```python
import torch

def _softmax(x, axis=-1):
    return torch.softmax(x, dim=axis)
def _gelu(x):
    return torch.nn.functional.gelu(x, approximate="tanh")
def _silu(x):
    return torch.nn.functional.silu(x)
def _sigmoid(x):
    return torch.sigmoid(x)
def _layernorm(x, axis=-1, eps=1e-5):
    m = x.mean(dim=axis, keepdim=True)
    v = x.var(dim=axis, keepdim=True, unbiased=False)
    return (x - m) / torch.sqrt(v + eps)
def _scatter_add(tgt, idx, src, axis=0):
    return tgt.clone().index_add_(axis, idx, src)
def _segment_sum(src, seg, n):
    out = torch.zeros((n,) + src.shape[1:], dtype=src.dtype)
    return out.index_add_(0, seg, src)
def _pad(x, pw):
    flat = tuple(v for pair in reversed(pw) for v in pair)
    return torch.nn.functional.pad(x, flat)

kernel_name = 'recurrent_scan__mha_block_cached__mixer_s_mlp__prompt'

def setup_inputs():

    _w = (lambda a: (lambda t: t.to(torch.bfloat16) if t.is_floating_point() else t)(torch.as_tensor(a)))
    def _A_setup():
        B, Sq, D = 1, 2048, 640
        H, Sk, Dh = 32, 2048, 192
        return {
            "x": _w((lambda *s: torch.randn(*s, dtype=torch.bfloat16) * 0.05)(B, Sq, D)),
            "k_cache": _w((lambda *s: torch.randn(*s, dtype=torch.bfloat16) * 0.8)(B, H, Sk, Dh)),
            "v_cache": _w((lambda *s: torch.randn(*s, dtype=torch.bfloat16) * 0.8)(B, H, Sk, Dh)),
            "wq": _w((lambda *s: torch.randn(*s, dtype=torch.bfloat16) * 0.8)(D, H * Dh) * (D ** -0.5)),
            "wo": _w((lambda *s: torch.randn(*s, dtype=torch.bfloat16) * 0.8)(H * Dh, D) * ((H * Dh) ** -0.5)),
        }
    _A_in = _A_setup()

    def _B_setup():
        B, S, D, F = 1, 2048, 640, 1536
        return {
            "x":  _w((lambda *s: torch.randn(*s, dtype=torch.bfloat16) * 0.05)(B, S, D)),
            "w1": _w((lambda *s: torch.randn(*s, dtype=torch.bfloat16) * 3.2)(D, D) * (D ** -0.5)),
            "w2": _w((lambda *s: torch.randn(*s, dtype=torch.bfloat16) * 3.2)(D, D) * (D ** -0.5)),
            "w3": _w((lambda *s: torch.randn(*s, dtype=torch.bfloat16) * 3.2)(D, F) * (D ** -0.5)),
            "w4": _w((lambda *s: torch.randn(*s, dtype=torch.bfloat16) * 3.2)(F, D) * (F ** -0.5)),
        }
    _B_in = _B_setup()

    return {
        "A_x": _A_in["x"],
        "A_k_cache": _A_in["k_cache"],
        "A_v_cache": _A_in["v_cache"],
        "A_wq": _A_in["wq"],
        "A_wo": _A_in["wo"],
        "B_x": _B_in["x"],
        "B_w1": _B_in["w1"],
        "B_w2": _B_in["w2"],
        "B_w3": _B_in["w3"],
        "B_w4": _B_in["w4"],
    }

def reference(A_x, A_k_cache, A_v_cache, A_wq, A_wo, B_x, B_w1, B_w2, B_w3, B_w4):
    def _A_ref(x, k_cache, v_cache, wq, wo):
        B, Sq, D = x.shape
        H, Dh = 32, 192
        q = (lambda x, *d: x.permute(*d))((x @ wq).reshape(B, Sq, H, Dh), 0, 2, 1, 3)
        scores = torch.einsum("bhqd,bhkd->bhqk", q, k_cache) * (Dh ** -0.5)
        p = _softmax(scores, axis=-1)
        o = torch.einsum("bhqk,bhkd->bhqd", p, v_cache)
        return (lambda x, *d: x.permute(*d))(o, 0, 2, 1, 3).reshape(B, Sq, H * Dh) @ wo

    def _B_ref(x, w1, w2, w3, w4):
        B, S, D = x.shape
        h = _gelu(_layernorm(x, axis=-1) @ w1)
        c = D // 4
        g0 = _pad(h[:, :, 0*c:1*c], ((0, 0), (1, 0), (0, 0)))[:, :S, :]
        g1 = _pad(h[:, :, 1*c:2*c], ((0, 0), (0, 1), (0, 0)))[:, 1:, :]
        g2 = _pad(h[:, :, 2*c:3*c], ((0, 0), (2, 0), (0, 0)))[:, :S, :]
        g3 = _pad(h[:, :, 3*c:   ], ((0, 0), (0, 2), (0, 0)))[:, 2:, :]
        hs = torch.concatenate([g0, g1, g2, g3], axis=-1) @ w2
        x1 = x + hs
        h2 = _gelu(_layernorm(x1, axis=-1) @ w3) @ w4
        return x1 + h2

    x = A_x
    xn = _layernorm(x, axis=-1)
    s = _A_ref(xn, A_k_cache, A_v_cache, A_wq, A_wo)
    g = _silu(_B_ref(xn, B_w1, B_w2, B_w3, B_w4))
    return x + s * g
```

```cpp
#include <hip/hip_runtime.h>
#include <hip/hip_bf16.h>
#include <stdint.h>

typedef unsigned short u16;
typedef __attribute__((ext_vector_type(8))) __bf16 bf16x8;
typedef __attribute__((ext_vector_type(16))) float f32x16;
typedef __attribute__((ext_vector_type(4))) unsigned u32x4;

constexpr int S = 2048, D = 640, H = 32, DH = 192, HD = H * DH, F = 1536;

__device__ __forceinline__ float bf2f(u16 x) { return __uint_as_float(((unsigned)x) << 16); }
__device__ __forceinline__ u16 f2bf(float f) { __bf16 b = (__bf16)f; return __builtin_bit_cast(u16, b); }
__device__ __forceinline__ float gelu_t(float x) {
  const float k = 0.7978845608028654f;
  float u = k * (x + 0.044715f * x * x * x);
  return x / (1.f + __expf(-2.f * u));
}

__global__ void ln_kernel(const u16* __restrict__ in, u16* __restrict__ o1, u16* __restrict__ o2) {
  int row = blockIdx.x * 4 + (threadIdx.x >> 6), lane = threadIdx.x & 63;
  float v[10]; float s = 0;
  for (int i = 0; i < 10; ++i) { v[i] = bf2f(in[row * D + lane + 64 * i]); s += v[i]; }
  for (int o = 32; o; o >>= 1) s += __shfl_xor(s, o);
  float m = s / D, q = 0;
  for (int i = 0; i < 10; ++i) { v[i] -= m; q += v[i] * v[i]; }
  for (int o = 32; o; o >>= 1) q += __shfl_xor(q, o);
  float r = rsqrtf(q / D + 1e-5f);
  s = 0;
  for (int i = 0; i < 10; ++i) { u16 b = f2bf(v[i] * r); o1[row * D + lane + 64 * i] = b; v[i] = bf2f(b); s += v[i]; }
  if (o2) {
    for (int o = 32; o; o >>= 1) s += __shfl_xor(s, o);
    m = s / D; q = 0;
    for (int i = 0; i < 10; ++i) { v[i] -= m; q += v[i] * v[i]; }
    for (int o = 32; o; o >>= 1) q += __shfl_xor(q, o);
    r = rsqrtf(q / D + 1e-5f);
    for (int i = 0; i < 10; ++i) o2[row * D + lane + 64 * i] = f2bf(v[i] * r);
  }
}

template <int EPI, int AMODE>
__global__ __launch_bounds__(256) void gemm_kernel(const u16* __restrict__ A, const u16* __restrict__ B, u16* __restrict__ C,
                                                   const u16* __restrict__ r1, const u16* __restrict__ r2, int M, int N, int K, float scale) {
  __shared__ u16 As[64][40];
  __shared__ u16 Bs[64][40];
  int tid = threadIdx.x, wid = tid >> 6, lane = tid & 63;
  int m0 = blockIdx.y * 64, n0 = blockIdx.x * 64;
  int wm = (wid >> 1) * 32, wn = (wid & 1) * 32;
  f32x16 acc = {};
  for (int k0 = 0; k0 < K; k0 += 32) {
    {
      int r = tid >> 2, c = (tid & 3) * 8;
      int gr = m0 + r;
      if (AMODE == 1) { int g = (k0 + c) / 160; int sh = g == 0 ? -1 : g == 1 ? 1 : g == 2 ? -2 : 2; gr += sh; }
      u32x4 v = {0, 0, 0, 0};
      if (gr >= 0 && gr < M) v = *(const u32x4*)(A + (size_t)gr * K + k0 + c);
      *(u32x4*)&As[r][c] = v;
      int kr = tid >> 3, nc = (tid & 7) * 8;
      u32x4 w = *(const u32x4*)(B + (size_t)(k0 + kr) * N + n0 + nc);
      const u16* wp = (const u16*)&w;
      for (int j = 0; j < 8; ++j) Bs[nc + j][kr] = wp[j];
    }
    __syncthreads();
    for (int ks = 0; ks < 2; ++ks) {
      bf16x8 a = *(const bf16x8*)&As[wm + (lane & 31)][ks * 16 + (lane >> 5) * 8];
      bf16x8 b = *(const bf16x8*)&Bs[wn + (lane & 31)][ks * 16 + (lane >> 5) * 8];
      acc = __builtin_amdgcn_mfma_f32_32x32x16_bf16(a, b, acc, 0, 0, 0);
    }
    __syncthreads();
  }
  for (int r = 0; r < 16; ++r) {
    int row = m0 + wm + (r & 3) + 8 * (r >> 2) + 4 * (lane >> 5), col = n0 + wn + (lane & 31);
    float v = acc[r];
    size_t idx = (size_t)row * N + col;
    if (EPI == 0) v *= scale;
    if (EPI == 1) v = gelu_t(v);
    if (EPI == 2) v += bf2f(r1[idx]);
    if (EPI == 3) { v += bf2f(r1[idx]); v = v / (1.f + __expf(-v)); }
    if (EPI == 4) { v = bf2f(r1[idx]) + v * bf2f(r2[idx]); }
    C[idx] = f2bf(v);
  }
}

__global__ __launch_bounds__(256) void attn_kernel(const u16* __restrict__ Q, const u16* __restrict__ Kc, const u16* __restrict__ Vc, u16* __restrict__ O) {
  __shared__ u16 Ks[32][200];
  __shared__ u16 Vt[192][40];
  int tid = threadIdx.x, wid = tid >> 6, lane = tid & 63, l32 = lane & 31, hi = lane >> 5;
  int h = blockIdx.y, q0 = blockIdx.x * 128 + wid * 32;
  bf16x8 qf[12];
#pragma unroll
  for (int d = 0; d < 12; ++d) qf[d] = *(const bf16x8*)(Q + (size_t)(q0 + l32) * HD + h * DH + d * 16 + hi * 8);
  f32x16 o[6];
#pragma unroll
  for (int i = 0; i < 6; ++i) o[i] = f32x16{};
  float m = -1e30f, l = 0;
  for (int k0 = 0; k0 < S; k0 += 32) {
    for (int i = tid; i < 32 * 24; i += 256) {
      int r = i / 24, c = (i % 24) * 8;
      *(u32x4*)&Ks[r][c] = *(const u32x4*)(Kc + ((size_t)h * S + k0 + r) * DH + c);
      u32x4 w = *(const u32x4*)(Vc + ((size_t)h * S + k0 + r) * DH + c);
      const u16* wp = (const u16*)&w;
      for (int j = 0; j < 8; ++j) Vt[c + j][r] = wp[j];
    }
    __syncthreads();
    f32x16 s = {};
#pragma unroll
    for (int d = 0; d < 12; ++d) {
      bf16x8 kf = *(const bf16x8*)&Ks[l32][d * 16 + hi * 8];
      s = __builtin_amdgcn_mfma_f32_32x32x16_bf16(kf, qf[d], s, 0, 0, 0);
    }
    float mx = s[0];
#pragma unroll
    for (int r = 1; r < 16; ++r) mx = fmaxf(mx, s[r]);
    mx = fmaxf(mx, __shfl_xor(mx, 32));
    float mn = fmaxf(m, mx), al = exp2f(m - mn); m = mn;
    float ps = 0;
    bf16x8 p[2];
#pragma unroll
    for (int r = 0; r < 16; ++r) { float e = exp2f(s[r] - mn); ps += e; p[r >> 3][r & 7] = (__bf16)e; }
    ps += __shfl_xor(ps, 32);
    l = l * al + ps;
#pragma unroll
    for (int i = 0; i < 6; ++i) {
#pragma unroll
      for (int r = 0; r < 16; ++r) o[i][r] *= al;
#pragma unroll
      for (int ks = 0; ks < 2; ++ks) {
        bf16x8 vf;
#pragma unroll
        for (int j = 0; j < 8; ++j) vf[j] = __builtin_bit_cast(__bf16, Vt[i * 32 + l32][16 * ks + 8 * (j >> 2) + 4 * hi + (j & 3)]);
        o[i] = __builtin_amdgcn_mfma_f32_32x32x16_bf16(vf, p[ks], o[i], 0, 0, 0);
      }
    }
    __syncthreads();
  }
  float il = 1.f / l;
#pragma unroll
  for (int i = 0; i < 6; ++i)
#pragma unroll
    for (int r = 0; r < 16; ++r) {
      int d = i * 32 + (r & 3) + 8 * (r >> 2) + 4 * hi;
      O[(size_t)(q0 + l32) * HD + h * DH + d] = f2bf(o[i][r] * il);
    }
}

extern "C" void kernel_launch(void* const* d_in, const int* in_sizes, int n_in, void* d_out, int out_size, void* d_ws, size_t ws_size,
                              hipStream_t stream) {
  const u16* x = (const u16*)d_in[0];
  const u16* kc = (const u16*)d_in[1];
  const u16* vc = (const u16*)d_in[2];
  const u16* wq = (const u16*)d_in[3];
  const u16* wo = (const u16*)d_in[4];
  const u16* w1 = (const u16*)d_in[6];
  const u16* w2 = (const u16*)d_in[7];
  const u16* w3 = (const u16*)d_in[8];
  const u16* w4 = (const u16*)d_in[9];
  u16* out = (u16*)d_out;
  char* ws = (char*)d_ws;
  u16* xn = (u16*)(ws);
  u16* xnn = (u16*)(ws + (4 << 20));
  u16* hh = (u16*)(ws + (8 << 20));
  u16* x1 = (u16*)(ws + (12 << 20));
  u16* x1n = (u16*)(ws + (16 << 20));
  u16* g = (u16*)(ws + (20 << 20));
  u16* u = (u16*)(ws + (24 << 20));
  u16* q = (u16*)(ws + (32 << 20));
  u16* o = (u16*)(ws + (64 << 20));
  const float qs = 0.07216878364870322f * 1.4426950408889634f;
  ln_kernel<<<S / 4, 256, 0, stream>>>(x, xn, xnn);
  gemm_kernel<0, 0><<<dim3(HD / 64, S / 64), 256, 0, stream>>>(xn, wq, q, nullptr, nullptr, S, HD, D, qs);
  attn_kernel<<<dim3(S / 128, H), 256, 0, stream>>>(q, kc, vc, o);
  gemm_kernel<1, 0><<<dim3(D / 64, S / 64), 256, 0, stream>>>(xnn, w1, hh, nullptr, nullptr, S, D, D, 1.f);
  gemm_kernel<2, 1><<<dim3(D / 64, S / 64), 256, 0, stream>>>(hh, w2, x1, xn, nullptr, S, D, D, 1.f);
  ln_kernel<<<S / 4, 256, 0, stream>>>(x1, x1n, nullptr);
  gemm_kernel<1, 0><<<dim3(F / 64, S / 64), 256, 0, stream>>>(x1n, w3, u, nullptr, nullptr, S, F, D, 1.f);
  gemm_kernel<3, 0><<<dim3(D / 64, S / 64), 256, 0, stream>>>(u, w4, g, x1, nullptr, S, D, F, 1.f);
  gemm_kernel<4, 0><<<dim3(D / 64, S / 64), 256, 0, stream>>>(o, wo, out, x, g, S, D, HD, 1.f);
}
```

```cpp
#include <hip/hip_runtime.h>

__global__ __launch_bounds__(256, 1) void fused_kernel(const void* x, const void* k, const void* v, const void* wq, const void* wo, const void* w3,
                                                       const void* w4, void* out, void* ws, const void* w1, const void* w2) {
  unsigned wid = __builtin_amdgcn_readfirstlane(threadIdx.x >> 6);
  unsigned bid = blockIdx.x;
  asm volatile(
"s_load_dwordx16 s[24:39], %0, 0x0\n"
"s_load_dwordx4 s[40:43], %0, 0x40\n"
"s_load_dwordx2 s[6:7], %0, 0x50\n"
"s_getpc_b64 s[16:17]\n"
"fused_code_head:\n"
"s_mov_b32 s52, %1\n"
"s_and_b32 s55, %2, 7\n"
"s_lshr_b32 s56, %2, 3\n"
"s_and_b32 s88, s56, 3\n"
"s_lshl2_add_u32 s53, s55, s88\n"
"s_lshr_b32 s54, s56, 2\n"
"v_mbcnt_lo_u32_b32 v248, -1, 0\n"
"v_mbcnt_hi_u32_b32 v248, -1, v248\n"
"v_lshrrev_b32 v40, 5, v248\n"
"v_and_b32 v41, 31, v248\n"
"v_mul_u32_u24 v42, 1280, v40\n"
"v_lshl_add_u32 v42, v41, 3, v42\n"
"s_lshl_b32 s4, s54, 5\n"
"s_add_u32 s4, s4, s53\n"
"s_lshl2_add_u32 s4, s4, s52\n"
"s_mul_i32 s88, s4, 2560\n"
"v_lshlrev_b32 v212, 2, v248\n"
"v_lshlrev_b32 v244, 4, v248\n"
"s_lshl_b32 s5, %2, 8\n"
"s_add_u32 s5, s5, 393216\n"
"s_mov_b32 s50, 2621440\n"
"s_mov_b32 s51, 0x20000\n"
"s_mov_b32 s10, 80740352\n"
"s_mov_b32 s11, 0x20000\n"
"s_waitcnt lgkmcnt(0)\n"
"s_mov_b32 s48, s24\n"
"s_and_b32 s49, s25, 0xffff\n"
"buffer_load_dwordx2 v[0:1], v42, s[48:51], s88 offen offset:0\n"
"buffer_load_dwordx2 v[2:3], v42, s[48:51], s88 offen offset:256\n"
"buffer_load_dwordx2 v[4:5], v42, s[48:51], s88 offen offset:512\n"
"buffer_load_dwordx2 v[6:7], v42, s[48:51], s88 offen offset:768\n"
"buffer_load_dwordx2 v[8:9], v42, s[48:51], s88 offen offset:1024\n"
"s_mov_b32 s8, s40\n"
"s_and_b32 s9, s41, 0xffff\n"
"s_min_u32 s90, s52, 1\n"
"s_lshl_b32 s90, s90, 30\n"
"s_add_u32 s5, s5, s90\n"
"v_and_b32 v48, 3, v41\n"
"v_lshrrev_b32 v49, 2, v41\n"
"v_lshl_add_u32 v48, v48, 3, v49\n"
"v_lshlrev_b32 v48, 8, v48\n"
"s_lshl_b32 s88, s54, 13\n"
"s_lshl2_add_u32 s88, s53, s88\n"
"v_add_u32 v48, s88, v48\n"
"s_lshl_b32 s88, s55, 8\n"
"s_lshl2_add_u32 s88, s56, s88\n"
"s_add_u32 s88, s88, 128\n"
"v_lshlrev_b32 v49, 11, v41\n"
"v_add_u32 v49, s88, v49\n"
"s_mov_b32 s88, 0\n"
"s_mov_b32 s89, -1\n"
"v_cndmask_b32 v48, v48, v49, s[88:89]\n"
"v_add_u32 v210, s90, v48\n"
"s_mov_b32 s74, 393216\n"
"s_mov_b32 s75, 458752\n"
"buffer_load_dword v44, v212, s[8:11], s5 offen sc1\n"
"v_lshrrev_b32 v46, 3, v248\n"
"v_and_b32 v47, 7, v248\n"
"v_and_b32 v48, 3, v46\n"
"v_lshrrev_b32 v49, 2, v46\n"
"v_lshl_add_u32 v235, v49, 4, v48\n"
"v_lshlrev_b32 v236, 4, v47\n"
"v_mul_u32_u24 v234, 1280, v235\n"
"v_add_u32 v234, v234, v236\n"
"v_lshrrev_b32 v48, 1, v248\n"
"v_mul_u32_u24 v48, 1280, v48\n"
"v_and_b32 v49, 1, v248\n"
"v_lshl_add_u32 v48, v49, 4, v48\n"
"s_mov_b64 s[44:45], s[42:43]\n"
"s_and_b32 s45, s45, 0xffff\n"
"s_mov_b32 s46, 819200\n"
"s_mov_b32 s47, 0x20000\n"
"s_lshl2_add_u32 s43, s56, s52\n"
"s_lshl3_add_u32 s88, s56, s52\n"
"s_mul_i32 s88, s88, 5120\n"
"s_lshl_b32 s89, s55, 7\n"
"s_add_u32 s88, s88, s89\n"
"s_sub_u32 s90, s43, 80\n"
"s_mul_i32 s89, s90, 40960\n"
"s_lshl_b32 s91, s55, 5\n"
"s_add_u32 s89, s89, s91\n"
"s_add_u32 s89, s89, 1024\n"
"s_lshl_b32 s87, s52, 10\n"
"s_add_u32 m0, s87, 16384\n"
"s_cmp_lt_u32 s56, 20\n"
"s_cselect_b32 s88, s88, s89\n"
"s_cselect_b64 vcc, -1, 0\n"
"v_cndmask_b32 v48, v48, v234, vcc\n"
"buffer_load_dwordx4 v48, s[44:47], s88 offen lds\n"
"s_mul_i32 s88, s53, 384\n"
"s_mov_b64 s[12:13], s[30:31]\n"
"s_add_u32 s12, s12, s88\n"
"s_addc_u32 s13, s13, 0\n"
"s_sub_u32 s12, s12, 1792\n"
"s_subb_u32 s13, s13, 0\n"
"s_and_b32 s13, s13, 0xffff\n"
"s_mov_b32 s14, 7854208\n"
"s_mov_b32 s15, 0x20000\n"
"v_mul_u32_u24 v48, 86, v248\n"
"v_lshrrev_b32 v48, 8, v48\n"
"v_mul_u32_u24 v48, 11904, v48\n"
"v_lshlrev_b32 v49, 7, v248\n"
"v_add_u32 v48, v48, v49\n"
"s_lshl2_add_u32 s88, s54, s52\n"
"s_mul_i32 s88, s88, 245760\n"
"s_mov_b32 s18, fused_code_end-fused_code_head\n"
"s_mov_b32 s19, 0x20000\n"
"s_lshl_b32 s43, s43, 13\n"
"buffer_load_dword v67, v49, s[16:19], s43 offen offset:2048\n"
"s_min_u32 s90, s52, 1\n"
"s_lshl_b32 s90, s90, 30\n"
"v_mov_b32 v52, 0\n"
"v_mov_b32 v53, 0\n"
"v_mov_b32 v54, 0\n"
"v_mov_b32 v55, 0\n"
"s_lshl_b32 s88, %2, 10\n"
"s_add_u32 s88, s88, s90\n"
"buffer_store_dwordx4 v[52:55], v244, s[8:11], s88 offen sc1\n"
"v_lshrrev_b32 v56, 4, v248\n"
"v_and_b32 v57, 15, v248\n"
"v_lshlrev_b32 v48, 4, v57\n"
"v_lshl_add_u32 v238, v56, 10, v48\n"
"v_cndmask_b32 v58, v244, v238, vcc\n"
"v_lshrrev_b32 v48, 2, v57\n"
"v_lshlrev_b32 v48, 7, v48\n"
"v_lshl_add_u32 v48, v56, 5, v48\n"
"v_and_b32 v60, 3, v57\n"
"v_lshl_add_u32 v48, v60, 3, v48\n"
"v_add_u32 v237, s87, v48\n"
"v_lshlrev_b32 v49, 3, v248\n"
"v_add_u32 v49, s87, v49\n"
"v_cndmask_b32 v59, v49, v237, vcc\n"
"s_mul_i32 s88, s56, 5120\n"
"s_lshl_b32 s89, s52, 8\n"
"s_add_u32 s88, s88, s89\n"
"s_lshr_b32 s89, s43, 13\n"
"s_mul_i32 s90, s89, 5120\n"
"s_sub_u32 s90, s90, 405504\n"
"s_cmp_lt_u32 s89, 100\n"
"s_cselect_b32 s90, s90, 1073741824\n"
"s_cmp_lt_u32 s56, 20\n"
"s_cselect_b32 s88, s88, s90\n"
"s_mul_i32 s89, s55, 102400\n"
"s_add_u32 s88, s88, s89\n"
"s_add_u32 s61, s88, 4194304\n"
"s_lshr_b32 s88, s4, 3\n"
"s_mul_i32 s88, s88, 20480\n"
"s_and_b32 s89, s53, 1\n"
"s_lshl_b32 s89, s89, 7\n"
"s_add_u32 s88, s88, s89\n"
"s_lshl1_add_u32 s88, s87, s88\n"
"s_add_u32 s62, s88, 1048576\n"
"s_add_u32 s63, s62, 8192\n"
"s_add_u32 s64, s62, 16384\n"
"s_cmp_lt_u32 s52, 2\n"
"s_cselect_b32 s64, s64, 1073741824\n"
"s_lshl_b32 s88, s4, 3\n"
"s_add_u32 s65, s88, 524288\n"
"s_add_u32 s4, s5, 65536\n"
"v_lshrrev_b32 v48, 3, v41\n"
"v_and_b32 v49, 3, v41\n"
"v_lshl_add_u32 v48, v48, 2, v49\n"
"v_lshlrev_b32 v48, 7, v48\n"
"v_bfe_u32 v49, v41, 2, 1\n"
"v_lshl_add_u32 v48, v49, 3, v48\n"
"v_lshl_add_u32 v48, v40, 4, v48\n"
"s_lshl_b32 s88, s52, 5\n"
"v_add_u32 v61, s88, v48\n"
"v_min_u32 v48, 1, v41\n"
"v_lshlrev_b32 v48, 30, v48\n"
"v_lshl_add_u32 v62, v40, 2, v48\n"
"v_lshlrev_b32 v48, 4, v47\n"
"v_lshl_add_u32 v63, v46, 8, v48\n"
"v_add_u32 v64, s87, v244\n"
"v_and_b32 v48, 6, v46\n"
"v_xor_b32 v48, v48, v47\n"
"v_lshlrev_b32 v226, 4, v48\n"
"v_mul_u32_u24 v48, 12288, v46\n"
"v_add_u32 v194, v48, v226\n"
"v_add_u32 v193, 896, v194\n"
"v_add_u32 v192, 1792, v194\n"
"s_mul_i32 s84, s52, 196608\n"
"s_add_u32 s85, s84, 98304\n"
"s_mul_i32 s57, s52, 6144\n"
"s_add_u32 m0, s57, 106496\n"
"s_nop 0\n"
"buffer_load_dwordx4 v192, s[12:15], s84 offen offset:0 lds\n"
"buffer_load_dwordx4 v193, s[12:15], s84 offen offset:1024 lds\n"
"buffer_load_dwordx4 v194, s[12:15], s84 offen offset:2048 lds\n"
"s_add_u32 s84, s84, 786432\n"
"s_add_u32 m0, s57, 109568\n"
"s_nop 0\n"
"buffer_load_dwordx4 v192, s[12:15], s85 offen offset:0 lds\n"
"buffer_load_dwordx4 v193, s[12:15], s85 offen offset:1024 lds\n"
"buffer_load_dwordx4 v194, s[12:15], s85 offen offset:2048 lds\n"
"s_add_u32 s85, s85, 786432\n"
"s_waitcnt vmcnt(9)\n"
"v_add_u32 v43, 1, v44\n"
"buffer_store_dword v43, v210, s[8:11], s75 offen sc1\n"
"v_lshlrev_b32 v10, 16, v0\n"
"v_and_b32 v11, 0xffff0000, v0\n"
"v_lshlrev_b32 v12, 16, v1\n"
"v_and_b32 v13, 0xffff0000, v1\n"
"v_lshlrev_b32 v14, 16, v2\n"
"v_and_b32 v15, 0xffff0000, v2\n"
"v_lshlrev_b32 v16, 16, v3\n"
"v_and_b32 v17, 0xffff0000, v3\n"
"v_lshlrev_b32 v18, 16, v4\n"
"v_and_b32 v19, 0xffff0000, v4\n"
"v_lshlrev_b32 v20, 16, v5\n"
"v_and_b32 v21, 0xffff0000, v5\n"
"v_lshlrev_b32 v22, 16, v6\n"
"v_and_b32 v23, 0xffff0000, v6\n"
"v_lshlrev_b32 v24, 16, v7\n"
"v_and_b32 v25, 0xffff0000, v7\n"
"v_lshlrev_b32 v26, 16, v8\n"
"v_and_b32 v27, 0xffff0000, v8\n"
"v_lshlrev_b32 v28, 16, v9\n"
"v_and_b32 v29, 0xffff0000, v9\n"
"v_pk_add_f32 v[30:31], v[10:11], v[12:13]\n"
"v_pk_add_f32 v[32:33], v[14:15], v[16:17]\n"
"v_pk_add_f32 v[30:31], v[30:31], v[18:19]\n"
"v_pk_add_f32 v[32:33], v[32:33], v[20:21]\n"
"v_pk_add_f32 v[30:31], v[30:31], v[22:23]\n"
"v_pk_add_f32 v[32:33], v[32:33], v[24:25]\n"
"v_pk_add_f32 v[30:31], v[30:31], v[26:27]\n"
"v_pk_add_f32 v[32:33], v[32:33], v[28:29]\n"
"v_pk_add_f32 v[30:31], v[30:31], v[32:33]\n"
"v_add_f32 v30, v30, v31\n"
"s_nop 1\n"
"v_add_f32_dpp v30, v30, v30 quad_perm:[1,0,3,2] row_mask:0xf bank_mask:0xf\n"
"s_nop 1\n"
"v_add_f32_dpp v30, v30, v30 quad_perm:[2,3,0,1] row_mask:0xf bank_mask:0xf\n"
"s_nop 1\n"
"v_add_f32_dpp v30, v30, v30 row_half_mirror row_mask:0xf bank_mask:0xf\n"
"s_nop 1\n"
"v_add_f32_dpp v30, v30, v30 row_mirror row_mask:0xf bank_mask:0xf\n"
"v_mov_b32 v31, v30\n"
"s_nop 1\n"
"v_permlane16_swap_b32 v30, v31\n"
"v_add_f32 v30, v30, v31\n"
"v_mul_f32 v30, 0xbacccccd, v30\n"
"v_mov_b32 v31, v30\n"
"v_pk_add_f32 v[10:11], v[10:11], v[30:31]\n"
"v_pk_add_f32 v[12:13], v[12:13], v[30:31]\n"
"v_pk_add_f32 v[14:15], v[14:15], v[30:31]\n"
"v_pk_add_f32 v[16:17], v[16:17], v[30:31]\n"
"v_pk_add_f32 v[18:19], v[18:19], v[30:31]\n"
"v_pk_add_f32 v[20:21], v[20:21], v[30:31]\n"
"v_pk_add_f32 v[22:23], v[22:23], v[30:31]\n"
"v_pk_add_f32 v[24:25], v[24:25], v[30:31]\n"
"v_pk_add_f32 v[26:27], v[26:27], v[30:31]\n"
"v_pk_add_f32 v[28:29], v[28:29], v[30:31]\n"
"v_pk_mul_f32 v[32:33], v[10:11], v[10:11]\n"
"v_pk_mul_f32 v[34:35], v[12:13], v[12:13]\n"
"v_pk_fma_f32 v[32:33], v[14:15], v[14:15], v[32:33]\n"
"v_pk_fma_f32 v[34:35], v[16:17], v[16:17], v[34:35]\n"
"v_pk_fma_f32 v[32:33], v[18:19], v[18:19], v[32:33]\n"
"v_pk_fma_f32 v[34:35], v[20:21], v[20:21], v[34:35]\n"
"v_pk_fma_f32 v[32:33], v[22:23], v[22:23], v[32:33]\n"
"v_pk_fma_f32 v[34:35], v[24:25], v[24:25], v[34:35]\n"
"v_pk_fma_f32 v[32:33], v[26:27], v[26:27], v[32:33]\n"
"v_pk_fma_f32 v[34:35], v[28:29], v[28:29], v[34:35]\n"
"v_pk_add_f32 v[32:33], v[32:33], v[34:35]\n"
"v_add_f32 v32, v32, v33\n"
"s_nop 1\n"
"v_add_f32_dpp v32, v32, v32 quad_perm:[1,0,3,2] row_mask:0xf bank_mask:0xf\n"
"s_nop 1\n"
"v_add_f32_dpp v32, v32, v32 quad_perm:[2,3,0,1] row_mask:0xf bank_mask:0xf\n"
"s_nop 1\n"
"v_add_f32_dpp v32, v32, v32 row_half_mirror row_mask:0xf bank_mask:0xf\n"
"s_nop 1\n"
"v_add_f32_dpp v32, v32, v32 row_mirror row_mask:0xf bank_mask:0xf\n"
"v_mov_b32 v33, v32\n"
"s_nop 1\n"
"v_permlane16_swap_b32 v32, v33\n"
"v_add_f32 v32, v32, v33\n"
"v_mul_f32 v32, 0x3acccccd, v32\n"
"v_add_f32 v33, 0x3727c5ac, v32\n"
"v_rsq_f32 v33, v33\n"
"s_nop 0\n"
"v_mul_f32 v34, 0x3dd53b95, v33\n"
"v_mov_b32 v35, v34\n"
"v_pk_mul_f32 v[10:11], v[10:11], v[34:35]\n"
"v_pk_mul_f32 v[12:13], v[12:13], v[34:35]\n"
"v_pk_mul_f32 v[14:15], v[14:15], v[34:35]\n"
"v_pk_mul_f32 v[16:17], v[16:17], v[34:35]\n"
"v_pk_mul_f32 v[18:19], v[18:19], v[34:35]\n"
"v_pk_mul_f32 v[20:21], v[20:21], v[34:35]\n"
"v_pk_mul_f32 v[22:23], v[22:23], v[34:35]\n"
"v_pk_mul_f32 v[24:25], v[24:25], v[34:35]\n"
"v_pk_mul_f32 v[26:27], v[26:27], v[34:35]\n"
"v_pk_mul_f32 v[28:29], v[28:29], v[34:35]\n"
"v_cvt_pk_bf16_f32 v0, v10, v11\n"
"v_cvt_pk_bf16_f32 v1, v12, v13\n"
"v_cvt_pk_bf16_f32 v2, v14, v15\n"
"v_cvt_pk_bf16_f32 v3, v16, v17\n"
"v_cvt_pk_bf16_f32 v4, v18, v19\n"
"v_cvt_pk_bf16_f32 v5, v20, v21\n"
"v_cvt_pk_bf16_f32 v6, v22, v23\n"
"v_cvt_pk_bf16_f32 v7, v24, v25\n"
"v_cvt_pk_bf16_f32 v8, v26, v27\n"
"v_cvt_pk_bf16_f32 v9, v28, v29\n"
"ds_write_b64 v61, v[0:1] offset:0\n"
"ds_write_b64 v61, v[2:3] offset:2048\n"
"ds_write_b64 v61, v[4:5] offset:4096\n"
"ds_write_b64 v61, v[6:7] offset:6144\n"
"ds_write_b64 v61, v[8:9] offset:8192\n"
"v_mul_f32 v32, v32, v33\n"
"v_mul_f32 v32, v32, v33\n"
"v_add_f32 v32, 0x3727c5ac, v32\n"
"v_rsq_f32 v32, v32\n"
"s_nop 0\n"
"v_mul_f32 v32, 0x4119ac27, v32\n"
"buffer_store_dword v32, v62, s[8:11], s65 offen sc1\n"
"s_waitcnt vmcnt(10)\n"
"ds_read_b64_tr_b16 v[68:69], v59 offset:16384\n"
"ds_read_b64_tr_b16 v[70:71], v59 offset:16896\n"
"s_waitcnt lgkmcnt(0)\n"
"s_barrier\n"
"ds_read_b128 v[72:75], v64 offset:0\n"
"ds_read_b128 v[76:79], v64 offset:4096\n"
"ds_read_b128 v[80:83], v64 offset:8192\n"
"buffer_store_dwordx4 v[68:71], v58, s[8:11], s61 offen sc1\n"
"s_waitcnt lgkmcnt(2)\n"
"buffer_store_dwordx4 v[72:75], v63, s[8:11], s62 offen sc1\n"
"s_waitcnt lgkmcnt(1)\n"
"buffer_store_dwordx4 v[76:79], v63, s[8:11], s63 offen sc1\n"
"s_waitcnt lgkmcnt(0)\n"
"buffer_store_dwordx4 v[80:83], v63, s[8:11], s64 offen sc1\n"
"buffer_load_dword v211, v212, s[8:11], s4 offen sc1\n"
"s_add_u32 m0, s57, 135168\n"
"s_nop 0\n"
"buffer_load_dwordx4 v192, s[12:15], s84 offen offset:0 lds\n"
"buffer_load_dwordx4 v193, s[12:15], s84 offen offset:1024 lds\n"
"buffer_load_dwordx4 v194, s[12:15], s84 offen offset:2048 lds\n"
"s_add_u32 s84, s84, 786432\n"
"s_add_u32 m0, s57, 138240\n"
"s_nop 0\n"
"buffer_load_dwordx4 v192, s[12:15], s85 offen offset:0 lds\n"
"buffer_load_dwordx4 v193, s[12:15], s85 offen offset:1024 lds\n"
"buffer_load_dwordx4 v194, s[12:15], s85 offen offset:2048 lds\n"
"s_add_u32 s85, s85, 786432\n"
"s_lshl2_add_u32 s88, s54, s52\n"
"s_lshl_b32 s88, s88, 2\n"
"s_add_u32 s89, s53, 0\n"
"s_and_b32 s89, s89, 3\n"
"s_add_u32 s76, s88, s89\n"
"s_mul_i32 s80, s76, 20480\n"
"s_add_u32 s80, s80, 1048576\n"
"s_add_u32 s89, s53, 1\n"
"s_and_b32 s89, s89, 3\n"
"s_add_u32 s77, s88, s89\n"
"s_mul_i32 s81, s77, 20480\n"
"s_add_u32 s81, s81, 1048576\n"
"s_add_u32 s89, s53, 2\n"
"s_and_b32 s89, s89, 3\n"
"s_add_u32 s78, s88, s89\n"
"s_mul_i32 s82, s78, 20480\n"
"s_add_u32 s82, s82, 1048576\n"
"s_add_u32 s89, s53, 3\n"
"s_and_b32 s89, s89, 3\n"
"s_add_u32 s79, s88, s89\n"
"s_mul_i32 s83, s79, 20480\n"
"s_add_u32 s83, s83, 1048576\n"
"s_and_b32 s88, s53, 3\n"
"s_cmp_lt_u32 s52, 2\n"
"s_cselect_b32 s88, s88, 4\n"
"s_and_b32 s89, s52, 1\n"
"s_mul_i32 s89, s89, 5\n"
"s_add_u32 s88, s88, s89\n"
"s_mul_i32 s89, s55, 100\n"
"s_add_u32 s88, s88, s89\n"
"s_lshl_b32 s88, s88, 10\n"
"s_add_u32 s86, s88, 4194304\n"
"v_bfe_u32 v48, v248, 2, 3\n"
"v_mul_u32_u24 v49, 3072, v40\n"
"v_lshl_add_u32 v49, v48, 7, v49\n"
"v_add_u32 v49, 49152, v49\n"
"v_and_b32 v48, 6, v48\n"
"v_lshl_add_u32 v49, v48, 4, v49\n"
"v_lshl_add_u32 v229, v60, 3, v49\n"
"v_and_b32 v48, 1, v60\n"
"v_lshl_add_u32 v49, v48, 4, v49\n"
"v_lshrrev_b32 v48, 1, v60\n"
"v_lshl_add_u32 v195, v48, 3, v49\n"
"v_xor_b32 v230, 32, v229\n"
"v_xor_b32 v196, 32, v195\n"
"v_xor_b32 v231, 64, v229\n"
"v_xor_b32 v197, 64, v195\n"
"v_xor_b32 v232, 96, v229\n"
"v_xor_b32 v198, 96, v195\n"
"v_add_u32 v199, 57344, v195\n"
"v_add_u32 v200, 57344, v196\n"
"v_add_u32 v201, 57344, v197\n"
"v_add_u32 v202, 57344, v198\n"
"v_add_u32 v203, 73728, v244\n"
"v_add_u32 v204, 131072, v244\n"
"s_mov_b32 s58, 0x8000\n"
"v_mov_b32 v219, 1073741824\n"
"s_waitcnt vmcnt(6)\n"
"s_barrier\n"
"s_cmp_lg_u32 s52, 0\n"
"s_cbranch_scc1 L_hs_done_%=\n"
"buffer_store_dword v211, v210, s[8:11], s74 offen sc1\n"
"v_mov_b32 v218, v211\n"
"buffer_load_dword v220, v212, s[8:11], s5 offen sc1\n"
"s_waitcnt vmcnt(0)\n"
"v_cmp_eq_u32 vcc, v220, v44\n"
"s_nop 4\n"
"s_cbranch_vccz L_hs_ok_%=\n"
"L_hs_%=:\n"
"buffer_load_dword v49, v212, s[8:11], s5 offen sc1\n"
"buffer_load_dword v211, v212, s[8:11], s4 offen sc1\n"
"s_waitcnt vmcnt(0)\n"
"v_cmp_eq_u32 vcc, v49, v44\n"
"s_sub_u32 s58, s58, 1\n"
"s_nop 3\n"
"s_cbranch_vccz L_hs_ok_%=\n"
"v_cmp_ne_u32 vcc, v218, v211\n"
"v_mov_b32 v218, v211\n"
"s_cmp_lg_u32 s58, 0\n"
"v_cndmask_b32 v48, v219, v210, vcc\n"
"buffer_store_dword v218, v48, s[8:11], s74 offen sc1\n"
"s_cbranch_scc1 L_hs_%=\n"
"L_hs_done_%=:\n"
"L_hs_ok_%=:\n"
"s_barrier\n"
"buffer_load_dwordx4 a[0:3], v244, s[8:11], s80 offen offset:0 sc1\n"
"buffer_load_dwordx4 a[4:7], v244, s[8:11], s80 offen offset:1024 sc1\n"
"buffer_load_dwordx4 a[8:11], v244, s[8:11], s81 offen offset:0 sc1\n"
"buffer_load_dwordx4 a[12:15], v244, s[8:11], s81 offen offset:1024 sc1\n"
"buffer_load_dwordx4 a[16:19], v244, s[8:11], s82 offen offset:0 sc1\n"
"buffer_load_dwordx4 a[20:23], v244, s[8:11], s82 offen offset:1024 sc1\n"
"buffer_load_dwordx4 a[24:27], v244, s[8:11], s83 offen offset:0 sc1\n"
"buffer_load_dwordx4 a[28:31], v244, s[8:11], s83 offen offset:1024 sc1\n"
"ds_read_b64_tr_b16 a[144:145], v199 offset:0\n"
"ds_read_b64_tr_b16 a[146:147], v199 offset:6144\n"
"ds_read_b64_tr_b16 a[148:149], v199 offset:12288\n"
"ds_read_b64_tr_b16 a[150:151], v199 offset:18432\n"
"ds_read_b64_tr_b16 a[152:153], v200 offset:0\n"
"ds_read_b64_tr_b16 a[154:155], v200 offset:6144\n"
"ds_read_b64_tr_b16 a[156:157], v200 offset:12288\n"
"ds_read_b64_tr_b16 a[158:159], v200 offset:18432\n"
"ds_read_b64_tr_b16 a[160:161], v201 offset:0\n"
"ds_read_b64_tr_b16 a[162:163], v201 offset:6144\n"
"ds_read_b64_tr_b16 a[164:165], v201 offset:12288\n"
"ds_read_b64_tr_b16 a[166:167], v201 offset:18432\n"
"s_waitcnt vmcnt(6)\n"
"s_waitcnt lgkmcnt(10)\n"
"v_mfma_f32_16x16x32_bf16 v[96:99], a[144:147], a[0:3], 0\n"
"ds_read_b64_tr_b16 a[168:169], v202 offset:0\n"
"buffer_load_dword v211, v212, s[8:11], s4 offen sc1\n"
"s_waitcnt lgkmcnt(9)\n"
"v_mfma_f32_16x16x32_bf16 v[96:99], a[148:151], a[4:7], v[96:99]\n"
"ds_read_b64_tr_b16 a[170:171], v202 offset:6144\n"
"s_waitcnt vmcnt(5)\n"
"v_mfma_f32_16x16x32_bf16 v[100:103], a[144:147], a[8:11], 0\n"
"ds_read_b64_tr_b16 a[172:173], v202 offset:12288\n"
"v_mfma_f32_16x16x32_bf16 v[100:103], a[148:151], a[12:15], v[100:103]\n"
"ds_read_b64_tr_b16 a[174:175], v202 offset:18432\n"
"s_waitcnt vmcnt(3)\n"
"v_mfma_f32_16x16x32_bf16 v[104:107], a[144:147], a[16:19], 0\n"
"s_mul_i32 s88, %2, 6554\n"
"v_mfma_f32_16x16x32_bf16 v[104:107], a[148:151], a[20:23], v[104:107]\n"
"s_lshr_b32 s88, s88, 16\n"
"s_add_u32 m0, s87, 131072\n"
"s_waitcnt vmcnt(1)\n"
"v_mfma_f32_16x16x32_bf16 v[108:111], a[144:147], a[24:27], 0\n"
"s_mul_i32 s89, s88, 10\n"
"buffer_load_dwordx4 v244, s[8:11], s86 offen sc1 lds\n"
"v_mfma_f32_16x16x32_bf16 v[108:111], a[148:151], a[28:31], v[108:111]\n"
"ds_read_b64_tr_b16 a[144:145], v199 offset:1024\n"
"s_add_u32 s86, s86, 10240\n"
"s_waitcnt lgkmcnt(3)\n"
"v_mfma_f32_16x16x32_bf16 v[112:115], a[152:155], a[0:3], 0\n"
"ds_read_b64_tr_b16 a[146:147], v199 offset:7168\n"
"v_mfma_f32_16x16x32_bf16 v[112:115], a[156:159], a[4:7], v[112:115]\n"
"ds_read_b64_tr_b16 a[148:149], v199 offset:13312\n"
"v_mfma_f32_16x16x32_bf16 v[116:119], a[152:155], a[8:11], 0\n"
"ds_read_b64_tr_b16 a[150:151], v199 offset:19456\n"
"v_mfma_f32_16x16x32_bf16 v[116:119], a[156:159], a[12:15], v[116:119]\n"
"s_sub_u32 s89, %2, s89\n"
"buffer_load_dwordx4 a[32:35], v244, s[8:11], s80 offen offset:2048 sc1\n"
"v_mfma_f32_16x16x32_bf16 v[120:123], a[152:155], a[16:19], 0\n"
"s_lshl3_add_u32 s90, s88, s52\n"
"v_mfma_f32_16x16x32_bf16 v[120:123], a[156:159], a[20:23], v[120:123]\n"
"s_mul_i32 s90, s90, 5120\n"
"v_mfma_f32_16x16x32_bf16 v[124:127], a[152:155], a[24:27], 0\n"
"s_lshl_b32 s91, s89, 7\n"
"v_mfma_f32_16x16x32_bf16 v[124:127], a[156:159], a[28:31], v[124:127]\n"
"ds_read_b64_tr_b16 a[152:153], v200 offset:1024\n"
"v_mfma_f32_16x16x32_bf16 v[128:131], a[160:163], a[0:3], 0\n"
"ds_read_b64_tr_b16 a[154:155], v200 offset:7168\n"
"buffer_load_dwordx4 a[36:39], v244, s[8:11], s80 offen offset:3072 sc1\n"
"v_mfma_f32_16x16x32_bf16 v[128:131], a[164:167], a[4:7], v[128:131]\n"
"ds_read_b64_tr_b16 a[156:157], v200 offset:13312\n"
"s_add_u32 s80, s80, 4096\n"
"v_mfma_f32_16x16x32_bf16 v[132:135], a[160:163], a[8:11], 0\n"
"ds_read_b64_tr_b16 a[158:159], v200 offset:19456\n"
"v_mfma_f32_16x16x32_bf16 v[132:135], a[164:167], a[12:15], v[132:135]\n"
"s_add_u32 s92, s90, s91\n"
"v_mfma_f32_16x16x32_bf16 v[136:139], a[160:163], a[16:19], 0\n"
"s_mul_i32 s90, s88, 40960\n"
"v_mfma_f32_16x16x32_bf16 v[136:139], a[164:167], a[20:23], v[136:139]\n"
"s_lshl_b32 s91, s89, 12\n"
"buffer_load_dwordx4 a[40:43], v244, s[8:11], s81 offen offset:2048 sc1\n"
"v_mfma_f32_16x16x32_bf16 v[140:143], a[160:163], a[24:27], 0\n"
"s_add_u32 s90, s90, s91\n"
"v_mfma_f32_16x16x32_bf16 v[140:143], a[164:167], a[28:31], v[140:143]\n"
"ds_read_b64_tr_b16 a[160:161], v201 offset:1024\n"
"v_mfma_f32_16x16x32_bf16 v[144:147], a[168:171], a[0:3], 0\n"
"ds_read_b64_tr_b16 a[162:163], v201 offset:7168\n"
"s_waitcnt lgkmcnt(2)\n"
"v_mfma_f32_16x16x32_bf16 v[144:147], a[172:175], a[4:7], v[144:147]\n"
"ds_read_b64_tr_b16 a[164:165], v201 offset:13312\n"
"v_mfma_f32_16x16x32_bf16 v[148:151], a[168:171], a[8:11], 0\n"
"ds_read_b64_tr_b16 a[166:167], v201 offset:19456\n"
"buffer_load_dwordx4 a[44:47], v244, s[8:11], s81 offen offset:3072 sc1\n"
"v_mfma_f32_16x16x32_bf16 v[148:151], a[172:175], a[12:15], v[148:151]\n"
"s_lshl_b32 s91, s52, 8\n"
"s_add_u32 s81, s81, 4096\n"
"v_mfma_f32_16x16x32_bf16 v[152:155], a[168:171], a[16:19], 0\n"
"s_add_u32 s93, s90, s91\n"
"v_mfma_f32_16x16x32_bf16 v[152:155], a[172:175], a[20:23], v[152:155]\n"
"s_mul_i32 s88, %2, 2731\n"
"v_mfma_f32_16x16x32_bf16 v[156:159], a[168:171], a[24:27], 0\n"
"s_lshr_b32 s88, s88, 16\n"
"v_mfma_f32_16x16x32_bf16 v[156:159], a[172:175], a[28:31], v[156:159]\n"
"ds_read_b64_tr_b16 a[168:169], v202 offset:1024\n"
"buffer_load_dwordx4 a[48:51], v244, s[8:11], s82 offen offset:2048 sc1\n"
"v_mfma_f32_16x16x32_bf16 v[160:163], a[144:147], a[0:3], 0\n"
"ds_read_b64_tr_b16 a[170:171], v202 offset:7168\n"
"v_mfma_f32_16x16x32_bf16 v[160:163], a[148:151], a[4:7], v[160:163]\n"
"ds_read_b64_tr_b16 a[172:173], v202 offset:13312\n"
"v_mfma_f32_16x16x32_bf16 v[164:167], a[144:147], a[8:11], 0\n"
"ds_read_b64_tr_b16 a[174:175], v202 offset:19456\n"
"v_mfma_f32_16x16x32_bf16 v[164:167], a[148:151], a[12:15], v[164:167]\n"
"s_mul_i32 s89, s88, 24\n"
"v_mfma_f32_16x16x32_bf16 v[168:171], a[144:147], a[16:19], 0\n"
"s_sub_u32 s89, %2, s89\n"
"buffer_load_dwordx4 a[52:55], v244, s[8:11], s82 offen offset:3072 sc1\n"
"v_mfma_f32_16x16x32_bf16 v[168:171], a[148:151], a[20:23], v[168:171]\n"
"s_lshl3_add_u32 s90, s88, s52\n"
"s_add_u32 s82, s82, 4096\n"
"v_mfma_f32_16x16x32_bf16 v[172:175], a[144:147], a[24:27], 0\n"
"s_mul_i32 s90, s90, 12288\n"
"v_mfma_f32_16x16x32_bf16 v[172:175], a[148:151], a[28:31], v[172:175]\n"
"ds_read_b64_tr_b16 a[144:145], v199 offset:2048\n"
"v_mfma_f32_16x16x32_bf16 v[176:179], a[152:155], a[0:3], 0\n"
"ds_read_b64_tr_b16 a[146:147], v199 offset:8192\n"
"v_mfma_f32_16x16x32_bf16 v[176:179], a[156:159], a[4:7], v[176:179]\n"
"ds_read_b64_tr_b16 a[148:149], v199 offset:14336\n"
"buffer_load_dwordx4 a[56:59], v244, s[8:11], s83 offen offset:2048 sc1\n"
"v_mfma_f32_16x16x32_bf16 v[180:183], a[152:155], a[8:11], 0\n"
"ds_read_b64_tr_b16 a[150:151], v199 offset:20480\n"
"v_mfma_f32_16x16x32_bf16 v[180:183], a[156:159], a[12:15], v[180:183]\n"
"s_lshl_b32 s91, s89, 7\n"
"v_mfma_f32_16x16x32_bf16 v[184:187], a[152:155], a[16:19], 0\n"
"s_add_u32 s94, s90, s91\n"
"v_mfma_f32_16x16x32_bf16 v[184:187], a[156:159], a[20:23], v[184:187]\n"
"s_mul_i32 s90, s88, 98304\n"
"v_mfma_f32_16x16x32_bf16 v[188:191], a[152:155], a[24:27], 0\n"
"s_lshl_b32 s91, s89, 12\n"
"buffer_load_dwordx4 a[60:63], v244, s[8:11], s83 offen offset:3072 sc1\n"
"v_mfma_f32_16x16x32_bf16 v[188:191], a[156:159], a[28:31], v[188:191]\n"
"ds_read_b64_tr_b16 a[152:153], v200 offset:2048\n"
"s_add_u32 s83, s83, 4096\n"
"s_waitcnt lgkmcnt(2)\n"
"v_mfma_f32_16x16x32_bf16 v[0:3], a[160:163], a[0:3], 0\n"
"ds_read_b64_tr_b16 a[154:155], v200 offset:8192\n"
"v_mfma_f32_16x16x32_bf16 v[0:3], a[164:167], a[4:7], v[0:3]\n"
"ds_read_b64_tr_b16 a[156:157], v200 offset:14336\n"
"v_mfma_f32_16x16x32_bf16 v[4:7], a[160:163], a[8:11], 0\n"
"ds_read_b64_tr_b16 a[158:159], v200 offset:20480\n"
"v_mfma_f32_16x16x32_bf16 v[4:7], a[164:167], a[12:15], v[4:7]\n"
"s_add_u32 s90, s90, s91\n"
"s_add_u32 m0, s57, 49152\n"
"v_mfma_f32_16x16x32_bf16 v[8:11], a[160:163], a[16:19], 0\n"
"s_lshl_b32 s91, s52, 8\n"
"buffer_load_dwordx4 v192, s[12:15], s84 offen offset:0 lds\n"
"v_mfma_f32_16x16x32_bf16 v[8:11], a[164:167], a[20:23], v[8:11]\n"
"s_add_u32 s95, s90, s91\n"
"v_mfma_f32_16x16x32_bf16 v[12:15], a[160:163], a[24:27], 0\n"
"s_mov_b32 s44, s6\n"
"v_mfma_f32_16x16x32_bf16 v[12:15], a[164:167], a[28:31], v[12:15]\n"
"ds_read_b64_tr_b16 a[160:161], v201 offset:2048\n"
"v_mfma_f32_16x16x32_bf16 v[16:19], a[168:171], a[0:3], 0\n"
"ds_read_b64_tr_b16 a[162:163], v201 offset:8192\n"
"v_mfma_f32_16x16x32_bf16 v[16:19], a[172:175], a[4:7], v[16:19]\n"
"ds_read_b64_tr_b16 a[164:165], v201 offset:14336\n"
"buffer_load_dwordx4 v193, s[12:15], s84 offen offset:1024 lds\n"
"v_mfma_f32_16x16x32_bf16 v[20:23], a[168:171], a[8:11], 0\n"
"ds_read_b64_tr_b16 a[166:167], v201 offset:20480\n"
"v_mfma_f32_16x16x32_bf16 v[20:23], a[172:175], a[12:15], v[20:23]\n"
"s_and_b32 s45, s7, 0xffff\n"
"v_mfma_f32_16x16x32_bf16 v[24:27], a[168:171], a[16:19], 0\n"
"s_add_u32 s48, s8, 17825792\n"
"s_addc_u32 s49, s9, 0\n"
"v_mfma_f32_16x16x32_bf16 v[24:27], a[172:175], a[20:23], v[24:27]\n"
"s_mov_b32 s50, 819200\n"
"v_mfma_f32_16x16x32_bf16 v[28:31], a[168:171], a[24:27], 0\n"
"buffer_load_dwordx4 v194, s[12:15], s84 offen offset:2048 lds\n"
"v_mfma_f32_16x16x32_bf16 v[28:31], a[172:175], a[28:31], v[28:31]\n"
"ds_read_b64_tr_b16 a[168:169], v202 offset:2048\n"
"s_add_u32 s84, s84, 786432\n"
"v_mfma_f32_16x16x32_bf16 v[32:35], a[144:147], a[0:3], 0\n"
"ds_read_b64_tr_b16 a[170:171], v202 offset:8192\n"
"s_waitcnt lgkmcnt(2)\n"
"v_mfma_f32_16x16x32_bf16 v[32:35], a[148:151], a[4:7], v[32:35]\n"
"ds_read_b64_tr_b16 a[172:173], v202 offset:14336\n"
"v_mfma_f32_16x16x32_bf16 v[36:39], a[144:147], a[8:11], 0\n"
"ds_read_b64_tr_b16 a[174:175], v202 offset:20480\n"
"v_mfma_f32_16x16x32_bf16 v[36:39], a[148:151], a[12:15], v[36:39]\n"
"s_add_u32 m0, s57, 52224\n"
"v_mfma_f32_16x16x32_bf16 v[40:43], a[144:147], a[16:19], 0\n"
"buffer_load_dwordx4 v192, s[12:15], s85 offen offset:0 lds\n"
"v_mfma_f32_16x16x32_bf16 v[40:43], a[148:151], a[20:23], v[40:43]\n"
"v_mfma_f32_16x16x32_bf16 v[44:47], a[144:147], a[24:27], 0\n"
"v_mfma_f32_16x16x32_bf16 v[44:47], a[148:151], a[28:31], v[44:47]\n"
"s_waitcnt vmcnt(12) lgkmcnt(0)\n"
"s_barrier\n"
"v_mfma_f32_16x16x32_bf16 v[48:51], a[152:155], a[0:3], 0\n"
"ds_read_b128 a[176:179], v204 offset:0\n"
"ds_read_b128 a[180:183], v204 offset:1024\n"
"v_mfma_f32_16x16x32_bf16 v[48:51], a[156:159], a[4:7], v[48:51]\n"
"ds_read_b128 a[184:187], v204 offset:2048\n"
"ds_read_b128 a[188:191], v204 offset:3072\n"
"v_mfma_f32_16x16x32_bf16 v[52:55], a[152:155], a[8:11], 0\n"
"ds_read_b64_tr_b16 a[144:145], v199 offset:28672\n"
"buffer_load_dwordx4 v193, s[12:15], s85 offen offset:1024 lds\n"
"v_mfma_f32_16x16x32_bf16 v[52:55], a[156:159], a[12:15], v[52:55]\n"
"ds_read_b64_tr_b16 a[146:147], v199 offset:34816\n"
"v_mfma_f32_16x16x32_bf16 v[56:59], a[152:155], a[16:19], 0\n"
"ds_read_b64_tr_b16 a[148:149], v199 offset:40960\n"
"v_mfma_f32_16x16x32_bf16 v[56:59], a[156:159], a[20:23], v[56:59]\n"
"ds_read_b64_tr_b16 a[150:151], v199 offset:47104\n"
"v_mfma_f32_16x16x32_bf16 v[60:63], a[152:155], a[24:27], 0\n"
"v_mfma_f32_16x16x32_bf16 v[60:63], a[156:159], a[28:31], v[60:63]\n"
"ds_read_b64_tr_b16 a[152:153], v200 offset:28672\n"
"buffer_load_dwordx4 v194, s[12:15], s85 offen offset:2048 lds\n"
"v_mfma_f32_16x16x32_bf16 v[64:67], a[160:163], a[0:3], 0\n"
"ds_read_b64_tr_b16 a[154:155], v200 offset:34816\n"
"s_add_u32 s85, s85, 786432\n"
"v_mfma_f32_16x16x32_bf16 v[64:67], a[164:167], a[4:7], v[64:67]\n"
"ds_read_b64_tr_b16 a[156:157], v200 offset:40960\n"
"v_cmp_ne_u32 vcc, v211, v218\n"
"s_nop 1\n"
"v_cndmask_b32 v210, v219, v210, vcc\n"
"v_mfma_f32_16x16x32_bf16 v[68:71], a[160:163], a[8:11], 0\n"
"ds_read_b64_tr_b16 a[158:159], v200 offset:47104\n"
"v_mfma_f32_16x16x32_bf16 v[68:71], a[164:167], a[12:15], v[68:71]\n"
"s_mul_i32 s88, s53, 786432\n"
"v_mfma_f32_16x16x32_bf16 v[72:75], a[160:163], a[16:19], 0\n"
"s_mov_b64 s[16:17], s[26:27]\n"
"s_add_u32 m0, s87, 159744\n"
"v_mfma_f32_16x16x32_bf16 v[72:75], a[164:167], a[20:23], v[72:75]\n"
"s_add_u32 s16, s16, s88\n"
"s_addc_u32 s17, s17, 0\n"
"buffer_load_dwordx4 v244, s[8:11], s86 offen sc1 lds\n"
"v_mfma_f32_16x16x32_bf16 v[76:79], a[160:163], a[24:27], 0\n"
"s_sub_u32 s16, s16, 1792\n"
"s_subb_u32 s17, s17, 0\n"
"s_add_u32 s86, s86, 10240\n"
"v_mfma_f32_16x16x32_bf16 v[76:79], a[164:167], a[28:31], v[76:79]\n"
"ds_read_b64_tr_b16 a[160:161], v201 offset:28672\n"
"s_waitcnt lgkmcnt(2)\n"
"v_mfma_f32_16x16x32_bf16 a[120:123], a[176:179], a[0:3], 0\n"
"ds_read_b64_tr_b16 a[162:163], v201 offset:34816\n"
"v_mfma_f32_16x16x32_bf16 a[120:123], a[180:183], a[4:7], a[120:123]\n"
"ds_read_b64_tr_b16 a[164:165], v201 offset:40960\n"
"v_mfma_f32_16x16x32_bf16 a[124:127], a[176:179], a[8:11], 0\n"
"ds_read_b64_tr_b16 a[166:167], v201 offset:47104\n"
"buffer_load_dwordx4 a[64:67], v244, s[8:11], s80 offen offset:0 sc1\n"
"v_mfma_f32_16x16x32_bf16 a[124:127], a[180:183], a[12:15], a[124:127]\n"
"s_and_b32 s17, s17, 0xffff\n"
"v_mfma_f32_16x16x32_bf16 a[128:131], a[176:179], a[16:19], 0\n"
"s_mov_b32 s18, 788224\n"
"v_mfma_f32_16x16x32_bf16 a[128:131], a[180:183], a[20:23], a[128:131]\n"
"s_mov_b32 s19, 0x20000\n"
"v_mfma_f32_16x16x32_bf16 a[132:135], a[176:179], a[24:27], 0\n"
"s_mov_b64 s[20:21], s[28:29]\n"
"v_mfma_f32_16x16x32_bf16 a[132:135], a[180:183], a[28:31], a[132:135]\n"
"s_add_u32 s20, s20, s88\n"
"s_addc_u32 s21, s21, 0\n"
"buffer_load_dwordx4 a[68:71], v244, s[8:11], s80 offen offset:1024 sc1\n"
"v_mfma_f32_16x16x32_bf16 a[136:139], a[184:187], a[0:3], 0\n"
"s_sub_u32 s20, s20, 1792\n"
"s_subb_u32 s21, s21, 0\n"
"v_mfma_f32_16x16x32_bf16 a[136:139], a[188:191], a[4:7], a[136:139]\n"
"s_and_b32 s21, s21, 0xffff\n"
"v_mfma_f32_16x16x32_bf16 v[80:83], a[168:171], a[0:3], 0\n"
"s_mov_b32 s22, 788224\n"
"v_mfma_f32_16x16x32_bf16 v[80:83], a[172:175], a[4:7], v[80:83]\n"
"s_mov_b32 s23, 0x20000\n"
"v_mfma_f32_16x16x32_bf16 v[84:87], a[168:171], a[8:11], 0\n"
"s_mov_b32 s61, s57\n"
"buffer_load_dwordx4 a[72:75], v244, s[8:11], s81 offen offset:0 sc1\n"
"v_mfma_f32_16x16x32_bf16 v[84:87], a[172:175], a[12:15], v[84:87]\n"
"s_add_u32 s62, s57, 3072\n"
"v_mfma_f32_16x16x32_bf16 v[88:91], a[168:171], a[16:19], 0\n"
"s_add_u32 s63, s57, 0\n"
"v_mfma_f32_16x16x32_bf16 v[88:91], a[172:175], a[20:23], v[88:91]\n"
"s_add_u32 s67, s57, 49152\n"
"v_mfma_f32_16x16x32_bf16 v[92:95], a[168:171], a[24:27], 0\n"
"s_add_u32 s64, s57, 3072\n"
"v_mfma_f32_16x16x32_bf16 v[92:95], a[172:175], a[28:31], v[92:95]\n"
"s_add_u32 s68, s57, 52224\n"
"buffer_store_dword v211, v210, s[8:11], s74 offen sc1\n"
"s_waitcnt vmcnt(11)\n"
"v_mfma_f32_16x16x32_bf16 v[96:99], a[144:147], a[32:35], v[96:99]\n"
"ds_read_b64_tr_b16 a[168:169], v202 offset:28672\n"
"v_mfma_f32_16x16x32_bf16 v[96:99], a[148:151], a[36:39], v[96:99]\n"
"ds_read_b64_tr_b16 a[170:171], v202 offset:34816\n"
"v_mfma_f32_16x16x32_bf16 v[100:103], a[144:147], a[40:43], v[100:103]\n"
"ds_read_b64_tr_b16 a[172:173], v202 offset:40960\n"
"v_mfma_f32_16x16x32_bf16 v[100:103], a[148:151], a[44:47], v[100:103]\n"
"ds_read_b64_tr_b16 a[174:175], v202 offset:47104\n"
"v_mfma_f32_16x16x32_bf16 v[104:107], a[144:147], a[48:51], v[104:107]\n"
"s_add_u32 s65, s57, 24576\n"
"buffer_load_dwordx4 a[76:79], v244, s[8:11], s81 offen offset:1024 sc1\n"
"v_mfma_f32_16x16x32_bf16 v[104:107], a[148:151], a[52:55], v[104:107]\n"
"s_add_u32 s69, s57, 73728\n"
"v_mfma_f32_16x16x32_bf16 v[108:111], a[144:147], a[56:59], v[108:111]\n"
"s_add_u32 s66, s57, 27648\n"
"v_mfma_f32_16x16x32_bf16 v[108:111], a[148:151], a[60:63], v[108:111]\n"
"ds_read_b64_tr_b16 a[144:145], v199 offset:29696\n"
"v_mfma_f32_16x16x32_bf16 v[112:115], a[152:155], a[32:35], v[112:115]\n"
"ds_read_b64_tr_b16 a[146:147], v199 offset:35840\n"
"s_waitcnt lgkmcnt(3)\n"
"v_mfma_f32_16x16x32_bf16 v[112:115], a[156:159], a[36:39], v[112:115]\n"
"ds_read_b64_tr_b16 a[148:149], v199 offset:41984\n"
"buffer_load_dwordx4 a[80:83], v244, s[8:11], s82 offen offset:0 sc1\n"
"v_mfma_f32_16x16x32_bf16 v[116:119], a[152:155], a[40:43], v[116:119]\n"
"ds_read_b64_tr_b16 a[150:151], v199 offset:48128\n"
"v_mfma_f32_16x16x32_bf16 v[116:119], a[156:159], a[44:47], v[116:119]\n"
"s_add_u32 s70, s57, 76800\n"
"v_mfma_f32_16x16x32_bf16 v[120:123], a[152:155], a[48:51], v[120:123]\n"
"s_mov_b32 s60, 0\n"
"v_mfma_f32_16x16x32_bf16 v[120:123], a[156:159], a[52:55], v[120:123]\n"
"v_lshrrev_b32 v220, 3, v248\n"
"v_mfma_f32_16x16x32_bf16 v[124:127], a[152:155], a[56:59], v[124:127]\n"
"v_mul_u32_u24 v220, 384, v220\n"
"buffer_load_dwordx4 a[84:87], v244, s[8:11], s82 offen offset:1024 sc1\n"
"v_mfma_f32_16x16x32_bf16 v[124:127], a[156:159], a[60:63], v[124:127]\n"
"ds_read_b64_tr_b16 a[152:153], v200 offset:29696\n"
"v_mfma_f32_16x16x32_bf16 v[128:131], a[160:163], a[32:35], v[128:131]\n"
"ds_read_b64_tr_b16 a[154:155], v200 offset:35840\n"
"v_mfma_f32_16x16x32_bf16 v[128:131], a[164:167], a[36:39], v[128:131]\n"
"ds_read_b64_tr_b16 a[156:157], v200 offset:41984\n"
"v_mfma_f32_16x16x32_bf16 v[132:135], a[160:163], a[40:43], v[132:135]\n"
"ds_read_b64_tr_b16 a[158:159], v200 offset:48128\n"
"v_mfma_f32_16x16x32_bf16 v[132:135], a[164:167], a[44:47], v[132:135]\n"
"v_add_u32 v226, v220, v226\n"
"buffer_load_dwordx4 a[88:91], v244, s[8:11], s83 offen offset:0 sc1\n"
"v_mfma_f32_16x16x32_bf16 v[136:139], a[160:163], a[48:51], v[136:139]\n"
"v_add_u32 v225, 896, v226\n"
"v_mfma_f32_16x16x32_bf16 v[136:139], a[164:167], a[52:55], v[136:139]\n"
"v_add_u32 v224, 1792, v226\n"
"v_mfma_f32_16x16x32_bf16 v[140:143], a[160:163], a[56:59], v[140:143]\n"
"v_bfe_u32 v220, v248, 3, 1\n"
"v_mfma_f32_16x16x32_bf16 v[140:143], a[164:167], a[60:63], v[140:143]\n"
"ds_read_b64_tr_b16 a[160:161], v201 offset:29696\n"
"v_mfma_f32_16x16x32_bf16 v[144:147], a[168:171], a[32:35], v[144:147]\n"
"ds_read_b64_tr_b16 a[162:163], v201 offset:35840\n"
"buffer_load_dwordx4 a[92:95], v244, s[8:11], s83 offen offset:1024 sc1\n"
"s_waitcnt lgkmcnt(2)\n"
"v_mfma_f32_16x16x32_bf16 v[144:147], a[172:175], a[36:39], v[144:147]\n"
"ds_read_b64_tr_b16 a[164:165], v201 offset:41984\n"
"v_mfma_f32_16x16x32_bf16 v[148:151], a[168:171], a[40:43], v[148:151]\n"
"ds_read_b64_tr_b16 a[166:167], v201 offset:48128\n"
"v_mfma_f32_16x16x32_bf16 v[148:151], a[172:175], a[44:47], v[148:151]\n"
"v_mul_u32_u24 v220, 3072, v220\n"
"v_mfma_f32_16x16x32_bf16 v[152:155], a[168:171], a[48:51], v[152:155]\n"
"v_lshl_add_u32 v220, v236, 3, v220\n"
"v_mfma_f32_16x16x32_bf16 v[152:155], a[172:175], a[52:55], v[152:155]\n"
"v_lshrrev_b32 v222, 4, v248\n"
"s_add_u32 m0, s57, 77824\n"
"v_mfma_f32_16x16x32_bf16 v[156:159], a[168:171], a[56:59], v[156:159]\n"
"v_and_b32 v223, 15, v248\n"
"buffer_load_dwordx4 v192, s[12:15], s84 offen offset:0 lds\n"
"v_mfma_f32_16x16x32_bf16 v[156:159], a[172:175], a[60:63], v[156:159]\n"
"ds_read_b64_tr_b16 a[168:169], v202 offset:29696\n"
"v_mfma_f32_16x16x32_bf16 v[160:163], a[144:147], a[32:35], v[160:163]\n"
"ds_read_b64_tr_b16 a[170:171], v202 offset:35840\n"
"v_mfma_f32_16x16x32_bf16 v[160:163], a[148:151], a[36:39], v[160:163]\n"
"ds_read_b64_tr_b16 a[172:173], v202 offset:41984\n"
"v_mfma_f32_16x16x32_bf16 v[164:167], a[144:147], a[40:43], v[164:167]\n"
"ds_read_b64_tr_b16 a[174:175], v202 offset:48128\n"
"v_mfma_f32_16x16x32_bf16 v[164:167], a[148:151], a[44:47], v[164:167]\n"
"v_and_b32 v221, 6, v248\n"
"buffer_load_dwordx4 v193, s[12:15], s84 offen offset:1024 lds\n"
"v_mfma_f32_16x16x32_bf16 v[168:171], a[144:147], a[48:51], v[168:171]\n"
"v_xor_b32 v221, v221, v222\n"
"v_mfma_f32_16x16x32_bf16 v[168:171], a[148:151], a[52:55], v[168:171]\n"
"v_lshl_add_u32 v227, v221, 4, v220\n"
"v_mfma_f32_16x16x32_bf16 v[172:175], a[144:147], a[56:59], v[172:175]\n"
"v_xor_b32 v228, 64, v227\n"
"v_mfma_f32_16x16x32_bf16 v[172:175], a[148:151], a[60:63], v[172:175]\n"
"ds_read_b64_tr_b16 a[144:145], v199 offset:30720\n"
"v_mfma_f32_16x16x32_bf16 v[176:179], a[152:155], a[32:35], v[176:179]\n"
"ds_read_b64_tr_b16 a[146:147], v199 offset:36864\n"
"buffer_load_dwordx4 v194, s[12:15], s84 offen offset:2048 lds\n"
"v_mfma_f32_16x16x32_bf16 v[176:179], a[156:159], a[36:39], v[176:179]\n"
"ds_read_b64_tr_b16 a[148:149], v199 offset:43008\n"
"s_add_u32 s84, s84, 786432\n"
"v_mfma_f32_16x16x32_bf16 v[180:183], a[152:155], a[40:43], v[180:183]\n"
"ds_read_b64_tr_b16 a[150:151], v199 offset:49152\n"
"v_mfma_f32_16x16x32_bf16 v[180:183], a[156:159], a[44:47], v[180:183]\n"
"v_add_u32 v250, 4096, v244\n"
"v_mfma_f32_16x16x32_bf16 v[184:187], a[152:155], a[48:51], v[184:187]\n"
"v_add_u32 v251, 0x10000, v244\n"
"v_mfma_f32_16x16x32_bf16 v[184:187], a[156:159], a[52:55], v[184:187]\n"
"v_add_u32 v252, 0x20000, v244\n"
"s_add_u32 m0, s57, 80896\n"
"v_mfma_f32_16x16x32_bf16 v[188:191], a[152:155], a[56:59], v[188:191]\n"
"v_and_b32 v233, 1, v248\n"
"buffer_load_dwordx4 v192, s[12:15], s85 offen offset:0 lds\n"
"v_mfma_f32_16x16x32_bf16 v[188:191], a[156:159], a[60:63], v[188:191]\n"
"ds_read_b64_tr_b16 a[152:153], v200 offset:30720\n"
"s_waitcnt lgkmcnt(2)\n"
"v_mfma_f32_16x16x32_bf16 v[0:3], a[160:163], a[32:35], v[0:3]\n"
"ds_read_b64_tr_b16 a[154:155], v200 offset:36864\n"
"v_mfma_f32_16x16x32_bf16 v[0:3], a[164:167], a[36:39], v[0:3]\n"
"ds_read_b64_tr_b16 a[156:157], v200 offset:43008\n"
"v_mfma_f32_16x16x32_bf16 v[4:7], a[160:163], a[40:43], v[4:7]\n"
"ds_read_b64_tr_b16 a[158:159], v200 offset:49152\n"
"v_mfma_f32_16x16x32_bf16 v[4:7], a[164:167], a[44:47], v[4:7]\n"
"v_mul_u32_u24 v233, 0xaaaa, v233\n"
"buffer_load_dwordx4 v193, s[12:15], s85 offen offset:1024 lds\n"
"v_mfma_f32_16x16x32_bf16 v[8:11], a[160:163], a[48:51], v[8:11]\n"
"v_add_u32 v233, 0x4444, v233\n"
"v_mfma_f32_16x16x32_bf16 v[8:11], a[164:167], a[52:55], v[8:11]\n"
"v_lshl_or_b32 v233, v233, 16, v233\n"
"v_mfma_f32_16x16x32_bf16 v[12:15], a[160:163], a[56:59], v[12:15]\n"
"v_lshl_or_b32 v220, s52, 6, v248\n"
"v_mfma_f32_16x16x32_bf16 v[12:15], a[164:167], a[60:63], v[12:15]\n"
"ds_read_b64_tr_b16 a[160:161], v201 offset:30720\n"
"v_mfma_f32_16x16x32_bf16 v[16:19], a[168:171], a[32:35], v[16:19]\n"
"ds_read_b64_tr_b16 a[162:163], v201 offset:36864\n"
"buffer_load_dwordx4 v194, s[12:15], s85 offen offset:2048 lds\n"
"v_mfma_f32_16x16x32_bf16 v[16:19], a[172:175], a[36:39], v[16:19]\n"
"ds_read_b64_tr_b16 a[164:165], v201 offset:43008\n"
"s_add_u32 s85, s85, 786432\n"
"v_mfma_f32_16x16x32_bf16 v[20:23], a[168:171], a[40:43], v[20:23]\n"
"ds_read_b64_tr_b16 a[166:167], v201 offset:49152\n"
"v_mfma_f32_16x16x32_bf16 v[20:23], a[172:175], a[44:47], v[20:23]\n"
"v_min_u32 v221, 1, v220\n"
"v_mfma_f32_16x16x32_bf16 v[24:27], a[168:171], a[48:51], v[24:27]\n"
"v_lshlrev_b32 v245, 30, v221\n"
"v_mfma_f32_16x16x32_bf16 v[24:27], a[172:175], a[52:55], v[24:27]\n"
"v_mov_b32 v246, 1\n"
"s_add_u32 m0, s87, 73728\n"
"v_mfma_f32_16x16x32_bf16 v[28:31], a[168:171], a[56:59], v[28:31]\n"
"v_mov_b32 v247, 0xbdd2d3e8\n"
"buffer_load_dwordx4 v244, s[8:11], s86 offen sc1 lds\n"
"v_mfma_f32_16x16x32_bf16 v[28:31], a[172:175], a[60:63], v[28:31]\n"
"ds_read_b64_tr_b16 a[168:169], v202 offset:30720\n"
"s_add_u32 s86, s86, 10240\n"
"v_mfma_f32_16x16x32_bf16 v[32:35], a[144:147], a[32:35], v[32:35]\n"
"ds_read_b64_tr_b16 a[170:171], v202 offset:36864\n"
"s_waitcnt lgkmcnt(2)\n"
"v_mfma_f32_16x16x32_bf16 v[32:35], a[148:151], a[36:39], v[32:35]\n"
"ds_read_b64_tr_b16 a[172:173], v202 offset:43008\n"
"v_mfma_f32_16x16x32_bf16 v[36:39], a[144:147], a[40:43], v[36:39]\n"
"ds_read_b64_tr_b16 a[174:175], v202 offset:49152\n"
"v_mfma_f32_16x16x32_bf16 v[36:39], a[148:151], a[44:47], v[36:39]\n"
"s_lshl_b32 s88, s54, 15\n"
"buffer_load_dwordx4 a[192:195], v244, s[8:11], s80 offen offset:2048 sc1\n"
"v_mfma_f32_16x16x32_bf16 v[40:43], a[144:147], a[48:51], v[40:43]\n"
"v_lshlrev_b32 v221, 6, v236\n"
"v_mfma_f32_16x16x32_bf16 v[40:43], a[148:151], a[52:55], v[40:43]\n"
"v_add_u32 v239, s88, v221\n"
"v_mfma_f32_16x16x32_bf16 v[44:47], a[144:147], a[56:59], v[44:47]\n"
"v_mul_u32_u24 v235, 3072, v235\n"
"v_mfma_f32_16x16x32_bf16 v[44:47], a[148:151], a[60:63], v[44:47]\n"
"s_waitcnt vmcnt(17) lgkmcnt(0)\n"
"s_barrier\n"
"v_mfma_f32_16x16x32_bf16 v[48:51], a[152:155], a[32:35], v[48:51]\n"
"ds_read_b128 a[176:179], v204 offset:28672\n"
"ds_read_b128 a[180:183], v204 offset:29696\n"
"buffer_load_dwordx4 a[196:199], v244, s[8:11], s80 offen offset:3072 sc1\n"
"v_mfma_f32_16x16x32_bf16 v[48:51], a[156:159], a[36:39], v[48:51]\n"
"ds_read_b128 a[184:187], v204 offset:30720\n"
"ds_read_b128 a[188:191], v204 offset:31744\n"
"s_add_u32 s80, s80, 4096\n"
"v_mfma_f32_16x16x32_bf16 v[52:55], a[152:155], a[40:43], v[52:55]\n"
"ds_read_b64_tr_b16 a[144:145], v195 offset:0\n"
"v_mfma_f32_16x16x32_bf16 v[52:55], a[156:159], a[44:47], v[52:55]\n"
"ds_read_b64_tr_b16 a[146:147], v195 offset:6144\n"
"v_mfma_f32_16x16x32_bf16 v[56:59], a[152:155], a[48:51], v[56:59]\n"
"ds_read_b64_tr_b16 a[148:149], v195 offset:12288\n"
"v_mfma_f32_16x16x32_bf16 v[56:59], a[156:159], a[52:55], v[56:59]\n"
"ds_read_b64_tr_b16 a[150:151], v195 offset:18432\n"
"buffer_load_dwordx4 a[200:203], v244, s[8:11], s81 offen offset:2048 sc1\n"
"v_mfma_f32_16x16x32_bf16 v[60:63], a[152:155], a[56:59], v[60:63]\n"
"v_add_u32 v235, v235, v236\n"
"v_mfma_f32_16x16x32_bf16 v[60:63], a[156:159], a[60:63], v[60:63]\n"
"ds_read_b64_tr_b16 a[152:153], v196 offset:0\n"
"v_mfma_f32_16x16x32_bf16 v[64:67], a[160:163], a[32:35], v[64:67]\n"
"ds_read_b64_tr_b16 a[154:155], v196 offset:6144\n"
"v_mfma_f32_16x16x32_bf16 v[64:67], a[164:167], a[36:39], v[64:67]\n"
"ds_read_b64_tr_b16 a[156:157], v196 offset:12288\n"
"v_mfma_f32_16x16x32_bf16 v[68:71], a[160:163], a[40:43], v[68:71]\n"
"ds_read_b64_tr_b16 a[158:159], v196 offset:18432\n"
"buffer_load_dwordx4 a[204:207], v244, s[8:11], s81 offen offset:3072 sc1\n"
"v_mfma_f32_16x16x32_bf16 v[68:71], a[164:167], a[44:47], v[68:71]\n"
"v_lshrrev_b32 v220, 3, v220\n"
"s_add_u32 s81, s81, 4096\n"
"v_mfma_f32_16x16x32_bf16 v[72:75], a[160:163], a[48:51], v[72:75]\n"
"v_min_u32 v220, 1, v220\n"
"v_mfma_f32_16x16x32_bf16 v[72:75], a[164:167], a[52:55], v[72:75]\n"
"v_lshlrev_b32 v220, 30, v220\n"
"v_mfma_f32_16x16x32_bf16 v[76:79], a[160:163], a[56:59], v[76:79]\n"
"v_lshl_add_u32 v236, v236, 11, v220\n"
"v_mfma_f32_16x16x32_bf16 v[76:79], a[164:167], a[60:63], v[76:79]\n"
"ds_read_b64_tr_b16 a[160:161], v197 offset:0\n"
"buffer_load_dwordx4 a[208:211], v244, s[8:11], s82 offen offset:2048 sc1\n"
"s_waitcnt lgkmcnt(2)\n"
"v_mfma_f32_16x16x32_bf16 a[120:123], a[176:179], a[32:35], a[120:123]\n"
"ds_read_b64_tr_b16 a[162:163], v197 offset:6144\n"
"v_mfma_f32_16x16x32_bf16 a[120:123], a[180:183], a[36:39], a[120:123]\n"
"ds_read_b64_tr_b16 a[164:165], v197 offset:12288\n"
"v_mfma_f32_16x16x32_bf16 a[124:127], a[176:179], a[40:43], a[124:127]\n"
"ds_read_b64_tr_b16 a[166:167], v197 offset:18432\n"
"v_mfma_f32_16x16x32_bf16 a[124:127], a[180:183], a[44:47], a[124:127]\n"
"v_lshlrev_b32 v254, 2, v223\n"
"v_mfma_f32_16x16x32_bf16 a[128:131], a[176:179], a[48:51], a[128:131]\n"
"s_lshr_b32 s89, s53, 2\n"
"buffer_load_dwordx4 a[212:215], v244, s[8:11], s82 offen offset:3072 sc1\n"
"v_mfma_f32_16x16x32_bf16 a[128:131], a[180:183], a[52:55], a[128:131]\n"
"s_add_u32 s89, s89, 32\n"
"s_add_u32 s82, s82, 4096\n"
"v_mfma_f32_16x16x32_bf16 a[132:135], a[176:179], a[56:59], a[132:135]\n"
"v_lshlrev_b32 v222, 8, v222\n"
"v_mfma_f32_16x16x32_bf16 a[132:135], a[180:183], a[60:63], a[132:135]\n"
"s_mov_b32 s88, 1\n"
"v_mfma_f32_16x16x32_bf16 a[136:139], a[184:187], a[32:35], a[136:139]\n"
"s_cmp_ge_u32 s53, 10\n"
"s_cselect_b32 s88, -1, s88\n"
"v_mfma_f32_16x16x32_bf16 a[136:139], a[188:191], a[36:39], a[136:139]\n"
"s_cmp_ge_u32 s53, 20\n"
"s_cselect_b32 s88, 2, s88\n"
"buffer_load_dwordx4 a[216:219], v244, s[8:11], s83 offen offset:2048 sc1\n"
"v_mfma_f32_16x16x32_bf16 v[80:83], a[168:171], a[32:35], v[80:83]\n"
"s_cmp_ge_u32 s53, 30\n"
"s_cselect_b32 s88, -2, s88\n"
"v_mfma_f32_16x16x32_bf16 v[80:83], a[172:175], a[36:39], v[80:83]\n"
"s_lshr_b32 s91, s53, 1\n"
"v_mfma_f32_16x16x32_bf16 v[84:87], a[168:171], a[40:43], v[84:87]\n"
"s_lshl_b32 s91, s91, 10\n"
"v_mfma_f32_16x16x32_bf16 v[84:87], a[172:175], a[44:47], v[84:87]\n"
"s_and_b32 s90, s53, 1\n"
"v_mfma_f32_16x16x32_bf16 v[88:91], a[168:171], a[48:51], v[88:91]\n"
"s_lshl3_add_u32 s91, s90, s91\n"
"buffer_load_dwordx4 a[220:223], v244, s[8:11], s83 offen offset:3072 sc1\n"
"v_mfma_f32_16x16x32_bf16 v[88:91], a[172:175], a[52:55], v[88:91]\n"
"s_add_u32 s91, s91, 5263360\n"
"s_add_u32 s83, s83, 4096\n"
"v_mfma_f32_16x16x32_bf16 v[92:95], a[168:171], a[56:59], v[92:95]\n"
"s_lshl4_add_u32 s90, s76, s88\n"
"v_mfma_f32_16x16x32_bf16 v[92:95], a[172:175], a[60:63], v[92:95]\n"
"v_add_u32 v220, s90, v223\n"
"s_waitcnt vmcnt(15)\n"
"v_mfma_f32_16x16x32_bf16 v[96:99], a[144:147], a[64:67], v[96:99]\n"
"ds_read_b64_tr_b16 a[168:169], v198 offset:0\n"
"v_mfma_f32_16x16x32_bf16 v[96:99], a[148:151], a[68:71], v[96:99]\n"
"ds_read_b64_tr_b16 a[170:171], v198 offset:6144\n"
"s_add_u32 m0, s57, 106496\n"
"v_mfma_f32_16x16x32_bf16 v[100:103], a[144:147], a[72:75], v[100:103]\n"
"ds_read_b64_tr_b16 a[172:173], v198 offset:12288\n"
"buffer_load_dwordx4 v192, s[12:15], s84 offen offset:0 lds\n"
"v_mfma_f32_16x16x32_bf16 v[100:103], a[148:151], a[76:79], v[100:103]\n"
"ds_read_b64_tr_b16 a[174:175], v198 offset:18432\n"
"v_mfma_f32_16x16x32_bf16 v[104:107], a[144:147], a[80:83], v[104:107]\n"
"v_cmp_gt_u32 vcc, 0x800, v220\n"
"v_mfma_f32_16x16x32_bf16 v[104:107], a[148:151], a[84:87], v[104:107]\n"
"v_bfe_u32 v221, v220, 4, 7\n"
"v_mfma_f32_16x16x32_bf16 v[108:111], a[144:147], a[88:91], v[108:111]\n"
"v_and_b32 v220, 15, v220\n"
"v_mfma_f32_16x16x32_bf16 v[108:111], a[148:151], a[92:95], v[108:111]\n"
"ds_read_b64_tr_b16 a[144:145], v195 offset:1024\n"
"buffer_load_dwordx4 v193, s[12:15], s84 offen offset:1024 lds\n"
"v_mfma_f32_16x16x32_bf16 v[112:115], a[152:155], a[64:67], v[112:115]\n"
"ds_read_b64_tr_b16 a[146:147], v195 offset:7168\n"
"s_waitcnt lgkmcnt(3)\n"
"v_mfma_f32_16x16x32_bf16 v[112:115], a[156:159], a[68:71], v[112:115]\n"
"ds_read_b64_tr_b16 a[148:149], v195 offset:13312\n"
"v_mfma_f32_16x16x32_bf16 v[116:119], a[152:155], a[72:75], v[116:119]\n"
"ds_read_b64_tr_b16 a[150:151], v195 offset:19456\n"
"v_mfma_f32_16x16x32_bf16 v[116:119], a[156:159], a[76:79], v[116:119]\n"
"v_cndmask_b32 v213, 0, 1.0, vcc\n"
"v_mfma_f32_16x16x32_bf16 v[120:123], a[152:155], a[80:83], v[120:123]\n"
"v_mul_u32_u24 v221, 20480, v221\n"
"buffer_load_dwordx4 v194, s[12:15], s84 offen offset:2048 lds\n"
"v_mfma_f32_16x16x32_bf16 v[120:123], a[156:159], a[84:87], v[120:123]\n"
"v_lshl_add_u32 v220, v220, 4, v222\n"
"s_add_u32 s84, s84, 786432\n"
"v_mfma_f32_16x16x32_bf16 v[124:127], a[152:155], a[88:91], v[124:127]\n"
"v_add3_u32 v240, v220, v221, s91\n"
"v_mfma_f32_16x16x32_bf16 v[124:127], a[156:159], a[92:95], v[124:127]\n"
"ds_read_b64_tr_b16 a[152:153], v196 offset:1024\n"
"v_mfma_f32_16x16x32_bf16 v[128:131], a[160:163], a[64:67], v[128:131]\n"
"ds_read_b64_tr_b16 a[154:155], v196 offset:7168\n"
"v_mfma_f32_16x16x32_bf16 v[128:131], a[164:167], a[68:71], v[128:131]\n"
"ds_read_b64_tr_b16 a[156:157], v196 offset:13312\n"
"s_add_u32 m0, s57, 109568\n"
"v_mfma_f32_16x16x32_bf16 v[132:135], a[160:163], a[72:75], v[132:135]\n"
"ds_read_b64_tr_b16 a[158:159], v196 offset:19456\n"
"buffer_load_dwordx4 v192, s[12:15], s85 offen offset:0 lds\n"
"v_mfma_f32_16x16x32_bf16 v[132:135], a[164:167], a[76:79], v[132:135]\n"
"s_lshl4_add_u32 s90, s77, s88\n"
"v_mfma_f32_16x16x32_bf16 v[136:139], a[160:163], a[80:83], v[136:139]\n"
"v_add_u32 v220, s90, v223\n"
"v_mfma_f32_16x16x32_bf16 v[136:139], a[164:167], a[84:87], v[136:139]\n"
"v_cmp_gt_u32 vcc, 0x800, v220\n"
"v_mfma_f32_16x16x32_bf16 v[140:143], a[160:163], a[88:91], v[140:143]\n"
"v_bfe_u32 v221, v220, 4, 7\n"
"v_mfma_f32_16x16x32_bf16 v[140:143], a[164:167], a[92:95], v[140:143]\n"
"ds_read_b64_tr_b16 a[160:161], v197 offset:1024\n"
"buffer_load_dwordx4 v193, s[12:15], s85 offen offset:1024 lds\n"
"v_mfma_f32_16x16x32_bf16 v[144:147], a[168:171], a[64:67], v[144:147]\n"
"ds_read_b64_tr_b16 a[162:163], v197 offset:7168\n"
"s_waitcnt lgkmcnt(2)\n"
"v_mfma_f32_16x16x32_bf16 v[144:147], a[172:175], a[68:71], v[144:147]\n"
"ds_read_b64_tr_b16 a[164:165], v197 offset:13312\n"
"v_mfma_f32_16x16x32_bf16 v[148:151], a[168:171], a[72:75], v[148:151]\n"
"ds_read_b64_tr_b16 a[166:167], v197 offset:19456\n"
"v_mfma_f32_16x16x32_bf16 v[148:151], a[172:175], a[76:79], v[148:151]\n"
"v_and_b32 v220, 15, v220\n"
"v_mfma_f32_16x16x32_bf16 v[152:155], a[168:171], a[80:83], v[152:155]\n"
"v_cndmask_b32 v214, 0, 1.0, vcc\n"
"buffer_load_dwordx4 v194, s[12:15], s85 offen offset:2048 lds\n"
"v_mfma_f32_16x16x32_bf16 v[152:155], a[172:175], a[84:87], v[152:155]\n"
"v_mul_u32_u24 v221, 20480, v221\n"
"s_add_u32 s85, s85, 786432\n"
"v_mfma_f32_16x16x32_bf16 v[156:159], a[168:171], a[88:91], v[156:159]\n"
"v_lshl_add_u32 v220, v220, 4, v222\n"
"v_mfma_f32_16x16x32_bf16 v[156:159], a[172:175], a[92:95], v[156:159]\n"
"ds_read_b64_tr_b16 a[168:169], v198 offset:1024\n"
"v_mfma_f32_16x16x32_bf16 v[160:163], a[144:147], a[64:67], v[160:163]\n"
"ds_read_b64_tr_b16 a[170:171], v198 offset:7168\n"
"v_mfma_f32_16x16x32_bf16 v[160:163], a[148:151], a[68:71], v[160:163]\n"
"ds_read_b64_tr_b16 a[172:173], v198 offset:13312\n"
"s_add_u32 m0, s87, 102400\n"
"v_mfma_f32_16x16x32_bf16 v[164:167], a[144:147], a[72:75], v[164:167]\n"
"ds_read_b64_tr_b16 a[174:175], v198 offset:19456\n"
"buffer_load_dwordx4 v244, s[8:11], s86 offen sc1 lds\n"
"v_mfma_f32_16x16x32_bf16 v[164:167], a[148:151], a[76:79], v[164:167]\n"
"v_add3_u32 v241, v220, v221, s91\n"
"s_add_u32 s86, s86, 10240\n"
"v_mfma_f32_16x16x32_bf16 v[168:171], a[144:147], a[80:83], v[168:171]\n"
"s_lshl4_add_u32 s90, s78, s88\n"
"v_mfma_f32_16x16x32_bf16 v[168:171], a[148:151], a[84:87], v[168:171]\n"
"v_add_u32 v220, s90, v223\n"
"v_mfma_f32_16x16x32_bf16 v[172:175], a[144:147], a[88:91], v[172:175]\n"
"v_cmp_gt_u32 vcc, 0x800, v220\n"
"v_mfma_f32_16x16x32_bf16 v[172:175], a[148:151], a[92:95], v[172:175]\n"
"ds_read_b64_tr_b16 a[144:145], v195 offset:2048\n"
"s_add_u32 m0, s87, 20480\n"
"v_mfma_f32_16x16x32_bf16 v[176:179], a[152:155], a[64:67], v[176:179]\n"
"ds_read_b64_tr_b16 a[146:147], v195 offset:8192\n"
"buffer_load_dwordx4 v234, s[44:47], s92 offen lds\n"
"v_mfma_f32_16x16x32_bf16 v[176:179], a[156:159], a[68:71], v[176:179]\n"
"ds_read_b64_tr_b16 a[148:149], v195 offset:14336\n"
"v_mfma_f32_16x16x32_bf16 v[180:183], a[152:155], a[72:75], v[180:183]\n"
"ds_read_b64_tr_b16 a[150:151], v195 offset:20480\n"
"v_mfma_f32_16x16x32_bf16 v[180:183], a[156:159], a[76:79], v[180:183]\n"
"v_bfe_u32 v221, v220, 4, 7\n"
"v_mfma_f32_16x16x32_bf16 v[184:187], a[152:155], a[80:83], v[184:187]\n"
"v_and_b32 v220, 15, v220\n"
"v_mfma_f32_16x16x32_bf16 v[184:187], a[156:159], a[84:87], v[184:187]\n"
"v_cndmask_b32 v215, 0, 1.0, vcc\n"
"buffer_load_dwordx4 a[224:227], v244, s[8:11], s80 offen offset:0 sc1\n"
"v_mfma_f32_16x16x32_bf16 v[188:191], a[152:155], a[88:91], v[188:191]\n"
"v_mul_u32_u24 v221, 20480, v221\n"
"v_mfma_f32_16x16x32_bf16 v[188:191], a[156:159], a[92:95], v[188:191]\n"
"ds_read_b64_tr_b16 a[152:153], v196 offset:2048\n"
"s_waitcnt lgkmcnt(2)\n"
"v_mfma_f32_16x16x32_bf16 v[0:3], a[160:163], a[64:67], v[0:3]\n"
"ds_read_b64_tr_b16 a[154:155], v196 offset:8192\n"
"v_mfma_f32_16x16x32_bf16 v[0:3], a[164:167], a[68:71], v[0:3]\n"
"ds_read_b64_tr_b16 a[156:157], v196 offset:14336\n"
"v_mfma_f32_16x16x32_bf16 v[4:7], a[160:163], a[72:75], v[4:7]\n"
"ds_read_b64_tr_b16 a[158:159], v196 offset:20480\n"
"buffer_load_dwordx4 a[228:231], v244, s[8:11], s80 offen offset:1024 sc1\n"
"v_mfma_f32_16x16x32_bf16 v[4:7], a[164:167], a[76:79], v[4:7]\n"
"v_lshl_add_u32 v220, v220, 4, v222\n"
"v_mfma_f32_16x16x32_bf16 v[8:11], a[160:163], a[80:83], v[8:11]\n"
"v_add3_u32 v242, v220, v221, s91\n"
"v_mfma_f32_16x16x32_bf16 v[8:11], a[164:167], a[84:87], v[8:11]\n"
"s_lshl4_add_u32 s90, s79, s88\n"
"v_mfma_f32_16x16x32_bf16 v[12:15], a[160:163], a[88:91], v[12:15]\n"
"v_add_u32 v220, s90, v223\n"
"v_mfma_f32_16x16x32_bf16 v[12:15], a[164:167], a[92:95], v[12:15]\n"
"ds_read_b64_tr_b16 a[160:161], v197 offset:2048\n"
"buffer_load_dwordx4 a[232:235], v244, s[8:11], s81 offen offset:0 sc1\n"
"v_mfma_f32_16x16x32_bf16 v[16:19], a[168:171], a[64:67], v[16:19]\n"
"ds_read_b64_tr_b16 a[162:163], v197 offset:8192\n"
"v_mfma_f32_16x16x32_bf16 v[16:19], a[172:175], a[68:71], v[16:19]\n"
"ds_read_b64_tr_b16 a[164:165], v197 offset:14336\n"
"v_mfma_f32_16x16x32_bf16 v[20:23], a[168:171], a[72:75], v[20:23]\n"
"ds_read_b64_tr_b16 a[166:167], v197 offset:20480\n"
"v_mfma_f32_16x16x32_bf16 v[20:23], a[172:175], a[76:79], v[20:23]\n"
"v_cmp_gt_u32 vcc, 0x800, v220\n"
"v_mfma_f32_16x16x32_bf16 v[24:27], a[168:171], a[80:83], v[24:27]\n"
"v_bfe_u32 v221, v220, 4, 7\n"
"buffer_load_dwordx4 a[236:239], v244, s[8:11], s81 offen offset:1024 sc1\n"
"v_mfma_f32_16x16x32_bf16 v[24:27], a[172:175], a[84:87], v[24:27]\n"
"v_and_b32 v220, 15, v220\n"
"v_mfma_f32_16x16x32_bf16 v[28:31], a[168:171], a[88:91], v[28:31]\n"
"v_cndmask_b32 v216, 0, 1.0, vcc\n"
"v_mfma_f32_16x16x32_bf16 v[28:31], a[172:175], a[92:95], v[28:31]\n"
"ds_read_b64_tr_b16 a[168:169], v198 offset:2048\n"
"v_mfma_f32_16x16x32_bf16 v[32:35], a[144:147], a[64:67], v[32:35]\n"
"ds_read_b64_tr_b16 a[170:171], v198 offset:8192\n"
"s_waitcnt lgkmcnt(2)\n"
"v_mfma_f32_16x16x32_bf16 v[32:35], a[148:151], a[68:71], v[32:35]\n"
"ds_read_b64_tr_b16 a[172:173], v198 offset:14336\n"
"buffer_load_dwordx4 a[240:243], v244, s[8:11], s82 offen offset:0 sc1\n"
"v_mfma_f32_16x16x32_bf16 v[36:39], a[144:147], a[72:75], v[36:39]\n"
"ds_read_b64_tr_b16 a[174:175], v198 offset:20480\n"
"v_mfma_f32_16x16x32_bf16 v[36:39], a[148:151], a[76:79], v[36:39]\n"
"v_mul_u32_u24 v221, 20480, v221\n"
"v_mfma_f32_16x16x32_bf16 v[40:43], a[144:147], a[80:83], v[40:43]\n"
"v_lshl_add_u32 v220, v220, 4, v222\n"
"v_mfma_f32_16x16x32_bf16 v[40:43], a[148:151], a[84:87], v[40:43]\n"
"v_add3_u32 v243, v220, v221, s91\n"
"v_mfma_f32_16x16x32_bf16 v[44:47], a[144:147], a[88:91], v[44:47]\n"
"s_mov_b32 s88, 1\n"
"buffer_load_dwordx4 a[244:247], v244, s[8:11], s82 offen offset:1024 sc1\n"
"v_mfma_f32_16x16x32_bf16 v[44:47], a[148:151], a[92:95], v[44:47]\n"
"s_waitcnt vmcnt(22) lgkmcnt(0)\n"
"s_barrier\n"
"v_mfma_f32_16x16x32_bf16 v[48:51], a[152:155], a[64:67], v[48:51]\n"
"ds_read_b128 a[176:179], v203 offset:0\n"
"ds_read_b128 a[180:183], v203 offset:1024\n"
"v_mfma_f32_16x16x32_bf16 v[48:51], a[156:159], a[68:71], v[48:51]\n"
"ds_read_b128 a[184:187], v203 offset:2048\n"
"ds_read_b128 a[188:191], v203 offset:3072\n"
"v_mfma_f32_16x16x32_bf16 v[52:55], a[152:155], a[72:75], v[52:55]\n"
"ds_read_b64_tr_b16 a[144:145], v195 offset:28672\n"
"v_mfma_f32_16x16x32_bf16 v[52:55], a[156:159], a[76:79], v[52:55]\n"
"ds_read_b64_tr_b16 a[146:147], v195 offset:34816\n"
"buffer_load_dwordx4 a[248:251], v244, s[8:11], s83 offen offset:0 sc1\n"
"v_mfma_f32_16x16x32_bf16 v[56:59], a[152:155], a[80:83], v[56:59]\n"
"ds_read_b64_tr_b16 a[148:149], v195 offset:40960\n"
"v_mfma_f32_16x16x32_bf16 v[56:59], a[156:159], a[84:87], v[56:59]\n"
"ds_read_b64_tr_b16 a[150:151], v195 offset:47104\n"
"v_mfma_f32_16x16x32_bf16 v[60:63], a[152:155], a[88:91], v[60:63]\n"
"s_cmp_ge_u32 s89, 10\n"
"s_cselect_b32 s88, -1, s88\n"
"v_mfma_f32_16x16x32_bf16 v[60:63], a[156:159], a[92:95], v[60:63]\n"
"ds_read_b64_tr_b16 a[152:153], v196 offset:28672\n"
"v_mfma_f32_16x16x32_bf16 v[64:67], a[160:163], a[64:67], v[64:67]\n"
"ds_read_b64_tr_b16 a[154:155], v196 offset:34816\n"
"buffer_load_dwordx4 a[252:255], v244, s[8:11], s83 offen offset:1024 sc1\n"
"v_mfma_f32_16x16x32_bf16 v[64:67], a[164:167], a[68:71], v[64:67]\n"
"ds_read_b64_tr_b16 a[156:157], v196 offset:40960\n"
"v_mfma_f32_16x16x32_bf16 v[68:71], a[160:163], a[72:75], v[68:71]\n"
"ds_read_b64_tr_b16 a[158:159], v196 offset:47104\n"
"v_mfma_f32_16x16x32_bf16 v[68:71], a[164:167], a[76:79], v[68:71]\n"
"s_cmp_ge_u32 s89, 20\n"
"s_cselect_b32 s88, 2, s88\n"
"v_mfma_f32_16x16x32_bf16 v[72:75], a[160:163], a[80:83], v[72:75]\n"
"s_cmp_ge_u32 s89, 30\n"
"s_cselect_b32 s88, -2, s88\n"
"v_mfma_f32_16x16x32_bf16 v[72:75], a[164:167], a[84:87], v[72:75]\n"
"s_lshr_b32 s91, s89, 1\n"
"s_add_u32 m0, s57, 135168\n"
"v_mfma_f32_16x16x32_bf16 v[76:79], a[160:163], a[88:91], v[76:79]\n"
"s_lshl_b32 s91, s91, 10\n"
"buffer_load_dwordx4 v192, s[12:15], s84 offen offset:0 lds\n"
"v_mfma_f32_16x16x32_bf16 v[76:79], a[164:167], a[92:95], v[76:79]\n"
"ds_read_b64_tr_b16 a[160:161], v197 offset:28672\n"
"s_waitcnt lgkmcnt(2)\n"
"v_mfma_f32_16x16x32_bf16 a[120:123], a[176:179], a[64:67], a[120:123]\n"
"ds_read_b64_tr_b16 a[162:163], v197 offset:34816\n"
"v_mfma_f32_16x16x32_bf16 a[120:123], a[180:183], a[68:71], a[120:123]\n"
"ds_read_b64_tr_b16 a[164:165], v197 offset:40960\n"
"v_mfma_f32_16x16x32_bf16 a[124:127], a[176:179], a[72:75], a[124:127]\n"
"ds_read_b64_tr_b16 a[166:167], v197 offset:47104\n"
"v_mfma_f32_16x16x32_bf16 a[124:127], a[180:183], a[76:79], a[124:127]\n"
"s_and_b32 s90, s89, 1\n"
"buffer_load_dwordx4 v193, s[12:15], s84 offen offset:1024 lds\n"
"v_mfma_f32_16x16x32_bf16 a[128:131], a[176:179], a[80:83], a[128:131]\n"
"s_lshl3_add_u32 s91, s90, s91\n"
"v_mfma_f32_16x16x32_bf16 a[128:131], a[180:183], a[84:87], a[128:131]\n"
"s_add_u32 s91, s91, 5263360\n"
"v_mfma_f32_16x16x32_bf16 a[132:135], a[176:179], a[88:91], a[132:135]\n"
"s_lshl4_add_u32 s90, s76, s88\n"
"v_mfma_f32_16x16x32_bf16 a[132:135], a[180:183], a[92:95], a[132:135]\n"
"v_add_u32 v220, s90, v223\n"
"v_mfma_f32_16x16x32_bf16 a[136:139], a[184:187], a[64:67], a[136:139]\n"
"v_cmp_gt_u32 vcc, 0x800, v220\n"
"buffer_load_dwordx4 v194, s[12:15], s84 offen offset:2048 lds\n"
"v_mfma_f32_16x16x32_bf16 a[136:139], a[188:191], a[68:71], a[136:139]\n"
"v_bfe_u32 v221, v220, 4, 7\n"
"s_add_u32 s84, s84, 786432\n"
"v_mfma_f32_16x16x32_bf16 v[80:83], a[168:171], a[64:67], v[80:83]\n"
"v_and_b32 v220, 15, v220\n"
"v_mfma_f32_16x16x32_bf16 v[80:83], a[172:175], a[68:71], v[80:83]\n"
"v_cndmask_b32 v217, 0, 1.0, vcc\n"
"v_mfma_f32_16x16x32_bf16 v[84:87], a[168:171], a[72:75], v[84:87]\n"
"v_mul_u32_u24 v221, 20480, v221\n"
"v_mfma_f32_16x16x32_bf16 v[84:87], a[172:175], a[76:79], v[84:87]\n"
"v_lshl_add_u32 v220, v220, 4, v222\n"
"s_add_u32 m0, s57, 138240\n"
"v_mfma_f32_16x16x32_bf16 v[88:91], a[168:171], a[80:83], v[88:91]\n"
"v_add3_u32 v253, v220, v221, s91\n"
"v_mfma_f32_16x16x32_bf16 v[88:91], a[172:175], a[84:87], v[88:91]\n"
"buffer_load_dwordx4 v192, s[12:15], s85 offen offset:0 lds\n"
"v_mfma_f32_16x16x32_bf16 v[92:95], a[168:171], a[88:91], v[92:95]\n"
"v_mfma_f32_16x16x32_bf16 v[92:95], a[172:175], a[92:95], v[92:95]\n"
"s_waitcnt vmcnt(20)\n"
"v_mfma_f32_16x16x32_bf16 v[96:99], a[144:147], a[192:195], v[96:99]\n"
"ds_read_b64_tr_b16 a[168:169], v198 offset:28672\n"
"v_mfma_f32_16x16x32_bf16 v[96:99], a[148:151], a[196:199], v[96:99]\n"
"ds_read_b64_tr_b16 a[170:171], v198 offset:34816\n"
"v_mfma_f32_16x16x32_bf16 v[100:103], a[144:147], a[200:203], v[100:103]\n"
"ds_read_b64_tr_b16 a[172:173], v198 offset:40960\n"
"buffer_load_dwordx4 v193, s[12:15], s85 offen offset:1024 lds\n"
"v_mfma_f32_16x16x32_bf16 v[100:103], a[148:151], a[204:207], v[100:103]\n"
"ds_read_b64_tr_b16 a[174:175], v198 offset:47104\n"
"v_mfma_f32_16x16x32_bf16 v[104:107], a[144:147], a[208:211], v[104:107]\n"
"v_mfma_f32_16x16x32_bf16 v[104:107], a[148:151], a[212:215], v[104:107]\n"
"v_mfma_f32_16x16x32_bf16 v[108:111], a[144:147], a[216:219], v[108:111]\n"
"v_mfma_f32_16x16x32_bf16 v[108:111], a[148:151], a[220:223], v[108:111]\n"
"ds_read_b64_tr_b16 a[144:145], v195 offset:29696\n"
"v_mfma_f32_16x16x32_bf16 v[112:115], a[152:155], a[192:195], v[112:115]\n"
"ds_read_b64_tr_b16 a[146:147], v195 offset:35840\n"
"buffer_load_dwordx4 v194, s[12:15], s85 offen offset:2048 lds\n"
"s_waitcnt lgkmcnt(3)\n"
"v_mfma_f32_16x16x32_bf16 v[112:115], a[156:159], a[196:199], v[112:115]\n"
"ds_read_b64_tr_b16 a[148:149], v195 offset:41984\n"
"s_add_u32 s85, s85, 786432\n"
"v_mfma_f32_16x16x32_bf16 v[116:119], a[152:155], a[200:203], v[116:119]\n"
"ds_read_b64_tr_b16 a[150:151], v195 offset:48128\n"
"v_mfma_f32_16x16x32_bf16 v[116:119], a[156:159], a[204:207], v[116:119]\n"
"v_mfma_f32_16x16x32_bf16 v[120:123], a[152:155], a[208:211], v[120:123]\n"
"v_mfma_f32_16x16x32_bf16 v[120:123], a[156:159], a[212:215], v[120:123]\n"
"v_mfma_f32_16x16x32_bf16 v[124:127], a[152:155], a[216:219], v[124:127]\n"
"s_add_u32 m0, s87, 131072\n"
"v_mfma_f32_16x16x32_bf16 v[124:127], a[156:159], a[220:223], v[124:127]\n"
"ds_read_b64_tr_b16 a[152:153], v196 offset:29696\n"
"buffer_load_dwordx4 v244, s[8:11], s86 offen sc1 lds\n"
"v_mfma_f32_16x16x32_bf16 v[128:131], a[160:163], a[192:195], v[128:131]\n"
"ds_read_b64_tr_b16 a[154:155], v196 offset:35840\n"
"s_add_u32 s86, s86, 10240\n"
"v_mfma_f32_16x16x32_bf16 v[128:131], a[164:167], a[196:199], v[128:131]\n"
"ds_read_b64_tr_b16 a[156:157], v196 offset:41984\n"
"v_mfma_f32_16x16x32_bf16 v[132:135], a[160:163], a[200:203], v[132:135]\n"
"ds_read_b64_tr_b16 a[158:159], v196 offset:48128\n"
"v_mfma_f32_16x16x32_bf16 v[132:135], a[164:167], a[204:207], v[132:135]\n"
"v_mfma_f32_16x16x32_bf16 v[136:139], a[160:163], a[208:211], v[136:139]\n"
"v_mfma_f32_16x16x32_bf16 v[136:139], a[164:167], a[212:215], v[136:139]\n"
"buffer_load_dwordx4 a[0:3], v244, s[8:11], s80 offen offset:2048 sc1\n"
"v_mfma_f32_16x16x32_bf16 v[140:143], a[160:163], a[216:219], v[140:143]\n"
"v_mfma_f32_16x16x32_bf16 v[140:143], a[164:167], a[220:223], v[140:143]\n"
"ds_read_b64_tr_b16 a[160:161], v197 offset:29696\n"
"v_mfma_f32_16x16x32_bf16 v[144:147], a[168:171], a[192:195], v[144:147]\n"
"ds_read_b64_tr_b16 a[162:163], v197 offset:35840\n"
"s_waitcnt lgkmcnt(2)\n"
"v_mfma_f32_16x16x32_bf16 v[144:147], a[172:175], a[196:199], v[144:147]\n"
"ds_read_b64_tr_b16 a[164:165], v197 offset:41984\n"
"v_mfma_f32_16x16x32_bf16 v[148:151], a[168:171], a[200:203], v[148:151]\n"
"ds_read_b64_tr_b16 a[166:167], v197 offset:48128\n"
"v_mfma_f32_16x16x32_bf16 v[148:151], a[172:175], a[204:207], v[148:151]\n"
"buffer_load_dwordx4 a[4:7], v244, s[8:11], s80 offen offset:3072 sc1\n"
"v_mfma_f32_16x16x32_bf16 v[152:155], a[168:171], a[208:211], v[152:155]\n"
"s_add_u32 s80, s80, 4096\n"
"v_mfma_f32_16x16x32_bf16 v[152:155], a[172:175], a[212:215], v[152:155]\n"
"v_mfma_f32_16x16x32_bf16 v[156:159], a[168:171], a[216:219], v[156:159]\n"
"v_mfma_f32_16x16x32_bf16 v[156:159], a[172:175], a[220:223], v[156:159]\n"
"ds_read_b64_tr_b16 a[168:169], v198 offset:29696\n"
"v_mfma_f32_16x16x32_bf16 v[160:163], a[144:147], a[192:195], v[160:163]\n"
"ds_read_b64_tr_b16 a[170:171], v198 offset:35840\n"
"v_mfma_f32_16x16x32_bf16 v[160:163], a[148:151], a[196:199], v[160:163]\n"
"ds_read_b64_tr_b16 a[172:173], v198 offset:41984\n"
"buffer_load_dwordx4 a[8:11], v244, s[8:11], s81 offen offset:2048 sc1\n"
"v_mfma_f32_16x16x32_bf16 v[164:167], a[144:147], a[200:203], v[164:167]\n"
"ds_read_b64_tr_b16 a[174:175], v198 offset:48128\n"
"v_mfma_f32_16x16x32_bf16 v[164:167], a[148:151], a[204:207], v[164:167]\n"
"v_mfma_f32_16x16x32_bf16 v[168:171], a[144:147], a[208:211], v[168:171]\n"
"v_mfma_f32_16x16x32_bf16 v[168:171], a[148:151], a[212:215], v[168:171]\n"
"v_mfma_f32_16x16x32_bf16 v[172:175], a[144:147], a[216:219], v[172:175]\n"
"v_mfma_f32_16x16x32_bf16 v[172:175], a[148:151], a[220:223], v[172:175]\n"
"ds_read_b64_tr_b16 a[144:145], v195 offset:30720\n"
"buffer_load_dwordx4 a[12:15], v244, s[8:11], s81 offen offset:3072 sc1\n"
"v_mfma_f32_16x16x32_bf16 v[176:179], a[152:155], a[192:195], v[176:179]\n"
"ds_read_b64_tr_b16 a[146:147], v195 offset:36864\n"
"s_add_u32 s81, s81, 4096\n"
"v_mfma_f32_16x16x32_bf16 v[176:179], a[156:159], a[196:199], v[176:179]\n"
"ds_read_b64_tr_b16 a[148:149], v195 offset:43008\n"
"v_mfma_f32_16x16x32_bf16 v[180:183], a[152:155], a[200:203], v[180:183]\n"
"ds_read_b64_tr_b16 a[150:151], v195 offset:49152\n"
"v_mfma_f32_16x16x32_bf16 v[180:183], a[156:159], a[204:207], v[180:183]\n"
"v_mfma_f32_16x16x32_bf16 v[184:187], a[152:155], a[208:211], v[184:187]\n"
"v_mfma_f32_16x16x32_bf16 v[184:187], a[156:159], a[212:215], v[184:187]\n"
"buffer_load_dwordx4 a[16:19], v244, s[8:11], s82 offen offset:2048 sc1\n"
"v_mfma_f32_16x16x32_bf16 v[188:191], a[152:155], a[216:219], v[188:191]\n"
"v_mfma_f32_16x16x32_bf16 v[188:191], a[156:159], a[220:223], v[188:191]\n"
"ds_read_b64_tr_b16 a[152:153], v196 offset:30720\n"
"s_waitcnt lgkmcnt(2)\n"
"v_mfma_f32_16x16x32_bf16 v[0:3], a[160:163], a[192:195], v[0:3]\n"
"ds_read_b64_tr_b16 a[154:155], v196 offset:36864\n"
"v_mfma_f32_16x16x32_bf16 v[0:3], a[164:167], a[196:199], v[0:3]\n"
"ds_read_b64_tr_b16 a[156:157], v196 offset:43008\n"
"v_mfma_f32_16x16x32_bf16 v[4:7], a[160:163], a[200:203], v[4:7]\n"
"ds_read_b64_tr_b16 a[158:159], v196 offset:49152\n"
"v_mfma_f32_16x16x32_bf16 v[4:7], a[164:167], a[204:207], v[4:7]\n"
"buffer_load_dwordx4 a[20:23], v244, s[8:11], s82 offen offset:3072 sc1\n"
"v_mfma_f32_16x16x32_bf16 v[8:11], a[160:163], a[208:211], v[8:11]\n"
"s_add_u32 s82, s82, 4096\n"
"v_mfma_f32_16x16x32_bf16 v[8:11], a[164:167], a[212:215], v[8:11]\n"
"v_mfma_f32_16x16x32_bf16 v[12:15], a[160:163], a[216:219], v[12:15]\n"
"v_mfma_f32_16x16x32_bf16 v[12:15], a[164:167], a[220:223], v[12:15]\n"
"ds_read_b64_tr_b16 a[160:161], v197 offset:30720\n"
"v_mfma_f32_16x16x32_bf16 v[16:19], a[168:171], a[192:195], v[16:19]\n"
"ds_read_b64_tr_b16 a[162:163], v197 offset:36864\n"
"v_mfma_f32_16x16x32_bf16 v[16:19], a[172:175], a[196:199], v[16:19]\n"
"ds_read_b64_tr_b16 a[164:165], v197 offset:43008\n"
"buffer_load_dwordx4 a[24:27], v244, s[8:11], s83 offen offset:2048 sc1\n"
"v_mfma_f32_16x16x32_bf16 v[20:23], a[168:171], a[200:203], v[20:23]\n"
"ds_read_b64_tr_b16 a[166:167], v197 offset:49152\n"
"v_mfma_f32_16x16x32_bf16 v[20:23], a[172:175], a[204:207], v[20:23]\n"
"v_mfma_f32_16x16x32_bf16 v[24:27], a[168:171], a[208:211], v[24:27]\n"
"v_mfma_f32_16x16x32_bf16 v[24:27], a[172:175], a[212:215], v[24:27]\n"
"v_mfma_f32_16x16x32_bf16 v[28:31], a[168:171], a[216:219], v[28:31]\n"
"v_mfma_f32_16x16x32_bf16 v[28:31], a[172:175], a[220:223], v[28:31]\n"
"ds_read_b64_tr_b16 a[168:169], v198 offset:30720\n"
"buffer_load_dwordx4 a[28:31], v244, s[8:11], s83 offen offset:3072 sc1\n"
"v_mfma_f32_16x16x32_bf16 v[32:35], a[144:147], a[192:195], v[32:35]\n"
"ds_read_b64_tr_b16 a[170:171], v198 offset:36864\n"
"s_add_u32 s83, s83, 4096\n"
"s_waitcnt lgkmcnt(2)\n"
"v_mfma_f32_16x16x32_bf16 v[32:35], a[148:151], a[196:199], v[32:35]\n"
"ds_read_b64_tr_b16 a[172:173], v198 offset:43008\n"
"v_mfma_f32_16x16x32_bf16 v[36:39], a[144:147], a[200:203], v[36:39]\n"
"ds_read_b64_tr_b16 a[174:175], v198 offset:49152\n"
"v_mfma_f32_16x16x32_bf16 v[36:39], a[148:151], a[204:207], v[36:39]\n"
"v_mfma_f32_16x16x32_bf16 v[40:43], a[144:147], a[208:211], v[40:43]\n"
"v_mfma_f32_16x16x32_bf16 v[40:43], a[148:151], a[212:215], v[40:43]\n"
"s_add_u32 m0, s57, 49152\n"
"v_mfma_f32_16x16x32_bf16 v[44:47], a[144:147], a[216:219], v[44:47]\n"
"buffer_load_dwordx4 v192, s[12:15], s84 offen offset:0 lds\n"
"v_mfma_f32_16x16x32_bf16 v[44:47], a[148:151], a[220:223], v[44:47]\n"
"s_waitcnt vmcnt(25) lgkmcnt(0)\n"
"s_barrier\n"
"v_mfma_f32_16x16x32_bf16 v[48:51], a[152:155], a[192:195], v[48:51]\n"
"ds_read_b128 a[176:179], v203 offset:28672\n"
"ds_read_b128 a[180:183], v203 offset:29696\n"
"v_mfma_f32_16x16x32_bf16 v[48:51], a[156:159], a[196:199], v[48:51]\n"
"ds_read_b128 a[184:187], v203 offset:30720\n"
"ds_read_b128 a[188:191], v203 offset:31744\n"
"v_mfma_f32_16x16x32_bf16 v[52:55], a[152:155], a[200:203], v[52:55]\n"
"ds_read_b64_tr_b16 a[144:145], v199 offset:0\n"
"v_mfma_f32_16x16x32_bf16 v[52:55], a[156:159], a[204:207], v[52:55]\n"
"ds_read_b64_tr_b16 a[146:147], v199 offset:6144\n"
"v_mfma_f32_16x16x32_bf16 v[56:59], a[152:155], a[208:211], v[56:59]\n"
"ds_read_b64_tr_b16 a[148:149], v199 offset:12288\n"
"buffer_load_dwordx4 v193, s[12:15], s84 offen offset:1024 lds\n"
"v_mfma_f32_16x16x32_bf16 v[56:59], a[156:159], a[212:215], v[56:59]\n"
"ds_read_b64_tr_b16 a[150:151], v199 offset:18432\n"
"v_mfma_f32_16x16x32_bf16 v[60:63], a[152:155], a[216:219], v[60:63]\n"
"v_mfma_f32_16x16x32_bf16 v[60:63], a[156:159], a[220:223], v[60:63]\n"
"ds_read_b64_tr_b16 a[152:153], v200 offset:0\n"
"v_mfma_f32_16x16x32_bf16 v[64:67], a[160:163], a[192:195], v[64:67]\n"
"ds_read_b64_tr_b16 a[154:155], v200 offset:6144\n"
"v_mfma_f32_16x16x32_bf16 v[64:67], a[164:167], a[196:199], v[64:67]\n"
"ds_read_b64_tr_b16 a[156:157], v200 offset:12288\n"
"v_mfma_f32_16x16x32_bf16 v[68:71], a[160:163], a[200:203], v[68:71]\n"
"ds_read_b64_tr_b16 a[158:159], v200 offset:18432\n"
"v_mfma_f32_16x16x32_bf16 v[68:71], a[164:167], a[204:207], v[68:71]\n"
"buffer_load_dwordx4 v194, s[12:15], s84 offen offset:2048 lds\n"
"v_mfma_f32_16x16x32_bf16 v[72:75], a[160:163], a[208:211], v[72:75]\n"
"s_add_u32 s84, s84, 786432\n"
"v_mfma_f32_16x16x32_bf16 v[72:75], a[164:167], a[212:215], v[72:75]\n"
"v_mfma_f32_16x16x32_bf16 v[76:79], a[160:163], a[216:219], v[76:79]\n"
"v_mfma_f32_16x16x32_bf16 v[76:79], a[164:167], a[220:223], v[76:79]\n"
"ds_read_b64_tr_b16 a[160:161], v201 offset:0\n"
"s_waitcnt lgkmcnt(2)\n"
"v_mfma_f32_16x16x32_bf16 a[120:123], a[176:179], a[192:195], a[120:123]\n"
"ds_read_b64_tr_b16 a[162:163], v201 offset:6144\n"
"v_mfma_f32_16x16x32_bf16 a[120:123], a[180:183], a[196:199], a[120:123]\n"
"ds_read_b64_tr_b16 a[164:165], v201 offset:12288\n"
"s_add_u32 m0, s57, 52224\n"
"v_mfma_f32_16x16x32_bf16 a[124:127], a[176:179], a[200:203], a[124:127]\n"
"ds_read_b64_tr_b16 a[166:167], v201 offset:18432\n"
"v_mfma_f32_16x16x32_bf16 a[124:127], a[180:183], a[204:207], a[124:127]\n"
"buffer_load_dwordx4 v192, s[12:15], s85 offen offset:0 lds\n"
"v_mfma_f32_16x16x32_bf16 a[128:131], a[176:179], a[208:211], a[128:131]\n"
"v_mfma_f32_16x16x32_bf16 a[128:131], a[180:183], a[212:215], a[128:131]\n"
"v_mfma_f32_16x16x32_bf16 a[132:135], a[176:179], a[216:219], a[132:135]\n"
"v_mfma_f32_16x16x32_bf16 a[132:135], a[180:183], a[220:223], a[132:135]\n"
"v_mfma_f32_16x16x32_bf16 a[136:139], a[184:187], a[192:195], a[136:139]\n"
"v_mfma_f32_16x16x32_bf16 a[136:139], a[188:191], a[196:199], a[136:139]\n"
"buffer_load_dwordx4 v193, s[12:15], s85 offen offset:1024 lds\n"
"v_mfma_f32_16x16x32_bf16 v[80:83], a[168:171], a[192:195], v[80:83]\n"
"v_mfma_f32_16x16x32_bf16 v[80:83], a[172:175], a[196:199], v[80:83]\n"
"v_mfma_f32_16x16x32_bf16 v[84:87], a[168:171], a[200:203], v[84:87]\n"
"v_mfma_f32_16x16x32_bf16 v[84:87], a[172:175], a[204:207], v[84:87]\n"
"v_mfma_f32_16x16x32_bf16 v[88:91], a[168:171], a[208:211], v[88:91]\n"
"v_mfma_f32_16x16x32_bf16 v[88:91], a[172:175], a[212:215], v[88:91]\n"
"buffer_load_dwordx4 v194, s[12:15], s85 offen offset:2048 lds\n"
"v_mfma_f32_16x16x32_bf16 v[92:95], a[168:171], a[216:219], v[92:95]\n"
"s_add_u32 s85, s85, 786432\n"
"v_mfma_f32_16x16x32_bf16 v[92:95], a[172:175], a[220:223], v[92:95]\n"
"s_waitcnt vmcnt(21)\n"
"v_mfma_f32_16x16x32_bf16 v[96:99], a[144:147], a[224:227], v[96:99]\n"
"ds_read_b64_tr_b16 a[168:169], v202 offset:0\n"
"v_mfma_f32_16x16x32_bf16 v[96:99], a[148:151], a[228:231], v[96:99]\n"
"ds_read_b64_tr_b16 a[170:171], v202 offset:6144\n"
"v_mfma_f32_16x16x32_bf16 v[100:103], a[144:147], a[232:235], v[100:103]\n"
"ds_read_b64_tr_b16 a[172:173], v202 offset:12288\n"
"v_mfma_f32_16x16x32_bf16 v[100:103], a[148:151], a[236:239], v[100:103]\n"
"ds_read_b64_tr_b16 a[174:175], v202 offset:18432\n"
"s_add_u32 m0, s87, 159744\n"
"v_mfma_f32_16x16x32_bf16 v[104:107], a[144:147], a[240:243], v[104:107]\n"
"buffer_load_dwordx4 v244, s[8:11], s86 offen sc1 lds\n"
"v_mfma_f32_16x16x32_bf16 v[104:107], a[148:151], a[244:247], v[104:107]\n"
"s_add_u32 s86, s86, 10240\n"
"v_mfma_f32_16x16x32_bf16 v[108:111], a[144:147], a[248:251], v[108:111]\n"
"v_mfma_f32_16x16x32_bf16 v[108:111], a[148:151], a[252:255], v[108:111]\n"
"ds_read_b64_tr_b16 a[144:145], v199 offset:1024\n"
"v_mfma_f32_16x16x32_bf16 v[112:115], a[152:155], a[224:227], v[112:115]\n"
"ds_read_b64_tr_b16 a[146:147], v199 offset:7168\n"
"s_waitcnt lgkmcnt(3)\n"
"v_mfma_f32_16x16x32_bf16 v[112:115], a[156:159], a[228:231], v[112:115]\n"
"ds_read_b64_tr_b16 a[148:149], v199 offset:13312\n"
"v_mfma_f32_16x16x32_bf16 v[116:119], a[152:155], a[232:235], v[116:119]\n"
"ds_read_b64_tr_b16 a[150:151], v199 offset:19456\n"
"v_mfma_f32_16x16x32_bf16 v[116:119], a[156:159], a[236:239], v[116:119]\n"
"s_lshl_b32 s4, s76, 6\n"
"s_add_u32 s4, s4, 524288\n"
"buffer_load_dword v205, v254, s[8:11], s4 offen sc1\n"
"v_mfma_f32_16x16x32_bf16 v[120:123], a[152:155], a[240:243], v[120:123]\n"
"v_mfma_f32_16x16x32_bf16 v[120:123], a[156:159], a[244:247], v[120:123]\n"
"v_mfma_f32_16x16x32_bf16 v[124:127], a[152:155], a[248:251], v[124:127]\n"
"v_mfma_f32_16x16x32_bf16 v[124:127], a[156:159], a[252:255], v[124:127]\n"
"ds_read_b64_tr_b16 a[152:153], v200 offset:1024\n"
"v_mfma_f32_16x16x32_bf16 v[128:131], a[160:163], a[224:227], v[128:131]\n"
"ds_read_b64_tr_b16 a[154:155], v200 offset:7168\n"
"v_mfma_f32_16x16x32_bf16 v[128:131], a[164:167], a[228:231], v[128:131]\n"
"ds_read_b64_tr_b16 a[156:157], v200 offset:13312\n"
"s_lshl_b32 s4, s77, 6\n"
"s_add_u32 s4, s4, 524288\n"
"buffer_load_dword v206, v254, s[8:11], s4 offen sc1\n"
"v_mfma_f32_16x16x32_bf16 v[132:135], a[160:163], a[232:235], v[132:135]\n"
"ds_read_b64_tr_b16 a[158:159], v200 offset:19456\n"
"v_mfma_f32_16x16x32_bf16 v[132:135], a[164:167], a[236:239], v[132:135]\n"
"v_mfma_f32_16x16x32_bf16 v[136:139], a[160:163], a[240:243], v[136:139]\n"
"v_mfma_f32_16x16x32_bf16 v[136:139], a[164:167], a[244:247], v[136:139]\n"
"v_mfma_f32_16x16x32_bf16 v[140:143], a[160:163], a[248:251], v[140:143]\n"
"v_mfma_f32_16x16x32_bf16 v[140:143], a[164:167], a[252:255], v[140:143]\n"
"ds_read_b64_tr_b16 a[160:161], v201 offset:1024\n"
"s_lshl_b32 s4, s78, 6\n"
"s_add_u32 s4, s4, 524288\n"
"buffer_load_dword v207, v254, s[8:11], s4 offen sc1\n"
"v_mfma_f32_16x16x32_bf16 v[144:147], a[168:171], a[224:227], v[144:147]\n"
"ds_read_b64_tr_b16 a[162:163], v201 offset:7168\n"
"s_waitcnt lgkmcnt(2)\n"
"v_mfma_f32_16x16x32_bf16 v[144:147], a[172:175], a[228:231], v[144:147]\n"
"ds_read_b64_tr_b16 a[164:165], v201 offset:13312\n"
"v_mfma_f32_16x16x32_bf16 v[148:151], a[168:171], a[232:235], v[148:151]\n"
"ds_read_b64_tr_b16 a[166:167], v201 offset:19456\n"
"v_mfma_f32_16x16x32_bf16 v[148:151], a[172:175], a[236:239], v[148:151]\n"
"v_mfma_f32_16x16x32_bf16 v[152:155], a[168:171], a[240:243], v[152:155]\n"
"v_mfma_f32_16x16x32_bf16 v[152:155], a[172:175], a[244:247], v[152:155]\n"
"s_lshl_b32 s4, s79, 6\n"
"s_add_u32 s4, s4, 524288\n"
"buffer_load_dword v208, v254, s[8:11], s4 offen sc1\n"
"v_mfma_f32_16x16x32_bf16 v[156:159], a[168:171], a[248:251], v[156:159]\n"
"v_mfma_f32_16x16x32_bf16 v[156:159], a[172:175], a[252:255], v[156:159]\n"
"ds_read_b64_tr_b16 a[168:169], v202 offset:1024\n"
"v_mfma_f32_16x16x32_bf16 v[160:163], a[144:147], a[224:227], v[160:163]\n"
"ds_read_b64_tr_b16 a[170:171], v202 offset:7168\n"
"v_mfma_f32_16x16x32_bf16 v[160:163], a[148:151], a[228:231], v[160:163]\n"
"ds_read_b64_tr_b16 a[172:173], v202 offset:13312\n"
"v_mfma_f32_16x16x32_bf16 v[164:167], a[144:147], a[232:235], v[164:167]\n"
"ds_read_b64_tr_b16 a[174:175], v202 offset:19456\n"
"v_mfma_f32_16x16x32_bf16 v[164:167], a[148:151], a[236:239], v[164:167]\n"
"buffer_load_dwordx4 a[32:35], v244, s[8:11], s80 offen offset:0 sc1\n"
"v_mfma_f32_16x16x32_bf16 v[168:171], a[144:147], a[240:243], v[168:171]\n"
"v_mfma_f32_16x16x32_bf16 v[168:171], a[148:151], a[244:247], v[168:171]\n"
"v_mfma_f32_16x16x32_bf16 v[172:175], a[144:147], a[248:251], v[172:175]\n"
"v_mfma_f32_16x16x32_bf16 v[172:175], a[148:151], a[252:255], v[172:175]\n"
"ds_read_b64_tr_b16 a[144:145], v199 offset:2048\n"
"v_mfma_f32_16x16x32_bf16 v[176:179], a[152:155], a[224:227], v[176:179]\n"
"ds_read_b64_tr_b16 a[146:147], v199 offset:8192\n"
"v_mfma_f32_16x16x32_bf16 v[176:179], a[156:159], a[228:231], v[176:179]\n"
"ds_read_b64_tr_b16 a[148:149], v199 offset:14336\n"
"buffer_load_dwordx4 a[36:39], v244, s[8:11], s80 offen offset:1024 sc1\n"
"v_mfma_f32_16x16x32_bf16 v[180:183], a[152:155], a[232:235], v[180:183]\n"
"ds_read_b64_tr_b16 a[150:151], v199 offset:20480\n"
"v_mfma_f32_16x16x32_bf16 v[180:183], a[156:159], a[236:239], v[180:183]\n"
"v_mfma_f32_16x16x32_bf16 v[184:187], a[152:155], a[240:243], v[184:187]\n"
"v_mfma_f32_16x16x32_bf16 v[184:187], a[156:159], a[244:247], v[184:187]\n"
"v_mfma_f32_16x16x32_bf16 v[188:191], a[152:155], a[248:251], v[188:191]\n"
"v_mfma_f32_16x16x32_bf16 v[188:191], a[156:159], a[252:255], v[188:191]\n"
"ds_read_b64_tr_b16 a[152:153], v200 offset:2048\n"
"buffer_load_dwordx4 a[40:43], v244, s[8:11], s81 offen offset:0 sc1\n"
"s_waitcnt lgkmcnt(2)\n"
"v_mfma_f32_16x16x32_bf16 v[0:3], a[160:163], a[224:227], v[0:3]\n"
"ds_read_b64_tr_b16 a[154:155], v200 offset:8192\n"
"v_mfma_f32_16x16x32_bf16 v[0:3], a[164:167], a[228:231], v[0:3]\n"
"ds_read_b64_tr_b16 a[156:157], v200 offset:14336\n"
"v_mfma_f32_16x16x32_bf16 v[4:7], a[160:163], a[232:235], v[4:7]\n"
"ds_read_b64_tr_b16 a[158:159], v200 offset:20480\n"
"v_mfma_f32_16x16x32_bf16 v[4:7], a[164:167], a[236:239], v[4:7]\n"
"v_mfma_f32_16x16x32_bf16 v[8:11], a[160:163], a[240:243], v[8:11]\n"
"v_mfma_f32_16x16x32_bf16 v[8:11], a[164:167], a[244:247], v[8:11]\n"
"buffer_load_dwordx4 a[44:47], v244, s[8:11], s81 offen offset:1024 sc1\n"
"v_mfma_f32_16x16x32_bf16 v[12:15], a[160:163], a[248:251], v[12:15]\n"
"v_mfma_f32_16x16x32_bf16 v[12:15], a[164:167], a[252:255], v[12:15]\n"
"ds_read_b64_tr_b16 a[160:161], v201 offset:2048\n"
"v_mfma_f32_16x16x32_bf16 v[16:19], a[168:171], a[224:227], v[16:19]\n"
"ds_read_b64_tr_b16 a[162:163], v201 offset:8192\n"
"v_mfma_f32_16x16x32_bf16 v[16:19], a[172:175], a[228:231], v[16:19]\n"
"ds_read_b64_tr_b16 a[164:165], v201 offset:14336\n"
"v_mfma_f32_16x16x32_bf16 v[20:23], a[168:171], a[232:235], v[20:23]\n"
"ds_read_b64_tr_b16 a[166:167], v201 offset:20480\n"
"v_mfma_f32_16x16x32_bf16 v[20:23], a[172:175], a[236:239], v[20:23]\n"
"buffer_load_dwordx4 a[48:51], v244, s[8:11], s82 offen offset:0 sc1\n"
"v_mfma_f32_16x16x32_bf16 v[24:27], a[168:171], a[240:243], v[24:27]\n"
"v_mfma_f32_16x16x32_bf16 v[24:27], a[172:175], a[244:247], v[24:27]\n"
"v_mfma_f32_16x16x32_bf16 v[28:31], a[168:171], a[248:251], v[28:31]\n"
"v_mfma_f32_16x16x32_bf16 v[28:31], a[172:175], a[252:255], v[28:31]\n"
"ds_read_b64_tr_b16 a[168:169], v202 offset:2048\n"
"v_mfma_f32_16x16x32_bf16 v[32:35], a[144:147], a[224:227], v[32:35]\n"
"ds_read_b64_tr_b16 a[170:171], v202 offset:8192\n"
"s_waitcnt lgkmcnt(2)\n"
"v_mfma_f32_16x16x32_bf16 v[32:35], a[148:151], a[228:231], v[32:35]\n"
"ds_read_b64_tr_b16 a[172:173], v202 offset:14336\n"
"buffer_load_dwordx4 a[52:55], v244, s[8:11], s82 offen offset:1024 sc1\n"
"v_mfma_f32_16x16x32_bf16 v[36:39], a[144:147], a[232:235], v[36:39]\n"
"ds_read_b64_tr_b16 a[174:175], v202 offset:20480\n"
"v_mfma_f32_16x16x32_bf16 v[36:39], a[148:151], a[236:239], v[36:39]\n"
"v_mfma_f32_16x16x32_bf16 v[40:43], a[144:147], a[240:243], v[40:43]\n"
"v_mfma_f32_16x16x32_bf16 v[40:43], a[148:151], a[244:247], v[40:43]\n"
"v_mfma_f32_16x16x32_bf16 v[44:47], a[144:147], a[248:251], v[44:47]\n"
"v_mfma_f32_16x16x32_bf16 v[44:47], a[148:151], a[252:255], v[44:47]\n"
"s_waitcnt vmcnt(25) lgkmcnt(0)\n"
"s_barrier\n"
"buffer_load_dwordx4 a[56:59], v244, s[8:11], s83 offen offset:0 sc1\n"
"v_mfma_f32_16x16x32_bf16 v[48:51], a[152:155], a[224:227], v[48:51]\n"
"ds_read_b128 a[176:179], v204 offset:0\n"
"ds_read_b128 a[180:183], v204 offset:1024\n"
"v_mfma_f32_16x16x32_bf16 v[48:51], a[156:159], a[228:231], v[48:51]\n"
"ds_read_b128 a[184:187], v204 offset:2048\n"
"ds_read_b128 a[188:191], v204 offset:3072\n"
"v_mfma_f32_16x16x32_bf16 v[52:55], a[152:155], a[232:235], v[52:55]\n"
"ds_read_b64_tr_b16 a[144:145], v199 offset:28672\n"
"v_mfma_f32_16x16x32_bf16 v[52:55], a[156:159], a[236:239], v[52:55]\n"
"ds_read_b64_tr_b16 a[146:147], v199 offset:34816\n"
"v_mfma_f32_16x16x32_bf16 v[56:59], a[152:155], a[240:243], v[56:59]\n"
"ds_read_b64_tr_b16 a[148:149], v199 offset:40960\n"
"v_mfma_f32_16x16x32_bf16 v[56:59], a[156:159], a[244:247], v[56:59]\n"
"ds_read_b64_tr_b16 a[150:151], v199 offset:47104\n"
"v_mfma_f32_16x16x32_bf16 v[60:63], a[152:155], a[248:251], v[60:63]\n"
"buffer_load_dwordx4 a[60:63], v244, s[8:11], s83 offen offset:1024 sc1\n"
"v_mfma_f32_16x16x32_bf16 v[60:63], a[156:159], a[252:255], v[60:63]\n"
"ds_read_b64_tr_b16 a[152:153], v200 offset:28672\n"
"v_mfma_f32_16x16x32_bf16 v[64:67], a[160:163], a[224:227], v[64:67]\n"
"ds_read_b64_tr_b16 a[154:155], v200 offset:34816\n"
"v_mfma_f32_16x16x32_bf16 v[64:67], a[164:167], a[228:231], v[64:67]\n"
"ds_read_b64_tr_b16 a[156:157], v200 offset:40960\n"
"v_mfma_f32_16x16x32_bf16 v[68:71], a[160:163], a[232:235], v[68:71]\n"
"ds_read_b64_tr_b16 a[158:159], v200 offset:47104\n"
"v_mfma_f32_16x16x32_bf16 v[68:71], a[164:167], a[236:239], v[68:71]\n"
"v_mfma_f32_16x16x32_bf16 v[72:75], a[160:163], a[240:243], v[72:75]\n"
"s_add_u32 m0, s57, 77824\n"
"v_mfma_f32_16x16x32_bf16 v[72:75], a[164:167], a[244:247], v[72:75]\n"
"buffer_load_dwordx4 v192, s[12:15], s84 offen offset:0 lds\n"
"v_mfma_f32_16x16x32_bf16 v[76:79], a[160:163], a[248:251], v[76:79]\n"
"v_mfma_f32_16x16x32_bf16 v[76:79], a[164:167], a[252:255], v[76:79]\n"
"ds_read_b64_tr_b16 a[160:161], v201 offset:28672\n"
"s_waitcnt lgkmcnt(2)\n"
"v_mfma_f32_16x16x32_bf16 a[120:123], a[176:179], a[224:227], a[120:123]\n"
"ds_read_b64_tr_b16 a[162:163], v201 offset:34816\n"
"v_mfma_f32_16x16x32_bf16 a[120:123], a[180:183], a[228:231], a[120:123]\n"
"ds_read_b64_tr_b16 a[164:165], v201 offset:40960\n"
"v_mfma_f32_16x16x32_bf16 a[124:127], a[176:179], a[232:235], a[124:127]\n"
"ds_read_b64_tr_b16 a[166:167], v201 offset:47104\n"
"v_mfma_f32_16x16x32_bf16 a[124:127], a[180:183], a[236:239], a[124:127]\n"
"buffer_load_dwordx4 v193, s[12:15], s84 offen offset:1024 lds\n"
"v_mfma_f32_16x16x32_bf16 a[128:131], a[176:179], a[240:243], a[128:131]\n"
"v_mfma_f32_16x16x32_bf16 a[128:131], a[180:183], a[244:247], a[128:131]\n"
"v_mfma_f32_16x16x32_bf16 a[132:135], a[176:179], a[248:251], a[132:135]\n"
"v_mfma_f32_16x16x32_bf16 a[132:135], a[180:183], a[252:255], a[132:135]\n"
"v_mfma_f32_16x16x32_bf16 a[136:139], a[184:187], a[224:227], a[136:139]\n"
"v_mfma_f32_16x16x32_bf16 a[136:139], a[188:191], a[228:231], a[136:139]\n"
"buffer_load_dwordx4 v194, s[12:15], s84 offen offset:2048 lds\n"
"v_mfma_f32_16x16x32_bf16 v[80:83], a[168:171], a[224:227], v[80:83]\n"
"s_add_u32 s84, s84, 786432\n"
"v_mfma_f32_16x16x32_bf16 v[80:83], a[172:175], a[228:231], v[80:83]\n"
"v_mfma_f32_16x16x32_bf16 v[84:87], a[168:171], a[232:235], v[84:87]\n"
"v_mfma_f32_16x16x32_bf16 v[84:87], a[172:175], a[236:239], v[84:87]\n"
"v_mfma_f32_16x16x32_bf16 v[88:91], a[168:171], a[240:243], v[88:91]\n"
"ds_read_b64_tr_b16 v[220:221], v237 offset:20480\n"
"v_mfma_f32_16x16x32_bf16 v[88:91], a[172:175], a[244:247], v[88:91]\n"
"ds_read_b64_tr_b16 v[222:223], v237 offset:20992\n"
"s_add_u32 m0, s57, 80896\n"
"v_mfma_f32_16x16x32_bf16 v[92:95], a[168:171], a[248:251], v[92:95]\n"
"buffer_load_dwordx4 v192, s[12:15], s85 offen offset:0 lds\n"
"v_mfma_f32_16x16x32_bf16 v[92:95], a[172:175], a[252:255], v[92:95]\n"
"s_waitcnt vmcnt(23)\n"
"v_mfma_f32_16x16x32_bf16 v[96:99], a[144:147], a[0:3], v[96:99]\n"
"ds_read_b64_tr_b16 a[168:169], v202 offset:28672\n"
"v_mfma_f32_16x16x32_bf16 v[96:99], a[148:151], a[4:7], v[96:99]\n"
"ds_read_b64_tr_b16 a[170:171], v202 offset:34816\n"
"v_mfma_f32_16x16x32_bf16 v[100:103], a[144:147], a[8:11], v[100:103]\n"
"ds_read_b64_tr_b16 a[172:173], v202 offset:40960\n"
"v_mfma_f32_16x16x32_bf16 v[100:103], a[148:151], a[12:15], v[100:103]\n"
"ds_read_b64_tr_b16 a[174:175], v202 offset:47104\n"
"v_mfma_f32_16x16x32_bf16 v[104:107], a[144:147], a[16:19], v[104:107]\n"
"buffer_load_dwordx4 v193, s[12:15], s85 offen offset:1024 lds\n"
"v_mfma_f32_16x16x32_bf16 v[104:107], a[148:151], a[20:23], v[104:107]\n"
"v_mfma_f32_16x16x32_bf16 v[108:111], a[144:147], a[24:27], v[108:111]\n"
"v_mfma_f32_16x16x32_bf16 v[108:111], a[148:151], a[28:31], v[108:111]\n"
"ds_read_b64_tr_b16 a[144:145], v199 offset:29696\n"
"v_mfma_f32_16x16x32_bf16 v[112:115], a[152:155], a[0:3], v[112:115]\n"
"ds_read_b64_tr_b16 a[146:147], v199 offset:35840\n"
"s_waitcnt lgkmcnt(3)\n"
"v_mfma_f32_16x16x32_bf16 v[112:115], a[156:159], a[4:7], v[112:115]\n"
"ds_read_b64_tr_b16 a[148:149], v199 offset:41984\n"
"v_mfma_f32_16x16x32_bf16 v[116:119], a[152:155], a[8:11], v[116:119]\n"
"ds_read_b64_tr_b16 a[150:151], v199 offset:48128\n"
"v_mfma_f32_16x16x32_bf16 v[116:119], a[156:159], a[12:15], v[116:119]\n"
"buffer_load_dwordx4 v194, s[12:15], s85 offen offset:2048 lds\n"
"v_mfma_f32_16x16x32_bf16 v[120:123], a[152:155], a[16:19], v[120:123]\n"
"s_add_u32 s85, s85, 786432\n"
"v_mfma_f32_16x16x32_bf16 v[120:123], a[156:159], a[20:23], v[120:123]\n"
"v_mfma_f32_16x16x32_bf16 v[124:127], a[152:155], a[24:27], v[124:127]\n"
"v_mfma_f32_16x16x32_bf16 v[124:127], a[156:159], a[28:31], v[124:127]\n"
"ds_read_b64_tr_b16 a[152:153], v200 offset:29696\n"
"v_mfma_f32_16x16x32_bf16 v[128:131], a[160:163], a[0:3], v[128:131]\n"
"ds_read_b64_tr_b16 a[154:155], v200 offset:35840\n"
"v_mfma_f32_16x16x32_bf16 v[128:131], a[164:167], a[4:7], v[128:131]\n"
"ds_read_b64_tr_b16 a[156:157], v200 offset:41984\n"
"buffer_store_dwordx4 v[220:223], v238, s[48:51], s93 offen sc1\n"
"v_mfma_f32_16x16x32_bf16 v[132:135], a[160:163], a[8:11], v[132:135]\n"
"ds_read_b64_tr_b16 a[158:159], v200 offset:48128\n"
"v_mfma_f32_16x16x32_bf16 v[132:135], a[164:167], a[12:15], v[132:135]\n"
"v_mfma_f32_16x16x32_bf16 v[136:139], a[160:163], a[16:19], v[136:139]\n"
"v_mfma_f32_16x16x32_bf16 v[136:139], a[164:167], a[20:23], v[136:139]\n"
"v_mfma_f32_16x16x32_bf16 v[140:143], a[160:163], a[24:27], v[140:143]\n"
"v_mfma_f32_16x16x32_bf16 v[140:143], a[164:167], a[28:31], v[140:143]\n"
"ds_read_b64_tr_b16 a[160:161], v201 offset:29696\n"
"s_add_u32 m0, s87, 73728\n"
"v_mfma_f32_16x16x32_bf16 v[144:147], a[168:171], a[0:3], v[144:147]\n"
"ds_read_b64_tr_b16 a[162:163], v201 offset:35840\n"
"buffer_load_dwordx4 v244, s[8:11], s86 offen sc1 lds\n"
"s_waitcnt lgkmcnt(2)\n"
"v_mfma_f32_16x16x32_bf16 v[144:147], a[172:175], a[4:7], v[144:147]\n"
"ds_read_b64_tr_b16 a[164:165], v201 offset:41984\n"
"s_add_u32 s86, s86, 10240\n"
"v_mfma_f32_16x16x32_bf16 v[148:151], a[168:171], a[8:11], v[148:151]\n"
"ds_read_b64_tr_b16 a[166:167], v201 offset:48128\n"
"v_mfma_f32_16x16x32_bf16 v[148:151], a[172:175], a[12:15], v[148:151]\n"
"v_mfma_f32_16x16x32_bf16 v[152:155], a[168:171], a[16:19], v[152:155]\n"
"v_mfma_f32_16x16x32_bf16 v[152:155], a[172:175], a[20:23], v[152:155]\n"
"v_mfma_f32_16x16x32_bf16 v[156:159], a[168:171], a[24:27], v[156:159]\n"
"buffer_load_dwordx4 a[64:67], v244, s[8:11], s80 offen offset:2048 sc1\n"
"v_mfma_f32_16x16x32_bf16 v[156:159], a[172:175], a[28:31], v[156:159]\n"
"ds_read_b64_tr_b16 a[168:169], v202 offset:29696\n"
"v_mfma_f32_16x16x32_bf16 v[160:163], a[144:147], a[0:3], v[160:163]\n"
"ds_read_b64_tr_b16 a[170:171], v202 offset:35840\n"
"v_mfma_f32_16x16x32_bf16 v[160:163], a[148:151], a[4:7], v[160:163]\n"
"ds_read_b64_tr_b16 a[172:173], v202 offset:41984\n"
"v_mfma_f32_16x16x32_bf16 v[164:167], a[144:147], a[8:11], v[164:167]\n"
"ds_read_b64_tr_b16 a[174:175], v202 offset:48128\n"
"v_mfma_f32_16x16x32_bf16 v[164:167], a[148:151], a[12:15], v[164:167]\n"
"v_mfma_f32_16x16x32_bf16 v[168:171], a[144:147], a[16:19], v[168:171]\n"
"buffer_load_dwordx4 a[68:71], v244, s[8:11], s80 offen offset:3072 sc1\n"
"v_mfma_f32_16x16x32_bf16 v[168:171], a[148:151], a[20:23], v[168:171]\n"
"s_add_u32 s80, s80, 4096\n"
"v_mfma_f32_16x16x32_bf16 v[172:175], a[144:147], a[24:27], v[172:175]\n"
"v_mfma_f32_16x16x32_bf16 v[172:175], a[148:151], a[28:31], v[172:175]\n"
"ds_read_b64_tr_b16 a[144:145], v199 offset:30720\n"
"v_mfma_f32_16x16x32_bf16 v[176:179], a[152:155], a[0:3], v[176:179]\n"
"ds_read_b64_tr_b16 a[146:147], v199 offset:36864\n"
"v_mfma_f32_16x16x32_bf16 v[176:179], a[156:159], a[4:7], v[176:179]\n"
"ds_read_b64_tr_b16 a[148:149], v199 offset:43008\n"
"v_mfma_f32_16x16x32_bf16 v[180:183], a[152:155], a[8:11], v[180:183]\n"
"ds_read_b64_tr_b16 a[150:151], v199 offset:49152\n"
"v_mfma_f32_16x16x32_bf16 v[180:183], a[156:159], a[12:15], v[180:183]\n"
"buffer_load_dwordx4 a[72:75], v244, s[8:11], s81 offen offset:2048 sc1\n"
"v_mfma_f32_16x16x32_bf16 v[184:187], a[152:155], a[16:19], v[184:187]\n"
"v_mfma_f32_16x16x32_bf16 v[184:187], a[156:159], a[20:23], v[184:187]\n"
"v_mfma_f32_16x16x32_bf16 v[188:191], a[152:155], a[24:27], v[188:191]\n"
"v_mfma_f32_16x16x32_bf16 v[188:191], a[156:159], a[28:31], v[188:191]\n"
"ds_read_b64_tr_b16 a[152:153], v200 offset:30720\n"
"s_waitcnt lgkmcnt(2)\n"
"v_mfma_f32_16x16x32_bf16 v[0:3], a[160:163], a[0:3], v[0:3]\n"
"ds_read_b64_tr_b16 a[154:155], v200 offset:36864\n"
"v_mfma_f32_16x16x32_bf16 v[0:3], a[164:167], a[4:7], v[0:3]\n"
"ds_read_b64_tr_b16 a[156:157], v200 offset:43008\n"
"buffer_load_dwordx4 a[76:79], v244, s[8:11], s81 offen offset:3072 sc1\n"
"v_mfma_f32_16x16x32_bf16 v[4:7], a[160:163], a[8:11], v[4:7]\n"
"ds_read_b64_tr_b16 a[158:159], v200 offset:49152\n"
"s_add_u32 s81, s81, 4096\n"
"v_mfma_f32_16x16x32_bf16 v[4:7], a[164:167], a[12:15], v[4:7]\n"
"v_mfma_f32_16x16x32_bf16 v[8:11], a[160:163], a[16:19], v[8:11]\n"
"v_mfma_f32_16x16x32_bf16 v[8:11], a[164:167], a[20:23], v[8:11]\n"
"v_mfma_f32_16x16x32_bf16 v[12:15], a[160:163], a[24:27], v[12:15]\n"
"v_mfma_f32_16x16x32_bf16 v[12:15], a[164:167], a[28:31], v[12:15]\n"
"ds_read_b64_tr_b16 a[160:161], v201 offset:30720\n"
"buffer_load_dwordx4 a[80:83], v244, s[8:11], s82 offen offset:2048 sc1\n"
"v_mfma_f32_16x16x32_bf16 v[16:19], a[168:171], a[0:3], v[16:19]\n"
"ds_read_b64_tr_b16 a[162:163], v201 offset:36864\n"
"v_mfma_f32_16x16x32_bf16 v[16:19], a[172:175], a[4:7], v[16:19]\n"
"ds_read_b64_tr_b16 a[164:165], v201 offset:43008\n"
"v_mfma_f32_16x16x32_bf16 v[20:23], a[168:171], a[8:11], v[20:23]\n"
"ds_read_b64_tr_b16 a[166:167], v201 offset:49152\n"
"v_mfma_f32_16x16x32_bf16 v[20:23], a[172:175], a[12:15], v[20:23]\n"
"v_mfma_f32_16x16x32_bf16 v[24:27], a[168:171], a[16:19], v[24:27]\n"
"v_mfma_f32_16x16x32_bf16 v[24:27], a[172:175], a[20:23], v[24:27]\n"
"buffer_load_dwordx4 a[84:87], v244, s[8:11], s82 offen offset:3072 sc1\n"
"v_mfma_f32_16x16x32_bf16 v[28:31], a[168:171], a[24:27], v[28:31]\n"
"s_add_u32 s82, s82, 4096\n"
"v_mfma_f32_16x16x32_bf16 v[28:31], a[172:175], a[28:31], v[28:31]\n"
"ds_read_b64_tr_b16 a[168:169], v202 offset:30720\n"
"v_mfma_f32_16x16x32_bf16 v[32:35], a[144:147], a[0:3], v[32:35]\n"
"ds_read_b64_tr_b16 a[170:171], v202 offset:36864\n"
"s_waitcnt lgkmcnt(2)\n"
"v_mfma_f32_16x16x32_bf16 v[32:35], a[148:151], a[4:7], v[32:35]\n"
"ds_read_b64_tr_b16 a[172:173], v202 offset:43008\n"
"v_mfma_f32_16x16x32_bf16 v[36:39], a[144:147], a[8:11], v[36:39]\n"
"ds_read_b64_tr_b16 a[174:175], v202 offset:49152\n"
"v_mfma_f32_16x16x32_bf16 v[36:39], a[148:151], a[12:15], v[36:39]\n"
"buffer_load_dwordx4 a[88:91], v244, s[8:11], s83 offen offset:2048 sc1\n"
"v_mfma_f32_16x16x32_bf16 v[40:43], a[144:147], a[16:19], v[40:43]\n"
"v_mfma_f32_16x16x32_bf16 v[40:43], a[148:151], a[20:23], v[40:43]\n"
"v_mfma_f32_16x16x32_bf16 v[44:47], a[144:147], a[24:27], v[44:47]\n"
"v_mfma_f32_16x16x32_bf16 v[44:47], a[148:151], a[28:31], v[44:47]\n"
"s_waitcnt vmcnt(27) lgkmcnt(0)\n"
"s_barrier\n"
"v_mfma_f32_16x16x32_bf16 v[48:51], a[152:155], a[0:3], v[48:51]\n"
"ds_read_b128 a[176:179], v204 offset:28672\n"
"ds_read_b128 a[180:183], v204 offset:29696\n"
"v_mfma_f32_16x16x32_bf16 v[48:51], a[156:159], a[4:7], v[48:51]\n"
"ds_read_b128 a[184:187], v204 offset:30720\n"
"ds_read_b128 a[188:191], v204 offset:31744\n"
"v_mfma_f32_16x16x32_bf16 v[52:55], a[152:155], a[8:11], v[52:55]\n"
"ds_read_b64_tr_b16 a[144:145], v195 offset:0\n"
"buffer_load_dwordx4 a[92:95], v244, s[8:11], s83 offen offset:3072 sc1\n"
"v_mfma_f32_16x16x32_bf16 v[52:55], a[156:159], a[12:15], v[52:55]\n"
"ds_read_b64_tr_b16 a[146:147], v195 offset:6144\n"
"s_add_u32 s83, s83, 4096\n"
"v_mfma_f32_16x16x32_bf16 v[56:59], a[152:155], a[16:19], v[56:59]\n"
"ds_read_b64_tr_b16 a[148:149], v195 offset:12288\n"
"v_mfma_f32_16x16x32_bf16 v[56:59], a[156:159], a[20:23], v[56:59]\n"
"ds_read_b64_tr_b16 a[150:151], v195 offset:18432\n"
"v_mfma_f32_16x16x32_bf16 v[60:63], a[152:155], a[24:27], v[60:63]\n"
"s_mov_b32 s44, s32\n"
"v_mfma_f32_16x16x32_bf16 v[60:63], a[156:159], a[28:31], v[60:63]\n"
"ds_read_b64_tr_b16 a[152:153], v196 offset:0\n"
"v_mfma_f32_16x16x32_bf16 v[64:67], a[160:163], a[0:3], v[64:67]\n"
"ds_read_b64_tr_b16 a[154:155], v196 offset:6144\n"
"s_add_u32 m0, s57, 106496\n"
"v_mfma_f32_16x16x32_bf16 v[64:67], a[164:167], a[4:7], v[64:67]\n"
"ds_read_b64_tr_b16 a[156:157], v196 offset:12288\n"
"buffer_load_dwordx4 v192, s[12:15], s84 offen offset:0 lds\n"
"v_mfma_f32_16x16x32_bf16 v[68:71], a[160:163], a[8:11], v[68:71]\n"
"ds_read_b64_tr_b16 a[158:159], v196 offset:18432\n"
"v_mfma_f32_16x16x32_bf16 v[68:71], a[164:167], a[12:15], v[68:71]\n"
"s_and_b32 s45, s33, 0xffff\n"
"v_mfma_f32_16x16x32_bf16 v[72:75], a[160:163], a[16:19], v[72:75]\n"
"s_mov_b32 s46, 7864320\n"
"v_mfma_f32_16x16x32_bf16 v[72:75], a[164:167], a[20:23], v[72:75]\n"
"s_add_u32 s48, s8, 23068672\n"
"s_addc_u32 s49, s9, 0\n"
"v_mfma_f32_16x16x32_bf16 v[76:79], a[160:163], a[24:27], v[76:79]\n"
"s_mov_b32 s50, 7864320\n"
"v_mfma_f32_16x16x32_bf16 v[76:79], a[164:167], a[28:31], v[76:79]\n"
"ds_read_b64_tr_b16 a[160:161], v197 offset:0\n"
"buffer_load_dwordx4 v193, s[12:15], s84 offen offset:1024 lds\n"
"s_waitcnt lgkmcnt(2)\n"
"v_mfma_f32_16x16x32_bf16 a[120:123], a[176:179], a[0:3], a[120:123]\n"
"ds_read_b64_tr_b16 a[162:163], v197 offset:6144\n"
"v_mfma_f32_16x16x32_bf16 a[120:123], a[180:183], a[4:7], a[120:123]\n"
"ds_read_b64_tr_b16 a[164:165], v197 offset:12288\n"
"v_mfma_f32_16x16x32_bf16 a[124:127], a[176:179], a[8:11], a[124:127]\n"
"ds_read_b64_tr_b16 a[166:167], v197 offset:18432\n"
"v_mfma_f32_16x16x32_bf16 a[124:127], a[180:183], a[12:15], a[124:127]\n"
"s_mov_b32 s71, s92\n"
"v_mfma_f32_16x16x32_bf16 a[128:131], a[176:179], a[16:19], a[128:131]\n"
"s_mov_b32 s72, s93\n"
"v_mfma_f32_16x16x32_bf16 a[128:131], a[180:183], a[20:23], a[128:131]\n"
"s_mov_b32 s73, 1024000\n"
"buffer_load_dwordx4 v194, s[12:15], s84 offen offset:2048 lds\n"
"v_mfma_f32_16x16x32_bf16 a[132:135], a[176:179], a[24:27], a[132:135]\n"
"s_mov_b32 s74, 1024000\n"
"s_add_u32 s84, s84, 786432\n"
"v_mfma_f32_16x16x32_bf16 a[132:135], a[180:183], a[28:31], a[132:135]\n"
"s_mov_b32 s59, 6\n"
"v_mfma_f32_16x16x32_bf16 a[136:139], a[184:187], a[0:3], a[136:139]\n"
"s_and_b32 s35, s35, 0xffff\n"
"v_mfma_f32_16x16x32_bf16 a[136:139], a[188:191], a[4:7], a[136:139]\n"
"s_and_b32 s37, s37, 0xffff\n"
"v_mfma_f32_16x16x32_bf16 v[80:83], a[168:171], a[0:3], v[80:83]\n"
"s_add_u32 s75, s87, 98304\n"
"v_mfma_f32_16x16x32_bf16 v[80:83], a[172:175], a[4:7], v[80:83]\n"
"v_add_u32 v237, 98304, v237\n"
"s_add_u32 m0, s57, 109568\n"
"v_mfma_f32_16x16x32_bf16 v[84:87], a[168:171], a[8:11], v[84:87]\n"
"buffer_load_dwordx4 v192, s[12:15], s85 offen offset:0 lds\n"
"v_mfma_f32_16x16x32_bf16 v[84:87], a[172:175], a[12:15], v[84:87]\n"
"v_mfma_f32_16x16x32_bf16 v[88:91], a[168:171], a[16:19], v[88:91]\n"
"v_mfma_f32_16x16x32_bf16 v[88:91], a[172:175], a[20:23], v[88:91]\n"
"v_mfma_f32_16x16x32_bf16 v[92:95], a[168:171], a[24:27], v[92:95]\n"
"v_mfma_f32_16x16x32_bf16 v[92:95], a[172:175], a[28:31], v[92:95]\n"
"s_waitcnt vmcnt(20)\n"
"v_mfma_f32_16x16x32_bf16 v[96:99], a[144:147], a[32:35], v[96:99]\n"
"ds_read_b64_tr_b16 a[168:169], v198 offset:0\n"
"buffer_load_dwordx4 v193, s[12:15], s85 offen offset:1024 lds\n"
"v_mfma_f32_16x16x32_bf16 v[96:99], a[148:151], a[36:39], v[96:99]\n"
"ds_read_b64_tr_b16 a[170:171], v198 offset:6144\n"
"v_mfma_f32_16x16x32_bf16 v[100:103], a[144:147], a[40:43], v[100:103]\n"
"ds_read_b64_tr_b16 a[172:173], v198 offset:12288\n"
"v_mfma_f32_16x16x32_bf16 v[100:103], a[148:151], a[44:47], v[100:103]\n"
"ds_read_b64_tr_b16 a[174:175], v198 offset:18432\n"
"v_mfma_f32_16x16x32_bf16 v[104:107], a[144:147], a[48:51], v[104:107]\n"
"v_mfma_f32_16x16x32_bf16 v[104:107], a[148:151], a[52:55], v[104:107]\n"
"v_mfma_f32_16x16x32_bf16 v[108:111], a[144:147], a[56:59], v[108:111]\n"
"buffer_load_dwordx4 v194, s[12:15], s85 offen offset:2048 lds\n"
"v_mfma_f32_16x16x32_bf16 v[108:111], a[148:151], a[60:63], v[108:111]\n"
"ds_read_b64_tr_b16 a[144:145], v195 offset:1024\n"
"s_add_u32 s85, s85, 786432\n"
"v_mfma_f32_16x16x32_bf16 v[112:115], a[152:155], a[32:35], v[112:115]\n"
"ds_read_b64_tr_b16 a[146:147], v195 offset:7168\n"
"s_waitcnt lgkmcnt(3)\n"
"v_mfma_f32_16x16x32_bf16 v[112:115], a[156:159], a[36:39], v[112:115]\n"
"ds_read_b64_tr_b16 a[148:149], v195 offset:13312\n"
"v_mfma_f32_16x16x32_bf16 v[116:119], a[152:155], a[40:43], v[116:119]\n"
"ds_read_b64_tr_b16 a[150:151], v195 offset:19456\n"
"v_mfma_f32_16x16x32_bf16 v[116:119], a[156:159], a[44:47], v[116:119]\n"
"v_mfma_f32_16x16x32_bf16 v[120:123], a[152:155], a[48:51], v[120:123]\n"
"s_add_u32 m0, s87, 102400\n"
"v_mfma_f32_16x16x32_bf16 v[120:123], a[156:159], a[52:55], v[120:123]\n"
"buffer_load_dwordx4 v244, s[8:11], s86 offen sc1 lds\n"
"v_mfma_f32_16x16x32_bf16 v[124:127], a[152:155], a[56:59], v[124:127]\n"
"s_add_u32 s86, s86, 10240\n"
"v_mfma_f32_16x16x32_bf16 v[124:127], a[156:159], a[60:63], v[124:127]\n"
"ds_read_b64_tr_b16 a[152:153], v196 offset:1024\n"
"v_mfma_f32_16x16x32_bf16 v[128:131], a[160:163], a[32:35], v[128:131]\n"
"ds_read_b64_tr_b16 a[154:155], v196 offset:7168\n"
"v_mfma_f32_16x16x32_bf16 v[128:131], a[164:167], a[36:39], v[128:131]\n"
"ds_read_b64_tr_b16 a[156:157], v196 offset:13312\n"
"v_mfma_f32_16x16x32_bf16 v[132:135], a[160:163], a[40:43], v[132:135]\n"
"ds_read_b64_tr_b16 a[158:159], v196 offset:19456\n"
"v_mfma_f32_16x16x32_bf16 v[132:135], a[164:167], a[44:47], v[132:135]\n"
"buffer_load_dwordx4 a[192:195], v244, s[8:11], s80 offen offset:0 sc1\n"
"v_mfma_f32_16x16x32_bf16 v[136:139], a[160:163], a[48:51], v[136:139]\n"
"v_mfma_f32_16x16x32_bf16 v[136:139], a[164:167], a[52:55], v[136:139]\n"
"v_mfma_f32_16x16x32_bf16 v[140:143], a[160:163], a[56:59], v[140:143]\n"
"v_mfma_f32_16x16x32_bf16 v[140:143], a[164:167], a[60:63], v[140:143]\n"
"ds_read_b64_tr_b16 a[160:161], v197 offset:1024\n"
"v_mfma_f32_16x16x32_bf16 v[144:147], a[168:171], a[32:35], v[144:147]\n"
"ds_read_b64_tr_b16 a[162:163], v197 offset:7168\n"
"s_waitcnt lgkmcnt(2)\n"
"v_mfma_f32_16x16x32_bf16 v[144:147], a[172:175], a[36:39], v[144:147]\n"
"ds_read_b64_tr_b16 a[164:165], v197 offset:13312\n"
"buffer_load_dwordx4 a[196:199], v244, s[8:11], s80 offen offset:1024 sc1\n"
"v_mfma_f32_16x16x32_bf16 v[148:151], a[168:171], a[40:43], v[148:151]\n"
"ds_read_b64_tr_b16 a[166:167], v197 offset:19456\n"
"v_mfma_f32_16x16x32_bf16 v[148:151], a[172:175], a[44:47], v[148:151]\n"
"v_mfma_f32_16x16x32_bf16 v[152:155], a[168:171], a[48:51], v[152:155]\n"
"v_mfma_f32_16x16x32_bf16 v[152:155], a[172:175], a[52:55], v[152:155]\n"
"v_mfma_f32_16x16x32_bf16 v[156:159], a[168:171], a[56:59], v[156:159]\n"
"v_mfma_f32_16x16x32_bf16 v[156:159], a[172:175], a[60:63], v[156:159]\n"
"ds_read_b64_tr_b16 a[168:169], v198 offset:1024\n"
"buffer_load_dwordx4 a[200:203], v244, s[8:11], s81 offen offset:0 sc1\n"
"v_mfma_f32_16x16x32_bf16 v[160:163], a[144:147], a[32:35], v[160:163]\n"
"ds_read_b64_tr_b16 a[170:171], v198 offset:7168\n"
"v_mfma_f32_16x16x32_bf16 v[160:163], a[148:151], a[36:39], v[160:163]\n"
"ds_read_b64_tr_b16 a[172:173], v198 offset:13312\n"
"v_mfma_f32_16x16x32_bf16 v[164:167], a[144:147], a[40:43], v[164:167]\n"
"ds_read_b64_tr_b16 a[174:175], v198 offset:19456\n"
"v_mfma_f32_16x16x32_bf16 v[164:167], a[148:151], a[44:47], v[164:167]\n"
"v_mfma_f32_16x16x32_bf16 v[168:171], a[144:147], a[48:51], v[168:171]\n"
"v_mfma_f32_16x16x32_bf16 v[168:171], a[148:151], a[52:55], v[168:171]\n"
"buffer_load_dwordx4 a[204:207], v244, s[8:11], s81 offen offset:1024 sc1\n"
"v_mfma_f32_16x16x32_bf16 v[172:175], a[144:147], a[56:59], v[172:175]\n"
"v_mfma_f32_16x16x32_bf16 v[172:175], a[148:151], a[60:63], v[172:175]\n"
"ds_read_b64_tr_b16 a[144:145], v195 offset:2048\n"
"v_mfma_f32_16x16x32_bf16 v[176:179], a[152:155], a[32:35], v[176:179]\n"
"ds_read_b64_tr_b16 a[146:147], v195 offset:8192\n"
"v_mfma_f32_16x16x32_bf16 v[176:179], a[156:159], a[36:39], v[176:179]\n"
"ds_read_b64_tr_b16 a[148:149], v195 offset:14336\n"
"v_mfma_f32_16x16x32_bf16 v[180:183], a[152:155], a[40:43], v[180:183]\n"
"ds_read_b64_tr_b16 a[150:151], v195 offset:20480\n"
"v_mfma_f32_16x16x32_bf16 v[180:183], a[156:159], a[44:47], v[180:183]\n"
"buffer_load_dwordx4 a[208:211], v244, s[8:11], s82 offen offset:0 sc1\n"
"v_mfma_f32_16x16x32_bf16 v[184:187], a[152:155], a[48:51], v[184:187]\n"
"v_mfma_f32_16x16x32_bf16 v[184:187], a[156:159], a[52:55], v[184:187]\n"
"v_mfma_f32_16x16x32_bf16 v[188:191], a[152:155], a[56:59], v[188:191]\n"
"v_mfma_f32_16x16x32_bf16 v[188:191], a[156:159], a[60:63], v[188:191]\n"
"ds_read_b64_tr_b16 a[152:153], v196 offset:2048\n"
"s_waitcnt lgkmcnt(2)\n"
"v_mfma_f32_16x16x32_bf16 v[0:3], a[160:163], a[32:35], v[0:3]\n"
"ds_read_b64_tr_b16 a[154:155], v196 offset:8192\n"
"v_mfma_f32_16x16x32_bf16 v[0:3], a[164:167], a[36:39], v[0:3]\n"
"ds_read_b64_tr_b16 a[156:157], v196 offset:14336\n"
"buffer_load_dwordx4 a[212:215], v244, s[8:11], s82 offen offset:1024 sc1\n"
"v_mfma_f32_16x16x32_bf16 v[4:7], a[160:163], a[40:43], v[4:7]\n"
"ds_read_b64_tr_b16 a[158:159], v196 offset:20480\n"
"v_mfma_f32_16x16x32_bf16 v[4:7], a[164:167], a[44:47], v[4:7]\n"
"v_mfma_f32_16x16x32_bf16 v[8:11], a[160:163], a[48:51], v[8:11]\n"
"v_mfma_f32_16x16x32_bf16 v[8:11], a[164:167], a[52:55], v[8:11]\n"
"v_mfma_f32_16x16x32_bf16 v[12:15], a[160:163], a[56:59], v[12:15]\n"
"v_mfma_f32_16x16x32_bf16 v[12:15], a[164:167], a[60:63], v[12:15]\n"
"ds_read_b64_tr_b16 a[160:161], v197 offset:2048\n"
"buffer_load_dwordx4 a[216:219], v244, s[8:11], s83 offen offset:0 sc1\n"
"v_mfma_f32_16x16x32_bf16 v[16:19], a[168:171], a[32:35], v[16:19]\n"
"ds_read_b64_tr_b16 a[162:163], v197 offset:8192\n"
"v_mfma_f32_16x16x32_bf16 v[16:19], a[172:175], a[36:39], v[16:19]\n"
"ds_read_b64_tr_b16 a[164:165], v197 offset:14336\n"
"v_mfma_f32_16x16x32_bf16 v[20:23], a[168:171], a[40:43], v[20:23]\n"
"ds_read_b64_tr_b16 a[166:167], v197 offset:20480\n"
"v_mfma_f32_16x16x32_bf16 v[20:23], a[172:175], a[44:47], v[20:23]\n"
"v_mfma_f32_16x16x32_bf16 v[24:27], a[168:171], a[48:51], v[24:27]\n"
"v_mfma_f32_16x16x32_bf16 v[24:27], a[172:175], a[52:55], v[24:27]\n"
"buffer_load_dwordx4 a[220:223], v244, s[8:11], s83 offen offset:1024 sc1\n"
"v_mfma_f32_16x16x32_bf16 v[28:31], a[168:171], a[56:59], v[28:31]\n"
"v_mfma_f32_16x16x32_bf16 v[28:31], a[172:175], a[60:63], v[28:31]\n"
"ds_read_b64_tr_b16 a[168:169], v198 offset:2048\n"
"v_mfma_f32_16x16x32_bf16 v[32:35], a[144:147], a[32:35], v[32:35]\n"
"ds_read_b64_tr_b16 a[170:171], v198 offset:8192\n"
"s_waitcnt lgkmcnt(2)\n"
"v_mfma_f32_16x16x32_bf16 v[32:35], a[148:151], a[36:39], v[32:35]\n"
"ds_read_b64_tr_b16 a[172:173], v198 offset:14336\n"
"v_mfma_f32_16x16x32_bf16 v[36:39], a[144:147], a[40:43], v[36:39]\n"
"ds_read_b64_tr_b16 a[174:175], v198 offset:20480\n"
"v_mfma_f32_16x16x32_bf16 v[36:39], a[148:151], a[44:47], v[36:39]\n"
"s_add_u32 m0, s57, 135168\n"
"v_mfma_f32_16x16x32_bf16 v[40:43], a[144:147], a[48:51], v[40:43]\n"
"buffer_load_dwordx4 v192, s[12:15], s84 offen offset:0 lds\n"
"v_mfma_f32_16x16x32_bf16 v[40:43], a[148:151], a[52:55], v[40:43]\n"
"v_mfma_f32_16x16x32_bf16 v[44:47], a[144:147], a[56:59], v[44:47]\n"
"v_mfma_f32_16x16x32_bf16 v[44:47], a[148:151], a[60:63], v[44:47]\n"
"s_waitcnt vmcnt(24) lgkmcnt(0)\n"
"s_barrier\n"
"v_mfma_f32_16x16x32_bf16 v[48:51], a[152:155], a[32:35], v[48:51]\n"
"ds_read_b128 a[176:179], v203 offset:0\n"
"ds_read_b128 a[180:183], v203 offset:1024\n"
"v_mfma_f32_16x16x32_bf16 v[48:51], a[156:159], a[36:39], v[48:51]\n"
"ds_read_b128 a[184:187], v203 offset:2048\n"
"ds_read_b128 a[188:191], v203 offset:3072\n"
"v_mfma_f32_16x16x32_bf16 v[52:55], a[152:155], a[40:43], v[52:55]\n"
"ds_read_b64_tr_b16 a[144:145], v195 offset:28672\n"
"buffer_load_dwordx4 v193, s[12:15], s84 offen offset:1024 lds\n"
"v_mfma_f32_16x16x32_bf16 v[52:55], a[156:159], a[44:47], v[52:55]\n"
"ds_read_b64_tr_b16 a[146:147], v195 offset:34816\n"
"v_mfma_f32_16x16x32_bf16 v[56:59], a[152:155], a[48:51], v[56:59]\n"
"ds_read_b64_tr_b16 a[148:149], v195 offset:40960\n"
"v_mfma_f32_16x16x32_bf16 v[56:59], a[156:159], a[52:55], v[56:59]\n"
"ds_read_b64_tr_b16 a[150:151], v195 offset:47104\n"
"v_mfma_f32_16x16x32_bf16 v[60:63], a[152:155], a[56:59], v[60:63]\n"
"v_mfma_f32_16x16x32_bf16 v[60:63], a[156:159], a[60:63], v[60:63]\n"
"ds_read_b64_tr_b16 a[152:153], v196 offset:28672\n"
"v_mfma_f32_16x16x32_bf16 v[64:67], a[160:163], a[32:35], v[64:67]\n"
"ds_read_b64_tr_b16 a[154:155], v196 offset:34816\n"
"buffer_load_dwordx4 v194, s[12:15], s84 offen offset:2048 lds\n"
"v_mfma_f32_16x16x32_bf16 v[64:67], a[164:167], a[36:39], v[64:67]\n"
"ds_read_b64_tr_b16 a[156:157], v196 offset:40960\n"
"s_add_u32 s84, s84, 786432\n"
"v_mfma_f32_16x16x32_bf16 v[68:71], a[160:163], a[40:43], v[68:71]\n"
"ds_read_b64_tr_b16 a[158:159], v196 offset:47104\n"
"v_mfma_f32_16x16x32_bf16 v[68:71], a[164:167], a[44:47], v[68:71]\n"
"v_mfma_f32_16x16x32_bf16 v[72:75], a[160:163], a[48:51], v[72:75]\n"
"v_mfma_f32_16x16x32_bf16 v[72:75], a[164:167], a[52:55], v[72:75]\n"
"v_mfma_f32_16x16x32_bf16 v[76:79], a[160:163], a[56:59], v[76:79]\n"
"s_add_u32 m0, s57, 138240\n"
"v_mfma_f32_16x16x32_bf16 v[76:79], a[164:167], a[60:63], v[76:79]\n"
"ds_read_b64_tr_b16 a[160:161], v197 offset:28672\n"
"buffer_load_dwordx4 v192, s[12:15], s85 offen offset:0 lds\n"
"s_waitcnt lgkmcnt(2)\n"
"v_mfma_f32_16x16x32_bf16 a[120:123], a[176:179], a[32:35], a[120:123]\n"
"ds_read_b64_tr_b16 a[162:163], v197 offset:34816\n"
"v_mfma_f32_16x16x32_bf16 a[120:123], a[180:183], a[36:39], a[120:123]\n"
"ds_read_b64_tr_b16 a[164:165], v197 offset:40960\n"
"v_mfma_f32_16x16x32_bf16 a[124:127], a[176:179], a[40:43], a[124:127]\n"
"ds_read_b64_tr_b16 a[166:167], v197 offset:47104\n"
"v_mfma_f32_16x16x32_bf16 a[124:127], a[180:183], a[44:47], a[124:127]\n"
"v_mfma_f32_16x16x32_bf16 a[128:131], a[176:179], a[48:51], a[128:131]\n"
"v_mfma_f32_16x16x32_bf16 a[128:131], a[180:183], a[52:55], a[128:131]\n"
"buffer_load_dwordx4 v193, s[12:15], s85 offen offset:1024 lds\n"
"v_mfma_f32_16x16x32_bf16 a[132:135], a[176:179], a[56:59], a[132:135]\n"
"v_mfma_f32_16x16x32_bf16 a[132:135], a[180:183], a[60:63], a[132:135]\n"
"v_mfma_f32_16x16x32_bf16 a[136:139], a[184:187], a[32:35], a[136:139]\n"
"v_mfma_f32_16x16x32_bf16 a[136:139], a[188:191], a[36:39], a[136:139]\n"
"v_mfma_f32_16x16x32_bf16 v[80:83], a[168:171], a[32:35], v[80:83]\n"
"v_mfma_f32_16x16x32_bf16 v[80:83], a[172:175], a[36:39], v[80:83]\n"
"buffer_load_dwordx4 v194, s[12:15], s85 offen offset:2048 lds\n"
"v_mfma_f32_16x16x32_bf16 v[84:87], a[168:171], a[40:43], v[84:87]\n"
"s_add_u32 s85, s85, 786432\n"
"v_mfma_f32_16x16x32_bf16 v[84:87], a[172:175], a[44:47], v[84:87]\n"
"v_mul_f32 v209, v205, v217\n"
"v_mfma_f32_16x16x32_bf16 v[88:91], a[168:171], a[48:51], v[88:91]\n"
"v_mul_f32 v205, v205, v213\n"
"v_mfma_f32_16x16x32_bf16 v[88:91], a[172:175], a[52:55], v[88:91]\n"
"v_mul_f32 v206, v206, v214\n"
"v_mfma_f32_16x16x32_bf16 v[92:95], a[168:171], a[56:59], v[92:95]\n"
"v_mul_f32 v207, v207, v215\n"
"v_mfma_f32_16x16x32_bf16 v[92:95], a[172:175], a[60:63], v[92:95]\n"
"v_mul_f32 v208, v208, v216\n"
"s_add_u32 m0, s87, 131072\n"
"s_waitcnt vmcnt(21)\n"
"v_mfma_f32_16x16x32_bf16 v[96:99], a[144:147], a[64:67], v[96:99]\n"
"ds_read_b64_tr_b16 a[168:169], v198 offset:28672\n"
"buffer_load_dwordx4 v244, s[8:11], s86 offen sc1 lds\n"
"v_mfma_f32_16x16x32_bf16 v[96:99], a[148:151], a[68:71], v[96:99]\n"
"ds_read_b64_tr_b16 a[170:171], v198 offset:34816\n"
"s_add_u32 s86, s86, 10240\n"
"v_mfma_f32_16x16x32_bf16 v[100:103], a[144:147], a[72:75], v[100:103]\n"
"ds_read_b64_tr_b16 a[172:173], v198 offset:40960\n"
"v_mfma_f32_16x16x32_bf16 v[100:103], a[148:151], a[76:79], v[100:103]\n"
"ds_read_b64_tr_b16 a[174:175], v198 offset:47104\n"
"v_mfma_f32_16x16x32_bf16 v[104:107], a[144:147], a[80:83], v[104:107]\n"
"v_mfma_f32_16x16x32_bf16 v[104:107], a[148:151], a[84:87], v[104:107]\n"
"v_mfma_f32_16x16x32_bf16 v[108:111], a[144:147], a[88:91], v[108:111]\n"
"s_add_u32 m0, s87, 159744\n"
"v_mfma_f32_16x16x32_bf16 v[108:111], a[148:151], a[92:95], v[108:111]\n"
"ds_read_b64_tr_b16 a[144:145], v195 offset:29696\n"
"buffer_load_dwordx4 v244, s[8:11], s86 offen sc1 lds\n"
"v_mfma_f32_16x16x32_bf16 v[112:115], a[152:155], a[64:67], v[112:115]\n"
"ds_read_b64_tr_b16 a[146:147], v195 offset:35840\n"
"s_add_u32 s86, s86, 10240\n"
"s_waitcnt lgkmcnt(3)\n"
"v_mfma_f32_16x16x32_bf16 v[112:115], a[156:159], a[68:71], v[112:115]\n"
"ds_read_b64_tr_b16 a[148:149], v195 offset:41984\n"
"v_mfma_f32_16x16x32_bf16 v[116:119], a[152:155], a[72:75], v[116:119]\n"
"ds_read_b64_tr_b16 a[150:151], v195 offset:48128\n"
"v_mfma_f32_16x16x32_bf16 v[116:119], a[156:159], a[76:79], v[116:119]\n"
"v_mfma_f32_16x16x32_bf16 v[120:123], a[152:155], a[80:83], v[120:123]\n"
"v_mfma_f32_16x16x32_bf16 v[120:123], a[156:159], a[84:87], v[120:123]\n"
"buffer_load_dwordx4 a[224:227], v244, s[8:11], s80 offen offset:2048 sc1\n"
"v_mfma_f32_16x16x32_bf16 v[124:127], a[152:155], a[88:91], v[124:127]\n"
"v_mfma_f32_16x16x32_bf16 v[124:127], a[156:159], a[92:95], v[124:127]\n"
"ds_read_b64_tr_b16 a[152:153], v196 offset:29696\n"
"v_mfma_f32_16x16x32_bf16 v[128:131], a[160:163], a[64:67], v[128:131]\n"
"ds_read_b64_tr_b16 a[154:155], v196 offset:35840\n"
"v_mfma_f32_16x16x32_bf16 v[128:131], a[164:167], a[68:71], v[128:131]\n"
"ds_read_b64_tr_b16 a[156:157], v196 offset:41984\n"
"v_mfma_f32_16x16x32_bf16 v[132:135], a[160:163], a[72:75], v[132:135]\n"
"ds_read_b64_tr_b16 a[158:159], v196 offset:48128\n"
"v_mfma_f32_16x16x32_bf16 v[132:135], a[164:167], a[76:79], v[132:135]\n"
"buffer_load_dwordx4 a[228:231], v244, s[8:11], s80 offen offset:3072 sc1\n"
"v_mfma_f32_16x16x32_bf16 v[136:139], a[160:163], a[80:83], v[136:139]\n"
"s_add_u32 s80, s80, 4096\n"
"v_mfma_f32_16x16x32_bf16 v[136:139], a[164:167], a[84:87], v[136:139]\n"
"v_mfma_f32_16x16x32_bf16 v[140:143], a[160:163], a[88:91], v[140:143]\n"
"v_mfma_f32_16x16x32_bf16 v[140:143], a[164:167], a[92:95], v[140:143]\n"
"ds_read_b64_tr_b16 a[160:161], v197 offset:29696\n"
"v_mfma_f32_16x16x32_bf16 v[144:147], a[168:171], a[64:67], v[144:147]\n"
"ds_read_b64_tr_b16 a[162:163], v197 offset:35840\n"
"s_waitcnt lgkmcnt(2)\n"
"v_mfma_f32_16x16x32_bf16 v[144:147], a[172:175], a[68:71], v[144:147]\n"
"ds_read_b64_tr_b16 a[164:165], v197 offset:41984\n"
"buffer_load_dwordx4 a[232:235], v244, s[8:11], s81 offen offset:2048 sc1\n"
"v_mfma_f32_16x16x32_bf16 v[148:151], a[168:171], a[72:75], v[148:151]\n"
"ds_read_b64_tr_b16 a[166:167], v197 offset:48128\n"
"v_mfma_f32_16x16x32_bf16 v[148:151], a[172:175], a[76:79], v[148:151]\n"
"v_mfma_f32_16x16x32_bf16 v[152:155], a[168:171], a[80:83], v[152:155]\n"
"v_mfma_f32_16x16x32_bf16 v[152:155], a[172:175], a[84:87], v[152:155]\n"
"v_mfma_f32_16x16x32_bf16 v[156:159], a[168:171], a[88:91], v[156:159]\n"
"v_mfma_f32_16x16x32_bf16 v[156:159], a[172:175], a[92:95], v[156:159]\n"
"ds_read_b64_tr_b16 a[168:169], v198 offset:29696\n"
"buffer_load_dwordx4 a[236:239], v244, s[8:11], s81 offen offset:3072 sc1\n"
"v_mfma_f32_16x16x32_bf16 v[160:163], a[144:147], a[64:67], v[160:163]\n"
"ds_read_b64_tr_b16 a[170:171], v198 offset:35840\n"
"s_add_u32 s81, s81, 4096\n"
"v_mfma_f32_16x16x32_bf16 v[160:163], a[148:151], a[68:71], v[160:163]\n"
"ds_read_b64_tr_b16 a[172:173], v198 offset:41984\n"
"v_mfma_f32_16x16x32_bf16 v[164:167], a[144:147], a[72:75], v[164:167]\n"
"ds_read_b64_tr_b16 a[174:175], v198 offset:48128\n"
"v_mfma_f32_16x16x32_bf16 v[164:167], a[148:151], a[76:79], v[164:167]\n"
"v_mfma_f32_16x16x32_bf16 v[168:171], a[144:147], a[80:83], v[168:171]\n"
"v_mfma_f32_16x16x32_bf16 v[168:171], a[148:151], a[84:87], v[168:171]\n"
"buffer_load_dwordx4 a[240:243], v244, s[8:11], s82 offen offset:2048 sc1\n"
"v_mfma_f32_16x16x32_bf16 v[172:175], a[144:147], a[88:91], v[172:175]\n"
"v_mfma_f32_16x16x32_bf16 v[172:175], a[148:151], a[92:95], v[172:175]\n"
"ds_read_b64_tr_b16 a[144:145], v195 offset:30720\n"
"v_mfma_f32_16x16x32_bf16 v[176:179], a[152:155], a[64:67], v[176:179]\n"
"ds_read_b64_tr_b16 a[146:147], v195 offset:36864\n"
"v_mfma_f32_16x16x32_bf16 v[176:179], a[156:159], a[68:71], v[176:179]\n"
"ds_read_b64_tr_b16 a[148:149], v195 offset:43008\n"
"v_mfma_f32_16x16x32_bf16 v[180:183], a[152:155], a[72:75], v[180:183]\n"
"ds_read_b64_tr_b16 a[150:151], v195 offset:49152\n"
"v_mfma_f32_16x16x32_bf16 v[180:183], a[156:159], a[76:79], v[180:183]\n"
"buffer_load_dwordx4 a[244:247], v244, s[8:11], s82 offen offset:3072 sc1\n"
"v_mfma_f32_16x16x32_bf16 v[184:187], a[152:155], a[80:83], v[184:187]\n"
"s_add_u32 s82, s82, 4096\n"
"v_mfma_f32_16x16x32_bf16 v[184:187], a[156:159], a[84:87], v[184:187]\n"
"v_mfma_f32_16x16x32_bf16 v[188:191], a[152:155], a[88:91], v[188:191]\n"
"v_mfma_f32_16x16x32_bf16 v[188:191], a[156:159], a[92:95], v[188:191]\n"
"ds_read_b64_tr_b16 a[152:153], v196 offset:30720\n"
"s_waitcnt lgkmcnt(2)\n"
"v_mfma_f32_16x16x32_bf16 v[0:3], a[160:163], a[64:67], v[0:3]\n"
"ds_read_b64_tr_b16 a[154:155], v196 offset:36864\n"
"v_mfma_f32_16x16x32_bf16 v[0:3], a[164:167], a[68:71], v[0:3]\n"
"ds_read_b64_tr_b16 a[156:157], v196 offset:43008\n"
"buffer_load_dwordx4 a[248:251], v244, s[8:11], s83 offen offset:2048 sc1\n"
"v_mfma_f32_16x16x32_bf16 v[4:7], a[160:163], a[72:75], v[4:7]\n"
"ds_read_b64_tr_b16 a[158:159], v196 offset:49152\n"
"v_mfma_f32_16x16x32_bf16 v[4:7], a[164:167], a[76:79], v[4:7]\n"
"v_mfma_f32_16x16x32_bf16 v[8:11], a[160:163], a[80:83], v[8:11]\n"
"v_mfma_f32_16x16x32_bf16 v[8:11], a[164:167], a[84:87], v[8:11]\n"
"v_mfma_f32_16x16x32_bf16 v[12:15], a[160:163], a[88:91], v[12:15]\n"
"v_mfma_f32_16x16x32_bf16 v[12:15], a[164:167], a[92:95], v[12:15]\n"
"ds_read_b64_tr_b16 a[160:161], v197 offset:30720\n"
"buffer_load_dwordx4 a[252:255], v244, s[8:11], s83 offen offset:3072 sc1\n"
"v_mfma_f32_16x16x32_bf16 v[16:19], a[168:171], a[64:67], v[16:19]\n"
"ds_read_b64_tr_b16 a[162:163], v197 offset:36864\n"
"s_add_u32 s83, s83, 4096\n"
"v_mfma_f32_16x16x32_bf16 v[16:19], a[172:175], a[68:71], v[16:19]\n"
"ds_read_b64_tr_b16 a[164:165], v197 offset:43008\n"
"v_mfma_f32_16x16x32_bf16 v[20:23], a[168:171], a[72:75], v[20:23]\n"
"ds_read_b64_tr_b16 a[166:167], v197 offset:49152\n"
"s_mul_i32 s6, s52, 5120\n"
"v_mfma_f32_16x16x32_bf16 v[20:23], a[172:175], a[76:79], v[20:23]\n"
"s_lshr_b32 s88, s56, 3\n"
"s_lshl2_add_u32 s88, s55, s88\n"
"v_mfma_f32_16x16x32_bf16 v[24:27], a[168:171], a[80:83], v[24:27]\n"
"s_lshl2_add_u32 s89, s88, s52\n"
"s_lshr_b32 s88, s56, 4\n"
"v_mfma_f32_16x16x32_bf16 v[24:27], a[172:175], a[84:87], v[24:27]\n"
"s_mov_b32 m0, s63\n"
"s_lshl3_add_u32 s88, s88, s55\n"
"v_mfma_f32_16x16x32_bf16 v[28:31], a[168:171], a[88:91], v[28:31]\n"
"buffer_load_dwordx4 v224, s[16:19], s61 offen offset:0 lds\n"
"v_mfma_f32_16x16x32_bf16 v[28:31], a[172:175], a[92:95], v[28:31]\n"
"ds_read_b64_tr_b16 a[168:169], v198 offset:30720\n"
"s_lshl_b32 s33, s88, 10\n"
"v_mfma_f32_16x16x32_bf16 v[32:35], a[144:147], a[64:67], v[32:35]\n"
"ds_read_b64_tr_b16 a[170:171], v198 offset:36864\n"
"s_and_b32 s90, s56, 7\n"
"s_waitcnt lgkmcnt(2)\n"
"v_mfma_f32_16x16x32_bf16 v[32:35], a[148:151], a[68:71], v[32:35]\n"
"ds_read_b64_tr_b16 a[172:173], v198 offset:43008\n"
"s_mul_i32 s5, s89, 20480\n"
"v_mfma_f32_16x16x32_bf16 v[36:39], a[144:147], a[72:75], v[36:39]\n"
"ds_read_b64_tr_b16 a[174:175], v198 offset:49152\n"
"s_lshl3_add_u32 s89, s89, s90\n"
"v_mfma_f32_16x16x32_bf16 v[36:39], a[148:151], a[76:79], v[36:39]\n"
"s_lshl_b32 s89, s89, 7\n"
"s_and_b32 s88, s90, 1\n"
"v_mfma_f32_16x16x32_bf16 v[40:43], a[144:147], a[80:83], v[40:43]\n"
"buffer_load_dwordx4 v225, s[16:19], s61 offen offset:1024 lds\n"
"v_mfma_f32_16x16x32_bf16 v[40:43], a[148:151], a[84:87], v[40:43]\n"
"s_mul_i32 s88, s88, 120\n"
"s_sub_u32 s89, s89, s88\n"
"v_mfma_f32_16x16x32_bf16 v[44:47], a[144:147], a[88:91], v[44:47]\n"
"s_add_u32 s31, s89, 262144\n"
"s_mul_i32 s91, s90, 5120\n"
"v_mfma_f32_16x16x32_bf16 v[44:47], a[148:151], a[92:95], v[44:47]\n"
"s_waitcnt vmcnt(26) lgkmcnt(0)\n"
"s_barrier\n"
"v_mfma_f32_16x16x32_bf16 v[48:51], a[152:155], a[64:67], v[48:51]\n"
"ds_read_b128 a[176:179], v203 offset:28672\n"
"ds_read_b128 a[180:183], v203 offset:29696\n"
"v_mfma_f32_16x16x32_bf16 v[48:51], a[156:159], a[68:71], v[48:51]\n"
"ds_read_b128 a[184:187], v203 offset:30720\n"
"ds_read_b128 a[188:191], v203 offset:31744\n"
"v_mfma_f32_16x16x32_bf16 v[52:55], a[152:155], a[72:75], v[52:55]\n"
"ds_read_b64_tr_b16 a[144:145], v199 offset:0\n"
"buffer_load_dwordx4 v226, s[16:19], s61 offen offset:2048 lds\n"
"v_mfma_f32_16x16x32_bf16 v[52:55], a[156:159], a[76:79], v[52:55]\n"
"ds_read_b64_tr_b16 a[146:147], v199 offset:6144\n"
"s_lshl3_add_u32 s88, s6, s91\n"
"v_mfma_f32_16x16x32_bf16 v[56:59], a[152:155], a[80:83], v[56:59]\n"
"ds_read_b64_tr_b16 a[148:149], v199 offset:12288\n"
"s_add_u32 s4, s88, 17825792\n"
"v_mfma_f32_16x16x32_bf16 v[56:59], a[156:159], a[84:87], v[56:59]\n"
"ds_read_b64_tr_b16 a[150:151], v199 offset:18432\n"
"s_and_b32 s88, s90, 1\n"
"v_mfma_f32_16x16x32_bf16 v[60:63], a[152:155], a[88:91], v[60:63]\n"
"s_mul_i32 s88, s88, 504\n"
"s_lshr_b32 s91, s91, 1\n"
"v_mfma_f32_16x16x32_bf16 v[60:63], a[156:159], a[92:95], v[60:63]\n"
"ds_read_b64_tr_b16 a[152:153], v200 offset:0\n"
"s_add_u32 s91, s91, s5\n"
"v_mfma_f32_16x16x32_bf16 v[64:67], a[160:163], a[64:67], v[64:67]\n"
"ds_read_b64_tr_b16 a[154:155], v200 offset:6144\n"
"s_mov_b32 m0, s64\n"
"v_mfma_f32_16x16x32_bf16 v[64:67], a[164:167], a[68:71], v[64:67]\n"
"ds_read_b64_tr_b16 a[156:157], v200 offset:12288\n"
"buffer_load_dwordx4 v224, s[16:19], s62 offen offset:0 lds\n"
"v_mfma_f32_16x16x32_bf16 v[68:71], a[160:163], a[72:75], v[68:71]\n"
"ds_read_b64_tr_b16 a[158:159], v200 offset:18432\n"
"s_sub_u32 s87, s91, s88\n"
"v_mfma_f32_16x16x32_bf16 v[68:71], a[164:167], a[76:79], v[68:71]\n"
"s_add_u32 s91, s91, s88\n"
"s_add_u32 s30, s91, 8\n"
"v_mfma_f32_16x16x32_bf16 v[72:75], a[160:163], a[80:83], v[72:75]\n"
"s_lshr_b32 s88, s56, 4\n"
"s_lshl1_add_u32 s88, s55, s88\n"
"v_mfma_f32_16x16x32_bf16 v[72:75], a[164:167], a[84:87], v[72:75]\n"
"s_lshl2_add_u32 s88, s88, s52\n"
"s_mul_i32 s88, s88, 40960\n"
"v_mfma_f32_16x16x32_bf16 v[76:79], a[160:163], a[88:91], v[76:79]\n"
"s_add_u32 s41, s88, 8388608\n"
"s_and_b32 s89, s56, 15\n"
"v_mfma_f32_16x16x32_bf16 v[76:79], a[164:167], a[92:95], v[76:79]\n"
"ds_read_b64_tr_b16 a[160:161], v201 offset:0\n"
"buffer_load_dwordx4 v225, s[16:19], s62 offen offset:1024 lds\n"
"s_waitcnt lgkmcnt(2)\n"
"v_mfma_f32_16x16x32_bf16 a[120:123], a[176:179], a[64:67], a[120:123]\n"
"ds_read_b64_tr_b16 a[162:163], v201 offset:6144\n"
"s_lshl4_add_u32 s89, s52, s89\n"
"v_mfma_f32_16x16x32_bf16 a[120:123], a[180:183], a[68:71], a[120:123]\n"
"ds_read_b64_tr_b16 a[164:165], v201 offset:12288\n"
"s_mul_i32 s89, s89, 6144\n"
"v_mfma_f32_16x16x32_bf16 a[124:127], a[176:179], a[72:75], a[124:127]\n"
"ds_read_b64_tr_b16 a[166:167], v201 offset:18432\n"
"s_add_u32 s40, s89, 18874368\n"
"v_mfma_f32_16x16x32_bf16 a[124:127], a[180:183], a[76:79], a[124:127]\n"
"s_mul_i32 s88, s53, 6144\n"
"s_add_u32 s88, s88, 33554432\n"
"v_mfma_f32_16x16x32_bf16 a[128:131], a[176:179], a[80:83], a[128:131]\n"
"s_mul_i32 s12, s76, 196608\n"
"s_add_u32 s12, s12, s88\n"
"v_mfma_f32_16x16x32_bf16 a[128:131], a[180:183], a[84:87], a[128:131]\n"
"buffer_load_dwordx4 v226, s[16:19], s62 offen offset:2048 lds\n"
"v_mfma_f32_16x16x32_bf16 a[132:135], a[176:179], a[88:91], a[132:135]\n"
"s_mul_i32 s13, s77, 196608\n"
"s_add_u32 s13, s13, s88\n"
"v_mfma_f32_16x16x32_bf16 a[132:135], a[180:183], a[92:95], a[132:135]\n"
"s_mul_i32 s14, s78, 196608\n"
"s_add_u32 s14, s14, s88\n"
"v_mfma_f32_16x16x32_bf16 a[136:139], a[184:187], a[64:67], a[136:139]\n"
"s_mul_i32 s15, s79, 196608\n"
"s_add_u32 s15, s15, s88\n"
"v_mfma_f32_16x16x32_bf16 a[136:139], a[188:191], a[68:71], a[136:139]\n"
"s_and_b32 s88, s53, 3\n"
"s_lshr_b32 s90, s52, 1\n"
"v_mfma_f32_16x16x32_bf16 v[80:83], a[168:171], a[64:67], v[80:83]\n"
"s_lshl2_add_u32 s89, s90, s88\n"
"s_and_b32 s90, s52, 1\n"
"v_mfma_f32_16x16x32_bf16 v[80:83], a[172:175], a[68:71], v[80:83]\n"
"s_mov_b32 m0, s67\n"
"s_lshl1_add_u32 s89, s89, s90\n"
"v_mfma_f32_16x16x32_bf16 v[84:87], a[168:171], a[72:75], v[84:87]\n"
"buffer_load_dwordx4 v224, s[20:23], s61 offen offset:0 lds\n"
"v_mfma_f32_16x16x32_bf16 v[84:87], a[172:175], a[76:79], v[84:87]\n"
"s_mul_i32 s89, s89, 5120\n"
"s_mul_i32 s90, s53, 245760\n"
"v_mfma_f32_16x16x32_bf16 v[88:91], a[168:171], a[80:83], v[88:91]\n"
"s_add_u32 s89, s89, s90\n"
"s_add_u32 s80, s89, 23068672\n"
"v_mfma_f32_16x16x32_bf16 v[88:91], a[172:175], a[84:87], v[88:91]\n"
"s_add_u32 s90, s53, 1\n"
"s_and_b32 s90, s90, 3\n"
"v_mfma_f32_16x16x32_bf16 v[92:95], a[168:171], a[88:91], v[92:95]\n"
"s_sub_u32 s90, s90, s88\n"
"s_mul_i32 s84, s90, 6144\n"
"v_mfma_f32_16x16x32_bf16 v[92:95], a[172:175], a[92:95], v[92:95]\n"
"s_mul_i32 s90, s90, 245760\n"
"s_add_u32 s81, s80, s90\n"
"s_waitcnt vmcnt(23)\n"
"v_mfma_f32_16x16x32_bf16 v[96:99], a[144:147], a[192:195], v[96:99]\n"
"ds_read_b64_tr_b16 a[168:169], v202 offset:0\n"
"buffer_load_dwordx4 v225, s[20:23], s61 offen offset:1024 lds\n"
"v_mfma_f32_16x16x32_bf16 v[96:99], a[148:151], a[196:199], v[96:99]\n"
"ds_read_b64_tr_b16 a[170:171], v202 offset:6144\n"
"s_add_u32 s90, s53, 2\n"
"v_mfma_f32_16x16x32_bf16 v[100:103], a[144:147], a[200:203], v[100:103]\n"
"ds_read_b64_tr_b16 a[172:173], v202 offset:12288\n"
"s_and_b32 s90, s90, 3\n"
"v_mfma_f32_16x16x32_bf16 v[100:103], a[148:151], a[204:207], v[100:103]\n"
"ds_read_b64_tr_b16 a[174:175], v202 offset:18432\n"
"s_sub_u32 s90, s90, s88\n"
"v_mfma_f32_16x16x32_bf16 v[104:107], a[144:147], a[208:211], v[104:107]\n"
"s_mul_i32 s85, s90, 6144\n"
"s_mul_i32 s90, s90, 245760\n"
"v_mfma_f32_16x16x32_bf16 v[104:107], a[148:151], a[212:215], v[104:107]\n"
"s_add_u32 s82, s80, s90\n"
"s_add_u32 s90, s53, 3\n"
"v_mfma_f32_16x16x32_bf16 v[108:111], a[144:147], a[216:219], v[108:111]\n"
"buffer_load_dwordx4 v226, s[20:23], s61 offen offset:2048 lds\n"
"v_mfma_f32_16x16x32_bf16 v[108:111], a[148:151], a[220:223], v[108:111]\n"
"ds_read_b64_tr_b16 a[144:145], v199 offset:1024\n"
"s_and_b32 s90, s90, 3\n"
"v_mfma_f32_16x16x32_bf16 v[112:115], a[152:155], a[192:195], v[112:115]\n"
"ds_read_b64_tr_b16 a[146:147], v199 offset:7168\n"
"s_sub_u32 s90, s90, s88\n"
"s_waitcnt lgkmcnt(3)\n"
"v_mfma_f32_16x16x32_bf16 v[112:115], a[156:159], a[196:199], v[112:115]\n"
"ds_read_b64_tr_b16 a[148:149], v199 offset:13312\n"
"s_mul_i32 s86, s90, 6144\n"
"v_mfma_f32_16x16x32_bf16 v[116:119], a[152:155], a[200:203], v[116:119]\n"
"ds_read_b64_tr_b16 a[150:151], v199 offset:19456\n"
"s_mul_i32 s90, s90, 245760\n"
"v_mfma_f32_16x16x32_bf16 v[116:119], a[156:159], a[204:207], v[116:119]\n"
"s_add_u32 s83, s80, s90\n"
"s_lshl_b32 s90, s54, 5\n"
"v_mfma_f32_16x16x32_bf16 v[120:123], a[152:155], a[208:211], v[120:123]\n"
"s_mov_b32 m0, s68\n"
"s_add_u32 s90, s90, s55\n"
"v_mfma_f32_16x16x32_bf16 v[120:123], a[156:159], a[212:215], v[120:123]\n"
"buffer_load_dwordx4 v224, s[20:23], s62 offen offset:0 lds\n"
"v_mfma_f32_16x16x32_bf16 v[124:127], a[152:155], a[216:219], v[124:127]\n"
"s_lshl_b32 s7, s90, 10\n"
"v_mfma_f32_16x16x32_bf16 v[124:127], a[156:159], a[220:223], v[124:127]\n"
"ds_read_b64_tr_b16 a[152:153], v200 offset:1024\n"
"v_mfma_f32_16x16x32_bf16 v[128:131], a[160:163], a[192:195], v[128:131]\n"
"ds_read_b64_tr_b16 a[154:155], v200 offset:7168\n"
"v_mfma_f32_16x16x32_bf16 v[128:131], a[164:167], a[196:199], v[128:131]\n"
"ds_read_b64_tr_b16 a[156:157], v200 offset:13312\n"
"v_mfma_f32_16x16x32_bf16 v[132:135], a[160:163], a[200:203], v[132:135]\n"
"ds_read_b64_tr_b16 a[158:159], v200 offset:19456\n"
"v_mfma_f32_16x16x32_bf16 v[132:135], a[164:167], a[204:207], v[132:135]\n"
"buffer_load_dwordx4 v225, s[20:23], s62 offen offset:1024 lds\n"
"v_mfma_f32_16x16x32_bf16 v[136:139], a[160:163], a[208:211], v[136:139]\n"
"v_mfma_f32_16x16x32_bf16 v[136:139], a[164:167], a[212:215], v[136:139]\n"
"v_mfma_f32_16x16x32_bf16 v[140:143], a[160:163], a[216:219], v[140:143]\n"
"v_mfma_f32_16x16x32_bf16 v[140:143], a[164:167], a[220:223], v[140:143]\n"
"ds_read_b64_tr_b16 a[160:161], v201 offset:1024\n"
"v_mfma_f32_16x16x32_bf16 v[144:147], a[168:171], a[192:195], v[144:147]\n"
"ds_read_b64_tr_b16 a[162:163], v201 offset:7168\n"
"s_waitcnt lgkmcnt(2)\n"
"v_mfma_f32_16x16x32_bf16 v[144:147], a[172:175], a[196:199], v[144:147]\n"
"ds_read_b64_tr_b16 a[164:165], v201 offset:13312\n"
"buffer_load_dwordx4 v226, s[20:23], s62 offen offset:2048 lds\n"
"v_mfma_f32_16x16x32_bf16 v[148:151], a[168:171], a[200:203], v[148:151]\n"
"ds_read_b64_tr_b16 a[166:167], v201 offset:19456\n"
"v_mfma_f32_16x16x32_bf16 v[148:151], a[172:175], a[204:207], v[148:151]\n"
"v_mfma_f32_16x16x32_bf16 v[152:155], a[168:171], a[208:211], v[152:155]\n"
"v_mfma_f32_16x16x32_bf16 v[152:155], a[172:175], a[212:215], v[152:155]\n"
"v_mfma_f32_16x16x32_bf16 v[156:159], a[168:171], a[216:219], v[156:159]\n"
"v_mfma_f32_16x16x32_bf16 v[156:159], a[172:175], a[220:223], v[156:159]\n"
"ds_read_b64_tr_b16 a[168:169], v202 offset:1024\n"
"s_add_u32 s58, s57, 24576\n"
"s_mov_b32 m0, s65\n"
"v_mfma_f32_16x16x32_bf16 v[160:163], a[144:147], a[192:195], v[160:163]\n"
"ds_read_b64_tr_b16 a[170:171], v202 offset:7168\n"
"buffer_load_dwordx4 v224, s[16:19], s58 offen offset:0 lds\n"
"v_mfma_f32_16x16x32_bf16 v[160:163], a[148:151], a[196:199], v[160:163]\n"
"ds_read_b64_tr_b16 a[172:173], v202 offset:13312\n"
"v_mfma_f32_16x16x32_bf16 v[164:167], a[144:147], a[200:203], v[164:167]\n"
"ds_read_b64_tr_b16 a[174:175], v202 offset:19456\n"
"v_mfma_f32_16x16x32_bf16 v[164:167], a[148:151], a[204:207], v[164:167]\n"
"v_mfma_f32_16x16x32_bf16 v[168:171], a[144:147], a[208:211], v[168:171]\n"
"v_mfma_f32_16x16x32_bf16 v[168:171], a[148:151], a[212:215], v[168:171]\n"
"v_mfma_f32_16x16x32_bf16 v[172:175], a[144:147], a[216:219], v[172:175]\n"
"buffer_load_dwordx4 v225, s[16:19], s58 offen offset:1024 lds\n"
"v_mfma_f32_16x16x32_bf16 v[172:175], a[148:151], a[220:223], v[172:175]\n"
"ds_read_b64_tr_b16 a[144:145], v199 offset:2048\n"
"v_mfma_f32_16x16x32_bf16 v[176:179], a[152:155], a[192:195], v[176:179]\n"
"ds_read_b64_tr_b16 a[146:147], v199 offset:8192\n"
"v_mfma_f32_16x16x32_bf16 v[176:179], a[156:159], a[196:199], v[176:179]\n"
"ds_read_b64_tr_b16 a[148:149], v199 offset:14336\n"
"v_mfma_f32_16x16x32_bf16 v[180:183], a[152:155], a[200:203], v[180:183]\n"
"ds_read_b64_tr_b16 a[150:151], v199 offset:20480\n"
"v_mfma_f32_16x16x32_bf16 v[180:183], a[156:159], a[204:207], v[180:183]\n"
"v_mfma_f32_16x16x32_bf16 v[184:187], a[152:155], a[208:211], v[184:187]\n"
"buffer_load_dwordx4 v226, s[16:19], s58 offen offset:2048 lds\n"
"v_mfma_f32_16x16x32_bf16 v[184:187], a[156:159], a[212:215], v[184:187]\n"
"v_mfma_f32_16x16x32_bf16 v[188:191], a[152:155], a[216:219], v[188:191]\n"
"v_mfma_f32_16x16x32_bf16 v[188:191], a[156:159], a[220:223], v[188:191]\n"
"ds_read_b64_tr_b16 a[152:153], v200 offset:2048\n"
"s_waitcnt lgkmcnt(2)\n"
"v_mfma_f32_16x16x32_bf16 v[0:3], a[160:163], a[192:195], v[0:3]\n"
"ds_read_b64_tr_b16 a[154:155], v200 offset:8192\n"
"v_mfma_f32_16x16x32_bf16 v[0:3], a[164:167], a[196:199], v[0:3]\n"
"ds_read_b64_tr_b16 a[156:157], v200 offset:14336\n"
"v_mfma_f32_16x16x32_bf16 v[4:7], a[160:163], a[200:203], v[4:7]\n"
"ds_read_b64_tr_b16 a[158:159], v200 offset:20480\n"
"s_add_u32 s58, s57, 27648\n"
"s_mov_b32 m0, s66\n"
"v_mfma_f32_16x16x32_bf16 v[4:7], a[164:167], a[204:207], v[4:7]\n"
"buffer_load_dwordx4 v224, s[16:19], s58 offen offset:0 lds\n"
"v_mfma_f32_16x16x32_bf16 v[8:11], a[160:163], a[208:211], v[8:11]\n"
"v_mfma_f32_16x16x32_bf16 v[8:11], a[164:167], a[212:215], v[8:11]\n"
"v_mfma_f32_16x16x32_bf16 v[12:15], a[160:163], a[216:219], v[12:15]\n"
"v_mfma_f32_16x16x32_bf16 v[12:15], a[164:167], a[220:223], v[12:15]\n"
"ds_read_b64_tr_b16 a[160:161], v201 offset:2048\n"
"v_mfma_f32_16x16x32_bf16 v[16:19], a[168:171], a[192:195], v[16:19]\n"
"ds_read_b64_tr_b16 a[162:163], v201 offset:8192\n"
"v_mfma_f32_16x16x32_bf16 v[16:19], a[172:175], a[196:199], v[16:19]\n"
"ds_read_b64_tr_b16 a[164:165], v201 offset:14336\n"
"buffer_load_dwordx4 v225, s[16:19], s58 offen offset:1024 lds\n"
"v_mfma_f32_16x16x32_bf16 v[20:23], a[168:171], a[200:203], v[20:23]\n"
"ds_read_b64_tr_b16 a[166:167], v201 offset:20480\n"
"v_mfma_f32_16x16x32_bf16 v[20:23], a[172:175], a[204:207], v[20:23]\n"
"v_mfma_f32_16x16x32_bf16 v[24:27], a[168:171], a[208:211], v[24:27]\n"
"v_mfma_f32_16x16x32_bf16 v[24:27], a[172:175], a[212:215], v[24:27]\n"
"v_mfma_f32_16x16x32_bf16 v[28:31], a[168:171], a[216:219], v[28:31]\n"
"v_mfma_f32_16x16x32_bf16 v[28:31], a[172:175], a[220:223], v[28:31]\n"
"ds_read_b64_tr_b16 a[168:169], v202 offset:2048\n"
"buffer_load_dwordx4 v226, s[16:19], s58 offen offset:2048 lds\n"
"v_mfma_f32_16x16x32_bf16 v[32:35], a[144:147], a[192:195], v[32:35]\n"
"ds_read_b64_tr_b16 a[170:171], v202 offset:8192\n"
"s_waitcnt lgkmcnt(2)\n"
"v_mfma_f32_16x16x32_bf16 v[32:35], a[148:151], a[196:199], v[32:35]\n"
"ds_read_b64_tr_b16 a[172:173], v202 offset:14336\n"
"v_mfma_f32_16x16x32_bf16 v[36:39], a[144:147], a[200:203], v[36:39]\n"
"ds_read_b64_tr_b16 a[174:175], v202 offset:20480\n"
"v_mfma_f32_16x16x32_bf16 v[36:39], a[148:151], a[204:207], v[36:39]\n"
"v_mfma_f32_16x16x32_bf16 v[40:43], a[144:147], a[208:211], v[40:43]\n"
"v_mfma_f32_16x16x32_bf16 v[40:43], a[148:151], a[212:215], v[40:43]\n"
"v_mfma_f32_16x16x32_bf16 v[44:47], a[144:147], a[216:219], v[44:47]\n"
"v_mfma_f32_16x16x32_bf16 v[44:47], a[148:151], a[220:223], v[44:47]\n"
"s_waitcnt vmcnt(26) lgkmcnt(0)\n"
"s_barrier\n"
"v_mfma_f32_16x16x32_bf16 v[48:51], a[152:155], a[192:195], v[48:51]\n"
"ds_read_b128 a[176:179], v204 offset:0\n"
"ds_read_b128 a[180:183], v204 offset:1024\n"
"v_mfma_f32_16x16x32_bf16 v[48:51], a[156:159], a[196:199], v[48:51]\n"
"ds_read_b128 a[184:187], v204 offset:2048\n"
"ds_read_b128 a[188:191], v204 offset:3072\n"
"v_mfma_f32_16x16x32_bf16 v[52:55], a[152:155], a[200:203], v[52:55]\n"
"ds_read_b64_tr_b16 a[144:145], v199 offset:28672\n"
"v_mfma_f32_16x16x32_bf16 v[52:55], a[156:159], a[204:207], v[52:55]\n"
"ds_read_b64_tr_b16 a[146:147], v199 offset:34816\n"
"v_mfma_f32_16x16x32_bf16 v[56:59], a[152:155], a[208:211], v[56:59]\n"
"ds_read_b64_tr_b16 a[148:149], v199 offset:40960\n"
"v_mfma_f32_16x16x32_bf16 v[56:59], a[156:159], a[212:215], v[56:59]\n"
"ds_read_b64_tr_b16 a[150:151], v199 offset:47104\n"
"v_mfma_f32_16x16x32_bf16 v[60:63], a[152:155], a[216:219], v[60:63]\n"
"v_mfma_f32_16x16x32_bf16 v[60:63], a[156:159], a[220:223], v[60:63]\n"
"ds_read_b64_tr_b16 a[152:153], v200 offset:28672\n"
"v_mfma_f32_16x16x32_bf16 v[64:67], a[160:163], a[192:195], v[64:67]\n"
"ds_read_b64_tr_b16 a[154:155], v200 offset:34816\n"
"v_mfma_f32_16x16x32_bf16 v[64:67], a[164:167], a[196:199], v[64:67]\n"
"ds_read_b64_tr_b16 a[156:157], v200 offset:40960\n"
"v_mfma_f32_16x16x32_bf16 v[68:71], a[160:163], a[200:203], v[68:71]\n"
"ds_read_b64_tr_b16 a[158:159], v200 offset:47104\n"
"v_mfma_f32_16x16x32_bf16 v[68:71], a[164:167], a[204:207], v[68:71]\n"
"v_mfma_f32_16x16x32_bf16 v[72:75], a[160:163], a[208:211], v[72:75]\n"
"v_mfma_f32_16x16x32_bf16 v[72:75], a[164:167], a[212:215], v[72:75]\n"
"v_mfma_f32_16x16x32_bf16 v[76:79], a[160:163], a[216:219], v[76:79]\n"
"v_mfma_f32_16x16x32_bf16 v[76:79], a[164:167], a[220:223], v[76:79]\n"
"ds_read_b64_tr_b16 a[160:161], v201 offset:28672\n"
"s_waitcnt lgkmcnt(2)\n"
"v_mfma_f32_16x16x32_bf16 a[120:123], a[176:179], a[192:195], a[120:123]\n"
"ds_read_b64_tr_b16 a[162:163], v201 offset:34816\n"
"v_mfma_f32_16x16x32_bf16 a[120:123], a[180:183], a[196:199], a[120:123]\n"
"ds_read_b64_tr_b16 a[164:165], v201 offset:40960\n"
"v_mfma_f32_16x16x32_bf16 a[124:127], a[176:179], a[200:203], a[124:127]\n"
"ds_read_b64_tr_b16 a[166:167], v201 offset:47104\n"
"v_mfma_f32_16x16x32_bf16 a[124:127], a[180:183], a[204:207], a[124:127]\n"
"v_mfma_f32_16x16x32_bf16 a[128:131], a[176:179], a[208:211], a[128:131]\n"
"v_mfma_f32_16x16x32_bf16 a[128:131], a[180:183], a[212:215], a[128:131]\n"
"v_mfma_f32_16x16x32_bf16 a[132:135], a[176:179], a[216:219], a[132:135]\n"
"v_mfma_f32_16x16x32_bf16 a[132:135], a[180:183], a[220:223], a[132:135]\n"
"v_mfma_f32_16x16x32_bf16 a[136:139], a[184:187], a[192:195], a[136:139]\n"
"v_mfma_f32_16x16x32_bf16 a[136:139], a[188:191], a[196:199], a[136:139]\n"
"v_mfma_f32_16x16x32_bf16 v[80:83], a[168:171], a[192:195], v[80:83]\n"
"v_mfma_f32_16x16x32_bf16 v[80:83], a[172:175], a[196:199], v[80:83]\n"
"v_mfma_f32_16x16x32_bf16 v[84:87], a[168:171], a[200:203], v[84:87]\n"
"v_mfma_f32_16x16x32_bf16 v[84:87], a[172:175], a[204:207], v[84:87]\n"
"v_mfma_f32_16x16x32_bf16 v[88:91], a[168:171], a[208:211], v[88:91]\n"
"v_mfma_f32_16x16x32_bf16 v[88:91], a[172:175], a[212:215], v[88:91]\n"
"v_mfma_f32_16x16x32_bf16 v[92:95], a[168:171], a[216:219], v[92:95]\n"
"v_mfma_f32_16x16x32_bf16 v[92:95], a[172:175], a[220:223], v[92:95]\n"
"s_waitcnt vmcnt(18)\n"
"v_mfma_f32_16x16x32_bf16 v[96:99], a[144:147], a[224:227], v[96:99]\n"
"ds_read_b64_tr_b16 a[168:169], v202 offset:28672\n"
"ds_read_b128 a[176:179], v204 offset:28672\n"
"v_mfma_f32_16x16x32_bf16 v[96:99], a[148:151], a[228:231], v[96:99]\n"
"ds_read_b64_tr_b16 a[170:171], v202 offset:34816\n"
"ds_read_b128 a[180:183], v204 offset:29696\n"
"v_mfma_f32_16x16x32_bf16 v[100:103], a[144:147], a[232:235], v[100:103]\n"
"ds_read_b64_tr_b16 a[172:173], v202 offset:40960\n"
"ds_read_b128 a[184:187], v204 offset:30720\n"
"v_mfma_f32_16x16x32_bf16 v[100:103], a[148:151], a[236:239], v[100:103]\n"
"ds_read_b64_tr_b16 a[174:175], v202 offset:47104\n"
"ds_read_b128 a[188:191], v204 offset:31744\n"
"v_mfma_f32_16x16x32_bf16 v[104:107], a[144:147], a[240:243], v[104:107]\n"
"v_mfma_f32_16x16x32_bf16 v[104:107], a[148:151], a[244:247], v[104:107]\n"
"v_mfma_f32_16x16x32_bf16 v[108:111], a[144:147], a[248:251], v[108:111]\n"
"v_mfma_f32_16x16x32_bf16 v[108:111], a[148:151], a[252:255], v[108:111]\n"
"ds_read_b64_tr_b16 a[144:145], v199 offset:29696\n"
"v_mfma_f32_16x16x32_bf16 v[112:115], a[152:155], a[224:227], v[112:115]\n"
"ds_read_b64_tr_b16 a[146:147], v199 offset:35840\n"
"s_waitcnt lgkmcnt(4)\n"
"v_mfma_f32_16x16x32_bf16 v[112:115], a[156:159], a[228:231], v[112:115]\n"
"ds_read_b64_tr_b16 a[148:149], v199 offset:41984\n"
"v_mfma_f32_16x16x32_bf16 v[116:119], a[152:155], a[232:235], v[116:119]\n"
"ds_read_b64_tr_b16 a[150:151], v199 offset:48128\n"
"v_mfma_f32_16x16x32_bf16 v[116:119], a[156:159], a[236:239], v[116:119]\n"
"v_mfma_f32_16x16x32_bf16 v[120:123], a[152:155], a[240:243], v[120:123]\n"
"v_mfma_f32_16x16x32_bf16 v[120:123], a[156:159], a[244:247], v[120:123]\n"
"v_mfma_f32_16x16x32_bf16 v[124:127], a[152:155], a[248:251], v[124:127]\n"
"v_mfma_f32_16x16x32_bf16 v[124:127], a[156:159], a[252:255], v[124:127]\n"
"ds_read_b64_tr_b16 a[152:153], v200 offset:29696\n"
"v_mfma_f32_16x16x32_bf16 a[120:123], a[176:179], a[224:227], a[120:123]\n"
"ds_read_b64_tr_b16 a[154:155], v200 offset:35840\n"
"v_mfma_f32_16x16x32_bf16 a[120:123], a[180:183], a[228:231], a[120:123]\n"
"ds_read_b64_tr_b16 a[156:157], v200 offset:41984\n"
"v_mfma_f32_16x16x32_bf16 a[124:127], a[176:179], a[232:235], a[124:127]\n"
"ds_read_b64_tr_b16 a[158:159], v200 offset:48128\n"
"v_mfma_f32_16x16x32_bf16 a[124:127], a[180:183], a[236:239], a[124:127]\n"
"v_cvt_pk_bf16_f32 v96, v96, v97\n"
"v_cvt_pk_bf16_f32 v97, v98, v99\n"
"v_cvt_pk_bf16_f32 v98, v112, v113\n"
"v_mfma_f32_16x16x32_bf16 a[128:131], a[176:179], a[240:243], a[128:131]\n"
"v_cvt_pk_bf16_f32 v99, v114, v115\n"
"v_cvt_pk_bf16_f32 v100, v100, v101\n"
"v_cvt_pk_bf16_f32 v101, v102, v103\n"
"v_cvt_pk_bf16_f32 v102, v116, v117\n"
"v_mfma_f32_16x16x32_bf16 a[128:131], a[180:183], a[244:247], a[128:131]\n"
"v_cvt_pk_bf16_f32 v103, v118, v119\n"
"v_permlane32_swap_b32 v96, v98\n"
"v_permlane32_swap_b32 v97, v99\n"
"v_mfma_f32_16x16x32_bf16 a[132:135], a[176:179], a[248:251], a[132:135]\n"
"v_accvgpr_write_b32 a0, v96\n"
"v_accvgpr_write_b32 a1, v97\n"
"v_accvgpr_write_b32 a2, v98\n"
"v_mfma_f32_16x16x32_bf16 a[132:135], a[180:183], a[252:255], a[132:135]\n"
"v_accvgpr_write_b32 a3, v99\n"
"v_cvt_pk_bf16_f32 v104, v104, v105\n"
"v_cvt_pk_bf16_f32 v105, v106, v107\n"
"v_cvt_pk_bf16_f32 v106, v120, v121\n"
"v_mfma_f32_16x16x32_bf16 a[136:139], a[184:187], a[224:227], a[136:139]\n"
"v_cvt_pk_bf16_f32 v107, v122, v123\n"
"v_permlane32_swap_b32 v100, v102\n"
"v_permlane32_swap_b32 v101, v103\n"
"s_waitcnt lgkmcnt(0)\n"
"v_mfma_f32_16x16x32_bf16 a[136:139], a[188:191], a[228:231], a[136:139]\n"
"v_accvgpr_write_b32 a24, v100\n"
"v_accvgpr_write_b32 a25, v101\n"
"v_accvgpr_write_b32 a26, v102\n"
"v_mfma_f32_16x16x32_bf16 v[128:131], a[160:163], a[224:227], v[128:131]\n"
"v_accvgpr_write_b32 a27, v103\n"
"v_cvt_pk_bf16_f32 v108, v108, v109\n"
"v_cvt_pk_bf16_f32 v109, v110, v111\n"
"v_cvt_pk_bf16_f32 v110, v124, v125\n"
"v_mfma_f32_16x16x32_bf16 v[128:131], a[164:167], a[228:231], v[128:131]\n"
"v_cvt_pk_bf16_f32 v111, v126, v127\n"
"v_permlane32_swap_b32 v104, v106\n"
"v_permlane32_swap_b32 v105, v107\n"
"v_mfma_f32_16x16x32_bf16 v[132:135], a[160:163], a[232:235], v[132:135]\n"
"v_accvgpr_write_b32 a48, v104\n"
"v_accvgpr_write_b32 a49, v105\n"
"v_accvgpr_write_b32 a50, v106\n"
"v_accvgpr_write_b32 a51, v107\n"
"v_mfma_f32_16x16x32_bf16 v[132:135], a[164:167], a[236:239], v[132:135]\n"
"v_permlane32_swap_b32 v108, v110\n"
"v_permlane32_swap_b32 v109, v111\n"
"v_accvgpr_write_b32 a72, v108\n"
"v_mfma_f32_16x16x32_bf16 v[136:139], a[160:163], a[240:243], v[136:139]\n"
"v_accvgpr_write_b32 a73, v109\n"
"v_accvgpr_write_b32 a74, v110\n"
"v_accvgpr_write_b32 a75, v111\n"
"v_mfma_f32_16x16x32_bf16 v[136:139], a[164:167], a[244:247], v[136:139]\n"
"v_accvgpr_read_b32 v96, a120\n"
"v_accvgpr_read_b32 v97, a121\n"
"v_accvgpr_read_b32 v98, a122\n"
"v_accvgpr_read_b32 v99, a123\n"
"v_mfma_f32_16x16x32_bf16 v[140:143], a[160:163], a[248:251], v[140:143]\n"
"v_accvgpr_read_b32 v100, a124\n"
"v_accvgpr_read_b32 v101, a125\n"
"v_accvgpr_read_b32 v102, a126\n"
"v_mfma_f32_16x16x32_bf16 v[140:143], a[164:167], a[252:255], v[140:143]\n"
"ds_read_b64_tr_b16 a[160:161], v201 offset:29696\n"
"v_accvgpr_read_b32 v103, a127\n"
"v_accvgpr_read_b32 v104, a128\n"
"v_mfma_f32_16x16x32_bf16 v[144:147], a[168:171], a[224:227], v[144:147]\n"
"ds_read_b64_tr_b16 a[162:163], v201 offset:35840\n"
"v_accvgpr_read_b32 v105, a129\n"
"v_accvgpr_read_b32 v106, a130\n"
"v_accvgpr_read_b32 v107, a131\n"
"v_mfma_f32_16x16x32_bf16 v[144:147], a[172:175], a[228:231], v[144:147]\n"
"ds_read_b64_tr_b16 a[164:165], v201 offset:41984\n"
"v_accvgpr_read_b32 v108, a132\n"
"v_accvgpr_read_b32 v109, a133\n"
"v_mfma_f32_16x16x32_bf16 v[148:151], a[168:171], a[232:235], v[148:151]\n"
"ds_read_b64_tr_b16 a[166:167], v201 offset:48128\n"
"v_accvgpr_read_b32 v110, a134\n"
"v_accvgpr_read_b32 v111, a135\n"
"v_mfma_f32_16x16x32_bf16 v[148:151], a[172:175], a[236:239], v[148:151]\n"
"v_accvgpr_read_b32 v112, a136\n"
"v_accvgpr_read_b32 v113, a137\n"
"v_accvgpr_read_b32 v114, a138\n"
"v_accvgpr_read_b32 v115, a139\n"
"v_mfma_f32_16x16x32_bf16 v[152:155], a[168:171], a[240:243], v[152:155]\n"
"v_mul_f32 v96, v96, v205\n"
"v_mul_f32 v97, v97, v205\n"
"v_mul_f32 v98, v98, v205\n"
"v_mfma_f32_16x16x32_bf16 v[152:155], a[172:175], a[244:247], v[152:155]\n"
"v_mul_f32 v99, v99, v205\n"
"v_mul_f32 v100, v100, v206\n"
"v_mul_f32 v101, v101, v206\n"
"v_mfma_f32_16x16x32_bf16 v[156:159], a[168:171], a[248:251], v[156:159]\n"
"v_mul_f32 v102, v102, v206\n"
"v_mul_f32 v103, v103, v206\n"
"v_mul_f32 v104, v104, v207\n"
"v_mul_f32 v105, v105, v207\n"
"v_mfma_f32_16x16x32_bf16 v[156:159], a[172:175], a[252:255], v[156:159]\n"
"ds_read_b64_tr_b16 a[168:169], v202 offset:29696\n"
"v_mul_f32 v106, v106, v207\n"
"v_mul_f32 v107, v107, v207\n"
"v_mfma_f32_16x16x32_bf16 v[160:163], a[144:147], a[224:227], v[160:163]\n"
"ds_read_b64_tr_b16 a[170:171], v202 offset:35840\n"
"v_mul_f32 v108, v108, v208\n"
"v_mul_f32 v109, v109, v208\n"
"v_mfma_f32_16x16x32_bf16 v[160:163], a[148:151], a[228:231], v[160:163]\n"
"ds_read_b64_tr_b16 a[172:173], v202 offset:41984\n"
"v_mul_f32 v110, v110, v208\n"
"v_mul_f32 v111, v111, v208\n"
"v_mul_f32 v112, v112, v209\n"
"v_mfma_f32_16x16x32_bf16 v[164:167], a[144:147], a[232:235], v[164:167]\n"
"ds_read_b64_tr_b16 a[174:175], v202 offset:48128\n"
"v_mul_f32 v113, v113, v209\n"
"v_mul_f32 v114, v114, v209\n"
"v_mfma_f32_16x16x32_bf16 v[164:167], a[148:151], a[236:239], v[164:167]\n"
"v_mul_f32 v115, v115, v209\n"
"v_cvt_pk_bf16_f32 v128, v128, v129\n"
"v_cvt_pk_bf16_f32 v129, v130, v131\n"
"v_mfma_f32_16x16x32_bf16 v[168:171], a[144:147], a[240:243], v[168:171]\n"
"v_cvt_pk_bf16_f32 v130, v144, v145\n"
"v_cvt_pk_bf16_f32 v131, v146, v147\n"
"v_cvt_pk_bf16_f32 v132, v132, v133\n"
"v_cvt_pk_bf16_f32 v133, v134, v135\n"
"v_mfma_f32_16x16x32_bf16 v[168:171], a[148:151], a[244:247], v[168:171]\n"
"v_cvt_pk_bf16_f32 v134, v148, v149\n"
"v_cvt_pk_bf16_f32 v135, v150, v151\n"
"v_permlane32_swap_b32 v128, v130\n"
"v_mfma_f32_16x16x32_bf16 v[172:175], a[144:147], a[248:251], v[172:175]\n"
"v_permlane32_swap_b32 v129, v131\n"
"v_accvgpr_write_b32 a4, v128\n"
"v_accvgpr_write_b32 a5, v129\n"
"v_accvgpr_write_b32 a6, v130\n"
"v_mfma_f32_16x16x32_bf16 v[172:175], a[148:151], a[252:255], v[172:175]\n"
"ds_read_b64_tr_b16 a[144:145], v199 offset:30720\n"
"v_accvgpr_write_b32 a7, v131\n"
"v_cvt_pk_bf16_f32 v136, v136, v137\n"
"v_mfma_f32_16x16x32_bf16 v[176:179], a[152:155], a[224:227], v[176:179]\n"
"ds_read_b64_tr_b16 a[146:147], v199 offset:36864\n"
"v_cvt_pk_bf16_f32 v137, v138, v139\n"
"v_cvt_pk_bf16_f32 v138, v152, v153\n"
"v_mfma_f32_16x16x32_bf16 v[176:179], a[156:159], a[228:231], v[176:179]\n"
"ds_read_b64_tr_b16 a[148:149], v199 offset:43008\n"
"v_cvt_pk_bf16_f32 v139, v154, v155\n"
"v_permlane32_swap_b32 v132, v134\n"
"v_permlane32_swap_b32 v133, v135\n"
"v_mfma_f32_16x16x32_bf16 v[180:183], a[152:155], a[232:235], v[180:183]\n"
"ds_read_b64_tr_b16 a[150:151], v199 offset:49152\n"
"v_accvgpr_write_b32 a28, v132\n"
"v_accvgpr_write_b32 a29, v133\n"
"v_mfma_f32_16x16x32_bf16 v[180:183], a[156:159], a[236:239], v[180:183]\n"
"v_accvgpr_write_b32 a30, v134\n"
"v_accvgpr_write_b32 a31, v135\n"
"v_cvt_pk_bf16_f32 v140, v140, v141\n"
"v_mfma_f32_16x16x32_bf16 v[184:187], a[152:155], a[240:243], v[184:187]\n"
"v_cvt_pk_bf16_f32 v141, v142, v143\n"
"v_cvt_pk_bf16_f32 v142, v156, v157\n"
"v_cvt_pk_bf16_f32 v143, v158, v159\n"
"v_permlane32_swap_b32 v136, v138\n"
"v_mfma_f32_16x16x32_bf16 v[184:187], a[156:159], a[244:247], v[184:187]\n"
"v_permlane32_swap_b32 v137, v139\n"
"v_accvgpr_write_b32 a52, v136\n"
"v_accvgpr_write_b32 a53, v137\n"
"v_mfma_f32_16x16x32_bf16 v[188:191], a[152:155], a[248:251], v[188:191]\n"
"v_accvgpr_write_b32 a54, v138\n"
"v_accvgpr_write_b32 a55, v139\n"
"v_permlane32_swap_b32 v140, v142\n"
"v_mfma_f32_16x16x32_bf16 v[188:191], a[156:159], a[252:255], v[188:191]\n"
"ds_read_b64_tr_b16 a[152:153], v200 offset:30720\n"
"v_permlane32_swap_b32 v141, v143\n"
"v_accvgpr_write_b32 a76, v140\n"
"v_accvgpr_write_b32 a77, v141\n"
"s_waitcnt lgkmcnt(2)\n"
"v_mfma_f32_16x16x32_bf16 v[0:3], a[160:163], a[224:227], v[0:3]\n"
"ds_read_b64_tr_b16 a[154:155], v200 offset:36864\n"
"v_accvgpr_write_b32 a78, v142\n"
"v_accvgpr_write_b32 a79, v143\n"
"v_mfma_f32_16x16x32_bf16 v[0:3], a[164:167], a[228:231], v[0:3]\n"
"ds_read_b64_tr_b16 a[156:157], v200 offset:43008\n"
"v_mfma_f32_16x16x32_bf16 v[4:7], a[160:163], a[232:235], v[4:7]\n"
"ds_read_b64_tr_b16 a[158:159], v200 offset:49152\n"
"v_mfma_f32_16x16x32_bf16 v[4:7], a[164:167], a[236:239], v[4:7]\n"
"v_cvt_pk_bf16_f32 v160, v160, v161\n"
"v_cvt_pk_bf16_f32 v161, v162, v163\n"
"v_cvt_pk_bf16_f32 v162, v176, v177\n"
"v_mfma_f32_16x16x32_bf16 v[8:11], a[160:163], a[240:243], v[8:11]\n"
"v_cvt_pk_bf16_f32 v163, v178, v179\n"
"v_cvt_pk_bf16_f32 v164, v164, v165\n"
"v_cvt_pk_bf16_f32 v165, v166, v167\n"
"v_mfma_f32_16x16x32_bf16 v[8:11], a[164:167], a[244:247], v[8:11]\n"
"v_cvt_pk_bf16_f32 v166, v180, v181\n"
"v_cvt_pk_bf16_f32 v167, v182, v183\n"
"v_permlane32_swap_b32 v160, v162\n"
"v_permlane32_swap_b32 v161, v163\n"
"v_mfma_f32_16x16x32_bf16 v[12:15], a[160:163], a[248:251], v[12:15]\n"
"v_accvgpr_write_b32 a8, v160\n"
"v_accvgpr_write_b32 a9, v161\n"
"v_accvgpr_write_b32 a10, v162\n"
"v_mfma_f32_16x16x32_bf16 v[12:15], a[164:167], a[252:255], v[12:15]\n"
"ds_read_b64_tr_b16 a[160:161], v201 offset:30720\n"
"v_accvgpr_write_b32 a11, v163\n"
"v_cvt_pk_bf16_f32 v168, v168, v169\n"
"v_mfma_f32_16x16x32_bf16 v[16:19], a[168:171], a[224:227], v[16:19]\n"
"ds_read_b64_tr_b16 a[162:163], v201 offset:36864\n"
"v_cvt_pk_bf16_f32 v169, v170, v171\n"
"v_cvt_pk_bf16_f32 v170, v184, v185\n"
"v_cvt_pk_bf16_f32 v171, v186, v187\n"
"v_mfma_f32_16x16x32_bf16 v[16:19], a[172:175], a[228:231], v[16:19]\n"
"ds_read_b64_tr_b16 a[164:165], v201 offset:43008\n"
"v_permlane32_swap_b32 v164, v166\n"
"v_permlane32_swap_b32 v165, v167\n"
"v_mfma_f32_16x16x32_bf16 v[20:23], a[168:171], a[232:235], v[20:23]\n"
"ds_read_b64_tr_b16 a[166:167], v201 offset:49152\n"
"v_accvgpr_write_b32 a32, v164\n"
"v_accvgpr_write_b32 a33, v165\n"
"v_accvgpr_write_b32 a34, v166\n"
"v_mfma_f32_16x16x32_bf16 v[20:23], a[172:175], a[236:239], v[20:23]\n"
"v_accvgpr_write_b32 a35, v167\n"
"v_cvt_pk_bf16_f32 v172, v172, v173\n"
"v_cvt_pk_bf16_f32 v173, v174, v175\n"
"v_mfma_f32_16x16x32_bf16 v[24:27], a[168:171], a[240:243], v[24:27]\n"
"v_cvt_pk_bf16_f32 v174, v188, v189\n"
"v_cvt_pk_bf16_f32 v175, v190, v191\n"
"v_permlane32_swap_b32 v168, v170\n"
"v_mfma_f32_16x16x32_bf16 v[24:27], a[172:175], a[244:247], v[24:27]\n"
"v_permlane32_swap_b32 v169, v171\n"
"v_accvgpr_write_b32 a56, v168\n"
"v_accvgpr_write_b32 a57, v169\n"
"v_accvgpr_write_b32 a58, v170\n"
"v_mfma_f32_16x16x32_bf16 v[28:31], a[168:171], a[248:251], v[28:31]\n"
"v_accvgpr_write_b32 a59, v171\n"
"v_permlane32_swap_b32 v172, v174\n"
"v_permlane32_swap_b32 v173, v175\n"
"v_mfma_f32_16x16x32_bf16 v[28:31], a[172:175], a[252:255], v[28:31]\n"
"ds_read_b64_tr_b16 a[168:169], v202 offset:30720\n"
"v_accvgpr_write_b32 a80, v172\n"
"v_accvgpr_write_b32 a81, v173\n"
"v_mfma_f32_16x16x32_bf16 v[32:35], a[144:147], a[224:227], v[32:35]\n"
"ds_read_b64_tr_b16 a[170:171], v202 offset:36864\n"
"v_accvgpr_write_b32 a82, v174\n"
"v_accvgpr_write_b32 a83, v175\n"
"s_waitcnt lgkmcnt(2)\n"
"v_mfma_f32_16x16x32_bf16 v[32:35], a[148:151], a[228:231], v[32:35]\n"
"ds_read_b64_tr_b16 a[172:173], v202 offset:43008\n"
"v_mfma_f32_16x16x32_bf16 v[36:39], a[144:147], a[232:235], v[36:39]\n"
"ds_read_b64_tr_b16 a[174:175], v202 offset:49152\n"
"v_mfma_f32_16x16x32_bf16 v[36:39], a[148:151], a[236:239], v[36:39]\n"
"v_cvt_pk_bf16_f32 v0, v0, v1\n"
"v_cvt_pk_bf16_f32 v1, v2, v3\n"
"v_cvt_pk_bf16_f32 v2, v16, v17\n"
"v_cvt_pk_bf16_f32 v3, v18, v19\n"
"v_mfma_f32_16x16x32_bf16 v[40:43], a[144:147], a[240:243], v[40:43]\n"
"v_cvt_pk_bf16_f32 v4, v4, v5\n"
"v_cvt_pk_bf16_f32 v5, v6, v7\n"
"v_cvt_pk_bf16_f32 v6, v20, v21\n"
"v_mfma_f32_16x16x32_bf16 v[40:43], a[148:151], a[244:247], v[40:43]\n"
"v_cvt_pk_bf16_f32 v7, v22, v23\n"
"v_permlane32_swap_b32 v0, v2\n"
"v_permlane32_swap_b32 v1, v3\n"
"v_mfma_f32_16x16x32_bf16 v[44:47], a[144:147], a[248:251], v[44:47]\n"
"v_accvgpr_write_b32 a12, v0\n"
"v_accvgpr_write_b32 a13, v1\n"
"v_accvgpr_write_b32 a14, v2\n"
"v_accvgpr_write_b32 a15, v3\n"
"v_mfma_f32_16x16x32_bf16 v[44:47], a[148:151], a[252:255], v[44:47]\n"
"s_waitcnt vmcnt(12) lgkmcnt(0)\n"
"s_barrier\n"
"v_cvt_pk_bf16_f32 v8, v8, v9\n"
"v_mfma_f32_16x16x32_bf16 v[48:51], a[152:155], a[224:227], v[48:51]\n"
"ds_read_b128 a[96:99], v227 offset:0\n"
"v_cvt_pk_bf16_f32 v9, v10, v11\n"
"v_cvt_pk_bf16_f32 v10, v24, v25\n"
"v_mfma_f32_16x16x32_bf16 v[48:51], a[156:159], a[228:231], v[48:51]\n"
"v_cvt_pk_bf16_f32 v11, v26, v27\n"
"v_permlane32_swap_b32 v4, v6\n"
"v_permlane32_swap_b32 v5, v7\n"
"v_accvgpr_write_b32 a36, v4\n"
"v_mfma_f32_16x16x32_bf16 v[52:55], a[152:155], a[232:235], v[52:55]\n"
"ds_read_b128 a[100:103], v228 offset:0\n"
"v_accvgpr_write_b32 a37, v5\n"
"v_accvgpr_write_b32 a38, v6\n"
"v_mfma_f32_16x16x32_bf16 v[52:55], a[156:159], a[236:239], v[52:55]\n"
"v_accvgpr_write_b32 a39, v7\n"
"v_cvt_pk_bf16_f32 v12, v12, v13\n"
"v_cvt_pk_bf16_f32 v13, v14, v15\n"
"v_mfma_f32_16x16x32_bf16 v[56:59], a[152:155], a[240:243], v[56:59]\n"
"ds_read_b128 a[104:107], v227 offset:1024\n"
"v_cvt_pk_bf16_f32 v14, v28, v29\n"
"v_cvt_pk_bf16_f32 v15, v30, v31\n"
"v_permlane32_swap_b32 v8, v10\n"
"v_mfma_f32_16x16x32_bf16 v[56:59], a[156:159], a[244:247], v[56:59]\n"
"v_permlane32_swap_b32 v9, v11\n"
"v_accvgpr_write_b32 a60, v8\n"
"v_accvgpr_write_b32 a61, v9\n"
"v_mfma_f32_16x16x32_bf16 v[60:63], a[152:155], a[248:251], v[60:63]\n"
"ds_read_b128 a[108:111], v228 offset:1024\n"
"v_accvgpr_write_b32 a62, v10\n"
"v_accvgpr_write_b32 a63, v11\n"
"v_permlane32_swap_b32 v12, v14\n"
"v_mfma_f32_16x16x32_bf16 v[60:63], a[156:159], a[252:255], v[60:63]\n"
"v_permlane32_swap_b32 v13, v15\n"
"v_accvgpr_write_b32 a84, v12\n"
"v_accvgpr_write_b32 a85, v13\n"
"v_mfma_f32_16x16x32_bf16 v[64:67], a[160:163], a[224:227], v[64:67]\n"
"ds_read_b128 a[112:115], v227 offset:2048\n"
"v_accvgpr_write_b32 a86, v14\n"
"v_accvgpr_write_b32 a87, v15\n"
"v_mfma_f32_16x16x32_bf16 v[64:67], a[164:167], a[228:231], v[64:67]\n"
"v_mfma_f32_16x16x32_bf16 v[68:71], a[160:163], a[232:235], v[68:71]\n"
"ds_read_b128 a[116:119], v228 offset:2048\n"
"v_mfma_f32_16x16x32_bf16 v[68:71], a[164:167], a[236:239], v[68:71]\n"
"v_cvt_pk_bf16_f32 v32, v32, v33\n"
"v_cvt_pk_bf16_f32 v33, v34, v35\n"
"v_cvt_pk_bf16_f32 v34, v48, v49\n"
"v_mfma_f32_16x16x32_bf16 v[72:75], a[160:163], a[240:243], v[72:75]\n"
"v_cvt_pk_bf16_f32 v35, v50, v51\n"
"v_cvt_pk_bf16_f32 v36, v36, v37\n"
"v_cvt_pk_bf16_f32 v37, v38, v39\n"
"v_cvt_pk_bf16_f32 v38, v52, v53\n"
"v_mfma_f32_16x16x32_bf16 v[72:75], a[164:167], a[244:247], v[72:75]\n"
"v_cvt_pk_bf16_f32 v39, v54, v55\n"
"v_permlane32_swap_b32 v32, v34\n"
"v_permlane32_swap_b32 v33, v35\n"
"v_mfma_f32_16x16x32_bf16 v[76:79], a[160:163], a[248:251], v[76:79]\n"
"v_accvgpr_write_b32 a16, v32\n"
"v_accvgpr_write_b32 a17, v33\n"
"v_accvgpr_write_b32 a18, v34\n"
"v_mfma_f32_16x16x32_bf16 v[76:79], a[164:167], a[252:255], v[76:79]\n"
"v_accvgpr_write_b32 a19, v35\n"
"v_cvt_pk_bf16_f32 v40, v40, v41\n"
"v_cvt_pk_bf16_f32 v41, v42, v43\n"
"v_cvt_pk_bf16_f32 v42, v56, v57\n"
"v_mfma_f32_16x16x32_bf16 v[80:83], a[168:171], a[224:227], v[80:83]\n"
"v_cvt_pk_bf16_f32 v43, v58, v59\n"
"v_permlane32_swap_b32 v36, v38\n"
"v_permlane32_swap_b32 v37, v39\n"
"v_mfma_f32_16x16x32_bf16 v[80:83], a[172:175], a[228:231], v[80:83]\n"
"v_accvgpr_write_b32 a40, v36\n"
"v_accvgpr_write_b32 a41, v37\n"
"v_accvgpr_write_b32 a42, v38\n"
"v_mfma_f32_16x16x32_bf16 v[84:87], a[168:171], a[232:235], v[84:87]\n"
"v_accvgpr_write_b32 a43, v39\n"
"v_cvt_pk_bf16_f32 v44, v44, v45\n"
"v_cvt_pk_bf16_f32 v45, v46, v47\n"
"v_cvt_pk_bf16_f32 v46, v60, v61\n"
"v_mfma_f32_16x16x32_bf16 v[84:87], a[172:175], a[236:239], v[84:87]\n"
"v_cvt_pk_bf16_f32 v47, v62, v63\n"
"v_permlane32_swap_b32 v40, v42\n"
"v_permlane32_swap_b32 v41, v43\n"
"v_mfma_f32_16x16x32_bf16 v[88:91], a[168:171], a[240:243], v[88:91]\n"
"v_accvgpr_write_b32 a64, v40\n"
"v_accvgpr_write_b32 a65, v41\n"
"v_accvgpr_write_b32 a66, v42\n"
"v_mfma_f32_16x16x32_bf16 v[88:91], a[172:175], a[244:247], v[88:91]\n"
"v_accvgpr_write_b32 a67, v43\n"
"v_permlane32_swap_b32 v44, v46\n"
"v_permlane32_swap_b32 v45, v47\n"
"v_accvgpr_write_b32 a88, v44\n"
"v_mfma_f32_16x16x32_bf16 v[92:95], a[168:171], a[248:251], v[92:95]\n"
"v_accvgpr_write_b32 a89, v45\n"
"v_accvgpr_write_b32 a90, v46\n"
"v_accvgpr_write_b32 a91, v47\n"
"v_mfma_f32_16x16x32_bf16 v[92:95], a[172:175], a[252:255], v[92:95]\n"
"v_cvt_pk_bf16_f32 v64, v64, v65\n"
"v_cvt_pk_bf16_f32 v65, v66, v67\n"
"v_cvt_pk_bf16_f32 v66, v80, v81\n"
"v_cvt_pk_bf16_f32 v67, v82, v83\n"
"s_waitcnt lgkmcnt(0)\n"
"v_mfma_f32_16x16x32_bf16 v[0:3], a[96:99], a[0:3], 0\n"
"ds_read_b128 a[120:123], v227 offset:6144\n"
"v_cvt_pk_bf16_f32 v68, v68, v69\n"
"v_cvt_pk_bf16_f32 v69, v70, v71\n"
"v_mfma_f32_16x16x32_bf16 v[0:3], a[100:103], a[4:7], v[0:3]\n"
"v_cvt_pk_bf16_f32 v70, v84, v85\n"
"v_cvt_pk_bf16_f32 v71, v86, v87\n"
"v_permlane32_swap_b32 v64, v66\n"
"v_mfma_f32_16x16x32_bf16 v[0:3], a[104:107], a[8:11], v[0:3]\n"
"ds_read_b128 a[124:127], v228 offset:6144\n"
"v_permlane32_swap_b32 v65, v67\n"
"v_accvgpr_write_b32 a20, v64\n"
"v_mfma_f32_16x16x32_bf16 v[0:3], a[108:111], a[12:15], v[0:3]\n"
"v_accvgpr_write_b32 a21, v65\n"
"v_accvgpr_write_b32 a22, v66\n"
"v_accvgpr_write_b32 a23, v67\n"
"v_mfma_f32_16x16x32_bf16 v[0:3], a[112:115], a[16:19], v[0:3]\n"
"ds_read_b128 a[128:131], v227 offset:7168\n"
"v_cvt_pk_bf16_f32 v72, v72, v73\n"
"v_cvt_pk_bf16_f32 v73, v74, v75\n"
"v_mfma_f32_16x16x32_bf16 v[0:3], a[116:119], a[20:23], v[0:3]\n"
"v_cvt_pk_bf16_f32 v74, v88, v89\n"
"v_cvt_pk_bf16_f32 v75, v90, v91\n"
"v_mfma_f32_16x16x32_bf16 v[4:7], a[96:99], a[24:27], 0\n"
"ds_read_b128 a[132:135], v228 offset:7168\n"
"v_permlane32_swap_b32 v68, v70\n"
"v_permlane32_swap_b32 v69, v71\n"
"v_mfma_f32_16x16x32_bf16 v[4:7], a[100:103], a[28:31], v[4:7]\n"
"v_accvgpr_write_b32 a44, v68\n"
"v_accvgpr_write_b32 a45, v69\n"
"v_mfma_f32_16x16x32_bf16 v[4:7], a[104:107], a[32:35], v[4:7]\n"
"ds_read_b128 a[136:139], v227 offset:8192\n"
"v_accvgpr_write_b32 a46, v70\n"
"v_accvgpr_write_b32 a47, v71\n"
"v_mfma_f32_16x16x32_bf16 v[4:7], a[108:111], a[36:39], v[4:7]\n"
"v_cvt_pk_bf16_f32 v76, v76, v77\n"
"v_cvt_pk_bf16_f32 v77, v78, v79\n"
"v_cvt_pk_bf16_f32 v78, v92, v93\n"
"v_mfma_f32_16x16x32_bf16 v[4:7], a[112:115], a[40:43], v[4:7]\n"
"ds_read_b128 a[140:143], v228 offset:8192\n"
"v_cvt_pk_bf16_f32 v79, v94, v95\n"
"v_permlane32_swap_b32 v72, v74\n"
"v_mfma_f32_16x16x32_bf16 v[4:7], a[116:119], a[44:47], v[4:7]\n"
"v_permlane32_swap_b32 v73, v75\n"
"v_accvgpr_write_b32 a68, v72\n"
"v_mfma_f32_16x16x32_bf16 v[8:11], a[96:99], a[48:51], 0\n"
"v_accvgpr_write_b32 a69, v73\n"
"v_accvgpr_write_b32 a70, v74\n"
"v_accvgpr_write_b32 a71, v75\n"
"v_mfma_f32_16x16x32_bf16 v[8:11], a[100:103], a[52:55], v[8:11]\n"
"v_permlane32_swap_b32 v76, v78\n"
"v_permlane32_swap_b32 v77, v79\n"
"v_accvgpr_write_b32 a92, v76\n"
"v_mfma_f32_16x16x32_bf16 v[8:11], a[104:107], a[56:59], v[8:11]\n"
"v_accvgpr_write_b32 a93, v77\n"
"v_accvgpr_write_b32 a94, v78\n"
"v_accvgpr_write_b32 a95, v79\n"
"v_mfma_f32_16x16x32_bf16 v[8:11], a[108:111], a[60:63], v[8:11]\n"
"v_max3_f32 v176, v0, v1, v2\n"
"v_max3_f32 v178, v4, v5, v6\n"
"v_max_f32 v176, v176, v3\n"
"v_max_f32 v178, v178, v7\n"
"v_mfma_f32_16x16x32_bf16 v[8:11], a[112:115], a[64:67], v[8:11]\n"
"v_mov_b32 v177, v176\n"
"v_mov_b32 v179, v178\n"
"v_accvgpr_write_b32 a187, 0\n"
"v_permlane32_swap_b32 v176, v177\n"
"v_mfma_f32_16x16x32_bf16 v[8:11], a[116:119], a[68:71], v[8:11]\n"
"v_permlane32_swap_b32 v178, v179\n"
"v_max_f32 v176, v176, v177\n"
"v_max_f32 v178, v178, v179\n"
"v_mfma_f32_16x16x32_bf16 v[12:15], a[96:99], a[72:75], 0\n"
"v_mov_b32 v177, v176\n"
"v_mov_b32 v179, v178\n"
"v_accvgpr_write_b32 a186, 0\n"
"v_permlane16_swap_b32 v176, v177\n"
"v_mfma_f32_16x16x32_bf16 v[12:15], a[100:103], a[76:79], v[12:15]\n"
"v_permlane16_swap_b32 v178, v179\n"
"v_max_f32 v176, v176, v177\n"
"v_max_f32 v178, v178, v179\n"
"v_mfma_f32_16x16x32_bf16 v[12:15], a[104:107], a[80:83], v[12:15]\n"
"v_sub_f32 v208, 0, v176\n"
"v_sub_f32 v209, 0, v176\n"
"v_sub_f32 v210, 0, v176\n"
"v_sub_f32 v211, 0, v176\n"
"v_mfma_f32_16x16x32_bf16 v[12:15], a[108:111], a[84:87], v[12:15]\n"
"v_sub_f32 v212, 0, v178\n"
"v_sub_f32 v213, 0, v178\n"
"v_sub_f32 v214, 0, v178\n"
"v_sub_f32 v215, 0, v178\n"
"v_mfma_f32_16x16x32_bf16 v[12:15], a[112:115], a[88:91], v[12:15]\n"
"v_add_f32 v0, v0, v208\n"
"v_add_f32 v1, v1, v208\n"
"v_add_f32 v2, v2, v208\n"
"v_mfma_f32_16x16x32_bf16 v[12:15], a[116:119], a[92:95], v[12:15]\n"
"v_add_f32 v3, v3, v208\n"
"v_exp_f32 v0, v0\n"
"s_waitcnt lgkmcnt(0)\n"
"v_mfma_f32_16x16x32_bf16 v[16:19], a[120:123], a[0:3], 0\n"
"ds_read_b128 a[96:99], v227 offset:12288\n"
"v_exp_f32 v1, v1\n"
"v_mfma_f32_16x16x32_bf16 v[16:19], a[124:127], a[4:7], v[16:19]\n"
"v_exp_f32 v2, v2\n"
"v_exp_f32 v3, v3\n"
"v_mfma_f32_16x16x32_bf16 v[16:19], a[128:131], a[8:11], v[16:19]\n"
"ds_read_b128 a[100:103], v228 offset:12288\n"
"v_cvt_pk_bf16_f32 v128, v0, v1\n"
"v_cvt_pk_bf16_f32 v129, v2, v3\n"
"v_mov_b32 v60, 0x3f803f80\n"
"v_mfma_f32_16x16x32_bf16 v[16:19], a[132:135], a[12:15], v[16:19]\n"
"v_accvgpr_write_b32 a168, v60\n"
"v_accvgpr_write_b32 a169, v60\n"
"v_accvgpr_write_b32 a170, v60\n"
"v_mfma_f32_16x16x32_bf16 v[16:19], a[136:139], a[16:19], v[16:19]\n"
"ds_read_b128 a[104:107], v227 offset:13312\n"
"v_accvgpr_write_b32 a171, v60\n"
"v_accvgpr_write_b32 a172, 0\n"
"v_accvgpr_write_b32 a173, 0\n"
"v_mfma_f32_16x16x32_bf16 v[16:19], a[140:143], a[20:23], v[16:19]\n"
"v_accvgpr_write_b32 a174, 0\n"
"v_accvgpr_write_b32 a175, 0\n"
"v_accvgpr_write_b32 a176, 0\n"
"v_mfma_f32_16x16x32_bf16 v[20:23], a[120:123], a[24:27], 0\n"
"ds_read_b128 a[108:111], v228 offset:13312\n"
"v_accvgpr_write_b32 a177, 0\n"
"v_accvgpr_write_b32 a178, 0\n"
"v_accvgpr_write_b32 a179, 0\n"
"v_mfma_f32_16x16x32_bf16 v[20:23], a[124:127], a[28:31], v[20:23]\n"
"v_accvgpr_write_b32 a180, 0\n"
"v_accvgpr_write_b32 a181, 0\n"
"v_accvgpr_write_b32 a182, 0\n"
"v_accvgpr_write_b32 a183, 0\n"
"v_mfma_f32_16x16x32_bf16 v[20:23], a[128:131], a[32:35], v[20:23]\n"
"ds_read_b128 a[112:115], v227 offset:14336\n"
"v_max3_f32 v180, v8, v9, v10\n"
"v_max3_f32 v182, v12, v13, v14\n"
"v_mfma_f32_16x16x32_bf16 v[20:23], a[132:135], a[36:39], v[20:23]\n"
"v_max_f32 v180, v180, v11\n"
"v_max_f32 v182, v182, v15\n"
"v_mov_b32 v181, v180\n"
"v_mov_b32 v183, v182\n"
"v_mfma_f32_16x16x32_bf16 v[20:23], a[136:139], a[40:43], v[20:23]\n"
"ds_read_b128 a[116:119], v228 offset:14336\n"
"v_accvgpr_write_b32 a185, 0\n"
"v_permlane32_swap_b32 v180, v181\n"
"v_mfma_f32_16x16x32_bf16 v[20:23], a[140:143], a[44:47], v[20:23]\n"
"v_permlane32_swap_b32 v182, v183\n"
"v_max_f32 v180, v180, v181\n"
"v_max_f32 v182, v182, v183\n"
"v_mov_b32 v181, v180\n"
"v_mfma_f32_16x16x32_bf16 v[24:27], a[120:123], a[48:51], 0\n"
"v_mov_b32 v183, v182\n"
"v_accvgpr_write_b32 a184, 0\n"
"v_permlane16_swap_b32 v180, v181\n"
"v_permlane16_swap_b32 v182, v183\n"
"v_mfma_f32_16x16x32_bf16 v[24:27], a[124:127], a[52:55], v[24:27]\n"
"v_max_f32 v180, v180, v181\n"
"v_max_f32 v182, v182, v183\n"
"v_sub_f32 v216, 0, v180\n"
"v_mfma_f32_16x16x32_bf16 v[24:27], a[128:131], a[56:59], v[24:27]\n"
"v_sub_f32 v217, 0, v180\n"
"v_sub_f32 v218, 0, v180\n"
"v_sub_f32 v219, 0, v180\n"
"v_sub_f32 v220, 0, v182\n"
"v_mfma_f32_16x16x32_bf16 v[24:27], a[132:135], a[60:63], v[24:27]\n"
"v_sub_f32 v221, 0, v182\n"
"v_sub_f32 v222, 0, v182\n"
"v_sub_f32 v223, 0, v182\n"
"v_mfma_f32_16x16x32_bf16 v[24:27], a[136:139], a[64:67], v[24:27]\n"
"v_add_f32 v4, v4, v212\n"
"v_add_f32 v5, v5, v212\n"
"v_add_f32 v6, v6, v212\n"
"v_add_f32 v7, v7, v212\n"
"v_mfma_f32_16x16x32_bf16 v[24:27], a[140:143], a[68:71], v[24:27]\n"
"v_exp_f32 v4, v4\n"
"v_exp_f32 v5, v5\n"
"v_mfma_f32_16x16x32_bf16 v[28:31], a[120:123], a[72:75], 0\n"
"v_exp_f32 v6, v6\n"
"v_mfma_f32_16x16x32_bf16 v[28:31], a[124:127], a[76:79], v[28:31]\n"
"v_exp_f32 v7, v7\n"
"v_cvt_pk_bf16_f32 v136, v4, v5\n"
"v_cvt_pk_bf16_f32 v137, v6, v7\n"
"v_mfma_f32_16x16x32_bf16 v[28:31], a[128:131], a[80:83], v[28:31]\n"
"v_add_f32 v8, v8, v216\n"
"v_add_f32 v9, v9, v216\n"
"v_add_f32 v10, v10, v216\n"
"v_mfma_f32_16x16x32_bf16 v[28:31], a[132:135], a[84:87], v[28:31]\n"
"v_add_f32 v11, v11, v216\n"
"v_exp_f32 v8, v8\n"
"v_mfma_f32_16x16x32_bf16 v[28:31], a[136:139], a[88:91], v[28:31]\n"
"v_exp_f32 v9, v9\n"
"v_exp_f32 v10, v10\n"
"v_mfma_f32_16x16x32_bf16 v[28:31], a[140:143], a[92:95], v[28:31]\n"
"v_exp_f32 v11, v11\n"
"v_cvt_pk_bf16_f32 v144, v8, v9\n"
"s_waitcnt lgkmcnt(0)\n"
"v_mfma_f32_16x16x32_bf16 v[32:35], a[96:99], a[0:3], v[208:211]\n"
"ds_read_b128 a[120:123], v227 offset:18432\n"
"v_cvt_pk_bf16_f32 v145, v10, v11\n"
"v_add_f32 v12, v12, v220\n"
"v_add_f32 v13, v13, v220\n"
"v_mfma_f32_16x16x32_bf16 v[32:35], a[100:103], a[4:7], v[32:35]\n"
"v_add_f32 v14, v14, v220\n"
"v_add_f32 v15, v15, v220\n"
"v_mfma_f32_16x16x32_bf16 v[32:35], a[104:107], a[8:11], v[32:35]\n"
"ds_read_b128 a[124:127], v228 offset:18432\n"
"v_exp_f32 v12, v12\n"
"v_mfma_f32_16x16x32_bf16 v[32:35], a[108:111], a[12:15], v[32:35]\n"
"v_exp_f32 v13, v13\n"
"v_exp_f32 v14, v14\n"
"v_mfma_f32_16x16x32_bf16 v[32:35], a[112:115], a[16:19], v[32:35]\n"
"ds_read_b128 a[128:131], v227 offset:19456\n"
"v_exp_f32 v15, v15\n"
"v_mfma_f32_16x16x32_bf16 v[32:35], a[116:119], a[20:23], v[32:35]\n"
"v_cvt_pk_bf16_f32 v152, v12, v13\n"
"v_cvt_pk_bf16_f32 v153, v14, v15\n"
"v_add_f32 v16, v16, v208\n"
"v_add_f32 v17, v17, v208\n"
"v_mfma_f32_16x16x32_bf16 v[36:39], a[96:99], a[24:27], v[212:215]\n"
"ds_read_b128 a[132:135], v228 offset:19456\n"
"v_add_f32 v18, v18, v208\n"
"v_add_f32 v19, v19, v208\n"
"v_mfma_f32_16x16x32_bf16 v[36:39], a[100:103], a[28:31], v[36:39]\n"
"v_exp_f32 v16, v16\n"
"v_exp_f32 v17, v17\n"
"v_mfma_f32_16x16x32_bf16 v[36:39], a[104:107], a[32:35], v[36:39]\n"
"ds_read_b128 a[136:139], v227 offset:20480\n"
"v_exp_f32 v18, v18\n"
"v_mfma_f32_16x16x32_bf16 v[36:39], a[108:111], a[36:39], v[36:39]\n"
"v_exp_f32 v19, v19\n"
"v_cvt_pk_bf16_f32 v130, v16, v17\n"
"v_mfma_f32_16x16x32_bf16 v[36:39], a[112:115], a[40:43], v[36:39]\n"
"ds_read_b128 a[140:143], v228 offset:20480\n"
"v_cvt_pk_bf16_f32 v131, v18, v19\n"
"v_add_f32 v20, v20, v212\n"
"v_add_f32 v21, v21, v212\n"
"v_mfma_f32_16x16x32_bf16 v[36:39], a[116:119], a[44:47], v[36:39]\n"
"v_add_f32 v22, v22, v212\n"
"v_add_f32 v23, v23, v212\n"
"v_mfma_f32_16x16x32_bf16 v[40:43], a[96:99], a[48:51], v[216:219]\n"
"v_exp_f32 v20, v20\n"
"v_exp_f32 v21, v21\n"
"v_mfma_f32_16x16x32_bf16 v[40:43], a[100:103], a[52:55], v[40:43]\n"
"v_exp_f32 v22, v22\n"
"v_exp_f32 v23, v23\n"
"v_mfma_f32_16x16x32_bf16 v[40:43], a[104:107], a[56:59], v[40:43]\n"
"v_cvt_pk_bf16_f32 v138, v20, v21\n"
"v_cvt_pk_bf16_f32 v139, v22, v23\n"
"v_add_f32 v24, v24, v216\n"
"v_mfma_f32_16x16x32_bf16 v[40:43], a[108:111], a[60:63], v[40:43]\n"
"v_add_f32 v25, v25, v216\n"
"v_add_f32 v26, v26, v216\n"
"v_add_f32 v27, v27, v216\n"
"v_mfma_f32_16x16x32_bf16 v[40:43], a[112:115], a[64:67], v[40:43]\n"
"v_exp_f32 v24, v24\n"
"v_mfma_f32_16x16x32_bf16 v[40:43], a[116:119], a[68:71], v[40:43]\n"
"v_exp_f32 v25, v25\n"
"v_exp_f32 v26, v26\n"
"v_mfma_f32_16x16x32_bf16 v[44:47], a[96:99], a[72:75], v[220:223]\n"
"v_exp_f32 v27, v27\n"
"v_cvt_pk_bf16_f32 v146, v24, v25\n"
"v_cvt_pk_bf16_f32 v147, v26, v27\n"
"v_mfma_f32_16x16x32_bf16 v[44:47], a[100:103], a[76:79], v[44:47]\n"
"v_add_f32 v28, v28, v220\n"
"v_add_f32 v29, v29, v220\n"
"v_add_f32 v30, v30, v220\n"
"v_mfma_f32_16x16x32_bf16 v[44:47], a[104:107], a[80:83], v[44:47]\n"
"v_add_f32 v31, v31, v220\n"
"v_exp_f32 v28, v28\n"
"v_mfma_f32_16x16x32_bf16 v[44:47], a[108:111], a[84:87], v[44:47]\n"
"v_exp_f32 v29, v29\n"
"v_mfma_f32_16x16x32_bf16 v[44:47], a[112:115], a[88:91], v[44:47]\n"
"v_exp_f32 v30, v30\n"
"v_exp_f32 v31, v31\n"
"v_mfma_f32_16x16x32_bf16 v[44:47], a[116:119], a[92:95], v[44:47]\n"
"v_cvt_pk_bf16_f32 v154, v28, v29\n"
"v_cvt_pk_bf16_f32 v155, v30, v31\n"
"v_exp_f32 v32, v32\n"
"s_waitcnt lgkmcnt(0)\n"
"v_mfma_f32_16x16x32_bf16 v[48:51], a[120:123], a[0:3], v[208:211]\n"
"v_exp_f32 v33, v33\n"
"v_mfma_f32_16x16x32_bf16 v[48:51], a[124:127], a[4:7], v[48:51]\n"
"v_exp_f32 v34, v34\n"
"v_exp_f32 v35, v35\n"
"v_mfma_f32_16x16x32_bf16 v[48:51], a[128:131], a[8:11], v[48:51]\n"
"v_cvt_pk_bf16_f32 v132, v32, v33\n"
"v_cvt_pk_bf16_f32 v133, v34, v35\n"
"v_mfma_f32_16x16x32_bf16 v[48:51], a[132:135], a[12:15], v[48:51]\n"
"v_exp_f32 v36, v36\n"
"v_exp_f32 v37, v37\n"
"v_mfma_f32_16x16x32_bf16 v[48:51], a[136:139], a[16:19], v[48:51]\n"
"v_exp_f32 v38, v38\n"
"v_exp_f32 v39, v39\n"
"v_mfma_f32_16x16x32_bf16 v[48:51], a[140:143], a[20:23], v[48:51]\n"
"s_waitcnt vmcnt(0) lgkmcnt(0)\n"
"s_barrier\n"
"v_cvt_pk_bf16_f32 v140, v36, v37\n"
"v_mfma_f32_16x16x32_bf16 v[52:55], a[120:123], a[24:27], v[212:215]\n"
"ds_read_b128 a[96:99], v227 offset:24576\n"
"v_cvt_pk_bf16_f32 v141, v38, v39\n"
"v_exp_f32 v40, v40\n"
"v_mfma_f32_16x16x32_bf16 v[52:55], a[124:127], a[28:31], v[52:55]\n"
"ds_read_b128 a[100:103], v228 offset:24576\n"
"v_exp_f32 v41, v41\n"
"v_mfma_f32_16x16x32_bf16 v[52:55], a[128:131], a[32:35], v[52:55]\n"
"ds_read_b128 a[104:107], v227 offset:25600\n"
"v_exp_f32 v42, v42\n"
"v_mfma_f32_16x16x32_bf16 v[52:55], a[132:135], a[36:39], v[52:55]\n"
"ds_read_b128 a[108:111], v228 offset:25600\n"
"v_exp_f32 v43, v43\n"
"v_cvt_pk_bf16_f32 v148, v40, v41\n"
"v_mfma_f32_16x16x32_bf16 v[52:55], a[136:139], a[40:43], v[52:55]\n"
"ds_read_b128 a[112:115], v227 offset:26624\n"
"v_cvt_pk_bf16_f32 v149, v42, v43\n"
"v_mfma_f32_16x16x32_bf16 v[52:55], a[140:143], a[44:47], v[52:55]\n"
"ds_read_b128 a[116:119], v228 offset:26624\n"
"v_exp_f32 v44, v44\n"
"v_mfma_f32_16x16x32_bf16 v[200:203], a[120:123], a[48:51], v[216:219]\n"
"ds_read_b64_tr_b16 a[144:145], v229 offset:0\n"
"v_exp_f32 v45, v45\n"
"v_mfma_f32_16x16x32_bf16 v[200:203], a[124:127], a[52:55], v[200:203]\n"
"ds_read_b64_tr_b16 a[146:147], v229 offset:6144\n"
"v_exp_f32 v46, v46\n"
"v_mfma_f32_16x16x32_bf16 v[200:203], a[128:131], a[56:59], v[200:203]\n"
"ds_read_b64_tr_b16 a[148:149], v229 offset:12288\n"
"v_exp_f32 v47, v47\n"
"v_cvt_pk_bf16_f32 v156, v44, v45\n"
"v_mfma_f32_16x16x32_bf16 v[200:203], a[132:135], a[60:63], v[200:203]\n"
"ds_read_b64_tr_b16 a[150:151], v229 offset:18432\n"
"v_cvt_pk_bf16_f32 v157, v46, v47\n"
"v_mfma_f32_16x16x32_bf16 v[200:203], a[136:139], a[64:67], v[200:203]\n"
"ds_read_b64_tr_b16 a[152:153], v230 offset:0\n"
"v_exp_f32 v48, v48\n"
"v_mfma_f32_16x16x32_bf16 v[200:203], a[140:143], a[68:71], v[200:203]\n"
"ds_read_b64_tr_b16 a[154:155], v230 offset:6144\n"
"v_exp_f32 v49, v49\n"
"v_mfma_f32_16x16x32_bf16 v[204:207], a[120:123], a[72:75], v[220:223]\n"
"ds_read_b64_tr_b16 a[156:157], v230 offset:12288\n"
"v_exp_f32 v50, v50\n"
"v_mfma_f32_16x16x32_bf16 v[204:207], a[124:127], a[76:79], v[204:207]\n"
"ds_read_b64_tr_b16 a[158:159], v230 offset:18432\n"
"v_exp_f32 v51, v51\n"
"v_cvt_pk_bf16_f32 v134, v48, v49\n"
"v_mfma_f32_16x16x32_bf16 v[204:207], a[128:131], a[80:83], v[204:207]\n"
"v_cvt_pk_bf16_f32 v135, v50, v51\n"
"v_exp_f32 v52, v52\n"
"v_mfma_f32_16x16x32_bf16 v[204:207], a[132:135], a[84:87], v[204:207]\n"
"v_exp_f32 v53, v53\n"
"v_exp_f32 v54, v54\n"
"v_mfma_f32_16x16x32_bf16 v[204:207], a[136:139], a[88:91], v[204:207]\n"
"v_exp_f32 v55, v55\n"
"v_cvt_pk_bf16_f32 v142, v52, v53\n"
"v_mfma_f32_16x16x32_bf16 v[204:207], a[140:143], a[92:95], v[204:207]\n"
"v_cvt_pk_bf16_f32 v143, v54, v55\n"
"v_exp_f32 v200, v200\n"
"v_exp_f32 v201, v201\n"
"v_exp_f32 v202, v202\n"
"v_exp_f32 v203, v203\n"
"v_cvt_pk_bf16_f32 v150, v200, v201\n"
"v_cvt_pk_bf16_f32 v151, v202, v203\n"
"s_nop 0\n"
"v_exp_f32 v204, v204\n"
"s_add_u32 s16, s16, 24576\n"
"s_addc_u32 s17, s17, 0\n"
"s_sub_u32 s18, s18, 24576\n"
"L_it0_%=:\n"
"s_waitcnt lgkmcnt(2)\n"
"v_mfma_f32_16x16x32_bf16 v[192:195], a[96:99], a[0:3], v[208:211]\n"
"s_add_u32 s61, s61, 24576\n"
"ds_read_b64_tr_b16 a[160:161], v231 offset:0\n"
"v_mfma_f32_16x16x32_bf16 v[192:195], a[100:103], a[4:7], v[192:195]\n"
"s_add_u32 s62, s62, 24576\n"
"ds_read_b64_tr_b16 a[162:163], v231 offset:6144\n"
"v_mfma_f32_16x16x32_bf16 v[192:195], a[104:107], a[8:11], v[192:195]\n"
"v_exp_f32 v205, v205\n"
"v_mul_f32 v116, v96, v96\n"
"v_mfma_f32_16x16x32_bf16 v[192:195], a[108:111], a[12:15], v[192:195]\n"
"s_mov_b32 m0, s69\n"
"ds_read_b64_tr_b16 a[164:165], v231 offset:12288\n"
"v_mul_f32 v117, v97, v97\n"
"v_mfma_f32_16x16x32_bf16 v[192:195], a[112:115], a[16:19], v[192:195]\n"
"v_cvt_pk_bf16_f32 v158, v204, v205\n"
"ds_read_b128 a[120:123], v227 offset:30720\n"
"v_mul_f32 v118, v98, v98\n"
"v_mfma_f32_16x16x32_bf16 v[192:195], a[116:119], a[20:23], v[192:195]\n"
"v_exp_f32 v206, v206\n"
"v_mul_f32 v119, v99, v99\n"
"v_mfma_f32_16x16x32_bf16 a[192:195], a[144:147], v[128:131], 0\n"
"buffer_load_dwordx4 v224, s[20:23], s61 offen offset:0 lds\n"
"v_mfma_f32_16x16x32_bf16 a[192:195], a[148:151], v[132:135], a[192:195]\n"
"ds_read_b64_tr_b16 a[166:167], v231 offset:18432\n"
"ds_read_b128 a[124:127], v228 offset:30720\n"
"v_fmaak_f32 v116, v247, v116, 0xc0135761\n"
"v_mfma_f32_16x16x32_bf16 a[196:199], a[144:147], v[136:139], 0\n"
"v_exp_f32 v207, v207\n"
"v_fmaak_f32 v117, v247, v117, 0xc0135761\n"
"v_mfma_f32_16x16x32_bf16 a[196:199], a[148:151], v[140:143], a[196:199]\n"
"buffer_load_dwordx4 v225, s[20:23], s61 offen offset:1024 lds\n"
"v_fmaak_f32 v118, v247, v118, 0xc0135761\n"
"v_mfma_f32_16x16x32_bf16 a[200:203], a[144:147], v[144:147], 0\n"
"v_cvt_pk_bf16_f32 v159, v206, v207\n"
"v_fmaak_f32 v119, v247, v119, 0xc0135761\n"
"v_mul_f32 v116, v116, v96\n"
"v_mfma_f32_16x16x32_bf16 a[200:203], a[148:151], v[148:151], a[200:203]\n"
"v_exp_f32 v192, v192\n"
"v_mul_f32 v117, v117, v97\n"
"v_mfma_f32_16x16x32_bf16 v[196:199], a[96:99], a[24:27], v[212:215]\n"
"ds_read_b128 a[128:131], v227 offset:31744\n"
"v_mul_f32 v118, v118, v98\n"
"v_mul_f32 v119, v119, v99\n"
"v_mfma_f32_16x16x32_bf16 v[196:199], a[100:103], a[28:31], v[196:199]\n"
"v_exp_f32 v116, v116\n"
"v_mfma_f32_16x16x32_bf16 v[196:199], a[104:107], a[32:35], v[196:199]\n"
"v_exp_f32 v193, v193\n"
"v_mfma_f32_16x16x32_bf16 v[196:199], a[108:111], a[36:39], v[196:199]\n"
"buffer_load_dwordx4 v226, s[20:23], s61 offen offset:2048 lds\n"
"v_mfma_f32_16x16x32_bf16 v[196:199], a[112:115], a[40:43], v[196:199]\n"
"v_cvt_pk_bf16_f32 v160, v192, v193\n"
"v_exp_f32 v117, v117\n"
"v_mfma_f32_16x16x32_bf16 v[196:199], a[116:119], a[44:47], v[196:199]\n"
"v_exp_f32 v194, v194\n"
"v_mfma_f32_16x16x32_bf16 a[204:207], a[144:147], v[152:155], 0\n"
"ds_read_b128 a[132:135], v228 offset:31744\n"
"v_exp_f32 v118, v118\n"
"v_mfma_f32_16x16x32_bf16 a[204:207], a[148:151], v[156:159], a[204:207]\n"
"ds_read_b64_tr_b16 a[144:145], v232 offset:0\n"
"v_mfma_f32_16x16x32_bf16 a[208:211], a[152:155], v[128:131], 0\n"
"v_exp_f32 v195, v195\n"
"s_waitcnt lgkmcnt(2)\n"
"v_mfma_f32_16x16x32_bf16 a[208:211], a[156:159], v[132:135], a[208:211]\n"
"ds_read_b64_tr_b16 a[146:147], v232 offset:6144\n"
"v_exp_f32 v119, v119\n"
"v_mfma_f32_16x16x32_bf16 a[212:215], a[152:155], v[136:139], 0\n"
"v_cvt_pk_bf16_f32 v161, v194, v195\n"
"ds_read_b64_tr_b16 a[148:149], v232 offset:12288\n"
"v_add_f32 v116, 1.0, v116\n"
"v_mfma_f32_16x16x32_bf16 a[212:215], a[156:159], v[140:143], a[212:215]\n"
"v_exp_f32 v196, v196\n"
"v_add_f32 v117, 1.0, v117\n"
"v_mfma_f32_16x16x32_bf16 v[200:203], a[96:99], a[48:51], v[216:219]\n"
"s_mov_b32 m0, s70\n"
"ds_read_b128 a[136:139], v227 offset:32768\n"
"v_add_f32 v118, 1.0, v118\n"
"v_mfma_f32_16x16x32_bf16 v[200:203], a[100:103], a[52:55], v[200:203]\n"
"buffer_load_dwordx4 v224, s[20:23], s62 offen offset:0 lds\n"
"v_add_f32 v119, 1.0, v119\n"
"v_mfma_f32_16x16x32_bf16 v[200:203], a[104:107], a[56:59], v[200:203]\n"
"v_exp_f32 v197, v197\n"
"v_mfma_f32_16x16x32_bf16 v[200:203], a[108:111], a[60:63], v[200:203]\n"
"ds_read_b64_tr_b16 a[150:151], v232 offset:18432\n"
"v_rcp_f32 v116, v116\n"
"v_mfma_f32_16x16x32_bf16 v[200:203], a[112:115], a[64:67], v[200:203]\n"
"v_cvt_pk_bf16_f32 v168, v196, v197\n"
"v_rcp_f32 v117, v117\n"
"v_mfma_f32_16x16x32_bf16 v[200:203], a[116:119], a[68:71], v[200:203]\n"
"v_exp_f32 v198, v198\n"
"v_mfma_f32_16x16x32_bf16 a[216:219], a[152:155], v[144:147], 0\n"
"buffer_load_dwordx4 v225, s[20:23], s62 offen offset:1024 lds\n"
"v_mfma_f32_16x16x32_bf16 a[216:219], a[156:159], v[148:151], a[216:219]\n"
"ds_read_b128 a[140:143], v228 offset:32768\n"
"v_rcp_f32 v118, v118\n"
"v_mfma_f32_16x16x32_bf16 a[220:223], a[152:155], v[152:155], 0\n"
"v_exp_f32 v199, v199\n"
"v_mfma_f32_16x16x32_bf16 a[220:223], a[156:159], v[156:159], a[220:223]\n"
"ds_read_b64_tr_b16 a[152:153], v229 offset:1024\n"
"v_mfma_f32_16x16x32_bf16 a[224:227], a[160:163], v[128:131], 0\n"
"v_cvt_pk_bf16_f32 v169, v198, v199\n"
"v_rcp_f32 v119, v119\n"
"v_mfma_f32_16x16x32_bf16 a[224:227], a[164:167], v[132:135], a[224:227]\n"
"v_exp_f32 v200, v200\n"
"v_mul_f32 v96, v96, v116\n"
"v_smfmac_f32_16x16x64_bf16 a[172:175], a[168:171], v[128:135], v233\n"
"buffer_load_dwordx4 v226, s[20:23], s62 offen offset:2048 lds\n"
"v_mul_f32 v97, v97, v117\n"
"v_mfma_f32_16x16x32_bf16 v[204:207], a[96:99], a[72:75], v[220:223]\n"
"ds_read_b64_tr_b16 a[154:155], v229 offset:7168\n"
"v_mul_f32 v98, v98, v118\n"
"v_mul_f32 v99, v99, v119\n"
"v_mfma_f32_16x16x32_bf16 v[204:207], a[100:103], a[76:79], v[204:207]\n"
"ds_read_b64_tr_b16 a[156:157], v229 offset:13312\n"
"v_cvt_pk_bf16_f32 v96, v96, v97\n"
"v_cvt_pk_bf16_f32 v97, v98, v99\n"
"v_mfma_f32_16x16x32_bf16 v[204:207], a[104:107], a[80:83], v[204:207]\n"
"v_exp_f32 v201, v201\n"
"v_mfma_f32_16x16x32_bf16 v[204:207], a[108:111], a[84:87], v[204:207]\n"
"ds_read_b64_tr_b16 a[158:159], v229 offset:19456\n"
"buffer_store_dwordx2 v[96:97], v240, s[8:11], s60 offen sc1\n"
"v_mfma_f32_16x16x32_bf16 v[204:207], a[112:115], a[88:91], v[204:207]\n"
"v_cvt_pk_bf16_f32 v176, v200, v201\n"
"v_mul_f32 v120, v100, v100\n"
"v_mul_f32 v121, v101, v101\n"
"v_mfma_f32_16x16x32_bf16 v[204:207], a[116:119], a[92:95], v[204:207]\n"
"v_exp_f32 v202, v202\n"
"v_mul_f32 v122, v102, v102\n"
"v_mfma_f32_16x16x32_bf16 a[228:231], a[160:163], v[136:139], 0\n"
"s_mov_b32 m0, s63\n"
"v_mul_f32 v123, v103, v103\n"
"v_fmaak_f32 v120, v247, v120, 0xc0135761\n"
"v_mfma_f32_16x16x32_bf16 a[228:231], a[164:167], v[140:143], a[228:231]\n"
"buffer_load_dwordx4 v224, s[16:19], s61 offen offset:0 lds\n"
"v_fmaak_f32 v121, v247, v121, 0xc0135761\n"
"v_mfma_f32_16x16x32_bf16 a[232:235], a[160:163], v[144:147], 0\n"
"v_exp_f32 v203, v203\n"
"v_fmaak_f32 v122, v247, v122, 0xc0135761\n"
"v_mfma_f32_16x16x32_bf16 a[232:235], a[164:167], v[148:151], a[232:235]\n"
"v_fmaak_f32 v123, v247, v123, 0xc0135761\n"
"v_mul_f32 v120, v120, v100\n"
"v_mfma_f32_16x16x32_bf16 a[236:239], a[160:163], v[152:155], 0\n"
"v_cvt_pk_bf16_f32 v177, v202, v203\n"
"v_mul_f32 v121, v121, v101\n"
"v_mul_f32 v122, v122, v102\n"
"v_mfma_f32_16x16x32_bf16 a[236:239], a[164:167], v[156:159], a[236:239]\n"
"v_exp_f32 v204, v204\n"
"v_mul_f32 v123, v123, v103\n"
"v_mfma_f32_16x16x32_bf16 v[192:195], a[120:123], a[0:3], v[208:211]\n"
"ds_read_b64_tr_b16 a[160:161], v230 offset:1024\n"
"ds_read_b128 a[96:99], v227 offset:36864\n"
"v_mfma_f32_16x16x32_bf16 v[192:195], a[124:127], a[4:7], v[192:195]\n"
"buffer_load_dwordx4 v225, s[16:19], s61 offen offset:1024 lds\n"
"v_mfma_f32_16x16x32_bf16 v[192:195], a[128:131], a[8:11], v[192:195]\n"
"v_exp_f32 v205, v205\n"
"s_waitcnt lgkmcnt(2)\n"
"v_mfma_f32_16x16x32_bf16 v[192:195], a[132:135], a[12:15], v[192:195]\n"
"ds_read_b64_tr_b16 a[162:163], v230 offset:7168\n"
"v_exp_f32 v120, v120\n"
"v_mfma_f32_16x16x32_bf16 v[192:195], a[136:139], a[16:19], v[192:195]\n"
"v_cvt_pk_bf16_f32 v184, v204, v205\n"
"v_mfma_f32_16x16x32_bf16 v[192:195], a[140:143], a[20:23], v[192:195]\n"
"v_exp_f32 v206, v206\n"
"v_mfma_f32_16x16x32_bf16 a[240:243], a[144:147], v[128:131], 0\n"
"ds_read_b64_tr_b16 a[164:165], v230 offset:13312\n"
"ds_read_b128 a[100:103], v228 offset:36864\n"
"v_mfma_f32_16x16x32_bf16 a[240:243], a[148:151], v[132:135], a[240:243]\n"
"ds_read_b64_tr_b16 a[166:167], v230 offset:19456\n"
"v_exp_f32 v121, v121\n"
"v_mfma_f32_16x16x32_bf16 a[244:247], a[144:147], v[136:139], 0\n"
"v_exp_f32 v207, v207\n"
"v_mfma_f32_16x16x32_bf16 a[244:247], a[148:151], v[140:143], a[244:247]\n"
"buffer_load_dwordx4 v226, s[16:19], s61 offen offset:2048 lds\n"
"v_mfma_f32_16x16x32_bf16 a[248:251], a[144:147], v[144:147], 0\n"
"v_cvt_pk_bf16_f32 v185, v206, v207\n"
"v_mfma_f32_16x16x32_bf16 a[248:251], a[148:151], v[148:151], a[248:251]\n"
"v_exp_f32 v192, v192\n"
"v_mfma_f32_16x16x32_bf16 v[196:199], a[120:123], a[24:27], v[212:215]\n"
"ds_read_b128 a[104:107], v227 offset:37888\n"
"v_exp_f32 v122, v122\n"
"v_mfma_f32_16x16x32_bf16 v[196:199], a[124:127], a[28:31], v[196:199]\n"
"v_exp_f32 v123, v123\n"
"v_add_f32 v120, 1.0, v120\n"
"v_mfma_f32_16x16x32_bf16 v[196:199], a[128:131], a[32:35], v[196:199]\n"
"v_exp_f32 v193, v193\n"
"v_add_f32 v121, 1.0, v121\n"
"v_mfma_f32_16x16x32_bf16 v[196:199], a[132:135], a[36:39], v[196:199]\n"
"s_mov_b32 m0, s64\n"
"v_add_f32 v122, 1.0, v122\n"
"v_add_f32 v123, 1.0, v123\n"
"v_mfma_f32_16x16x32_bf16 v[196:199], a[136:139], a[40:43], v[196:199]\n"
"v_cvt_pk_bf16_f32 v162, v192, v193\n"
"v_rcp_f32 v120, v120\n"
"v_mfma_f32_16x16x32_bf16 v[196:199], a[140:143], a[44:47], v[196:199]\n"
"v_exp_f32 v194, v194\n"
"v_mfma_f32_16x16x32_bf16 a[252:255], a[144:147], v[152:155], 0\n"
"buffer_load_dwordx4 v224, s[16:19], s62 offen offset:0 lds\n"
"v_mfma_f32_16x16x32_bf16 a[252:255], a[148:151], v[156:159], a[252:255]\n"
"ds_read_b64_tr_b16 a[144:145], v231 offset:1024\n"
"ds_read_b128 a[108:111], v228 offset:37888\n"
"v_mfma_f32_16x16x32_bf16 v[0:3], a[152:155], v[128:131], 0\n"
"v_exp_f32 v195, v195\n"
"v_mfma_f32_16x16x32_bf16 v[0:3], a[156:159], v[132:135], v[0:3]\n"
"ds_read_b64_tr_b16 a[146:147], v231 offset:7168\n"
"v_rcp_f32 v121, v121\n"
"v_mfma_f32_16x16x32_bf16 v[4:7], a[152:155], v[136:139], 0\n"
"v_cvt_pk_bf16_f32 v163, v194, v195\n"
"v_rcp_f32 v122, v122\n"
"v_mfma_f32_16x16x32_bf16 v[4:7], a[156:159], v[140:143], v[4:7]\n"
"v_exp_f32 v196, v196\n"
"v_mfma_f32_16x16x32_bf16 v[200:203], a[120:123], a[48:51], v[216:219]\n"
"buffer_load_dwordx4 v225, s[16:19], s62 offen offset:1024 lds\n"
"v_mfma_f32_16x16x32_bf16 v[200:203], a[124:127], a[52:55], v[200:203]\n"
"ds_read_b64_tr_b16 a[148:149], v231 offset:13312\n"
"ds_read_b128 a[112:115], v227 offset:38912\n"
"v_mfma_f32_16x16x32_bf16 v[200:203], a[128:131], a[56:59], v[200:203]\n"
"v_exp_f32 v197, v197\n"
"v_mfma_f32_16x16x32_bf16 v[200:203], a[132:135], a[60:63], v[200:203]\n"
"ds_read_b64_tr_b16 a[150:151], v231 offset:19456\n"
"v_rcp_f32 v123, v123\n"
"v_mfma_f32_16x16x32_bf16 v[200:203], a[136:139], a[64:67], v[200:203]\n"
"v_cvt_pk_bf16_f32 v170, v196, v197\n"
"v_mul_f32 v100, v100, v120\n"
"v_mul_f32 v101, v101, v121\n"
"v_mfma_f32_16x16x32_bf16 v[200:203], a[140:143], a[68:71], v[200:203]\n"
"v_exp_f32 v198, v198\n"
"v_mul_f32 v102, v102, v122\n"
"v_mfma_f32_16x16x32_bf16 v[8:11], a[152:155], v[144:147], 0\n"
"buffer_load_dwordx4 v226, s[16:19], s62 offen offset:2048 lds\n"
"v_mfma_f32_16x16x32_bf16 v[8:11], a[156:159], v[148:151], v[8:11]\n"
"ds_read_b128 a[116:119], v228 offset:38912\n"
"v_mul_f32 v103, v103, v123\n"
"v_cvt_pk_bf16_f32 v100, v100, v101\n"
"v_mfma_f32_16x16x32_bf16 v[12:15], a[152:155], v[152:155], 0\n"
"v_exp_f32 v199, v199\n"
"v_cvt_pk_bf16_f32 v101, v102, v103\n"
"v_mfma_f32_16x16x32_bf16 v[12:15], a[156:159], v[156:159], v[12:15]\n"
"ds_read_b64_tr_b16 a[152:153], v232 offset:1024\n"
"buffer_store_dwordx2 v[100:101], v241, s[8:11], s60 offen sc1\n"
"s_waitcnt lgkmcnt(2)\n"
"v_mfma_f32_16x16x32_bf16 v[16:19], a[160:163], v[128:131], 0\n"
"v_cvt_pk_bf16_f32 v171, v198, v199\n"
"v_mul_f32 v116, v104, v104\n"
"v_mul_f32 v117, v105, v105\n"
"v_mfma_f32_16x16x32_bf16 v[16:19], a[164:167], v[132:135], v[16:19]\n"
"v_exp_f32 v200, v200\n"
"v_mul_f32 v118, v106, v106\n"
"v_smfmac_f32_16x16x64_bf16 a[176:179], a[168:171], v[136:143], v233\n"
"ds_read_b64_tr_b16 a[154:155], v232 offset:7168\n"
"v_mul_f32 v119, v107, v107\n"
"v_fmaak_f32 v116, v247, v116, 0xc0135761\n"
"v_mfma_f32_16x16x32_bf16 v[204:207], a[120:123], a[72:75], v[220:223]\n"
"ds_read_b64_tr_b16 a[156:157], v232 offset:13312\n"
"v_fmaak_f32 v117, v247, v117, 0xc0135761\n"
"v_mfma_f32_16x16x32_bf16 v[204:207], a[124:127], a[76:79], v[204:207]\n"
"ds_read_b64_tr_b16 a[158:159], v232 offset:19456\n"
"v_fmaak_f32 v118, v247, v118, 0xc0135761\n"
"v_fmaak_f32 v119, v247, v119, 0xc0135761\n"
"v_mfma_f32_16x16x32_bf16 v[204:207], a[128:131], a[80:83], v[204:207]\n"
"v_exp_f32 v201, v201\n"
"v_mul_f32 v116, v116, v104\n"
"v_mfma_f32_16x16x32_bf16 v[204:207], a[132:135], a[84:87], v[204:207]\n"
"v_mul_f32 v117, v117, v105\n"
"v_mul_f32 v118, v118, v106\n"
"v_mul_f32 v119, v119, v107\n"
"v_mfma_f32_16x16x32_bf16 v[204:207], a[136:139], a[88:91], v[204:207]\n"
"v_cvt_pk_bf16_f32 v178, v200, v201\n"
"v_exp_f32 v116, v116\n"
"v_mfma_f32_16x16x32_bf16 v[204:207], a[140:143], a[92:95], v[204:207]\n"
"v_exp_f32 v202, v202\n"
"v_mfma_f32_16x16x32_bf16 v[20:23], a[160:163], v[136:139], 0\n"
"v_exp_f32 v117, v117\n"
"v_mfma_f32_16x16x32_bf16 v[20:23], a[164:167], v[140:143], v[20:23]\n"
"v_exp_f32 v118, v118\n"
"v_mfma_f32_16x16x32_bf16 v[24:27], a[160:163], v[144:147], 0\n"
"v_exp_f32 v203, v203\n"
"v_mfma_f32_16x16x32_bf16 v[24:27], a[164:167], v[148:151], v[24:27]\n"
"v_exp_f32 v119, v119\n"
"v_add_f32 v116, 1.0, v116\n"
"v_mfma_f32_16x16x32_bf16 v[28:31], a[160:163], v[152:155], 0\n"
"v_cvt_pk_bf16_f32 v179, v202, v203\n"
"v_add_f32 v117, 1.0, v117\n"
"v_add_f32 v118, 1.0, v118\n"
"v_mfma_f32_16x16x32_bf16 v[28:31], a[164:167], v[156:159], v[28:31]\n"
"v_exp_f32 v204, v204\n"
"v_add_f32 v119, 1.0, v119\n"
"v_mfma_f32_16x16x32_bf16 v[192:195], a[96:99], a[0:3], v[208:211]\n"
"ds_read_b64_tr_b16 a[160:161], v229 offset:2048\n"
"ds_read_b128 a[120:123], v227 offset:43008\n"
"v_mfma_f32_16x16x32_bf16 v[192:195], a[100:103], a[4:7], v[192:195]\n"
"ds_read_b64_tr_b16 a[162:163], v229 offset:8192\n"
"v_mfma_f32_16x16x32_bf16 v[192:195], a[104:107], a[8:11], v[192:195]\n"
"v_exp_f32 v205, v205\n"
"v_mfma_f32_16x16x32_bf16 v[192:195], a[108:111], a[12:15], v[192:195]\n"
"ds_read_b64_tr_b16 a[164:165], v229 offset:14336\n"
"v_rcp_f32 v116, v116\n"
"v_mfma_f32_16x16x32_bf16 v[192:195], a[112:115], a[16:19], v[192:195]\n"
"v_cvt_pk_bf16_f32 v186, v204, v205\n"
"v_rcp_f32 v117, v117\n"
"s_waitcnt lgkmcnt(4)\n"
"v_mfma_f32_16x16x32_bf16 v[192:195], a[116:119], a[20:23], v[192:195]\n"
"v_exp_f32 v206, v206\n"
"v_mfma_f32_16x16x32_bf16 v[32:35], a[144:147], v[128:131], 0\n"
"ds_read_b64_tr_b16 a[166:167], v229 offset:20480\n"
"v_rcp_f32 v118, v118\n"
"v_mfma_f32_16x16x32_bf16 v[32:35], a[148:151], v[132:135], v[32:35]\n"
"ds_read_b128 a[124:127], v228 offset:43008\n"
"v_rcp_f32 v119, v119\n"
"v_mfma_f32_16x16x32_bf16 v[36:39], a[144:147], v[136:139], 0\n"
"v_exp_f32 v207, v207\n"
"v_mfma_f32_16x16x32_bf16 v[36:39], a[148:151], v[140:143], v[36:39]\n"
"v_mul_f32 v104, v104, v116\n"
"v_mul_f32 v105, v105, v117\n"
"v_mul_f32 v106, v106, v118\n"
"v_mfma_f32_16x16x32_bf16 v[40:43], a[144:147], v[144:147], 0\n"
"v_cvt_pk_bf16_f32 v187, v206, v207\n"
"v_mul_f32 v107, v107, v119\n"
"v_cvt_pk_bf16_f32 v104, v104, v105\n"
"v_mfma_f32_16x16x32_bf16 v[40:43], a[148:151], v[148:151], v[40:43]\n"
"v_exp_f32 v192, v192\n"
"v_cvt_pk_bf16_f32 v105, v106, v107\n"
"v_mfma_f32_16x16x32_bf16 v[196:199], a[96:99], a[24:27], v[212:215]\n"
"ds_read_b128 a[128:131], v227 offset:44032\n"
"buffer_store_dwordx2 v[104:105], v242, s[8:11], s60 offen sc1\n"
"v_mfma_f32_16x16x32_bf16 v[196:199], a[100:103], a[28:31], v[196:199]\n"
"v_mul_f32 v120, v108, v108\n"
"v_mul_f32 v121, v109, v109\n"
"v_mul_f32 v122, v110, v110\n"
"v_mfma_f32_16x16x32_bf16 v[196:199], a[104:107], a[32:35], v[196:199]\n"
"v_exp_f32 v193, v193\n"
"v_mul_f32 v123, v111, v111\n"
"v_mfma_f32_16x16x32_bf16 v[196:199], a[108:111], a[36:39], v[196:199]\n"
"v_fmaak_f32 v120, v247, v120, 0xc0135761\n"
"v_fmaak_f32 v121, v247, v121, 0xc0135761\n"
"v_mfma_f32_16x16x32_bf16 v[196:199], a[112:115], a[40:43], v[196:199]\n"
"v_cvt_pk_bf16_f32 v164, v192, v193\n"
"v_fmaak_f32 v122, v247, v122, 0xc0135761\n"
"v_fmaak_f32 v123, v247, v123, 0xc0135761\n"
"v_mfma_f32_16x16x32_bf16 v[196:199], a[116:119], a[44:47], v[196:199]\n"
"v_exp_f32 v194, v194\n"
"v_mul_f32 v120, v120, v108\n"
"v_mfma_f32_16x16x32_bf16 v[44:47], a[144:147], v[152:155], 0\n"
"ds_read_b128 a[132:135], v228 offset:44032\n"
"v_mul_f32 v121, v121, v109\n"
"v_mul_f32 v122, v122, v110\n"
"v_mfma_f32_16x16x32_bf16 v[44:47], a[148:151], v[156:159], v[44:47]\n"
"ds_read_b64_tr_b16 a[144:145], v230 offset:2048\n"
"v_mul_f32 v123, v123, v111\n"
"v_mfma_f32_16x16x32_bf16 v[48:51], a[152:155], v[128:131], 0\n"
"v_exp_f32 v195, v195\n"
"v_mfma_f32_16x16x32_bf16 v[48:51], a[156:159], v[132:135], v[48:51]\n"
"ds_read_b64_tr_b16 a[146:147], v230 offset:8192\n"
"v_mfma_f32_16x16x32_bf16 v[52:55], a[152:155], v[136:139], 0\n"
"v_cvt_pk_bf16_f32 v165, v194, v195\n"
"v_exp_f32 v120, v120\n"
"v_mfma_f32_16x16x32_bf16 v[52:55], a[156:159], v[140:143], v[52:55]\n"
"v_exp_f32 v196, v196\n"
"v_mfma_f32_16x16x32_bf16 v[200:203], a[96:99], a[48:51], v[216:219]\n"
"ds_read_b64_tr_b16 a[148:149], v230 offset:14336\n"
"v_exp_f32 v121, v121\n"
"v_mfma_f32_16x16x32_bf16 v[200:203], a[100:103], a[52:55], v[200:203]\n"
"ds_read_b64_tr_b16 a[150:151], v230 offset:20480\n"
"v_exp_f32 v122, v122\n"
"v_mfma_f32_16x16x32_bf16 v[200:203], a[104:107], a[56:59], v[200:203]\n"
"v_exp_f32 v197, v197\n"
"v_mfma_f32_16x16x32_bf16 v[200:203], a[108:111], a[60:63], v[200:203]\n"
"ds_read_b128 a[136:139], v227 offset:45056\n"
"v_exp_f32 v123, v123\n"
"v_mfma_f32_16x16x32_bf16 v[200:203], a[112:115], a[64:67], v[200:203]\n"
"v_cvt_pk_bf16_f32 v172, v196, v197\n"
"v_add_f32 v120, 1.0, v120\n"
"v_mfma_f32_16x16x32_bf16 v[200:203], a[116:119], a[68:71], v[200:203]\n"
"v_exp_f32 v198, v198\n"
"v_add_f32 v121, 1.0, v121\n"
"v_mfma_f32_16x16x32_bf16 v[56:59], a[152:155], v[144:147], 0\n"
"ds_read_b128 a[140:143], v228 offset:45056\n"
"v_add_f32 v122, 1.0, v122\n"
"v_add_f32 v123, 1.0, v123\n"
"v_mfma_f32_16x16x32_bf16 v[56:59], a[156:159], v[148:151], v[56:59]\n"
"v_rcp_f32 v120, v120\n"
"v_mfma_f32_16x16x32_bf16 v[60:63], a[152:155], v[152:155], 0\n"
"v_exp_f32 v199, v199\n"
"v_mfma_f32_16x16x32_bf16 v[60:63], a[156:159], v[156:159], v[60:63]\n"
"ds_read_b64_tr_b16 a[152:153], v231 offset:2048\n"
"v_rcp_f32 v121, v121\n"
"s_waitcnt lgkmcnt(2)\n"
"v_mfma_f32_16x16x32_bf16 v[64:67], a[160:163], v[128:131], 0\n"
"v_cvt_pk_bf16_f32 v173, v198, v199\n"
"v_rcp_f32 v122, v122\n"
"v_mfma_f32_16x16x32_bf16 v[64:67], a[164:167], v[132:135], v[64:67]\n"
"v_exp_f32 v200, v200\n"
"v_smfmac_f32_16x16x64_bf16 a[180:183], a[168:171], v[144:151], v233\n"
"ds_read_b64_tr_b16 a[154:155], v231 offset:8192\n"
"v_rcp_f32 v123, v123\n"
"v_mfma_f32_16x16x32_bf16 v[204:207], a[96:99], a[72:75], v[220:223]\n"
"ds_read_b64_tr_b16 a[156:157], v231 offset:14336\n"
"v_mul_f32 v108, v108, v120\n"
"v_mul_f32 v109, v109, v121\n"
"v_mfma_f32_16x16x32_bf16 v[204:207], a[100:103], a[76:79], v[204:207]\n"
"ds_read_b64_tr_b16 a[158:159], v231 offset:20480\n"
"v_mul_f32 v110, v110, v122\n"
"v_mul_f32 v111, v111, v123\n"
"v_mfma_f32_16x16x32_bf16 v[204:207], a[104:107], a[80:83], v[204:207]\n"
"v_exp_f32 v201, v201\n"
"v_cvt_pk_bf16_f32 v108, v108, v109\n"
"v_mfma_f32_16x16x32_bf16 v[204:207], a[108:111], a[84:87], v[204:207]\n"
"v_cvt_pk_bf16_f32 v109, v110, v111\n"
"buffer_store_dwordx2 v[108:109], v243, s[8:11], s60 offen sc1\n"
"v_mfma_f32_16x16x32_bf16 v[204:207], a[112:115], a[88:91], v[204:207]\n"
"v_cvt_pk_bf16_f32 v180, v200, v201\n"
"v_mul_f32 v116, v112, v112\n"
"v_mfma_f32_16x16x32_bf16 v[204:207], a[116:119], a[92:95], v[204:207]\n"
"v_exp_f32 v202, v202\n"
"v_mul_f32 v117, v113, v113\n"
"v_mfma_f32_16x16x32_bf16 v[68:71], a[160:163], v[136:139], 0\n"
"v_mul_f32 v118, v114, v114\n"
"v_mul_f32 v119, v115, v115\n"
"v_fmaak_f32 v116, v247, v116, 0xc0135761\n"
"v_mfma_f32_16x16x32_bf16 v[68:71], a[164:167], v[140:143], v[68:71]\n"
"v_fmaak_f32 v117, v247, v117, 0xc0135761\n"
"v_fmaak_f32 v118, v247, v118, 0xc0135761\n"
"v_fmaak_f32 v119, v247, v119, 0xc0135761\n"
"v_mfma_f32_16x16x32_bf16 v[72:75], a[160:163], v[144:147], 0\n"
"v_exp_f32 v203, v203\n"
"v_mul_f32 v116, v116, v112\n"
"v_mfma_f32_16x16x32_bf16 v[72:75], a[164:167], v[148:151], v[72:75]\n"
"v_mul_f32 v117, v117, v113\n"
"v_mul_f32 v118, v118, v114\n"
"v_mul_f32 v119, v119, v115\n"
"v_mfma_f32_16x16x32_bf16 v[76:79], a[160:163], v[152:155], 0\n"
"v_cvt_pk_bf16_f32 v181, v202, v203\n"
"v_exp_f32 v116, v116\n"
"v_mfma_f32_16x16x32_bf16 v[76:79], a[164:167], v[156:159], v[76:79]\n"
"v_exp_f32 v204, v204\n"
"v_mfma_f32_16x16x32_bf16 v[192:195], a[120:123], a[0:3], v[208:211]\n"
"ds_read_b64_tr_b16 a[160:161], v232 offset:2048\n"
"v_exp_f32 v117, v117\n"
"v_mfma_f32_16x16x32_bf16 v[192:195], a[124:127], a[4:7], v[192:195]\n"
"ds_read_b64_tr_b16 a[162:163], v232 offset:8192\n"
"v_exp_f32 v118, v118\n"
"v_mfma_f32_16x16x32_bf16 v[192:195], a[128:131], a[8:11], v[192:195]\n"
"v_exp_f32 v205, v205\n"
"v_mfma_f32_16x16x32_bf16 v[192:195], a[132:135], a[12:15], v[192:195]\n"
"ds_read_b64_tr_b16 a[164:165], v232 offset:14336\n"
"v_exp_f32 v119, v119\n"
"v_mfma_f32_16x16x32_bf16 v[192:195], a[136:139], a[16:19], v[192:195]\n"
"v_cvt_pk_bf16_f32 v188, v204, v205\n"
"v_add_f32 v116, 1.0, v116\n"
"v_add_f32 v117, 1.0, v117\n"
"s_waitcnt lgkmcnt(3)\n"
"v_mfma_f32_16x16x32_bf16 v[192:195], a[140:143], a[20:23], v[192:195]\n"
"v_exp_f32 v206, v206\n"
"v_add_f32 v118, 1.0, v118\n"
"v_mfma_f32_16x16x32_bf16 v[80:83], a[144:147], v[128:131], 0\n"
"ds_read_b64_tr_b16 a[166:167], v232 offset:20480\n"
"v_add_f32 v119, 1.0, v119\n"
"v_mfma_f32_16x16x32_bf16 v[80:83], a[148:151], v[132:135], v[80:83]\n"
"v_rcp_f32 v116, v116\n"
"v_mfma_f32_16x16x32_bf16 v[84:87], a[144:147], v[136:139], 0\n"
"v_exp_f32 v207, v207\n"
"v_mfma_f32_16x16x32_bf16 v[84:87], a[148:151], v[140:143], v[84:87]\n"
"v_rcp_f32 v117, v117\n"
"v_mfma_f32_16x16x32_bf16 v[88:91], a[144:147], v[144:147], 0\n"
"v_cvt_pk_bf16_f32 v189, v206, v207\n"
"v_rcp_f32 v118, v118\n"
"v_mfma_f32_16x16x32_bf16 v[88:91], a[148:151], v[148:151], v[88:91]\n"
"v_exp_f32 v192, v192\n"
"v_mfma_f32_16x16x32_bf16 v[196:199], a[120:123], a[24:27], v[212:215]\n"
"v_rcp_f32 v119, v119\n"
"v_mfma_f32_16x16x32_bf16 v[196:199], a[124:127], a[28:31], v[196:199]\n"
"v_mul_f32 v112, v112, v116\n"
"v_mul_f32 v113, v113, v117\n"
"v_mul_f32 v114, v114, v118\n"
"v_mfma_f32_16x16x32_bf16 v[196:199], a[128:131], a[32:35], v[196:199]\n"
"v_exp_f32 v193, v193\n"
"v_mul_f32 v115, v115, v119\n"
"v_mfma_f32_16x16x32_bf16 v[196:199], a[132:135], a[36:39], v[196:199]\n"
"v_cvt_pk_bf16_f32 v112, v112, v113\n"
"v_cvt_pk_bf16_f32 v113, v114, v115\n"
"v_mfma_f32_16x16x32_bf16 v[196:199], a[136:139], a[40:43], v[196:199]\n"
"v_cvt_pk_bf16_f32 v166, v192, v193\n"
"buffer_store_dwordx2 v[112:113], v253, s[8:11], s60 offen sc1\n"
"v_mfma_f32_16x16x32_bf16 v[196:199], a[140:143], a[44:47], v[196:199]\n"
"v_exp_f32 v194, v194\n"
"v_mfma_f32_16x16x32_bf16 v[92:95], a[144:147], v[152:155], 0\n"
"v_mfma_f32_16x16x32_bf16 v[92:95], a[148:151], v[156:159], v[92:95]\n"
"v_mfma_f32_16x16x32_bf16 v[96:99], a[152:155], v[128:131], 0\n"
"v_exp_f32 v195, v195\n"
"v_mfma_f32_16x16x32_bf16 v[96:99], a[156:159], v[132:135], v[96:99]\n"
"s_waitcnt vmcnt(4) lgkmcnt(0)\n"
"s_barrier\n"
"v_mfma_f32_16x16x32_bf16 v[100:103], a[152:155], v[136:139], 0\n"
"v_cvt_pk_bf16_f32 v167, v194, v195\n"
"ds_read_b128 a[96:99], v227 offset:0\n"
"v_mfma_f32_16x16x32_bf16 v[100:103], a[156:159], v[140:143], v[100:103]\n"
"v_exp_f32 v196, v196\n"
"v_mfma_f32_16x16x32_bf16 v[200:203], a[120:123], a[48:51], v[216:219]\n"
"ds_read_b64_tr_b16 a[144:145], v229 offset:24576\n"
"ds_read_b128 a[100:103], v228 offset:0\n"
"v_mfma_f32_16x16x32_bf16 v[200:203], a[124:127], a[52:55], v[200:203]\n"
"ds_read_b64_tr_b16 a[146:147], v229 offset:30720\n"
"ds_read_b128 a[104:107], v227 offset:1024\n"
"v_mfma_f32_16x16x32_bf16 v[200:203], a[128:131], a[56:59], v[200:203]\n"
"v_exp_f32 v197, v197\n"
"v_mfma_f32_16x16x32_bf16 v[200:203], a[132:135], a[60:63], v[200:203]\n"
"ds_read_b64_tr_b16 a[148:149], v229 offset:36864\n"
"ds_read_b128 a[108:111], v228 offset:1024\n"
"v_mfma_f32_16x16x32_bf16 v[200:203], a[136:139], a[64:67], v[200:203]\n"
"v_cvt_pk_bf16_f32 v174, v196, v197\n"
"s_mul_i32 s88, s54, 6144\n"
"v_mfma_f32_16x16x32_bf16 v[200:203], a[140:143], a[68:71], v[200:203]\n"
"v_exp_f32 v198, v198\n"
"v_mfma_f32_16x16x32_bf16 v[104:107], a[152:155], v[144:147], 0\n"
"ds_read_b64_tr_b16 a[150:151], v229 offset:43008\n"
"s_mul_i32 s89, s52, -4608\n"
"v_mfma_f32_16x16x32_bf16 v[104:107], a[156:159], v[148:151], v[104:107]\n"
"ds_read_b128 a[112:115], v227 offset:2048\n"
"s_add_u32 s88, s88, s89\n"
"v_mfma_f32_16x16x32_bf16 v[108:111], a[152:155], v[152:155], 0\n"
"v_exp_f32 v199, v199\n"
"v_mfma_f32_16x16x32_bf16 v[108:111], a[156:159], v[156:159], v[108:111]\n"
"ds_read_b64_tr_b16 a[152:153], v230 offset:24576\n"
"ds_read_b128 a[116:119], v228 offset:2048\n"
"v_mfma_f32_16x16x32_bf16 v[112:115], a[160:163], v[128:131], 0\n"
"v_cvt_pk_bf16_f32 v175, v198, v199\n"
"s_add_u32 s88, s88, 50944\n"
"v_mfma_f32_16x16x32_bf16 v[112:115], a[164:167], v[132:135], v[112:115]\n"
"v_exp_f32 v200, v200\n"
"v_smfmac_f32_16x16x64_bf16 a[184:187], a[168:171], v[152:159], v233\n"
"ds_read_b64_tr_b16 a[154:155], v230 offset:30720\n"
"v_add_u32 v241, 52, v248\n"
"v_mfma_f32_16x16x32_bf16 v[204:207], a[120:123], a[72:75], v[220:223]\n"
"ds_read_b64_tr_b16 a[156:157], v230 offset:36864\n"
"v_lshrrev_b32 v241, 6, v241\n"
"v_mfma_f32_16x16x32_bf16 v[204:207], a[124:127], a[76:79], v[204:207]\n"
"ds_read_b64_tr_b16 a[158:159], v230 offset:43008\n"
"v_lshlrev_b32 v241, 30, v241\n"
"v_mfma_f32_16x16x32_bf16 v[204:207], a[128:131], a[80:83], v[204:207]\n"
"v_exp_f32 v201, v201\n"
"v_mfma_f32_16x16x32_bf16 v[204:207], a[132:135], a[84:87], v[204:207]\n"
"v_lshl_add_u32 v241, v248, 7, v241\n"
"v_add_u32 v241, s88, v241\n"
"v_mfma_f32_16x16x32_bf16 v[204:207], a[136:139], a[88:91], v[204:207]\n"
"v_cvt_pk_bf16_f32 v182, v200, v201\n"
"v_mfma_f32_16x16x32_bf16 v[204:207], a[140:143], a[92:95], v[204:207]\n"
"v_exp_f32 v202, v202\n"
"v_mfma_f32_16x16x32_bf16 v[116:119], a[160:163], v[136:139], 0\n"
"v_mfma_f32_16x16x32_bf16 v[116:119], a[164:167], v[140:143], v[116:119]\n"
"v_mfma_f32_16x16x32_bf16 v[120:123], a[160:163], v[144:147], 0\n"
"v_exp_f32 v203, v203\n"
"v_mfma_f32_16x16x32_bf16 v[120:123], a[164:167], v[148:151], v[120:123]\n"
"v_mfma_f32_16x16x32_bf16 v[124:127], a[160:163], v[152:155], 0\n"
"v_cvt_pk_bf16_f32 v183, v202, v203\n"
"v_mfma_f32_16x16x32_bf16 v[124:127], a[164:167], v[156:159], v[124:127]\n"
"v_exp_f32 v204, v204\n"
"s_mov_b32 s58, 14\n"
".p2align 6\n"
"L_loop_%=:\n"
"s_waitcnt lgkmcnt(0)\n"
"v_mfma_f32_16x16x32_bf16 v[192:195], a[96:99], a[0:3], v[208:211]\n"
"s_add_u32 s61, s61, 24576\n"
"ds_read_b64_tr_b16 a[160:161], v231 offset:24576\n"
"v_mfma_f32_16x16x32_bf16 v[192:195], a[100:103], a[4:7], v[192:195]\n"
"s_add_u32 s62, s62, 24576\n"
"ds_read_b64_tr_b16 a[162:163], v231 offset:30720\n"
"v_mfma_f32_16x16x32_bf16 v[192:195], a[104:107], a[8:11], v[192:195]\n"
"v_exp_f32 v205, v205\n"
"v_mfma_f32_16x16x32_bf16 v[192:195], a[108:111], a[12:15], v[192:195]\n"
"s_mov_b32 m0, s67\n"
"ds_read_b64_tr_b16 a[164:165], v231 offset:36864\n"
"v_mfma_f32_16x16x32_bf16 v[192:195], a[112:115], a[16:19], v[192:195]\n"
"v_cvt_pk_bf16_f32 v190, v204, v205\n"
"ds_read_b128 a[120:123], v227 offset:6144\n"
"v_mfma_f32_16x16x32_bf16 v[192:195], a[116:119], a[20:23], v[192:195]\n"
"v_exp_f32 v206, v206\n"
"v_mfma_f32_16x16x32_bf16 a[192:195], a[144:147], v[160:163], a[192:195]\n"
"buffer_load_dwordx4 v224, s[20:23], s61 offen offset:0 lds\n"
"v_mfma_f32_16x16x32_bf16 a[192:195], a[148:151], v[164:167], a[192:195]\n"
"ds_read_b64_tr_b16 a[166:167], v231 offset:43008\n"
"ds_read_b128 a[124:127], v228 offset:6144\n"
"v_mfma_f32_16x16x32_bf16 a[196:199], a[144:147], v[168:171], a[196:199]\n"
"v_exp_f32 v207, v207\n"
"v_mfma_f32_16x16x32_bf16 a[196:199], a[148:151], v[172:175], a[196:199]\n"
"buffer_load_dwordx4 v225, s[20:23], s61 offen offset:1024 lds\n"
"v_mfma_f32_16x16x32_bf16 a[200:203], a[144:147], v[176:179], a[200:203]\n"
"v_cvt_pk_bf16_f32 v191, v206, v207\n"
"v_mfma_f32_16x16x32_bf16 a[200:203], a[148:151], v[180:183], a[200:203]\n"
"v_exp_f32 v192, v192\n"
"v_mfma_f32_16x16x32_bf16 v[196:199], a[96:99], a[24:27], v[212:215]\n"
"ds_read_b128 a[128:131], v227 offset:7168\n"
"v_mfma_f32_16x16x32_bf16 v[196:199], a[100:103], a[28:31], v[196:199]\n"
"v_mfma_f32_16x16x32_bf16 v[196:199], a[104:107], a[32:35], v[196:199]\n"
"v_exp_f32 v193, v193\n"
"v_mfma_f32_16x16x32_bf16 v[196:199], a[108:111], a[36:39], v[196:199]\n"
"buffer_load_dwordx4 v226, s[20:23], s61 offen offset:2048 lds\n"
"v_mfma_f32_16x16x32_bf16 v[196:199], a[112:115], a[40:43], v[196:199]\n"
"v_cvt_pk_bf16_f32 v128, v192, v193\n"
"v_mfma_f32_16x16x32_bf16 v[196:199], a[116:119], a[44:47], v[196:199]\n"
"v_exp_f32 v194, v194\n"
"v_mfma_f32_16x16x32_bf16 a[204:207], a[144:147], v[184:187], a[204:207]\n"
"ds_read_b128 a[132:135], v228 offset:7168\n"
"v_mfma_f32_16x16x32_bf16 a[204:207], a[148:151], v[188:191], a[204:207]\n"
"ds_read_b64_tr_b16 a[144:145], v232 offset:24576\n"
"v_mfma_f32_16x16x32_bf16 a[208:211], a[152:155], v[160:163], a[208:211]\n"
"v_exp_f32 v195, v195\n"
"v_mfma_f32_16x16x32_bf16 a[208:211], a[156:159], v[164:167], a[208:211]\n"
"ds_read_b64_tr_b16 a[146:147], v232 offset:30720\n"
"v_mfma_f32_16x16x32_bf16 a[212:215], a[152:155], v[168:171], a[212:215]\n"
"v_cvt_pk_bf16_f32 v129, v194, v195\n"
"ds_read_b64_tr_b16 a[148:149], v232 offset:36864\n"
"v_mfma_f32_16x16x32_bf16 a[212:215], a[156:159], v[172:175], a[212:215]\n"
"v_exp_f32 v196, v196\n"
"v_mfma_f32_16x16x32_bf16 v[200:203], a[96:99], a[48:51], v[216:219]\n"
"s_mov_b32 m0, s68\n"
"ds_read_b128 a[136:139], v227 offset:8192\n"
"v_mfma_f32_16x16x32_bf16 v[200:203], a[100:103], a[52:55], v[200:203]\n"
"buffer_load_dwordx4 v224, s[20:23], s62 offen offset:0 lds\n"
"v_mfma_f32_16x16x32_bf16 v[200:203], a[104:107], a[56:59], v[200:203]\n"
"v_exp_f32 v197, v197\n"
"v_mfma_f32_16x16x32_bf16 v[200:203], a[108:111], a[60:63], v[200:203]\n"
"ds_read_b64_tr_b16 a[150:151], v232 offset:43008\n"
"v_mfma_f32_16x16x32_bf16 v[200:203], a[112:115], a[64:67], v[200:203]\n"
"v_cvt_pk_bf16_f32 v136, v196, v197\n"
"v_mfma_f32_16x16x32_bf16 v[200:203], a[116:119], a[68:71], v[200:203]\n"
"v_exp_f32 v198, v198\n"
"v_mfma_f32_16x16x32_bf16 a[216:219], a[152:155], v[176:179], a[216:219]\n"
"buffer_load_dwordx4 v225, s[20:23], s62 offen offset:1024 lds\n"
"v_mfma_f32_16x16x32_bf16 a[216:219], a[156:159], v[180:183], a[216:219]\n"
"ds_read_b128 a[140:143], v228 offset:8192\n"
"v_mfma_f32_16x16x32_bf16 a[220:223], a[152:155], v[184:187], a[220:223]\n"
"v_exp_f32 v199, v199\n"
"v_mfma_f32_16x16x32_bf16 a[220:223], a[156:159], v[188:191], a[220:223]\n"
"ds_read_b64_tr_b16 a[152:153], v229 offset:25600\n"
"s_waitcnt lgkmcnt(2)\n"
"v_mfma_f32_16x16x32_bf16 a[224:227], a[160:163], v[160:163], a[224:227]\n"
"v_cvt_pk_bf16_f32 v137, v198, v199\n"
"v_mfma_f32_16x16x32_bf16 a[224:227], a[164:167], v[164:167], a[224:227]\n"
"v_exp_f32 v200, v200\n"
"v_smfmac_f32_16x16x64_bf16 a[172:175], a[168:171], v[160:167], v233\n"
"buffer_load_dwordx4 v226, s[20:23], s62 offen offset:2048 lds\n"
"v_mfma_f32_16x16x32_bf16 v[204:207], a[96:99], a[72:75], v[220:223]\n"
"ds_read_b64_tr_b16 a[154:155], v229 offset:31744\n"
"v_mfma_f32_16x16x32_bf16 v[204:207], a[100:103], a[76:79], v[204:207]\n"
"ds_read_b64_tr_b16 a[156:157], v229 offset:37888\n"
"v_mfma_f32_16x16x32_bf16 v[204:207], a[104:107], a[80:83], v[204:207]\n"
"v_exp_f32 v201, v201\n"
"v_mfma_f32_16x16x32_bf16 v[204:207], a[108:111], a[84:87], v[204:207]\n"
"ds_read_b64_tr_b16 a[158:159], v229 offset:44032\n"
"v_mfma_f32_16x16x32_bf16 v[204:207], a[112:115], a[88:91], v[204:207]\n"
"v_cvt_pk_bf16_f32 v144, v200, v201\n"
"v_mfma_f32_16x16x32_bf16 v[204:207], a[116:119], a[92:95], v[204:207]\n"
"v_exp_f32 v202, v202\n"
"v_mfma_f32_16x16x32_bf16 a[228:231], a[160:163], v[168:171], a[228:231]\n"
"s_mov_b32 m0, s65\n"
"v_mfma_f32_16x16x32_bf16 a[228:231], a[164:167], v[172:175], a[228:231]\n"
"buffer_load_dwordx4 v224, s[16:19], s61 offen offset:0 lds\n"
"v_mfma_f32_16x16x32_bf16 a[232:235], a[160:163], v[176:179], a[232:235]\n"
"v_exp_f32 v203, v203\n"
"v_mfma_f32_16x16x32_bf16 a[232:235], a[164:167], v[180:183], a[232:235]\n"
"v_mfma_f32_16x16x32_bf16 a[236:239], a[160:163], v[184:187], a[236:239]\n"
"v_cvt_pk_bf16_f32 v145, v202, v203\n"
"v_mfma_f32_16x16x32_bf16 a[236:239], a[164:167], v[188:191], a[236:239]\n"
"v_exp_f32 v204, v204\n"
"v_mfma_f32_16x16x32_bf16 v[192:195], a[120:123], a[0:3], v[208:211]\n"
"ds_read_b64_tr_b16 a[160:161], v230 offset:25600\n"
"ds_read_b128 a[96:99], v227 offset:12288\n"
"v_mfma_f32_16x16x32_bf16 v[192:195], a[124:127], a[4:7], v[192:195]\n"
"buffer_load_dwordx4 v225, s[16:19], s61 offen offset:1024 lds\n"
"v_mfma_f32_16x16x32_bf16 v[192:195], a[128:131], a[8:11], v[192:195]\n"
"v_exp_f32 v205, v205\n"
"v_mfma_f32_16x16x32_bf16 v[192:195], a[132:135], a[12:15], v[192:195]\n"
"ds_read_b64_tr_b16 a[162:163], v230 offset:31744\n"
"v_mfma_f32_16x16x32_bf16 v[192:195], a[136:139], a[16:19], v[192:195]\n"
"v_cvt_pk_bf16_f32 v152, v204, v205\n"
"s_waitcnt lgkmcnt(3)\n"
"v_mfma_f32_16x16x32_bf16 v[192:195], a[140:143], a[20:23], v[192:195]\n"
"v_exp_f32 v206, v206\n"
"v_mfma_f32_16x16x32_bf16 a[240:243], a[144:147], v[160:163], a[240:243]\n"
"ds_read_b64_tr_b16 a[164:165], v230 offset:37888\n"
"ds_read_b128 a[100:103], v228 offset:12288\n"
"v_mfma_f32_16x16x32_bf16 a[240:243], a[148:151], v[164:167], a[240:243]\n"
"ds_read_b64_tr_b16 a[166:167], v230 offset:44032\n"
"v_mfma_f32_16x16x32_bf16 a[244:247], a[144:147], v[168:171], a[244:247]\n"
"v_exp_f32 v207, v207\n"
"v_mfma_f32_16x16x32_bf16 a[244:247], a[148:151], v[172:175], a[244:247]\n"
"buffer_load_dwordx4 v226, s[16:19], s61 offen offset:2048 lds\n"
"v_mfma_f32_16x16x32_bf16 a[248:251], a[144:147], v[176:179], a[248:251]\n"
"v_cvt_pk_bf16_f32 v153, v206, v207\n"
"v_mfma_f32_16x16x32_bf16 a[248:251], a[148:151], v[180:183], a[248:251]\n"
"v_exp_f32 v192, v192\n"
"v_mfma_f32_16x16x32_bf16 v[196:199], a[120:123], a[24:27], v[212:215]\n"
"ds_read_b128 a[104:107], v227 offset:13312\n"
"v_mfma_f32_16x16x32_bf16 v[196:199], a[124:127], a[28:31], v[196:199]\n"
"v_mfma_f32_16x16x32_bf16 v[196:199], a[128:131], a[32:35], v[196:199]\n"
"v_exp_f32 v193, v193\n"
"v_mfma_f32_16x16x32_bf16 v[196:199], a[132:135], a[36:39], v[196:199]\n"
"s_mov_b32 m0, s66\n"
"v_mfma_f32_16x16x32_bf16 v[196:199], a[136:139], a[40:43], v[196:199]\n"
"v_cvt_pk_bf16_f32 v130, v192, v193\n"
"v_mfma_f32_16x16x32_bf16 v[196:199], a[140:143], a[44:47], v[196:199]\n"
"v_exp_f32 v194, v194\n"
"v_mfma_f32_16x16x32_bf16 a[252:255], a[144:147], v[184:187], a[252:255]\n"
"buffer_load_dwordx4 v224, s[16:19], s62 offen offset:0 lds\n"
"v_mfma_f32_16x16x32_bf16 a[252:255], a[148:151], v[188:191], a[252:255]\n"
"ds_read_b64_tr_b16 a[144:145], v231 offset:25600\n"
"ds_read_b128 a[108:111], v228 offset:13312\n"
"v_mfma_f32_16x16x32_bf16 v[0:3], a[152:155], v[160:163], v[0:3]\n"
"v_exp_f32 v195, v195\n"
"v_mfma_f32_16x16x32_bf16 v[0:3], a[156:159], v[164:167], v[0:3]\n"
"ds_read_b64_tr_b16 a[146:147], v231 offset:31744\n"
"v_mfma_f32_16x16x32_bf16 v[4:7], a[152:155], v[168:171], v[4:7]\n"
"v_cvt_pk_bf16_f32 v131, v194, v195\n"
"v_mfma_f32_16x16x32_bf16 v[4:7], a[156:159], v[172:175], v[4:7]\n"
"v_exp_f32 v196, v196\n"
"v_mfma_f32_16x16x32_bf16 v[200:203], a[120:123], a[48:51], v[216:219]\n"
"buffer_load_dwordx4 v225, s[16:19], s62 offen offset:1024 lds\n"
"v_mfma_f32_16x16x32_bf16 v[200:203], a[124:127], a[52:55], v[200:203]\n"
"ds_read_b64_tr_b16 a[148:149], v231 offset:37888\n"
"ds_read_b128 a[112:115], v227 offset:14336\n"
"v_mfma_f32_16x16x32_bf16 v[200:203], a[128:131], a[56:59], v[200:203]\n"
"v_exp_f32 v197, v197\n"
"v_mfma_f32_16x16x32_bf16 v[200:203], a[132:135], a[60:63], v[200:203]\n"
"ds_read_b64_tr_b16 a[150:151], v231 offset:44032\n"
"v_mfma_f32_16x16x32_bf16 v[200:203], a[136:139], a[64:67], v[200:203]\n"
"v_cvt_pk_bf16_f32 v138, v196, v197\n"
"v_mfma_f32_16x16x32_bf16 v[200:203], a[140:143], a[68:71], v[200:203]\n"
"v_exp_f32 v198, v198\n"
"v_mfma_f32_16x16x32_bf16 v[8:11], a[152:155], v[176:179], v[8:11]\n"
"buffer_load_dwordx4 v226, s[16:19], s62 offen offset:2048 lds\n"
"v_mfma_f32_16x16x32_bf16 v[8:11], a[156:159], v[180:183], v[8:11]\n"
"ds_read_b128 a[116:119], v228 offset:14336\n"
"v_mfma_f32_16x16x32_bf16 v[12:15], a[152:155], v[184:187], v[12:15]\n"
"v_exp_f32 v199, v199\n"
"v_mfma_f32_16x16x32_bf16 v[12:15], a[156:159], v[188:191], v[12:15]\n"
"ds_read_b64_tr_b16 a[152:153], v232 offset:25600\n"
"s_waitcnt lgkmcnt(2)\n"
"v_mfma_f32_16x16x32_bf16 v[16:19], a[160:163], v[160:163], v[16:19]\n"
"v_cvt_pk_bf16_f32 v139, v198, v199\n"
"v_mfma_f32_16x16x32_bf16 v[16:19], a[164:167], v[164:167], v[16:19]\n"
"v_exp_f32 v200, v200\n"
"v_smfmac_f32_16x16x64_bf16 a[176:179], a[168:171], v[168:175], v233\n"
"buffer_load_dword v242, v241, s[16:19], s61 offen\n"
"v_mfma_f32_16x16x32_bf16 v[204:207], a[120:123], a[72:75], v[220:223]\n"
"ds_read_b64_tr_b16 a[154:155], v232 offset:31744\n"
"v_mfma_f32_16x16x32_bf16 v[204:207], a[124:127], a[76:79], v[204:207]\n"
"ds_read_b64_tr_b16 a[156:157], v232 offset:37888\n"
"v_mfma_f32_16x16x32_bf16 v[204:207], a[128:131], a[80:83], v[204:207]\n"
"v_exp_f32 v201, v201\n"
"v_mfma_f32_16x16x32_bf16 v[204:207], a[132:135], a[84:87], v[204:207]\n"
"ds_read_b64_tr_b16 a[158:159], v232 offset:44032\n"
"v_mfma_f32_16x16x32_bf16 v[204:207], a[136:139], a[88:91], v[204:207]\n"
"v_cvt_pk_bf16_f32 v146, v200, v201\n"
"v_mfma_f32_16x16x32_bf16 v[204:207], a[140:143], a[92:95], v[204:207]\n"
"v_exp_f32 v202, v202\n"
"v_mfma_f32_16x16x32_bf16 v[20:23], a[160:163], v[168:171], v[20:23]\n"
"s_mov_b32 m0, s75\n"
"v_mfma_f32_16x16x32_bf16 v[20:23], a[164:167], v[172:175], v[20:23]\n"
"buffer_load_dwordx4 v234, s[44:47], s71 offen lds\n"
"v_mfma_f32_16x16x32_bf16 v[24:27], a[160:163], v[176:179], v[24:27]\n"
"v_exp_f32 v203, v203\n"
"v_mfma_f32_16x16x32_bf16 v[24:27], a[164:167], v[180:183], v[24:27]\n"
"s_add_u32 s71, s71, s73\n"
"v_mfma_f32_16x16x32_bf16 v[28:31], a[160:163], v[184:187], v[28:31]\n"
"v_cvt_pk_bf16_f32 v147, v202, v203\n"
"v_mfma_f32_16x16x32_bf16 v[28:31], a[164:167], v[188:191], v[28:31]\n"
"v_exp_f32 v204, v204\n"
"v_mfma_f32_16x16x32_bf16 v[192:195], a[96:99], a[0:3], v[208:211]\n"
"ds_read_b64_tr_b16 a[160:161], v229 offset:26624\n"
"ds_read_b128 a[120:123], v227 offset:18432\n"
"v_mfma_f32_16x16x32_bf16 v[192:195], a[100:103], a[4:7], v[192:195]\n"
"ds_read_b64_tr_b16 a[162:163], v229 offset:32768\n"
"v_mfma_f32_16x16x32_bf16 v[192:195], a[104:107], a[8:11], v[192:195]\n"
"v_exp_f32 v205, v205\n"
"v_mfma_f32_16x16x32_bf16 v[192:195], a[108:111], a[12:15], v[192:195]\n"
"ds_read_b64_tr_b16 a[164:165], v229 offset:38912\n"
"v_mfma_f32_16x16x32_bf16 v[192:195], a[112:115], a[16:19], v[192:195]\n"
"v_cvt_pk_bf16_f32 v154, v204, v205\n"
"s_waitcnt lgkmcnt(4)\n"
"v_mfma_f32_16x16x32_bf16 v[192:195], a[116:119], a[20:23], v[192:195]\n"
"v_exp_f32 v206, v206\n"
"v_mfma_f32_16x16x32_bf16 v[32:35], a[144:147], v[160:163], v[32:35]\n"
"ds_read_b64_tr_b16 a[166:167], v229 offset:45056\n"
"v_mfma_f32_16x16x32_bf16 v[32:35], a[148:151], v[164:167], v[32:35]\n"
"ds_read_b128 a[124:127], v228 offset:18432\n"
"v_mfma_f32_16x16x32_bf16 v[36:39], a[144:147], v[168:171], v[36:39]\n"
"v_exp_f32 v207, v207\n"
"v_mfma_f32_16x16x32_bf16 v[36:39], a[148:151], v[172:175], v[36:39]\n"
"v_mfma_f32_16x16x32_bf16 v[40:43], a[144:147], v[176:179], v[40:43]\n"
"v_cvt_pk_bf16_f32 v155, v206, v207\n"
"v_mfma_f32_16x16x32_bf16 v[40:43], a[148:151], v[180:183], v[40:43]\n"
"v_exp_f32 v192, v192\n"
"v_mfma_f32_16x16x32_bf16 v[196:199], a[96:99], a[24:27], v[212:215]\n"
"ds_read_b128 a[128:131], v227 offset:19456\n"
"v_mfma_f32_16x16x32_bf16 v[196:199], a[100:103], a[28:31], v[196:199]\n"
"v_mfma_f32_16x16x32_bf16 v[196:199], a[104:107], a[32:35], v[196:199]\n"
"v_exp_f32 v193, v193\n"
"v_mfma_f32_16x16x32_bf16 v[196:199], a[108:111], a[36:39], v[196:199]\n"
"v_mfma_f32_16x16x32_bf16 v[196:199], a[112:115], a[40:43], v[196:199]\n"
"v_cvt_pk_bf16_f32 v132, v192, v193\n"
"v_mfma_f32_16x16x32_bf16 v[196:199], a[116:119], a[44:47], v[196:199]\n"
"v_exp_f32 v194, v194\n"
"v_mfma_f32_16x16x32_bf16 v[44:47], a[144:147], v[184:187], v[44:47]\n"
"ds_read_b128 a[132:135], v228 offset:19456\n"
"v_mfma_f32_16x16x32_bf16 v[44:47], a[148:151], v[188:191], v[44:47]\n"
"ds_read_b64_tr_b16 a[144:145], v230 offset:26624\n"
"v_mfma_f32_16x16x32_bf16 v[48:51], a[152:155], v[160:163], v[48:51]\n"
"v_exp_f32 v195, v195\n"
"v_mfma_f32_16x16x32_bf16 v[48:51], a[156:159], v[164:167], v[48:51]\n"
"ds_read_b64_tr_b16 a[146:147], v230 offset:32768\n"
"v_mfma_f32_16x16x32_bf16 v[52:55], a[152:155], v[168:171], v[52:55]\n"
"v_cvt_pk_bf16_f32 v133, v194, v195\n"
"v_mfma_f32_16x16x32_bf16 v[52:55], a[156:159], v[172:175], v[52:55]\n"
"v_exp_f32 v196, v196\n"
"v_mfma_f32_16x16x32_bf16 v[200:203], a[96:99], a[48:51], v[216:219]\n"
"ds_read_b64_tr_b16 a[148:149], v230 offset:38912\n"
"v_mfma_f32_16x16x32_bf16 v[200:203], a[100:103], a[52:55], v[200:203]\n"
"ds_read_b64_tr_b16 a[150:151], v230 offset:45056\n"
"v_mfma_f32_16x16x32_bf16 v[200:203], a[104:107], a[56:59], v[200:203]\n"
"v_exp_f32 v197, v197\n"
"v_mfma_f32_16x16x32_bf16 v[200:203], a[108:111], a[60:63], v[200:203]\n"
"ds_read_b128 a[136:139], v227 offset:20480\n"
"v_mfma_f32_16x16x32_bf16 v[200:203], a[112:115], a[64:67], v[200:203]\n"
"v_cvt_pk_bf16_f32 v140, v196, v197\n"
"v_mfma_f32_16x16x32_bf16 v[200:203], a[116:119], a[68:71], v[200:203]\n"
"v_exp_f32 v198, v198\n"
"v_mfma_f32_16x16x32_bf16 v[56:59], a[152:155], v[176:179], v[56:59]\n"
"ds_read_b128 a[140:143], v228 offset:20480\n"
"v_mfma_f32_16x16x32_bf16 v[56:59], a[156:159], v[180:183], v[56:59]\n"
"v_mfma_f32_16x16x32_bf16 v[60:63], a[152:155], v[184:187], v[60:63]\n"
"v_exp_f32 v199, v199\n"
"v_mfma_f32_16x16x32_bf16 v[60:63], a[156:159], v[188:191], v[60:63]\n"
"ds_read_b64_tr_b16 a[152:153], v231 offset:26624\n"
"s_waitcnt lgkmcnt(2)\n"
"v_mfma_f32_16x16x32_bf16 v[64:67], a[160:163], v[160:163], v[64:67]\n"
"v_cvt_pk_bf16_f32 v141, v198, v199\n"
"v_mfma_f32_16x16x32_bf16 v[64:67], a[164:167], v[164:167], v[64:67]\n"
"v_exp_f32 v200, v200\n"
"v_smfmac_f32_16x16x64_bf16 a[180:183], a[168:171], v[176:183], v233\n"
"ds_read_b64_tr_b16 a[154:155], v231 offset:32768\n"
"v_mfma_f32_16x16x32_bf16 v[204:207], a[96:99], a[72:75], v[220:223]\n"
"ds_read_b64_tr_b16 a[156:157], v231 offset:38912\n"
"v_mfma_f32_16x16x32_bf16 v[204:207], a[100:103], a[76:79], v[204:207]\n"
"ds_read_b64_tr_b16 a[158:159], v231 offset:45056\n"
"v_mfma_f32_16x16x32_bf16 v[204:207], a[104:107], a[80:83], v[204:207]\n"
"v_exp_f32 v201, v201\n"
"v_mfma_f32_16x16x32_bf16 v[204:207], a[108:111], a[84:87], v[204:207]\n"
"v_mfma_f32_16x16x32_bf16 v[204:207], a[112:115], a[88:91], v[204:207]\n"
"v_cvt_pk_bf16_f32 v148, v200, v201\n"
"v_mfma_f32_16x16x32_bf16 v[204:207], a[116:119], a[92:95], v[204:207]\n"
"v_exp_f32 v202, v202\n"
"v_mfma_f32_16x16x32_bf16 v[68:71], a[160:163], v[168:171], v[68:71]\n"
"v_mfma_f32_16x16x32_bf16 v[68:71], a[164:167], v[172:175], v[68:71]\n"
"v_mfma_f32_16x16x32_bf16 v[72:75], a[160:163], v[176:179], v[72:75]\n"
"v_exp_f32 v203, v203\n"
"v_mfma_f32_16x16x32_bf16 v[72:75], a[164:167], v[180:183], v[72:75]\n"
"v_mfma_f32_16x16x32_bf16 v[76:79], a[160:163], v[184:187], v[76:79]\n"
"v_cvt_pk_bf16_f32 v149, v202, v203\n"
"v_mfma_f32_16x16x32_bf16 v[76:79], a[164:167], v[188:191], v[76:79]\n"
"v_exp_f32 v204, v204\n"
"v_mfma_f32_16x16x32_bf16 v[192:195], a[120:123], a[0:3], v[208:211]\n"
"ds_read_b64_tr_b16 a[160:161], v232 offset:26624\n"
"v_mfma_f32_16x16x32_bf16 v[192:195], a[124:127], a[4:7], v[192:195]\n"
"ds_read_b64_tr_b16 a[162:163], v232 offset:32768\n"
"v_mfma_f32_16x16x32_bf16 v[192:195], a[128:131], a[8:11], v[192:195]\n"
"v_exp_f32 v205, v205\n"
"v_mfma_f32_16x16x32_bf16 v[192:195], a[132:135], a[12:15], v[192:195]\n"
"ds_read_b64_tr_b16 a[164:165], v232 offset:38912\n"
"v_mfma_f32_16x16x32_bf16 v[192:195], a[136:139], a[16:19], v[192:195]\n"
"v_cvt_pk_bf16_f32 v156, v204, v205\n"
"s_waitcnt lgkmcnt(3)\n"
"v_mfma_f32_16x16x32_bf16 v[192:195], a[140:143], a[20:23], v[192:195]\n"
"v_exp_f32 v206, v206\n"
"v_mfma_f32_16x16x32_bf16 v[80:83], a[144:147], v[160:163], v[80:83]\n"
"ds_read_b64_tr_b16 a[166:167], v232 offset:45056\n"
"v_mfma_f32_16x16x32_bf16 v[80:83], a[148:151], v[164:167], v[80:83]\n"
"v_mfma_f32_16x16x32_bf16 v[84:87], a[144:147], v[168:171], v[84:87]\n"
"v_exp_f32 v207, v207\n"
"v_mfma_f32_16x16x32_bf16 v[84:87], a[148:151], v[172:175], v[84:87]\n"
"v_mfma_f32_16x16x32_bf16 v[88:91], a[144:147], v[176:179], v[88:91]\n"
"v_cvt_pk_bf16_f32 v157, v206, v207\n"
"v_mfma_f32_16x16x32_bf16 v[88:91], a[148:151], v[180:183], v[88:91]\n"
"v_exp_f32 v192, v192\n"
"v_mfma_f32_16x16x32_bf16 v[196:199], a[120:123], a[24:27], v[212:215]\n"
"v_mfma_f32_16x16x32_bf16 v[196:199], a[124:127], a[28:31], v[196:199]\n"
"v_mfma_f32_16x16x32_bf16 v[196:199], a[128:131], a[32:35], v[196:199]\n"
"v_exp_f32 v193, v193\n"
"v_mfma_f32_16x16x32_bf16 v[196:199], a[132:135], a[36:39], v[196:199]\n"
"v_mfma_f32_16x16x32_bf16 v[196:199], a[136:139], a[40:43], v[196:199]\n"
"v_cvt_pk_bf16_f32 v134, v192, v193\n"
"v_mfma_f32_16x16x32_bf16 v[196:199], a[140:143], a[44:47], v[196:199]\n"
"v_exp_f32 v194, v194\n"
"v_mfma_f32_16x16x32_bf16 v[92:95], a[144:147], v[184:187], v[92:95]\n"
"v_mfma_f32_16x16x32_bf16 v[92:95], a[148:151], v[188:191], v[92:95]\n"
"v_mfma_f32_16x16x32_bf16 v[96:99], a[152:155], v[160:163], v[96:99]\n"
"v_exp_f32 v195, v195\n"
"v_mfma_f32_16x16x32_bf16 v[96:99], a[156:159], v[164:167], v[96:99]\n"
"s_waitcnt vmcnt(2) lgkmcnt(0)\n"
"s_barrier\n"
"v_mfma_f32_16x16x32_bf16 v[100:103], a[152:155], v[168:171], v[100:103]\n"
"v_cvt_pk_bf16_f32 v135, v194, v195\n"
"ds_read_b128 a[96:99], v227 offset:24576\n"
"v_mfma_f32_16x16x32_bf16 v[100:103], a[156:159], v[172:175], v[100:103]\n"
"v_exp_f32 v196, v196\n"
"v_mfma_f32_16x16x32_bf16 v[200:203], a[120:123], a[48:51], v[216:219]\n"
"ds_read_b64_tr_b16 a[144:145], v229 offset:0\n"
"ds_read_b128 a[100:103], v228 offset:24576\n"
"v_mfma_f32_16x16x32_bf16 v[200:203], a[124:127], a[52:55], v[200:203]\n"
"ds_read_b64_tr_b16 a[146:147], v229 offset:6144\n"
"ds_read_b128 a[104:107], v227 offset:25600\n"
"v_mfma_f32_16x16x32_bf16 v[200:203], a[128:131], a[56:59], v[200:203]\n"
"v_exp_f32 v197, v197\n"
"v_mfma_f32_16x16x32_bf16 v[200:203], a[132:135], a[60:63], v[200:203]\n"
"ds_read_b64_tr_b16 a[148:149], v229 offset:12288\n"
"ds_read_b128 a[108:111], v228 offset:25600\n"
"v_mfma_f32_16x16x32_bf16 v[200:203], a[136:139], a[64:67], v[200:203]\n"
"v_cvt_pk_bf16_f32 v142, v196, v197\n"
"v_mfma_f32_16x16x32_bf16 v[200:203], a[140:143], a[68:71], v[200:203]\n"
"v_exp_f32 v198, v198\n"
"v_mfma_f32_16x16x32_bf16 v[104:107], a[152:155], v[176:179], v[104:107]\n"
"ds_read_b64_tr_b16 a[150:151], v229 offset:18432\n"
"v_mfma_f32_16x16x32_bf16 v[104:107], a[156:159], v[180:183], v[104:107]\n"
"ds_read_b128 a[112:115], v227 offset:26624\n"
"v_mfma_f32_16x16x32_bf16 v[108:111], a[152:155], v[184:187], v[108:111]\n"
"v_exp_f32 v199, v199\n"
"v_mfma_f32_16x16x32_bf16 v[108:111], a[156:159], v[188:191], v[108:111]\n"
"ds_read_b64_tr_b16 a[152:153], v230 offset:0\n"
"ds_read_b128 a[116:119], v228 offset:26624\n"
"v_mfma_f32_16x16x32_bf16 v[112:115], a[160:163], v[160:163], v[112:115]\n"
"v_cvt_pk_bf16_f32 v143, v198, v199\n"
"v_mfma_f32_16x16x32_bf16 v[112:115], a[164:167], v[164:167], v[112:115]\n"
"v_exp_f32 v200, v200\n"
"v_smfmac_f32_16x16x64_bf16 a[184:187], a[168:171], v[184:191], v233\n"
"ds_read_b64_tr_b16 a[154:155], v230 offset:6144\n"
"v_mfma_f32_16x16x32_bf16 v[204:207], a[120:123], a[72:75], v[220:223]\n"
"ds_read_b64_tr_b16 a[156:157], v230 offset:12288\n"
"v_mfma_f32_16x16x32_bf16 v[204:207], a[124:127], a[76:79], v[204:207]\n"
"ds_read_b64_tr_b16 a[158:159], v230 offset:18432\n"
"v_mfma_f32_16x16x32_bf16 v[204:207], a[128:131], a[80:83], v[204:207]\n"
"v_exp_f32 v201, v201\n"
"v_mfma_f32_16x16x32_bf16 v[204:207], a[132:135], a[84:87], v[204:207]\n"
"v_mfma_f32_16x16x32_bf16 v[204:207], a[136:139], a[88:91], v[204:207]\n"
"v_cvt_pk_bf16_f32 v150, v200, v201\n"
"v_mfma_f32_16x16x32_bf16 v[204:207], a[140:143], a[92:95], v[204:207]\n"
"v_exp_f32 v202, v202\n"
"v_mfma_f32_16x16x32_bf16 v[116:119], a[160:163], v[168:171], v[116:119]\n"
"v_mfma_f32_16x16x32_bf16 v[116:119], a[164:167], v[172:175], v[116:119]\n"
"v_mfma_f32_16x16x32_bf16 v[120:123], a[160:163], v[176:179], v[120:123]\n"
"v_exp_f32 v203, v203\n"
"v_mfma_f32_16x16x32_bf16 v[120:123], a[164:167], v[180:183], v[120:123]\n"
"v_mfma_f32_16x16x32_bf16 v[124:127], a[160:163], v[184:187], v[124:127]\n"
"v_cvt_pk_bf16_f32 v151, v202, v203\n"
"v_mfma_f32_16x16x32_bf16 v[124:127], a[164:167], v[188:191], v[124:127]\n"
"v_exp_f32 v204, v204\n"
"s_waitcnt lgkmcnt(0)\n"
"v_mfma_f32_16x16x32_bf16 v[192:195], a[96:99], a[0:3], v[208:211]\n"
"s_add_u32 s61, s61, 24576\n"
"ds_read_b64_tr_b16 a[160:161], v231 offset:0\n"
"v_mfma_f32_16x16x32_bf16 v[192:195], a[100:103], a[4:7], v[192:195]\n"
"s_add_u32 s62, s62, 24576\n"
"ds_read_b64_tr_b16 a[162:163], v231 offset:6144\n"
"v_mfma_f32_16x16x32_bf16 v[192:195], a[104:107], a[8:11], v[192:195]\n"
"v_exp_f32 v205, v205\n"
"v_mfma_f32_16x16x32_bf16 v[192:195], a[108:111], a[12:15], v[192:195]\n"
"s_mov_b32 m0, s69\n"
"ds_read_b64_tr_b16 a[164:165], v231 offset:12288\n"
"v_mfma_f32_16x16x32_bf16 v[192:195], a[112:115], a[16:19], v[192:195]\n"
"v_cvt_pk_bf16_f32 v158, v204, v205\n"
"ds_read_b128 a[120:123], v227 offset:30720\n"
"v_mfma_f32_16x16x32_bf16 v[192:195], a[116:119], a[20:23], v[192:195]\n"
"v_exp_f32 v206, v206\n"
"v_mfma_f32_16x16x32_bf16 a[192:195], a[144:147], v[128:131], a[192:195]\n"
"buffer_load_dwordx4 v224, s[20:23], s61 offen offset:0 lds\n"
"v_mfma_f32_16x16x32_bf16 a[192:195], a[148:151], v[132:135], a[192:195]\n"
"ds_read_b64_tr_b16 a[166:167], v231 offset:18432\n"
"ds_read_b128 a[124:127], v228 offset:30720\n"
"v_mfma_f32_16x16x32_bf16 a[196:199], a[144:147], v[136:139], a[196:199]\n"
"v_exp_f32 v207, v207\n"
"v_mfma_f32_16x16x32_bf16 a[196:199], a[148:151], v[140:143], a[196:199]\n"
"buffer_load_dwordx4 v225, s[20:23], s61 offen offset:1024 lds\n"
"v_mfma_f32_16x16x32_bf16 a[200:203], a[144:147], v[144:147], a[200:203]\n"
"v_cvt_pk_bf16_f32 v159, v206, v207\n"
"v_mfma_f32_16x16x32_bf16 a[200:203], a[148:151], v[148:151], a[200:203]\n"
"v_exp_f32 v192, v192\n"
"v_mfma_f32_16x16x32_bf16 v[196:199], a[96:99], a[24:27], v[212:215]\n"
"ds_read_b128 a[128:131], v227 offset:31744\n"
"v_mfma_f32_16x16x32_bf16 v[196:199], a[100:103], a[28:31], v[196:199]\n"
"v_mfma_f32_16x16x32_bf16 v[196:199], a[104:107], a[32:35], v[196:199]\n"
"v_exp_f32 v193, v193\n"
"v_mfma_f32_16x16x32_bf16 v[196:199], a[108:111], a[36:39], v[196:199]\n"
"buffer_load_dwordx4 v226, s[20:23], s61 offen offset:2048 lds\n"
"v_mfma_f32_16x16x32_bf16 v[196:199], a[112:115], a[40:43], v[196:199]\n"
"v_cvt_pk_bf16_f32 v160, v192, v193\n"
"v_mfma_f32_16x16x32_bf16 v[196:199], a[116:119], a[44:47], v[196:199]\n"
"v_exp_f32 v194, v194\n"
"v_mfma_f32_16x16x32_bf16 a[204:207], a[144:147], v[152:155], a[204:207]\n"
"ds_read_b128 a[132:135], v228 offset:31744\n"
"v_mfma_f32_16x16x32_bf16 a[204:207], a[148:151], v[156:159], a[204:207]\n"
"ds_read_b64_tr_b16 a[144:145], v232 offset:0\n"
"v_mfma_f32_16x16x32_bf16 a[208:211], a[152:155], v[128:131], a[208:211]\n"
"v_exp_f32 v195, v195\n"
"v_mfma_f32_16x16x32_bf16 a[208:211], a[156:159], v[132:135], a[208:211]\n"
"ds_read_b64_tr_b16 a[146:147], v232 offset:6144\n"
"v_mfma_f32_16x16x32_bf16 a[212:215], a[152:155], v[136:139], a[212:215]\n"
"v_cvt_pk_bf16_f32 v161, v194, v195\n"
"ds_read_b64_tr_b16 a[148:149], v232 offset:12288\n"
"v_mfma_f32_16x16x32_bf16 a[212:215], a[156:159], v[140:143], a[212:215]\n"
"v_exp_f32 v196, v196\n"
"v_mfma_f32_16x16x32_bf16 v[200:203], a[96:99], a[48:51], v[216:219]\n"
"s_mov_b32 m0, s70\n"
"ds_read_b128 a[136:139], v227 offset:32768\n"
"v_mfma_f32_16x16x32_bf16 v[200:203], a[100:103], a[52:55], v[200:203]\n"
"buffer_load_dwordx4 v224, s[20:23], s62 offen offset:0 lds\n"
"v_mfma_f32_16x16x32_bf16 v[200:203], a[104:107], a[56:59], v[200:203]\n"
"v_exp_f32 v197, v197\n"
"v_mfma_f32_16x16x32_bf16 v[200:203], a[108:111], a[60:63], v[200:203]\n"
"ds_read_b64_tr_b16 a[150:151], v232 offset:18432\n"
"v_mfma_f32_16x16x32_bf16 v[200:203], a[112:115], a[64:67], v[200:203]\n"
"v_cvt_pk_bf16_f32 v168, v196, v197\n"
"v_mfma_f32_16x16x32_bf16 v[200:203], a[116:119], a[68:71], v[200:203]\n"
"v_exp_f32 v198, v198\n"
"v_mfma_f32_16x16x32_bf16 a[216:219], a[152:155], v[144:147], a[216:219]\n"
"buffer_load_dwordx4 v225, s[20:23], s62 offen offset:1024 lds\n"
"v_mfma_f32_16x16x32_bf16 a[216:219], a[156:159], v[148:151], a[216:219]\n"
"ds_read_b128 a[140:143], v228 offset:32768\n"
"v_mfma_f32_16x16x32_bf16 a[220:223], a[152:155], v[152:155], a[220:223]\n"
"v_exp_f32 v199, v199\n"
"v_mfma_f32_16x16x32_bf16 a[220:223], a[156:159], v[156:159], a[220:223]\n"
"ds_read_b64_tr_b16 a[152:153], v229 offset:1024\n"
"s_waitcnt lgkmcnt(2)\n"
"v_mfma_f32_16x16x32_bf16 a[224:227], a[160:163], v[128:131], a[224:227]\n"
"v_cvt_pk_bf16_f32 v169, v198, v199\n"
"v_mfma_f32_16x16x32_bf16 a[224:227], a[164:167], v[132:135], a[224:227]\n"
"v_exp_f32 v200, v200\n"
"v_smfmac_f32_16x16x64_bf16 a[172:175], a[168:171], v[128:135], v233\n"
"buffer_load_dwordx4 v226, s[20:23], s62 offen offset:2048 lds\n"
"v_mfma_f32_16x16x32_bf16 v[204:207], a[96:99], a[72:75], v[220:223]\n"
"ds_read_b64_tr_b16 a[154:155], v229 offset:7168\n"
"v_mfma_f32_16x16x32_bf16 v[204:207], a[100:103], a[76:79], v[204:207]\n"
"ds_read_b64_tr_b16 a[156:157], v229 offset:13312\n"
"v_mfma_f32_16x16x32_bf16 v[204:207], a[104:107], a[80:83], v[204:207]\n"
"v_exp_f32 v201, v201\n"
"v_mfma_f32_16x16x32_bf16 v[204:207], a[108:111], a[84:87], v[204:207]\n"
"ds_read_b64_tr_b16 a[158:159], v229 offset:19456\n"
"v_mfma_f32_16x16x32_bf16 v[204:207], a[112:115], a[88:91], v[204:207]\n"
"v_cvt_pk_bf16_f32 v176, v200, v201\n"
"v_mfma_f32_16x16x32_bf16 v[204:207], a[116:119], a[92:95], v[204:207]\n"
"v_exp_f32 v202, v202\n"
"v_mfma_f32_16x16x32_bf16 a[228:231], a[160:163], v[136:139], a[228:231]\n"
"s_mov_b32 m0, s63\n"
"v_mfma_f32_16x16x32_bf16 a[228:231], a[164:167], v[140:143], a[228:231]\n"
"buffer_load_dwordx4 v224, s[16:19], s61 offen offset:0 lds\n"
"v_mfma_f32_16x16x32_bf16 a[232:235], a[160:163], v[144:147], a[232:235]\n"
"v_exp_f32 v203, v203\n"
"v_mfma_f32_16x16x32_bf16 a[232:235], a[164:167], v[148:151], a[232:235]\n"
"v_mfma_f32_16x16x32_bf16 a[236:239], a[160:163], v[152:155], a[236:239]\n"
"v_cvt_pk_bf16_f32 v177, v202, v203\n"
"v_mfma_f32_16x16x32_bf16 a[236:239], a[164:167], v[156:159], a[236:239]\n"
"v_exp_f32 v204, v204\n"
"v_mfma_f32_16x16x32_bf16 v[192:195], a[120:123], a[0:3], v[208:211]\n"
"ds_read_b64_tr_b16 a[160:161], v230 offset:1024\n"
"ds_read_b128 a[96:99], v227 offset:36864\n"
"v_mfma_f32_16x16x32_bf16 v[192:195], a[124:127], a[4:7], v[192:195]\n"
"buffer_load_dwordx4 v225, s[16:19], s61 offen offset:1024 lds\n"
"v_mfma_f32_16x16x32_bf16 v[192:195], a[128:131], a[8:11], v[192:195]\n"
"v_exp_f32 v205, v205\n"
"v_mfma_f32_16x16x32_bf16 v[192:195], a[132:135], a[12:15], v[192:195]\n"
"ds_read_b64_tr_b16 a[162:163], v230 offset:7168\n"
"v_mfma_f32_16x16x32_bf16 v[192:195], a[136:139], a[16:19], v[192:195]\n"
"v_cvt_pk_bf16_f32 v184, v204, v205\n"
"s_waitcnt lgkmcnt(3)\n"
"v_mfma_f32_16x16x32_bf16 v[192:195], a[140:143], a[20:23], v[192:195]\n"
"v_exp_f32 v206, v206\n"
"v_mfma_f32_16x16x32_bf16 a[240:243], a[144:147], v[128:131], a[240:243]\n"
"ds_read_b64_tr_b16 a[164:165], v230 offset:13312\n"
"ds_read_b128 a[100:103], v228 offset:36864\n"
"v_mfma_f32_16x16x32_bf16 a[240:243], a[148:151], v[132:135], a[240:243]\n"
"ds_read_b64_tr_b16 a[166:167], v230 offset:19456\n"
"v_mfma_f32_16x16x32_bf16 a[244:247], a[144:147], v[136:139], a[244:247]\n"
"v_exp_f32 v207, v207\n"
"v_mfma_f32_16x16x32_bf16 a[244:247], a[148:151], v[140:143], a[244:247]\n"
"buffer_load_dwordx4 v226, s[16:19], s61 offen offset:2048 lds\n"
"v_mfma_f32_16x16x32_bf16 a[248:251], a[144:147], v[144:147], a[248:251]\n"
"v_cvt_pk_bf16_f32 v185, v206, v207\n"
"v_mfma_f32_16x16x32_bf16 a[248:251], a[148:151], v[148:151], a[248:251]\n"
"v_exp_f32 v192, v192\n"
"v_mfma_f32_16x16x32_bf16 v[196:199], a[120:123], a[24:27], v[212:215]\n"
"ds_read_b128 a[104:107], v227 offset:37888\n"
"v_mfma_f32_16x16x32_bf16 v[196:199], a[124:127], a[28:31], v[196:199]\n"
"v_mfma_f32_16x16x32_bf16 v[196:199], a[128:131], a[32:35], v[196:199]\n"
"v_exp_f32 v193, v193\n"
"v_mfma_f32_16x16x32_bf16 v[196:199], a[132:135], a[36:39], v[196:199]\n"
"s_mov_b32 m0, s64\n"
"v_mfma_f32_16x16x32_bf16 v[196:199], a[136:139], a[40:43], v[196:199]\n"
"v_cvt_pk_bf16_f32 v162, v192, v193\n"
"v_mfma_f32_16x16x32_bf16 v[196:199], a[140:143], a[44:47], v[196:199]\n"
"v_exp_f32 v194, v194\n"
"v_mfma_f32_16x16x32_bf16 a[252:255], a[144:147], v[152:155], a[252:255]\n"
"buffer_load_dwordx4 v224, s[16:19], s62 offen offset:0 lds\n"
"v_mfma_f32_16x16x32_bf16 a[252:255], a[148:151], v[156:159], a[252:255]\n"
"ds_read_b64_tr_b16 a[144:145], v231 offset:1024\n"
"ds_read_b128 a[108:111], v228 offset:37888\n"
"v_mfma_f32_16x16x32_bf16 v[0:3], a[152:155], v[128:131], v[0:3]\n"
"v_exp_f32 v195, v195\n"
"v_mfma_f32_16x16x32_bf16 v[0:3], a[156:159], v[132:135], v[0:3]\n"
"ds_read_b64_tr_b16 a[146:147], v231 offset:7168\n"
"v_mfma_f32_16x16x32_bf16 v[4:7], a[152:155], v[136:139], v[4:7]\n"
"v_cvt_pk_bf16_f32 v163, v194, v195\n"
"v_mfma_f32_16x16x32_bf16 v[4:7], a[156:159], v[140:143], v[4:7]\n"
"v_exp_f32 v196, v196\n"
"v_mfma_f32_16x16x32_bf16 v[200:203], a[120:123], a[48:51], v[216:219]\n"
"buffer_load_dwordx4 v225, s[16:19], s62 offen offset:1024 lds\n"
"v_mfma_f32_16x16x32_bf16 v[200:203], a[124:127], a[52:55], v[200:203]\n"
"ds_read_b64_tr_b16 a[148:149], v231 offset:13312\n"
"ds_read_b128 a[112:115], v227 offset:38912\n"
"v_mfma_f32_16x16x32_bf16 v[200:203], a[128:131], a[56:59], v[200:203]\n"
"v_exp_f32 v197, v197\n"
"v_mfma_f32_16x16x32_bf16 v[200:203], a[132:135], a[60:63], v[200:203]\n"
"ds_read_b64_tr_b16 a[150:151], v231 offset:19456\n"
"v_mfma_f32_16x16x32_bf16 v[200:203], a[136:139], a[64:67], v[200:203]\n"
"v_cvt_pk_bf16_f32 v170, v196, v197\n"
"v_mfma_f32_16x16x32_bf16 v[200:203], a[140:143], a[68:71], v[200:203]\n"
"v_exp_f32 v198, v198\n"
"v_mfma_f32_16x16x32_bf16 v[8:11], a[152:155], v[144:147], v[8:11]\n"
"buffer_load_dwordx4 v226, s[16:19], s62 offen offset:2048 lds\n"
"v_mfma_f32_16x16x32_bf16 v[8:11], a[156:159], v[148:151], v[8:11]\n"
"ds_read_b128 a[116:119], v228 offset:38912\n"
"v_mfma_f32_16x16x32_bf16 v[12:15], a[152:155], v[152:155], v[12:15]\n"
"v_exp_f32 v199, v199\n"
"v_mfma_f32_16x16x32_bf16 v[12:15], a[156:159], v[156:159], v[12:15]\n"
"ds_read_b64_tr_b16 a[152:153], v232 offset:1024\n"
"s_waitcnt lgkmcnt(2)\n"
"v_mfma_f32_16x16x32_bf16 v[16:19], a[160:163], v[128:131], v[16:19]\n"
"v_cvt_pk_bf16_f32 v171, v198, v199\n"
"v_mfma_f32_16x16x32_bf16 v[16:19], a[164:167], v[132:135], v[16:19]\n"
"v_exp_f32 v200, v200\n"
"v_smfmac_f32_16x16x64_bf16 a[176:179], a[168:171], v[136:143], v233\n"
"ds_read_b64_tr_b16 a[154:155], v232 offset:7168\n"
"v_mfma_f32_16x16x32_bf16 v[204:207], a[120:123], a[72:75], v[220:223]\n"
"ds_read_b64_tr_b16 a[156:157], v232 offset:13312\n"
"v_mfma_f32_16x16x32_bf16 v[204:207], a[124:127], a[76:79], v[204:207]\n"
"ds_read_b64_tr_b16 a[158:159], v232 offset:19456\n"
"v_mfma_f32_16x16x32_bf16 v[204:207], a[128:131], a[80:83], v[204:207]\n"
"v_exp_f32 v201, v201\n"
"v_mfma_f32_16x16x32_bf16 v[204:207], a[132:135], a[84:87], v[204:207]\n"
"v_mfma_f32_16x16x32_bf16 v[204:207], a[136:139], a[88:91], v[204:207]\n"
"v_cvt_pk_bf16_f32 v178, v200, v201\n"
"v_mfma_f32_16x16x32_bf16 v[204:207], a[140:143], a[92:95], v[204:207]\n"
"v_exp_f32 v202, v202\n"
"v_mfma_f32_16x16x32_bf16 v[20:23], a[160:163], v[136:139], v[20:23]\n"
"s_waitcnt vmcnt(12)\n"
"v_mfma_f32_16x16x32_bf16 v[20:23], a[164:167], v[140:143], v[20:23]\n"
"ds_read_b64_tr_b16 a[188:189], v237 offset:0\n"
"v_mfma_f32_16x16x32_bf16 v[24:27], a[160:163], v[144:147], v[24:27]\n"
"v_exp_f32 v203, v203\n"
"v_mfma_f32_16x16x32_bf16 v[24:27], a[164:167], v[148:151], v[24:27]\n"
"ds_read_b64_tr_b16 a[190:191], v237 offset:512\n"
"v_mfma_f32_16x16x32_bf16 v[28:31], a[160:163], v[152:155], v[28:31]\n"
"v_cvt_pk_bf16_f32 v179, v202, v203\n"
"v_mfma_f32_16x16x32_bf16 v[28:31], a[164:167], v[156:159], v[28:31]\n"
"v_exp_f32 v204, v204\n"
"v_mfma_f32_16x16x32_bf16 v[192:195], a[96:99], a[0:3], v[208:211]\n"
"ds_read_b64_tr_b16 a[160:161], v229 offset:2048\n"
"ds_read_b128 a[120:123], v227 offset:43008\n"
"v_mfma_f32_16x16x32_bf16 v[192:195], a[100:103], a[4:7], v[192:195]\n"
"ds_read_b64_tr_b16 a[162:163], v229 offset:8192\n"
"v_mfma_f32_16x16x32_bf16 v[192:195], a[104:107], a[8:11], v[192:195]\n"
"v_exp_f32 v205, v205\n"
"v_mfma_f32_16x16x32_bf16 v[192:195], a[108:111], a[12:15], v[192:195]\n"
"ds_read_b64_tr_b16 a[164:165], v229 offset:14336\n"
"v_mfma_f32_16x16x32_bf16 v[192:195], a[112:115], a[16:19], v[192:195]\n"
"v_cvt_pk_bf16_f32 v186, v204, v205\n"
"s_waitcnt lgkmcnt(4)\n"
"v_mfma_f32_16x16x32_bf16 v[192:195], a[116:119], a[20:23], v[192:195]\n"
"v_exp_f32 v206, v206\n"
"v_mfma_f32_16x16x32_bf16 v[32:35], a[144:147], v[128:131], v[32:35]\n"
"ds_read_b64_tr_b16 a[166:167], v229 offset:20480\n"
"ds_read_b128 a[124:127], v228 offset:43008\n"
"v_mfma_f32_16x16x32_bf16 v[32:35], a[148:151], v[132:135], v[32:35]\n"
"buffer_store_dwordx4 a[188:191], v238, s[48:51], s72 offen sc1\n"
"v_mfma_f32_16x16x32_bf16 v[36:39], a[144:147], v[136:139], v[36:39]\n"
"v_exp_f32 v207, v207\n"
"v_mfma_f32_16x16x32_bf16 v[36:39], a[148:151], v[140:143], v[36:39]\n"
"s_add_u32 s72, s72, s74\n"
"v_mfma_f32_16x16x32_bf16 v[40:43], a[144:147], v[144:147], v[40:43]\n"
"v_cvt_pk_bf16_f32 v187, v206, v207\n"
"v_mfma_f32_16x16x32_bf16 v[40:43], a[148:151], v[148:151], v[40:43]\n"
"v_exp_f32 v192, v192\n"
"v_mfma_f32_16x16x32_bf16 v[196:199], a[96:99], a[24:27], v[212:215]\n"
"ds_read_b128 a[128:131], v227 offset:44032\n"
"v_mfma_f32_16x16x32_bf16 v[196:199], a[100:103], a[28:31], v[196:199]\n"
"buffer_load_dword v243, v241, s[20:23], s61 offen\n"
"v_mfma_f32_16x16x32_bf16 v[196:199], a[104:107], a[32:35], v[196:199]\n"
"v_exp_f32 v193, v193\n"
"v_mfma_f32_16x16x32_bf16 v[196:199], a[108:111], a[36:39], v[196:199]\n"
"v_mfma_f32_16x16x32_bf16 v[196:199], a[112:115], a[40:43], v[196:199]\n"
"v_cvt_pk_bf16_f32 v164, v192, v193\n"
"v_mfma_f32_16x16x32_bf16 v[196:199], a[116:119], a[44:47], v[196:199]\n"
"v_exp_f32 v194, v194\n"
"v_mfma_f32_16x16x32_bf16 v[44:47], a[144:147], v[152:155], v[44:47]\n"
"ds_read_b128 a[132:135], v228 offset:44032\n"
"v_mfma_f32_16x16x32_bf16 v[44:47], a[148:151], v[156:159], v[44:47]\n"
"ds_read_b64_tr_b16 a[144:145], v230 offset:2048\n"
"v_mfma_f32_16x16x32_bf16 v[48:51], a[152:155], v[128:131], v[48:51]\n"
"v_exp_f32 v195, v195\n"
"v_mfma_f32_16x16x32_bf16 v[48:51], a[156:159], v[132:135], v[48:51]\n"
"ds_read_b64_tr_b16 a[146:147], v230 offset:8192\n"
"v_mfma_f32_16x16x32_bf16 v[52:55], a[152:155], v[136:139], v[52:55]\n"
"v_cvt_pk_bf16_f32 v165, v194, v195\n"
"v_mfma_f32_16x16x32_bf16 v[52:55], a[156:159], v[140:143], v[52:55]\n"
"v_exp_f32 v196, v196\n"
"v_mfma_f32_16x16x32_bf16 v[200:203], a[96:99], a[48:51], v[216:219]\n"
"ds_read_b64_tr_b16 a[148:149], v230 offset:14336\n"
"v_mfma_f32_16x16x32_bf16 v[200:203], a[100:103], a[52:55], v[200:203]\n"
"ds_read_b64_tr_b16 a[150:151], v230 offset:20480\n"
"v_mfma_f32_16x16x32_bf16 v[200:203], a[104:107], a[56:59], v[200:203]\n"
"v_exp_f32 v197, v197\n"
"v_mfma_f32_16x16x32_bf16 v[200:203], a[108:111], a[60:63], v[200:203]\n"
"ds_read_b128 a[136:139], v227 offset:45056\n"
"v_mfma_f32_16x16x32_bf16 v[200:203], a[112:115], a[64:67], v[200:203]\n"
"v_cvt_pk_bf16_f32 v172, v196, v197\n"
"v_mfma_f32_16x16x32_bf16 v[200:203], a[116:119], a[68:71], v[200:203]\n"
"v_exp_f32 v198, v198\n"
"v_mfma_f32_16x16x32_bf16 v[56:59], a[152:155], v[144:147], v[56:59]\n"
"ds_read_b128 a[140:143], v228 offset:45056\n"
"v_mfma_f32_16x16x32_bf16 v[56:59], a[156:159], v[148:151], v[56:59]\n"
"v_mfma_f32_16x16x32_bf16 v[60:63], a[152:155], v[152:155], v[60:63]\n"
"v_exp_f32 v199, v199\n"
"v_mfma_f32_16x16x32_bf16 v[60:63], a[156:159], v[156:159], v[60:63]\n"
"ds_read_b64_tr_b16 a[152:153], v231 offset:2048\n"
"s_waitcnt lgkmcnt(2)\n"
"v_mfma_f32_16x16x32_bf16 v[64:67], a[160:163], v[128:131], v[64:67]\n"
"v_cvt_pk_bf16_f32 v173, v198, v199\n"
"v_mfma_f32_16x16x32_bf16 v[64:67], a[164:167], v[132:135], v[64:67]\n"
"v_exp_f32 v200, v200\n"
"v_smfmac_f32_16x16x64_bf16 a[180:183], a[168:171], v[144:151], v233\n"
"ds_read_b64_tr_b16 a[154:155], v231 offset:8192\n"
"v_mfma_f32_16x16x32_bf16 v[204:207], a[96:99], a[72:75], v[220:223]\n"
"ds_read_b64_tr_b16 a[156:157], v231 offset:14336\n"
"v_mfma_f32_16x16x32_bf16 v[204:207], a[100:103], a[76:79], v[204:207]\n"
"ds_read_b64_tr_b16 a[158:159], v231 offset:20480\n"
"v_mfma_f32_16x16x32_bf16 v[204:207], a[104:107], a[80:83], v[204:207]\n"
"v_exp_f32 v201, v201\n"
"v_mfma_f32_16x16x32_bf16 v[204:207], a[108:111], a[84:87], v[204:207]\n"
"v_mfma_f32_16x16x32_bf16 v[204:207], a[112:115], a[88:91], v[204:207]\n"
"v_cvt_pk_bf16_f32 v180, v200, v201\n"
"v_mfma_f32_16x16x32_bf16 v[204:207], a[116:119], a[92:95], v[204:207]\n"
"v_exp_f32 v202, v202\n"
"v_mfma_f32_16x16x32_bf16 v[68:71], a[160:163], v[136:139], v[68:71]\n"
"v_mfma_f32_16x16x32_bf16 v[68:71], a[164:167], v[140:143], v[68:71]\n"
"v_mfma_f32_16x16x32_bf16 v[72:75], a[160:163], v[144:147], v[72:75]\n"
"v_exp_f32 v203, v203\n"
"v_mfma_f32_16x16x32_bf16 v[72:75], a[164:167], v[148:151], v[72:75]\n"
"v_mfma_f32_16x16x32_bf16 v[76:79], a[160:163], v[152:155], v[76:79]\n"
"v_cvt_pk_bf16_f32 v181, v202, v203\n"
"v_mfma_f32_16x16x32_bf16 v[76:79], a[164:167], v[156:159], v[76:79]\n"
"v_exp_f32 v204, v204\n"
"v_mfma_f32_16x16x32_bf16 v[192:195], a[120:123], a[0:3], v[208:211]\n"
"ds_read_b64_tr_b16 a[160:161], v232 offset:2048\n"
"v_mfma_f32_16x16x32_bf16 v[192:195], a[124:127], a[4:7], v[192:195]\n"
"ds_read_b64_tr_b16 a[162:163], v232 offset:8192\n"
"v_mfma_f32_16x16x32_bf16 v[192:195], a[128:131], a[8:11], v[192:195]\n"
"v_exp_f32 v205, v205\n"
"v_mfma_f32_16x16x32_bf16 v[192:195], a[132:135], a[12:15], v[192:195]\n"
"ds_read_b64_tr_b16 a[164:165], v232 offset:14336\n"
"v_mfma_f32_16x16x32_bf16 v[192:195], a[136:139], a[16:19], v[192:195]\n"
"v_cvt_pk_bf16_f32 v188, v204, v205\n"
"s_waitcnt lgkmcnt(3)\n"
"v_mfma_f32_16x16x32_bf16 v[192:195], a[140:143], a[20:23], v[192:195]\n"
"v_exp_f32 v206, v206\n"
"v_mfma_f32_16x16x32_bf16 v[80:83], a[144:147], v[128:131], v[80:83]\n"
"ds_read_b64_tr_b16 a[166:167], v232 offset:20480\n"
"v_mfma_f32_16x16x32_bf16 v[80:83], a[148:151], v[132:135], v[80:83]\n"
"v_mfma_f32_16x16x32_bf16 v[84:87], a[144:147], v[136:139], v[84:87]\n"
"v_exp_f32 v207, v207\n"
"v_mfma_f32_16x16x32_bf16 v[84:87], a[148:151], v[140:143], v[84:87]\n"
"v_mfma_f32_16x16x32_bf16 v[88:91], a[144:147], v[144:147], v[88:91]\n"
"v_cvt_pk_bf16_f32 v189, v206, v207\n"
"v_mfma_f32_16x16x32_bf16 v[88:91], a[148:151], v[148:151], v[88:91]\n"
"v_exp_f32 v192, v192\n"
"v_mfma_f32_16x16x32_bf16 v[196:199], a[120:123], a[24:27], v[212:215]\n"
"v_mfma_f32_16x16x32_bf16 v[196:199], a[124:127], a[28:31], v[196:199]\n"
"v_mfma_f32_16x16x32_bf16 v[196:199], a[128:131], a[32:35], v[196:199]\n"
"v_exp_f32 v193, v193\n"
"v_mfma_f32_16x16x32_bf16 v[196:199], a[132:135], a[36:39], v[196:199]\n"
"v_mfma_f32_16x16x32_bf16 v[196:199], a[136:139], a[40:43], v[196:199]\n"
"v_cvt_pk_bf16_f32 v166, v192, v193\n"
"v_mfma_f32_16x16x32_bf16 v[196:199], a[140:143], a[44:47], v[196:199]\n"
"v_exp_f32 v194, v194\n"
"v_mfma_f32_16x16x32_bf16 v[92:95], a[144:147], v[152:155], v[92:95]\n"
"v_mfma_f32_16x16x32_bf16 v[92:95], a[148:151], v[156:159], v[92:95]\n"
"v_mfma_f32_16x16x32_bf16 v[96:99], a[152:155], v[128:131], v[96:99]\n"
"v_exp_f32 v195, v195\n"
"v_mfma_f32_16x16x32_bf16 v[96:99], a[156:159], v[132:135], v[96:99]\n"
"s_waitcnt vmcnt(2) lgkmcnt(0)\n"
"s_barrier\n"
"v_mfma_f32_16x16x32_bf16 v[100:103], a[152:155], v[136:139], v[100:103]\n"
"v_cvt_pk_bf16_f32 v167, v194, v195\n"
"ds_read_b128 a[96:99], v227 offset:0\n"
"v_mfma_f32_16x16x32_bf16 v[100:103], a[156:159], v[140:143], v[100:103]\n"
"v_exp_f32 v196, v196\n"
"v_mfma_f32_16x16x32_bf16 v[200:203], a[120:123], a[48:51], v[216:219]\n"
"ds_read_b64_tr_b16 a[144:145], v229 offset:24576\n"
"ds_read_b128 a[100:103], v228 offset:0\n"
"v_mfma_f32_16x16x32_bf16 v[200:203], a[124:127], a[52:55], v[200:203]\n"
"ds_read_b64_tr_b16 a[146:147], v229 offset:30720\n"
"ds_read_b128 a[104:107], v227 offset:1024\n"
"v_mfma_f32_16x16x32_bf16 v[200:203], a[128:131], a[56:59], v[200:203]\n"
"v_exp_f32 v197, v197\n"
"v_mfma_f32_16x16x32_bf16 v[200:203], a[132:135], a[60:63], v[200:203]\n"
"ds_read_b64_tr_b16 a[148:149], v229 offset:36864\n"
"ds_read_b128 a[108:111], v228 offset:1024\n"
"v_mfma_f32_16x16x32_bf16 v[200:203], a[136:139], a[64:67], v[200:203]\n"
"v_cvt_pk_bf16_f32 v174, v196, v197\n"
"v_mfma_f32_16x16x32_bf16 v[200:203], a[140:143], a[68:71], v[200:203]\n"
"v_exp_f32 v198, v198\n"
"v_mfma_f32_16x16x32_bf16 v[104:107], a[152:155], v[144:147], v[104:107]\n"
"ds_read_b64_tr_b16 a[150:151], v229 offset:43008\n"
"v_mfma_f32_16x16x32_bf16 v[104:107], a[156:159], v[148:151], v[104:107]\n"
"ds_read_b128 a[112:115], v227 offset:2048\n"
"v_mfma_f32_16x16x32_bf16 v[108:111], a[152:155], v[152:155], v[108:111]\n"
"v_exp_f32 v199, v199\n"
"v_mfma_f32_16x16x32_bf16 v[108:111], a[156:159], v[156:159], v[108:111]\n"
"ds_read_b64_tr_b16 a[152:153], v230 offset:24576\n"
"ds_read_b128 a[116:119], v228 offset:2048\n"
"v_mfma_f32_16x16x32_bf16 v[112:115], a[160:163], v[128:131], v[112:115]\n"
"v_cvt_pk_bf16_f32 v175, v198, v199\n"
"v_mfma_f32_16x16x32_bf16 v[112:115], a[164:167], v[132:135], v[112:115]\n"
"v_exp_f32 v200, v200\n"
"v_smfmac_f32_16x16x64_bf16 a[184:187], a[168:171], v[152:159], v233\n"
"ds_read_b64_tr_b16 a[154:155], v230 offset:30720\n"
"v_mfma_f32_16x16x32_bf16 v[204:207], a[120:123], a[72:75], v[220:223]\n"
"ds_read_b64_tr_b16 a[156:157], v230 offset:36864\n"
"v_mfma_f32_16x16x32_bf16 v[204:207], a[124:127], a[76:79], v[204:207]\n"
"ds_read_b64_tr_b16 a[158:159], v230 offset:43008\n"
"v_mfma_f32_16x16x32_bf16 v[204:207], a[128:131], a[80:83], v[204:207]\n"
"v_exp_f32 v201, v201\n"
"v_mfma_f32_16x16x32_bf16 v[204:207], a[132:135], a[84:87], v[204:207]\n"
"s_sub_u32 s58, s58, 1\n"
"v_mfma_f32_16x16x32_bf16 v[204:207], a[136:139], a[88:91], v[204:207]\n"
"v_cvt_pk_bf16_f32 v182, v200, v201\n"
"v_mfma_f32_16x16x32_bf16 v[204:207], a[140:143], a[92:95], v[204:207]\n"
"v_exp_f32 v202, v202\n"
"v_mfma_f32_16x16x32_bf16 v[116:119], a[160:163], v[136:139], v[116:119]\n"
"v_mfma_f32_16x16x32_bf16 v[116:119], a[164:167], v[140:143], v[116:119]\n"
"s_cmp_lg_u32 s58, s59\n"
"v_mfma_f32_16x16x32_bf16 v[120:123], a[160:163], v[144:147], v[120:123]\n"
"v_exp_f32 v203, v203\n"
"v_mfma_f32_16x16x32_bf16 v[120:123], a[164:167], v[148:151], v[120:123]\n"
"v_mfma_f32_16x16x32_bf16 v[124:127], a[160:163], v[152:155], v[124:127]\n"
"v_cvt_pk_bf16_f32 v183, v202, v203\n"
"v_mfma_f32_16x16x32_bf16 v[124:127], a[164:167], v[156:159], v[124:127]\n"
"v_exp_f32 v204, v204\n"
"s_cbranch_scc1 L_loop_%=\n"
"s_cmp_lg_u32 s58, 0\n"
"s_cbranch_scc1 L_sw_%=\n"
"s_waitcnt lgkmcnt(0)\n"
"v_mfma_f32_16x16x32_bf16 v[192:195], a[96:99], a[0:3], v[208:211]\n"
"s_add_u32 s61, s61, 24576\n"
"ds_read_b64_tr_b16 a[160:161], v231 offset:24576\n"
"ds_read_b128 a[120:123], v227 offset:6144\n"
"v_mfma_f32_16x16x32_bf16 v[192:195], a[100:103], a[4:7], v[192:195]\n"
"s_add_u32 s62, s62, 24576\n"
"s_getpc_b64 s[44:45]\n"
"fused_code_tail:\n"
"ds_read_b64_tr_b16 a[162:163], v231 offset:30720\n"
"v_mfma_f32_16x16x32_bf16 v[192:195], a[104:107], a[8:11], v[192:195]\n"
"v_exp_f32 v205, v205\n"
"ds_read_b64_tr_b16 a[164:165], v231 offset:36864\n"
"v_mfma_f32_16x16x32_bf16 v[192:195], a[108:111], a[12:15], v[192:195]\n"
"v_cmp_gt_u32 vcc, 32, v249\n"
"s_cbranch_vccnz L_spin_pre0_%=\n"
"L_back_pre0_%=:\n"
"ds_read_b64_tr_b16 a[166:167], v231 offset:43008\n"
"v_mfma_f32_16x16x32_bf16 v[192:195], a[112:115], a[16:19], v[192:195]\n"
"v_cvt_pk_bf16_f32 v190, v204, v205\n"
"s_mov_b32 m0, s67\n"
"v_mfma_f32_16x16x32_bf16 v[192:195], a[116:119], a[20:23], v[192:195]\n"
"v_exp_f32 v206, v206\n"
"v_mfma_f32_16x16x32_bf16 a[192:195], a[144:147], v[160:163], a[192:195]\n"
"buffer_load_dwordx4 v224, s[20:23], s61 offen offset:0 lds\n"
"v_mfma_f32_16x16x32_bf16 a[192:195], a[148:151], v[164:167], a[192:195]\n"
"buffer_load_dwordx4 v225, s[20:23], s61 offen offset:1024 lds\n"
"v_mfma_f32_16x16x32_bf16 a[196:199], a[144:147], v[168:171], a[196:199]\n"
"v_exp_f32 v207, v207\n"
"v_mfma_f32_16x16x32_bf16 a[196:199], a[148:151], v[172:175], a[196:199]\n"
"ds_read_b128 a[124:127], v228 offset:6144\n"
"v_mfma_f32_16x16x32_bf16 a[200:203], a[144:147], v[176:179], a[200:203]\n"
"v_cvt_pk_bf16_f32 v191, v206, v207\n"
"v_mfma_f32_16x16x32_bf16 a[200:203], a[148:151], v[180:183], a[200:203]\n"
"v_exp_f32 v192, v192\n"
"v_mfma_f32_16x16x32_bf16 v[196:199], a[96:99], a[24:27], v[212:215]\n"
"buffer_load_dwordx4 v226, s[20:23], s61 offen offset:2048 lds\n"
"v_mfma_f32_16x16x32_bf16 v[196:199], a[100:103], a[28:31], v[196:199]\n"
"ds_read_b128 a[128:131], v227 offset:7168\n"
"v_mfma_f32_16x16x32_bf16 v[196:199], a[104:107], a[32:35], v[196:199]\n"
"v_exp_f32 v193, v193\n"
"v_mfma_f32_16x16x32_bf16 v[196:199], a[108:111], a[36:39], v[196:199]\n"
"v_mfma_f32_16x16x32_bf16 v[196:199], a[112:115], a[40:43], v[196:199]\n"
"v_cvt_pk_bf16_f32 v128, v192, v193\n"
"v_mfma_f32_16x16x32_bf16 v[196:199], a[116:119], a[44:47], v[196:199]\n"
"v_exp_f32 v194, v194\n"
"v_mfma_f32_16x16x32_bf16 a[204:207], a[144:147], v[184:187], a[204:207]\n"
"s_mov_b32 m0, s68\n"
"ds_read_b128 a[132:135], v228 offset:7168\n"
"v_mfma_f32_16x16x32_bf16 a[204:207], a[148:151], v[188:191], a[204:207]\n"
"buffer_load_dwordx4 v224, s[20:23], s62 offen offset:0 lds\n"
"v_mfma_f32_16x16x32_bf16 a[208:211], a[152:155], v[160:163], a[208:211]\n"
"v_exp_f32 v195, v195\n"
"v_mfma_f32_16x16x32_bf16 a[208:211], a[156:159], v[164:167], a[208:211]\n"
"buffer_load_dwordx4 v225, s[20:23], s62 offen offset:1024 lds\n"
"v_mfma_f32_16x16x32_bf16 a[212:215], a[152:155], v[168:171], a[212:215]\n"
"v_cvt_pk_bf16_f32 v129, v194, v195\n"
"ds_read_b64_tr_b16 a[144:145], v232 offset:24576\n"
"v_mfma_f32_16x16x32_bf16 a[212:215], a[156:159], v[172:175], a[212:215]\n"
"v_exp_f32 v196, v196\n"
"v_mfma_f32_16x16x32_bf16 v[200:203], a[96:99], a[48:51], v[216:219]\n"
"ds_read_b64_tr_b16 a[146:147], v232 offset:30720\n"
"ds_read_b128 a[136:139], v227 offset:8192\n"
"v_mfma_f32_16x16x32_bf16 v[200:203], a[100:103], a[52:55], v[200:203]\n"
"ds_read_b64_tr_b16 a[148:149], v232 offset:36864\n"
"v_mfma_f32_16x16x32_bf16 v[200:203], a[104:107], a[56:59], v[200:203]\n"
"v_exp_f32 v197, v197\n"
"v_mfma_f32_16x16x32_bf16 v[200:203], a[108:111], a[60:63], v[200:203]\n"
"buffer_load_dwordx4 v226, s[20:23], s62 offen offset:2048 lds\n"
"v_mfma_f32_16x16x32_bf16 v[200:203], a[112:115], a[64:67], v[200:203]\n"
"v_cvt_pk_bf16_f32 v136, v196, v197\n"
"v_mfma_f32_16x16x32_bf16 v[200:203], a[116:119], a[68:71], v[200:203]\n"
"v_exp_f32 v198, v198\n"
"v_mfma_f32_16x16x32_bf16 a[216:219], a[152:155], v[176:179], a[216:219]\n"
"ds_read_b64_tr_b16 a[150:151], v232 offset:43008\n"
"ds_read_b128 a[140:143], v228 offset:8192\n"
"v_mfma_f32_16x16x32_bf16 a[216:219], a[156:159], v[180:183], a[216:219]\n"
"s_mov_b32 m0, s65\n"
"v_mfma_f32_16x16x32_bf16 a[220:223], a[152:155], v[184:187], a[220:223]\n"
"v_exp_f32 v199, v199\n"
"v_mfma_f32_16x16x32_bf16 a[220:223], a[156:159], v[188:191], a[220:223]\n"
"buffer_load_dwordx4 v224, s[16:19], s61 offen offset:0 lds\n"
"s_waitcnt lgkmcnt(2)\n"
"v_mfma_f32_16x16x32_bf16 a[224:227], a[160:163], v[160:163], a[224:227]\n"
"v_cvt_pk_bf16_f32 v137, v198, v199\n"
"v_mfma_f32_16x16x32_bf16 a[224:227], a[164:167], v[164:167], a[224:227]\n"
"v_exp_f32 v200, v200\n"
"v_smfmac_f32_16x16x64_bf16 a[172:175], a[168:171], v[160:167], v233\n"
"buffer_load_dwordx4 v225, s[16:19], s61 offen offset:1024 lds\n"
"v_mfma_f32_16x16x32_bf16 v[204:207], a[96:99], a[72:75], v[220:223]\n"
"ds_read_b64_tr_b16 a[152:153], v229 offset:25600\n"
"v_mfma_f32_16x16x32_bf16 v[204:207], a[100:103], a[76:79], v[204:207]\n"
"ds_read_b64_tr_b16 a[154:155], v229 offset:31744\n"
"v_mfma_f32_16x16x32_bf16 v[204:207], a[104:107], a[80:83], v[204:207]\n"
"v_exp_f32 v201, v201\n"
"v_mfma_f32_16x16x32_bf16 v[204:207], a[108:111], a[84:87], v[204:207]\n"
"ds_read_b64_tr_b16 a[156:157], v229 offset:37888\n"
"v_mfma_f32_16x16x32_bf16 v[204:207], a[112:115], a[88:91], v[204:207]\n"
"v_cvt_pk_bf16_f32 v144, v200, v201\n"
"v_mfma_f32_16x16x32_bf16 v[204:207], a[116:119], a[92:95], v[204:207]\n"
"v_exp_f32 v202, v202\n"
"v_mfma_f32_16x16x32_bf16 a[228:231], a[160:163], v[168:171], a[228:231]\n"
"buffer_load_dwordx4 v226, s[16:19], s61 offen offset:2048 lds\n"
"v_mfma_f32_16x16x32_bf16 a[228:231], a[164:167], v[172:175], a[228:231]\n"
"ds_read_b64_tr_b16 a[158:159], v229 offset:44032\n"
"v_mfma_f32_16x16x32_bf16 a[232:235], a[160:163], v[176:179], a[232:235]\n"
"v_exp_f32 v203, v203\n"
"v_mfma_f32_16x16x32_bf16 a[232:235], a[164:167], v[180:183], a[232:235]\n"
"s_mov_b32 m0, s66\n"
"v_mfma_f32_16x16x32_bf16 a[236:239], a[160:163], v[184:187], a[236:239]\n"
"v_cvt_pk_bf16_f32 v145, v202, v203\n"
"v_mfma_f32_16x16x32_bf16 a[236:239], a[164:167], v[188:191], a[236:239]\n"
"v_exp_f32 v204, v204\n"
"v_mfma_f32_16x16x32_bf16 v[192:195], a[120:123], a[0:3], v[208:211]\n"
"buffer_load_dwordx4 v224, s[16:19], s62 offen offset:0 lds\n"
"v_mfma_f32_16x16x32_bf16 v[192:195], a[124:127], a[4:7], v[192:195]\n"
"ds_read_b64_tr_b16 a[160:161], v230 offset:25600\n"
"ds_read_b128 a[96:99], v227 offset:12288\n"
"v_mfma_f32_16x16x32_bf16 v[192:195], a[128:131], a[8:11], v[192:195]\n"
"v_exp_f32 v205, v205\n"
"v_mfma_f32_16x16x32_bf16 v[192:195], a[132:135], a[12:15], v[192:195]\n"
"buffer_load_dwordx4 v225, s[16:19], s62 offen offset:1024 lds\n"
"v_mfma_f32_16x16x32_bf16 v[192:195], a[136:139], a[16:19], v[192:195]\n"
"v_cvt_pk_bf16_f32 v152, v204, v205\n"
"s_waitcnt lgkmcnt(2)\n"
"v_mfma_f32_16x16x32_bf16 v[192:195], a[140:143], a[20:23], v[192:195]\n"
"v_exp_f32 v206, v206\n"
"v_mfma_f32_16x16x32_bf16 a[240:243], a[144:147], v[160:163], a[240:243]\n"
"ds_read_b64_tr_b16 a[162:163], v230 offset:31744\n"
"ds_read_b128 a[100:103], v228 offset:12288\n"
"v_mfma_f32_16x16x32_bf16 a[240:243], a[148:151], v[164:167], a[240:243]\n"
"buffer_load_dwordx4 v226, s[16:19], s62 offen offset:2048 lds\n"
"v_mfma_f32_16x16x32_bf16 a[244:247], a[144:147], v[168:171], a[244:247]\n"
"v_exp_f32 v207, v207\n"
"v_mfma_f32_16x16x32_bf16 a[244:247], a[148:151], v[172:175], a[244:247]\n"
"ds_read_b64_tr_b16 a[164:165], v230 offset:37888\n"
"v_mfma_f32_16x16x32_bf16 a[248:251], a[144:147], v[176:179], a[248:251]\n"
"v_cvt_pk_bf16_f32 v153, v206, v207\n"
"ds_read_b64_tr_b16 a[166:167], v230 offset:44032\n"
"v_mfma_f32_16x16x32_bf16 a[248:251], a[148:151], v[180:183], a[248:251]\n"
"v_exp_f32 v192, v192\n"
"v_mfma_f32_16x16x32_bf16 v[196:199], a[120:123], a[24:27], v[212:215]\n"
"s_add_u32 s88, s4, 0\n"
"ds_read_b128 a[104:107], v227 offset:13312\n"
"v_mfma_f32_16x16x32_bf16 v[196:199], a[124:127], a[28:31], v[196:199]\n"
"s_add_u32 m0, s6, 102400\n"
"v_mfma_f32_16x16x32_bf16 v[196:199], a[128:131], a[32:35], v[196:199]\n"
"v_exp_f32 v193, v193\n"
"v_mfma_f32_16x16x32_bf16 v[196:199], a[132:135], a[36:39], v[196:199]\n"
"buffer_load_dwordx4 v244, s[8:11], s88 offen offset:0 sc1 lds\n"
"v_mfma_f32_16x16x32_bf16 v[196:199], a[136:139], a[40:43], v[196:199]\n"
"v_cvt_pk_bf16_f32 v130, v192, v193\n"
"v_mfma_f32_16x16x32_bf16 v[196:199], a[140:143], a[44:47], v[196:199]\n"
"v_exp_f32 v194, v194\n"
"v_mfma_f32_16x16x32_bf16 a[252:255], a[144:147], v[184:187], a[252:255]\n"
"buffer_load_dwordx4 v244, s[8:11], s88 offen offset:1024 sc1 lds\n"
"v_mfma_f32_16x16x32_bf16 a[252:255], a[148:151], v[188:191], a[252:255]\n"
"ds_read_b64_tr_b16 a[144:145], v231 offset:25600\n"
"ds_read_b128 a[108:111], v228 offset:13312\n"
"v_mfma_f32_16x16x32_bf16 v[0:3], a[152:155], v[160:163], v[0:3]\n"
"v_exp_f32 v195, v195\n"
"v_mfma_f32_16x16x32_bf16 v[0:3], a[156:159], v[164:167], v[0:3]\n"
"ds_read_b64_tr_b16 a[146:147], v231 offset:31744\n"
"v_mfma_f32_16x16x32_bf16 v[4:7], a[152:155], v[168:171], v[4:7]\n"
"v_cvt_pk_bf16_f32 v131, v194, v195\n"
"v_mfma_f32_16x16x32_bf16 v[4:7], a[156:159], v[172:175], v[4:7]\n"
"v_exp_f32 v196, v196\n"
"v_mfma_f32_16x16x32_bf16 v[200:203], a[120:123], a[48:51], v[216:219]\n"
"buffer_load_dwordx4 v244, s[8:11], s88 offen offset:2048 sc1 lds\n"
"v_mfma_f32_16x16x32_bf16 v[200:203], a[124:127], a[52:55], v[200:203]\n"
"ds_read_b64_tr_b16 a[148:149], v231 offset:37888\n"
"ds_read_b128 a[112:115], v227 offset:14336\n"
"v_mfma_f32_16x16x32_bf16 v[200:203], a[128:131], a[56:59], v[200:203]\n"
"v_exp_f32 v197, v197\n"
"v_mfma_f32_16x16x32_bf16 v[200:203], a[132:135], a[60:63], v[200:203]\n"
"buffer_load_dwordx4 v244, s[8:11], s88 offen offset:3072 sc1 lds\n"
"v_mfma_f32_16x16x32_bf16 v[200:203], a[136:139], a[64:67], v[200:203]\n"
"v_cvt_pk_bf16_f32 v138, v196, v197\n"
"v_mfma_f32_16x16x32_bf16 v[200:203], a[140:143], a[68:71], v[200:203]\n"
"v_exp_f32 v198, v198\n"
"v_mfma_f32_16x16x32_bf16 v[8:11], a[152:155], v[176:179], v[8:11]\n"
"ds_read_b64_tr_b16 a[150:151], v231 offset:44032\n"
"v_mfma_f32_16x16x32_bf16 v[8:11], a[156:159], v[180:183], v[8:11]\n"
"ds_read_b128 a[116:119], v228 offset:14336\n"
"v_mfma_f32_16x16x32_bf16 v[12:15], a[152:155], v[184:187], v[12:15]\n"
"v_exp_f32 v199, v199\n"
"v_mfma_f32_16x16x32_bf16 v[12:15], a[156:159], v[188:191], v[12:15]\n"
"s_add_u32 m0, s6, 106496\n"
"ds_read_b64_tr_b16 a[152:153], v232 offset:25600\n"
"s_waitcnt lgkmcnt(3)\n"
"v_mfma_f32_16x16x32_bf16 v[16:19], a[160:163], v[160:163], v[16:19]\n"
"v_cvt_pk_bf16_f32 v139, v198, v199\n"
"ds_read_b64_tr_b16 a[154:155], v232 offset:31744\n"
"v_mfma_f32_16x16x32_bf16 v[16:19], a[164:167], v[164:167], v[16:19]\n"
"v_exp_f32 v200, v200\n"
"v_smfmac_f32_16x16x64_bf16 a[176:179], a[168:171], v[168:175], v233\n"
"buffer_load_dwordx4 v250, s[8:11], s88 offen offset:0 sc1 lds\n"
"v_mfma_f32_16x16x32_bf16 v[204:207], a[120:123], a[72:75], v[220:223]\n"
"s_add_u32 s88, s4, 163840\n"
"ds_read_b64_tr_b16 a[156:157], v232 offset:37888\n"
"v_mfma_f32_16x16x32_bf16 v[204:207], a[124:127], a[76:79], v[204:207]\n"
"s_add_u32 m0, s6, 122880\n"
"ds_read_b64_tr_b16 a[158:159], v232 offset:44032\n"
"v_mfma_f32_16x16x32_bf16 v[204:207], a[128:131], a[80:83], v[204:207]\n"
"v_exp_f32 v201, v201\n"
"v_mfma_f32_16x16x32_bf16 v[204:207], a[132:135], a[84:87], v[204:207]\n"
"buffer_load_dwordx4 v244, s[8:11], s88 offen offset:0 sc1 lds\n"
"v_mfma_f32_16x16x32_bf16 v[204:207], a[136:139], a[88:91], v[204:207]\n"
"v_cvt_pk_bf16_f32 v146, v200, v201\n"
"v_mfma_f32_16x16x32_bf16 v[204:207], a[140:143], a[92:95], v[204:207]\n"
"v_exp_f32 v202, v202\n"
"v_mfma_f32_16x16x32_bf16 v[20:23], a[160:163], v[168:171], v[20:23]\n"
"buffer_load_dwordx4 v244, s[8:11], s88 offen offset:1024 sc1 lds\n"
"v_mfma_f32_16x16x32_bf16 v[20:23], a[164:167], v[172:175], v[20:23]\n"
"v_mfma_f32_16x16x32_bf16 v[24:27], a[160:163], v[176:179], v[24:27]\n"
"v_exp_f32 v203, v203\n"
"v_mfma_f32_16x16x32_bf16 v[24:27], a[164:167], v[180:183], v[24:27]\n"
"v_mfma_f32_16x16x32_bf16 v[28:31], a[160:163], v[184:187], v[28:31]\n"
"v_cvt_pk_bf16_f32 v147, v202, v203\n"
"v_mfma_f32_16x16x32_bf16 v[28:31], a[164:167], v[188:191], v[28:31]\n"
"v_exp_f32 v204, v204\n"
"v_mfma_f32_16x16x32_bf16 v[192:195], a[96:99], a[0:3], v[208:211]\n"
"buffer_load_dwordx4 v244, s[8:11], s88 offen offset:2048 sc1 lds\n"
"v_mfma_f32_16x16x32_bf16 v[192:195], a[100:103], a[4:7], v[192:195]\n"
"ds_read_b64_tr_b16 a[160:161], v229 offset:26624\n"
"ds_read_b128 a[120:123], v227 offset:18432\n"
"v_mfma_f32_16x16x32_bf16 v[192:195], a[104:107], a[8:11], v[192:195]\n"
"v_exp_f32 v205, v205\n"
"v_mfma_f32_16x16x32_bf16 v[192:195], a[108:111], a[12:15], v[192:195]\n"
"buffer_load_dwordx4 v244, s[8:11], s88 offen offset:3072 sc1 lds\n"
"v_mfma_f32_16x16x32_bf16 v[192:195], a[112:115], a[16:19], v[192:195]\n"
"v_cvt_pk_bf16_f32 v154, v204, v205\n"
"s_waitcnt lgkmcnt(2)\n"
"v_mfma_f32_16x16x32_bf16 v[192:195], a[116:119], a[20:23], v[192:195]\n"
"v_exp_f32 v206, v206\n"
"v_mfma_f32_16x16x32_bf16 v[32:35], a[144:147], v[160:163], v[32:35]\n"
"ds_read_b64_tr_b16 a[162:163], v229 offset:32768\n"
"ds_read_b128 a[124:127], v228 offset:18432\n"
"v_mfma_f32_16x16x32_bf16 v[32:35], a[148:151], v[164:167], v[32:35]\n"
"ds_read_b64_tr_b16 a[164:165], v229 offset:38912\n"
"v_mfma_f32_16x16x32_bf16 v[36:39], a[144:147], v[168:171], v[36:39]\n"
"v_exp_f32 v207, v207\n"
"v_mfma_f32_16x16x32_bf16 v[36:39], a[148:151], v[172:175], v[36:39]\n"
"s_add_u32 m0, s6, 126976\n"
"ds_read_b64_tr_b16 a[166:167], v229 offset:45056\n"
"v_mfma_f32_16x16x32_bf16 v[40:43], a[144:147], v[176:179], v[40:43]\n"
"v_cvt_pk_bf16_f32 v155, v206, v207\n"
"v_mfma_f32_16x16x32_bf16 v[40:43], a[148:151], v[180:183], v[40:43]\n"
"v_exp_f32 v192, v192\n"
"v_mfma_f32_16x16x32_bf16 v[196:199], a[96:99], a[24:27], v[212:215]\n"
"buffer_load_dwordx4 v250, s[8:11], s88 offen offset:0 sc1 lds\n"
"v_mfma_f32_16x16x32_bf16 v[196:199], a[100:103], a[28:31], v[196:199]\n"
"s_mov_b32 s46, fused_code_end-fused_code_tail\n"
"ds_read_b128 a[128:131], v227 offset:19456\n"
"v_mfma_f32_16x16x32_bf16 v[196:199], a[104:107], a[32:35], v[196:199]\n"
"v_exp_f32 v193, v193\n"
"v_mfma_f32_16x16x32_bf16 v[196:199], a[108:111], a[36:39], v[196:199]\n"
"s_mov_b32 m0, s75\n"
"v_mfma_f32_16x16x32_bf16 v[196:199], a[112:115], a[40:43], v[196:199]\n"
"v_cvt_pk_bf16_f32 v132, v192, v193\n"
"v_mfma_f32_16x16x32_bf16 v[196:199], a[116:119], a[44:47], v[196:199]\n"
"v_exp_f32 v194, v194\n"
"v_mfma_f32_16x16x32_bf16 v[44:47], a[144:147], v[184:187], v[44:47]\n"
"buffer_load_dwordx4 v234, s[44:47], s43 offen lds\n"
"ds_read_b128 a[132:135], v228 offset:19456\n"
"v_mfma_f32_16x16x32_bf16 v[44:47], a[148:151], v[188:191], v[44:47]\n"
"s_mov_b64 s[44:45], s[8:9]\n"
"ds_read_b64_tr_b16 a[144:145], v230 offset:26624\n"
"v_mfma_f32_16x16x32_bf16 v[48:51], a[152:155], v[160:163], v[48:51]\n"
"v_exp_f32 v195, v195\n"
"v_mfma_f32_16x16x32_bf16 v[48:51], a[156:159], v[164:167], v[48:51]\n"
"s_mov_b32 s46, 20840448\n"
"ds_read_b64_tr_b16 a[146:147], v230 offset:32768\n"
"v_mfma_f32_16x16x32_bf16 v[52:55], a[152:155], v[168:171], v[52:55]\n"
"v_cvt_pk_bf16_f32 v133, v194, v195\n"
"ds_read_b64_tr_b16 a[148:149], v230 offset:38912\n"
"v_mfma_f32_16x16x32_bf16 v[52:55], a[156:159], v[172:175], v[52:55]\n"
"v_exp_f32 v196, v196\n"
"v_mfma_f32_16x16x32_bf16 v[200:203], a[96:99], a[48:51], v[216:219]\n"
"s_add_u32 s71, s43, 18874368\n"
"ds_read_b128 a[136:139], v227 offset:20480\n"
"v_mfma_f32_16x16x32_bf16 v[200:203], a[100:103], a[52:55], v[200:203]\n"
"buffer_load_dwordx4 v234, s[44:47], s71 offen lds\n"
"v_mfma_f32_16x16x32_bf16 v[200:203], a[104:107], a[56:59], v[200:203]\n"
"v_exp_f32 v197, v197\n"
"v_mfma_f32_16x16x32_bf16 v[200:203], a[108:111], a[60:63], v[200:203]\n"
"ds_read_b64_tr_b16 a[150:151], v230 offset:45056\n"
"v_mfma_f32_16x16x32_bf16 v[200:203], a[112:115], a[64:67], v[200:203]\n"
"v_cvt_pk_bf16_f32 v140, v196, v197\n"
"v_mfma_f32_16x16x32_bf16 v[200:203], a[116:119], a[68:71], v[200:203]\n"
"v_exp_f32 v198, v198\n"
"v_mfma_f32_16x16x32_bf16 v[56:59], a[152:155], v[176:179], v[56:59]\n"
"ds_read_b128 a[140:143], v228 offset:20480\n"
"v_mfma_f32_16x16x32_bf16 v[56:59], a[156:159], v[180:183], v[56:59]\n"
"v_mfma_f32_16x16x32_bf16 v[60:63], a[152:155], v[184:187], v[60:63]\n"
"v_exp_f32 v199, v199\n"
"v_mfma_f32_16x16x32_bf16 v[60:63], a[156:159], v[188:191], v[60:63]\n"
"ds_read_b64_tr_b16 a[152:153], v231 offset:26624\n"
"s_waitcnt lgkmcnt(2)\n"
"v_mfma_f32_16x16x32_bf16 v[64:67], a[160:163], v[160:163], v[64:67]\n"
"v_cvt_pk_bf16_f32 v141, v198, v199\n"
"v_mfma_f32_16x16x32_bf16 v[64:67], a[164:167], v[164:167], v[64:67]\n"
"v_exp_f32 v200, v200\n"
"v_smfmac_f32_16x16x64_bf16 a[180:183], a[168:171], v[176:183], v233\n"
"ds_read_b64_tr_b16 a[154:155], v231 offset:32768\n"
"v_mfma_f32_16x16x32_bf16 v[204:207], a[96:99], a[72:75], v[220:223]\n"
"ds_read_b64_tr_b16 a[156:157], v231 offset:38912\n"
"v_mfma_f32_16x16x32_bf16 v[204:207], a[100:103], a[76:79], v[204:207]\n"
"ds_read_b64_tr_b16 a[158:159], v231 offset:45056\n"
"v_mfma_f32_16x16x32_bf16 v[204:207], a[104:107], a[80:83], v[204:207]\n"
"v_exp_f32 v201, v201\n"
"v_mfma_f32_16x16x32_bf16 v[204:207], a[108:111], a[84:87], v[204:207]\n"
"v_mfma_f32_16x16x32_bf16 v[204:207], a[112:115], a[88:91], v[204:207]\n"
"v_cvt_pk_bf16_f32 v148, v200, v201\n"
"v_mfma_f32_16x16x32_bf16 v[204:207], a[116:119], a[92:95], v[204:207]\n"
"v_exp_f32 v202, v202\n"
"v_mfma_f32_16x16x32_bf16 v[68:71], a[160:163], v[168:171], v[68:71]\n"
"v_mfma_f32_16x16x32_bf16 v[68:71], a[164:167], v[172:175], v[68:71]\n"
"v_mfma_f32_16x16x32_bf16 v[72:75], a[160:163], v[176:179], v[72:75]\n"
"v_exp_f32 v203, v203\n"
"v_mfma_f32_16x16x32_bf16 v[72:75], a[164:167], v[180:183], v[72:75]\n"
"v_mfma_f32_16x16x32_bf16 v[76:79], a[160:163], v[184:187], v[76:79]\n"
"v_cvt_pk_bf16_f32 v149, v202, v203\n"
"v_mfma_f32_16x16x32_bf16 v[76:79], a[164:167], v[188:191], v[76:79]\n"
"v_exp_f32 v204, v204\n"
"v_mfma_f32_16x16x32_bf16 v[192:195], a[120:123], a[0:3], v[208:211]\n"
"ds_read_b64_tr_b16 a[160:161], v232 offset:26624\n"
"v_mfma_f32_16x16x32_bf16 v[192:195], a[124:127], a[4:7], v[192:195]\n"
"ds_read_b64_tr_b16 a[162:163], v232 offset:32768\n"
"v_mfma_f32_16x16x32_bf16 v[192:195], a[128:131], a[8:11], v[192:195]\n"
"v_exp_f32 v205, v205\n"
"v_mfma_f32_16x16x32_bf16 v[192:195], a[132:135], a[12:15], v[192:195]\n"
"ds_read_b64_tr_b16 a[164:165], v232 offset:38912\n"
"v_mfma_f32_16x16x32_bf16 v[192:195], a[136:139], a[16:19], v[192:195]\n"
"v_cvt_pk_bf16_f32 v156, v204, v205\n"
"s_waitcnt lgkmcnt(3)\n"
"v_mfma_f32_16x16x32_bf16 v[192:195], a[140:143], a[20:23], v[192:195]\n"
"v_exp_f32 v206, v206\n"
"v_mfma_f32_16x16x32_bf16 v[80:83], a[144:147], v[160:163], v[80:83]\n"
"ds_read_b64_tr_b16 a[166:167], v232 offset:45056\n"
"v_mfma_f32_16x16x32_bf16 v[80:83], a[148:151], v[164:167], v[80:83]\n"
"v_mfma_f32_16x16x32_bf16 v[84:87], a[144:147], v[168:171], v[84:87]\n"
"v_exp_f32 v207, v207\n"
"v_mfma_f32_16x16x32_bf16 v[84:87], a[148:151], v[172:175], v[84:87]\n"
"v_mfma_f32_16x16x32_bf16 v[88:91], a[144:147], v[176:179], v[88:91]\n"
"v_cvt_pk_bf16_f32 v157, v206, v207\n"
"v_mfma_f32_16x16x32_bf16 v[88:91], a[148:151], v[180:183], v[88:91]\n"
"v_exp_f32 v192, v192\n"
"v_mfma_f32_16x16x32_bf16 v[196:199], a[120:123], a[24:27], v[212:215]\n"
"v_mfma_f32_16x16x32_bf16 v[196:199], a[124:127], a[28:31], v[196:199]\n"
"v_mfma_f32_16x16x32_bf16 v[196:199], a[128:131], a[32:35], v[196:199]\n"
"v_exp_f32 v193, v193\n"
"v_mfma_f32_16x16x32_bf16 v[196:199], a[132:135], a[36:39], v[196:199]\n"
"v_mfma_f32_16x16x32_bf16 v[196:199], a[136:139], a[40:43], v[196:199]\n"
"v_cvt_pk_bf16_f32 v134, v192, v193\n"
"v_mfma_f32_16x16x32_bf16 v[196:199], a[140:143], a[44:47], v[196:199]\n"
"v_exp_f32 v194, v194\n"
"v_mfma_f32_16x16x32_bf16 v[92:95], a[144:147], v[184:187], v[92:95]\n"
"v_mfma_f32_16x16x32_bf16 v[92:95], a[148:151], v[188:191], v[92:95]\n"
"v_mfma_f32_16x16x32_bf16 v[96:99], a[152:155], v[160:163], v[96:99]\n"
"v_exp_f32 v195, v195\n"
"v_mfma_f32_16x16x32_bf16 v[96:99], a[156:159], v[164:167], v[96:99]\n"
"s_waitcnt vmcnt(2) lgkmcnt(0)\n"
"s_barrier\n"
"v_mfma_f32_16x16x32_bf16 v[100:103], a[152:155], v[168:171], v[100:103]\n"
"v_cvt_pk_bf16_f32 v135, v194, v195\n"
"ds_read_b128 a[96:99], v227 offset:24576\n"
"v_mfma_f32_16x16x32_bf16 v[100:103], a[156:159], v[172:175], v[100:103]\n"
"v_exp_f32 v196, v196\n"
"v_mfma_f32_16x16x32_bf16 v[200:203], a[120:123], a[48:51], v[216:219]\n"
"ds_read_b64_tr_b16 a[144:145], v229 offset:0\n"
"ds_read_b128 a[100:103], v228 offset:24576\n"
"v_mfma_f32_16x16x32_bf16 v[200:203], a[124:127], a[52:55], v[200:203]\n"
"ds_read_b64_tr_b16 a[146:147], v229 offset:6144\n"
"ds_read_b128 a[104:107], v227 offset:25600\n"
"v_mfma_f32_16x16x32_bf16 v[200:203], a[128:131], a[56:59], v[200:203]\n"
"v_exp_f32 v197, v197\n"
"v_mfma_f32_16x16x32_bf16 v[200:203], a[132:135], a[60:63], v[200:203]\n"
"ds_read_b64_tr_b16 a[148:149], v229 offset:12288\n"
"ds_read_b128 a[108:111], v228 offset:25600\n"
"v_mfma_f32_16x16x32_bf16 v[200:203], a[136:139], a[64:67], v[200:203]\n"
"v_cvt_pk_bf16_f32 v142, v196, v197\n"
"v_mfma_f32_16x16x32_bf16 v[200:203], a[140:143], a[68:71], v[200:203]\n"
"v_exp_f32 v198, v198\n"
"v_mfma_f32_16x16x32_bf16 v[104:107], a[152:155], v[176:179], v[104:107]\n"
"ds_read_b64_tr_b16 a[150:151], v229 offset:18432\n"
"v_mfma_f32_16x16x32_bf16 v[104:107], a[156:159], v[180:183], v[104:107]\n"
"ds_read_b128 a[112:115], v227 offset:26624\n"
"v_mfma_f32_16x16x32_bf16 v[108:111], a[152:155], v[184:187], v[108:111]\n"
"v_exp_f32 v199, v199\n"
"v_mfma_f32_16x16x32_bf16 v[108:111], a[156:159], v[188:191], v[108:111]\n"
"ds_read_b64_tr_b16 a[152:153], v230 offset:0\n"
"ds_read_b128 a[116:119], v228 offset:26624\n"
"v_mfma_f32_16x16x32_bf16 v[112:115], a[160:163], v[160:163], v[112:115]\n"
"v_cvt_pk_bf16_f32 v143, v198, v199\n"
"v_mfma_f32_16x16x32_bf16 v[112:115], a[164:167], v[164:167], v[112:115]\n"
"v_exp_f32 v200, v200\n"
"v_smfmac_f32_16x16x64_bf16 a[184:187], a[168:171], v[184:191], v233\n"
"ds_read_b64_tr_b16 a[154:155], v230 offset:6144\n"
"v_mfma_f32_16x16x32_bf16 v[204:207], a[120:123], a[72:75], v[220:223]\n"
"ds_read_b64_tr_b16 a[156:157], v230 offset:12288\n"
"v_mfma_f32_16x16x32_bf16 v[204:207], a[124:127], a[76:79], v[204:207]\n"
"ds_read_b64_tr_b16 a[158:159], v230 offset:18432\n"
"v_mfma_f32_16x16x32_bf16 v[204:207], a[128:131], a[80:83], v[204:207]\n"
"v_exp_f32 v201, v201\n"
"v_mfma_f32_16x16x32_bf16 v[204:207], a[132:135], a[84:87], v[204:207]\n"
"v_mfma_f32_16x16x32_bf16 v[204:207], a[136:139], a[88:91], v[204:207]\n"
"v_cvt_pk_bf16_f32 v150, v200, v201\n"
"v_mfma_f32_16x16x32_bf16 v[204:207], a[140:143], a[92:95], v[204:207]\n"
"v_exp_f32 v202, v202\n"
"v_mfma_f32_16x16x32_bf16 v[116:119], a[160:163], v[168:171], v[116:119]\n"
"v_mfma_f32_16x16x32_bf16 v[116:119], a[164:167], v[172:175], v[116:119]\n"
"v_mfma_f32_16x16x32_bf16 v[120:123], a[160:163], v[176:179], v[120:123]\n"
"v_exp_f32 v203, v203\n"
"v_mfma_f32_16x16x32_bf16 v[120:123], a[164:167], v[180:183], v[120:123]\n"
"v_mfma_f32_16x16x32_bf16 v[124:127], a[160:163], v[184:187], v[124:127]\n"
"v_cvt_pk_bf16_f32 v151, v202, v203\n"
"v_mfma_f32_16x16x32_bf16 v[124:127], a[164:167], v[188:191], v[124:127]\n"
"v_exp_f32 v204, v204\n"
"s_waitcnt lgkmcnt(0)\n"
"v_mfma_f32_16x16x32_bf16 v[192:195], a[96:99], a[0:3], v[208:211]\n"
"s_add_u32 s61, s61, 24576\n"
"ds_read_b64_tr_b16 a[160:161], v231 offset:0\n"
"ds_read_b128 a[120:123], v227 offset:30720\n"
"v_mfma_f32_16x16x32_bf16 v[192:195], a[100:103], a[4:7], v[192:195]\n"
"s_add_u32 s62, s62, 24576\n"
"ds_read_b64_tr_b16 a[162:163], v231 offset:6144\n"
"v_mfma_f32_16x16x32_bf16 v[192:195], a[104:107], a[8:11], v[192:195]\n"
"v_exp_f32 v205, v205\n"
"v_mfma_f32_16x16x32_bf16 v[192:195], a[108:111], a[12:15], v[192:195]\n"
"s_mov_b32 m0, s69\n"
"ds_read_b64_tr_b16 a[164:165], v231 offset:12288\n"
"v_mfma_f32_16x16x32_bf16 v[192:195], a[112:115], a[16:19], v[192:195]\n"
"v_cvt_pk_bf16_f32 v158, v204, v205\n"
"ds_read_b64_tr_b16 a[166:167], v231 offset:18432\n"
"v_mfma_f32_16x16x32_bf16 v[192:195], a[116:119], a[20:23], v[192:195]\n"
"v_exp_f32 v206, v206\n"
"v_mfma_f32_16x16x32_bf16 a[192:195], a[144:147], v[128:131], a[192:195]\n"
"buffer_load_dwordx4 v224, s[20:23], s61 offen offset:0 lds\n"
"v_mfma_f32_16x16x32_bf16 a[192:195], a[148:151], v[132:135], a[192:195]\n"
"buffer_load_dwordx4 v225, s[20:23], s61 offen offset:1024 lds\n"
"v_mfma_f32_16x16x32_bf16 a[196:199], a[144:147], v[136:139], a[196:199]\n"
"v_exp_f32 v207, v207\n"
"v_mfma_f32_16x16x32_bf16 a[196:199], a[148:151], v[140:143], a[196:199]\n"
"ds_read_b128 a[124:127], v228 offset:30720\n"
"v_mfma_f32_16x16x32_bf16 a[200:203], a[144:147], v[144:147], a[200:203]\n"
"v_cvt_pk_bf16_f32 v159, v206, v207\n"
"v_mfma_f32_16x16x32_bf16 a[200:203], a[148:151], v[148:151], a[200:203]\n"
"v_exp_f32 v192, v192\n"
"v_mfma_f32_16x16x32_bf16 v[196:199], a[96:99], a[24:27], v[212:215]\n"
"buffer_load_dwordx4 v226, s[20:23], s61 offen offset:2048 lds\n"
"v_mfma_f32_16x16x32_bf16 v[196:199], a[100:103], a[28:31], v[196:199]\n"
"ds_read_b128 a[128:131], v227 offset:31744\n"
"v_mfma_f32_16x16x32_bf16 v[196:199], a[104:107], a[32:35], v[196:199]\n"
"v_exp_f32 v193, v193\n"
"v_mfma_f32_16x16x32_bf16 v[196:199], a[108:111], a[36:39], v[196:199]\n"
"v_mfma_f32_16x16x32_bf16 v[196:199], a[112:115], a[40:43], v[196:199]\n"
"v_cvt_pk_bf16_f32 v160, v192, v193\n"
"v_mfma_f32_16x16x32_bf16 v[196:199], a[116:119], a[44:47], v[196:199]\n"
"v_exp_f32 v194, v194\n"
"v_mfma_f32_16x16x32_bf16 a[204:207], a[144:147], v[152:155], a[204:207]\n"
"s_mov_b32 m0, s70\n"
"ds_read_b128 a[132:135], v228 offset:31744\n"
"v_mfma_f32_16x16x32_bf16 a[204:207], a[148:151], v[156:159], a[204:207]\n"
"buffer_load_dwordx4 v224, s[20:23], s62 offen offset:0 lds\n"
"v_mfma_f32_16x16x32_bf16 a[208:211], a[152:155], v[128:131], a[208:211]\n"
"v_exp_f32 v195, v195\n"
"v_mfma_f32_16x16x32_bf16 a[208:211], a[156:159], v[132:135], a[208:211]\n"
"buffer_load_dwordx4 v225, s[20:23], s62 offen offset:1024 lds\n"
"v_mfma_f32_16x16x32_bf16 a[212:215], a[152:155], v[136:139], a[212:215]\n"
"v_cvt_pk_bf16_f32 v161, v194, v195\n"
"ds_read_b64_tr_b16 a[144:145], v232 offset:0\n"
"v_mfma_f32_16x16x32_bf16 a[212:215], a[156:159], v[140:143], a[212:215]\n"
"v_exp_f32 v196, v196\n"
"v_mfma_f32_16x16x32_bf16 v[200:203], a[96:99], a[48:51], v[216:219]\n"
"ds_read_b64_tr_b16 a[146:147], v232 offset:6144\n"
"ds_read_b128 a[136:139], v227 offset:32768\n"
"v_mfma_f32_16x16x32_bf16 v[200:203], a[100:103], a[52:55], v[200:203]\n"
"ds_read_b64_tr_b16 a[148:149], v232 offset:12288\n"
"v_mfma_f32_16x16x32_bf16 v[200:203], a[104:107], a[56:59], v[200:203]\n"
"v_exp_f32 v197, v197\n"
"v_mfma_f32_16x16x32_bf16 v[200:203], a[108:111], a[60:63], v[200:203]\n"
"buffer_load_dwordx4 v226, s[20:23], s62 offen offset:2048 lds\n"
"v_mfma_f32_16x16x32_bf16 v[200:203], a[112:115], a[64:67], v[200:203]\n"
"v_cvt_pk_bf16_f32 v168, v196, v197\n"
"v_mfma_f32_16x16x32_bf16 v[200:203], a[116:119], a[68:71], v[200:203]\n"
"v_exp_f32 v198, v198\n"
"v_mfma_f32_16x16x32_bf16 a[216:219], a[152:155], v[144:147], a[216:219]\n"
"ds_read_b64_tr_b16 a[150:151], v232 offset:18432\n"
"ds_read_b128 a[140:143], v228 offset:32768\n"
"v_mfma_f32_16x16x32_bf16 a[216:219], a[156:159], v[148:151], a[216:219]\n"
"buffer_load_dword v249, v239, s[8:11], 0 offen offset:384 sc1\n"
"v_mfma_f32_16x16x32_bf16 a[220:223], a[152:155], v[152:155], a[220:223]\n"
"v_exp_f32 v199, v199\n"
"v_mfma_f32_16x16x32_bf16 a[220:223], a[156:159], v[156:159], a[220:223]\n"
"ds_read_b64_tr_b16 a[152:153], v229 offset:1024\n"
"s_waitcnt lgkmcnt(3)\n"
"v_mfma_f32_16x16x32_bf16 a[224:227], a[160:163], v[128:131], a[224:227]\n"
"v_cvt_pk_bf16_f32 v169, v198, v199\n"
"ds_read_b64_tr_b16 a[154:155], v229 offset:7168\n"
"v_mfma_f32_16x16x32_bf16 a[224:227], a[164:167], v[132:135], a[224:227]\n"
"v_exp_f32 v200, v200\n"
"v_smfmac_f32_16x16x64_bf16 a[172:175], a[168:171], v[128:135], v233\n"
"s_add_u32 s88, s4, 327680\n"
"ds_read_b64_tr_b16 a[156:157], v229 offset:13312\n"
"v_mfma_f32_16x16x32_bf16 v[204:207], a[96:99], a[72:75], v[220:223]\n"
"s_add_u32 m0, s6, 143360\n"
"v_mfma_f32_16x16x32_bf16 v[204:207], a[100:103], a[76:79], v[204:207]\n"
"buffer_load_dwordx4 v244, s[8:11], s88 offen offset:0 sc1 lds\n"
"v_mfma_f32_16x16x32_bf16 v[204:207], a[104:107], a[80:83], v[204:207]\n"
"v_exp_f32 v201, v201\n"
"v_mfma_f32_16x16x32_bf16 v[204:207], a[108:111], a[84:87], v[204:207]\n"
"ds_read_b64_tr_b16 a[158:159], v229 offset:19456\n"
"v_mfma_f32_16x16x32_bf16 v[204:207], a[112:115], a[88:91], v[204:207]\n"
"v_cvt_pk_bf16_f32 v176, v200, v201\n"
"v_mfma_f32_16x16x32_bf16 v[204:207], a[116:119], a[92:95], v[204:207]\n"
"v_exp_f32 v202, v202\n"
"v_mfma_f32_16x16x32_bf16 a[228:231], a[160:163], v[136:139], a[228:231]\n"
"buffer_load_dwordx4 v244, s[8:11], s88 offen offset:1024 sc1 lds\n"
"v_mfma_f32_16x16x32_bf16 a[228:231], a[164:167], v[140:143], a[228:231]\n"
"v_mfma_f32_16x16x32_bf16 a[232:235], a[160:163], v[144:147], a[232:235]\n"
"v_exp_f32 v203, v203\n"
"v_mfma_f32_16x16x32_bf16 a[232:235], a[164:167], v[148:151], a[232:235]\n"
"buffer_load_dwordx4 v244, s[8:11], s88 offen offset:2048 sc1 lds\n"
"v_mfma_f32_16x16x32_bf16 a[236:239], a[160:163], v[152:155], a[236:239]\n"
"v_cvt_pk_bf16_f32 v177, v202, v203\n"
"v_mfma_f32_16x16x32_bf16 a[236:239], a[164:167], v[156:159], a[236:239]\n"
"v_exp_f32 v204, v204\n"
"v_mfma_f32_16x16x32_bf16 v[192:195], a[120:123], a[0:3], v[208:211]\n"
"ds_read_b64_tr_b16 a[160:161], v230 offset:1024\n"
"ds_read_b128 a[96:99], v227 offset:36864\n"
"v_mfma_f32_16x16x32_bf16 v[192:195], a[124:127], a[4:7], v[192:195]\n"
"ds_read_b64_tr_b16 a[162:163], v230 offset:7168\n"
"v_mfma_f32_16x16x32_bf16 v[192:195], a[128:131], a[8:11], v[192:195]\n"
"v_exp_f32 v205, v205\n"
"v_mfma_f32_16x16x32_bf16 v[192:195], a[132:135], a[12:15], v[192:195]\n"
"buffer_load_dwordx4 v244, s[8:11], s88 offen offset:3072 sc1 lds\n"
"v_mfma_f32_16x16x32_bf16 v[192:195], a[136:139], a[16:19], v[192:195]\n"
"v_cvt_pk_bf16_f32 v184, v204, v205\n"
"s_waitcnt lgkmcnt(3)\n"
"v_mfma_f32_16x16x32_bf16 v[192:195], a[140:143], a[20:23], v[192:195]\n"
"v_exp_f32 v206, v206\n"
"v_mfma_f32_16x16x32_bf16 a[240:243], a[144:147], v[128:131], a[240:243]\n"
"ds_read_b64_tr_b16 a[164:165], v230 offset:13312\n"
"ds_read_b128 a[100:103], v228 offset:36864\n"
"v_mfma_f32_16x16x32_bf16 a[240:243], a[148:151], v[132:135], a[240:243]\n"
"s_add_u32 m0, s6, 147456\n"
"ds_read_b64_tr_b16 a[166:167], v230 offset:19456\n"
"v_mfma_f32_16x16x32_bf16 a[244:247], a[144:147], v[136:139], a[244:247]\n"
"v_exp_f32 v207, v207\n"
"v_mfma_f32_16x16x32_bf16 a[244:247], a[148:151], v[140:143], a[244:247]\n"
"buffer_load_dwordx4 v250, s[8:11], s88 offen offset:0 sc1 lds\n"
"v_mfma_f32_16x16x32_bf16 a[248:251], a[144:147], v[144:147], a[248:251]\n"
"v_cvt_pk_bf16_f32 v185, v206, v207\n"
"v_mfma_f32_16x16x32_bf16 a[248:251], a[148:151], v[148:151], a[248:251]\n"
"v_exp_f32 v192, v192\n"
"v_mfma_f32_16x16x32_bf16 v[196:199], a[120:123], a[24:27], v[212:215]\n"
"s_add_u32 s88, s4, 491520\n"
"ds_read_b128 a[104:107], v227 offset:37888\n"
"v_mfma_f32_16x16x32_bf16 v[196:199], a[124:127], a[28:31], v[196:199]\n"
"s_add_u32 m0, s6, 0\n"
"v_mfma_f32_16x16x32_bf16 v[196:199], a[128:131], a[32:35], v[196:199]\n"
"v_exp_f32 v193, v193\n"
"v_mfma_f32_16x16x32_bf16 v[196:199], a[132:135], a[36:39], v[196:199]\n"
"buffer_load_dwordx4 v244, s[8:11], s88 offen offset:0 sc1 lds\n"
"v_mfma_f32_16x16x32_bf16 v[196:199], a[136:139], a[40:43], v[196:199]\n"
"v_cvt_pk_bf16_f32 v162, v192, v193\n"
"v_mfma_f32_16x16x32_bf16 v[196:199], a[140:143], a[44:47], v[196:199]\n"
"v_exp_f32 v194, v194\n"
"v_mfma_f32_16x16x32_bf16 a[252:255], a[144:147], v[152:155], a[252:255]\n"
"buffer_load_dwordx4 v244, s[8:11], s88 offen offset:1024 sc1 lds\n"
"v_mfma_f32_16x16x32_bf16 a[252:255], a[148:151], v[156:159], a[252:255]\n"
"ds_read_b64_tr_b16 a[144:145], v231 offset:1024\n"
"ds_read_b128 a[108:111], v228 offset:37888\n"
"v_mfma_f32_16x16x32_bf16 v[0:3], a[152:155], v[128:131], v[0:3]\n"
"v_exp_f32 v195, v195\n"
"v_mfma_f32_16x16x32_bf16 v[0:3], a[156:159], v[132:135], v[0:3]\n"
"ds_read_b64_tr_b16 a[146:147], v231 offset:7168\n"
"v_mfma_f32_16x16x32_bf16 v[4:7], a[152:155], v[136:139], v[4:7]\n"
"v_cvt_pk_bf16_f32 v163, v194, v195\n"
"v_mfma_f32_16x16x32_bf16 v[4:7], a[156:159], v[140:143], v[4:7]\n"
"v_exp_f32 v196, v196\n"
"v_mfma_f32_16x16x32_bf16 v[200:203], a[120:123], a[48:51], v[216:219]\n"
"buffer_load_dwordx4 v244, s[8:11], s88 offen offset:2048 sc1 lds\n"
"v_mfma_f32_16x16x32_bf16 v[200:203], a[124:127], a[52:55], v[200:203]\n"
"ds_read_b64_tr_b16 a[148:149], v231 offset:13312\n"
"ds_read_b128 a[112:115], v227 offset:38912\n"
"v_mfma_f32_16x16x32_bf16 v[200:203], a[128:131], a[56:59], v[200:203]\n"
"v_exp_f32 v197, v197\n"
"v_mfma_f32_16x16x32_bf16 v[200:203], a[132:135], a[60:63], v[200:203]\n"
"buffer_load_dwordx4 v244, s[8:11], s88 offen offset:3072 sc1 lds\n"
"v_mfma_f32_16x16x32_bf16 v[200:203], a[136:139], a[64:67], v[200:203]\n"
"v_cvt_pk_bf16_f32 v170, v196, v197\n"
"v_mfma_f32_16x16x32_bf16 v[200:203], a[140:143], a[68:71], v[200:203]\n"
"v_exp_f32 v198, v198\n"
"v_mfma_f32_16x16x32_bf16 v[8:11], a[152:155], v[144:147], v[8:11]\n"
"ds_read_b64_tr_b16 a[150:151], v231 offset:19456\n"
"v_mfma_f32_16x16x32_bf16 v[8:11], a[156:159], v[148:151], v[8:11]\n"
"ds_read_b128 a[116:119], v228 offset:38912\n"
"v_mfma_f32_16x16x32_bf16 v[12:15], a[152:155], v[152:155], v[12:15]\n"
"v_exp_f32 v199, v199\n"
"v_mfma_f32_16x16x32_bf16 v[12:15], a[156:159], v[156:159], v[12:15]\n"
"s_add_u32 m0, s6, 4096\n"
"ds_read_b64_tr_b16 a[152:153], v232 offset:1024\n"
"s_waitcnt lgkmcnt(3)\n"
"v_mfma_f32_16x16x32_bf16 v[16:19], a[160:163], v[128:131], v[16:19]\n"
"v_cvt_pk_bf16_f32 v171, v198, v199\n"
"ds_read_b64_tr_b16 a[154:155], v232 offset:7168\n"
"v_mfma_f32_16x16x32_bf16 v[16:19], a[164:167], v[132:135], v[16:19]\n"
"v_exp_f32 v200, v200\n"
"v_smfmac_f32_16x16x64_bf16 a[176:179], a[168:171], v[136:143], v233\n"
"buffer_load_dwordx4 v250, s[8:11], s88 offen offset:0 sc1 lds\n"
"v_mfma_f32_16x16x32_bf16 v[204:207], a[120:123], a[72:75], v[220:223]\n"
"s_add_u32 s71, s71, 1048576\n"
"ds_read_b64_tr_b16 a[156:157], v232 offset:13312\n"
"v_mfma_f32_16x16x32_bf16 v[204:207], a[124:127], a[76:79], v[204:207]\n"
"s_mov_b32 m0, s75\n"
"ds_read_b64_tr_b16 a[158:159], v232 offset:19456\n"
"v_mfma_f32_16x16x32_bf16 v[204:207], a[128:131], a[80:83], v[204:207]\n"
"v_exp_f32 v201, v201\n"
"v_mfma_f32_16x16x32_bf16 v[204:207], a[132:135], a[84:87], v[204:207]\n"
"buffer_load_dwordx4 v234, s[44:47], s71 offen lds\n"
"v_mfma_f32_16x16x32_bf16 v[204:207], a[136:139], a[88:91], v[204:207]\n"
"v_cvt_pk_bf16_f32 v178, v200, v201\n"
"v_mfma_f32_16x16x32_bf16 v[204:207], a[140:143], a[92:95], v[204:207]\n"
"v_exp_f32 v202, v202\n"
"v_mfma_f32_16x16x32_bf16 v[20:23], a[160:163], v[136:139], v[20:23]\n"
"s_mov_b32 s46, 0\n"
"v_mfma_f32_16x16x32_bf16 v[20:23], a[164:167], v[140:143], v[20:23]\n"
"v_mfma_f32_16x16x32_bf16 v[24:27], a[160:163], v[144:147], v[24:27]\n"
"v_exp_f32 v203, v203\n"
"v_mfma_f32_16x16x32_bf16 v[24:27], a[164:167], v[148:151], v[24:27]\n"
"v_mfma_f32_16x16x32_bf16 v[28:31], a[160:163], v[152:155], v[28:31]\n"
"v_cvt_pk_bf16_f32 v179, v202, v203\n"
"v_mfma_f32_16x16x32_bf16 v[28:31], a[164:167], v[156:159], v[28:31]\n"
"v_exp_f32 v204, v204\n"
"v_mfma_f32_16x16x32_bf16 v[192:195], a[96:99], a[0:3], v[208:211]\n"
"ds_read_b64_tr_b16 a[160:161], v229 offset:2048\n"
"ds_read_b128 a[120:123], v227 offset:43008\n"
"v_mfma_f32_16x16x32_bf16 v[192:195], a[100:103], a[4:7], v[192:195]\n"
"ds_read_b64_tr_b16 a[162:163], v229 offset:8192\n"
"v_mfma_f32_16x16x32_bf16 v[192:195], a[104:107], a[8:11], v[192:195]\n"
"v_exp_f32 v205, v205\n"
"v_mfma_f32_16x16x32_bf16 v[192:195], a[108:111], a[12:15], v[192:195]\n"
"ds_read_b64_tr_b16 a[164:165], v229 offset:14336\n"
"v_mfma_f32_16x16x32_bf16 v[192:195], a[112:115], a[16:19], v[192:195]\n"
"v_cvt_pk_bf16_f32 v186, v204, v205\n"
"s_waitcnt lgkmcnt(4)\n"
"v_mfma_f32_16x16x32_bf16 v[192:195], a[116:119], a[20:23], v[192:195]\n"
"v_exp_f32 v206, v206\n"
"v_mfma_f32_16x16x32_bf16 v[32:35], a[144:147], v[128:131], v[32:35]\n"
"ds_read_b64_tr_b16 a[166:167], v229 offset:20480\n"
"v_mfma_f32_16x16x32_bf16 v[32:35], a[148:151], v[132:135], v[32:35]\n"
"ds_read_b128 a[124:127], v228 offset:43008\n"
"v_mfma_f32_16x16x32_bf16 v[36:39], a[144:147], v[136:139], v[36:39]\n"
"v_exp_f32 v207, v207\n"
"v_mfma_f32_16x16x32_bf16 v[36:39], a[148:151], v[140:143], v[36:39]\n"
"v_mfma_f32_16x16x32_bf16 v[40:43], a[144:147], v[144:147], v[40:43]\n"
"v_cvt_pk_bf16_f32 v187, v206, v207\n"
"v_mfma_f32_16x16x32_bf16 v[40:43], a[148:151], v[148:151], v[40:43]\n"
"v_exp_f32 v192, v192\n"
"v_mfma_f32_16x16x32_bf16 v[196:199], a[96:99], a[24:27], v[212:215]\n"
"ds_read_b128 a[128:131], v227 offset:44032\n"
"v_mfma_f32_16x16x32_bf16 v[196:199], a[100:103], a[28:31], v[196:199]\n"
"v_mfma_f32_16x16x32_bf16 v[196:199], a[104:107], a[32:35], v[196:199]\n"
"v_exp_f32 v193, v193\n"
"v_mfma_f32_16x16x32_bf16 v[196:199], a[108:111], a[36:39], v[196:199]\n"
"v_mfma_f32_16x16x32_bf16 v[196:199], a[112:115], a[40:43], v[196:199]\n"
"v_cvt_pk_bf16_f32 v164, v192, v193\n"
"v_mfma_f32_16x16x32_bf16 v[196:199], a[116:119], a[44:47], v[196:199]\n"
"v_exp_f32 v194, v194\n"
"v_mfma_f32_16x16x32_bf16 v[44:47], a[144:147], v[152:155], v[44:47]\n"
"ds_read_b128 a[132:135], v228 offset:44032\n"
"v_mfma_f32_16x16x32_bf16 v[44:47], a[148:151], v[156:159], v[44:47]\n"
"ds_read_b64_tr_b16 a[144:145], v230 offset:2048\n"
"v_mfma_f32_16x16x32_bf16 v[48:51], a[152:155], v[128:131], v[48:51]\n"
"v_exp_f32 v195, v195\n"
"v_mfma_f32_16x16x32_bf16 v[48:51], a[156:159], v[132:135], v[48:51]\n"
"ds_read_b64_tr_b16 a[146:147], v230 offset:8192\n"
"v_mfma_f32_16x16x32_bf16 v[52:55], a[152:155], v[136:139], v[52:55]\n"
"v_cvt_pk_bf16_f32 v165, v194, v195\n"
"v_mfma_f32_16x16x32_bf16 v[52:55], a[156:159], v[140:143], v[52:55]\n"
"v_exp_f32 v196, v196\n"
"v_mfma_f32_16x16x32_bf16 v[200:203], a[96:99], a[48:51], v[216:219]\n"
"ds_read_b64_tr_b16 a[148:149], v230 offset:14336\n"
"v_mfma_f32_16x16x32_bf16 v[200:203], a[100:103], a[52:55], v[200:203]\n"
"ds_read_b64_tr_b16 a[150:151], v230 offset:20480\n"
"v_mfma_f32_16x16x32_bf16 v[200:203], a[104:107], a[56:59], v[200:203]\n"
"v_exp_f32 v197, v197\n"
"v_mfma_f32_16x16x32_bf16 v[200:203], a[108:111], a[60:63], v[200:203]\n"
"ds_read_b128 a[136:139], v227 offset:45056\n"
"v_mfma_f32_16x16x32_bf16 v[200:203], a[112:115], a[64:67], v[200:203]\n"
"v_cvt_pk_bf16_f32 v172, v196, v197\n"
"v_mfma_f32_16x16x32_bf16 v[200:203], a[116:119], a[68:71], v[200:203]\n"
"v_exp_f32 v198, v198\n"
"v_mfma_f32_16x16x32_bf16 v[56:59], a[152:155], v[144:147], v[56:59]\n"
"ds_read_b128 a[140:143], v228 offset:45056\n"
"v_mfma_f32_16x16x32_bf16 v[56:59], a[156:159], v[148:151], v[56:59]\n"
"v_mfma_f32_16x16x32_bf16 v[60:63], a[152:155], v[152:155], v[60:63]\n"
"v_exp_f32 v199, v199\n"
"v_mfma_f32_16x16x32_bf16 v[60:63], a[156:159], v[156:159], v[60:63]\n"
"ds_read_b64_tr_b16 a[152:153], v231 offset:2048\n"
"s_waitcnt lgkmcnt(2)\n"
"v_mfma_f32_16x16x32_bf16 v[64:67], a[160:163], v[128:131], v[64:67]\n"
"v_cvt_pk_bf16_f32 v173, v198, v199\n"
"v_mfma_f32_16x16x32_bf16 v[64:67], a[164:167], v[132:135], v[64:67]\n"
"v_exp_f32 v200, v200\n"
"v_smfmac_f32_16x16x64_bf16 a[180:183], a[168:171], v[144:151], v233\n"
"ds_read_b64_tr_b16 a[154:155], v231 offset:8192\n"
"v_mfma_f32_16x16x32_bf16 v[204:207], a[96:99], a[72:75], v[220:223]\n"
"ds_read_b64_tr_b16 a[156:157], v231 offset:14336\n"
"v_mfma_f32_16x16x32_bf16 v[204:207], a[100:103], a[76:79], v[204:207]\n"
"ds_read_b64_tr_b16 a[158:159], v231 offset:20480\n"
"v_mfma_f32_16x16x32_bf16 v[204:207], a[104:107], a[80:83], v[204:207]\n"
"v_exp_f32 v201, v201\n"
"v_mfma_f32_16x16x32_bf16 v[204:207], a[108:111], a[84:87], v[204:207]\n"
"v_mfma_f32_16x16x32_bf16 v[204:207], a[112:115], a[88:91], v[204:207]\n"
"v_cvt_pk_bf16_f32 v180, v200, v201\n"
"v_mfma_f32_16x16x32_bf16 v[204:207], a[116:119], a[92:95], v[204:207]\n"
"v_exp_f32 v202, v202\n"
"v_mfma_f32_16x16x32_bf16 v[68:71], a[160:163], v[136:139], v[68:71]\n"
"v_mfma_f32_16x16x32_bf16 v[68:71], a[164:167], v[140:143], v[68:71]\n"
"v_mfma_f32_16x16x32_bf16 v[72:75], a[160:163], v[144:147], v[72:75]\n"
"v_exp_f32 v203, v203\n"
"v_mfma_f32_16x16x32_bf16 v[72:75], a[164:167], v[148:151], v[72:75]\n"
"v_mfma_f32_16x16x32_bf16 v[76:79], a[160:163], v[152:155], v[76:79]\n"
"v_cvt_pk_bf16_f32 v181, v202, v203\n"
"v_mfma_f32_16x16x32_bf16 v[76:79], a[164:167], v[156:159], v[76:79]\n"
"v_exp_f32 v204, v204\n"
"v_mfma_f32_16x16x32_bf16 v[192:195], a[120:123], a[0:3], v[208:211]\n"
"ds_read_b64_tr_b16 a[160:161], v232 offset:2048\n"
"v_mfma_f32_16x16x32_bf16 v[192:195], a[124:127], a[4:7], v[192:195]\n"
"ds_read_b64_tr_b16 a[162:163], v232 offset:8192\n"
"v_mfma_f32_16x16x32_bf16 v[192:195], a[128:131], a[8:11], v[192:195]\n"
"v_exp_f32 v205, v205\n"
"v_mfma_f32_16x16x32_bf16 v[192:195], a[132:135], a[12:15], v[192:195]\n"
"ds_read_b64_tr_b16 a[164:165], v232 offset:14336\n"
"v_mfma_f32_16x16x32_bf16 v[192:195], a[136:139], a[16:19], v[192:195]\n"
"v_cvt_pk_bf16_f32 v188, v204, v205\n"
"s_waitcnt lgkmcnt(3)\n"
"v_mfma_f32_16x16x32_bf16 v[192:195], a[140:143], a[20:23], v[192:195]\n"
"v_exp_f32 v206, v206\n"
"v_mfma_f32_16x16x32_bf16 v[80:83], a[144:147], v[128:131], v[80:83]\n"
"ds_read_b64_tr_b16 a[166:167], v232 offset:20480\n"
"v_mfma_f32_16x16x32_bf16 v[80:83], a[148:151], v[132:135], v[80:83]\n"
"v_mfma_f32_16x16x32_bf16 v[84:87], a[144:147], v[136:139], v[84:87]\n"
"v_exp_f32 v207, v207\n"
"v_mfma_f32_16x16x32_bf16 v[84:87], a[148:151], v[140:143], v[84:87]\n"
"v_mfma_f32_16x16x32_bf16 v[88:91], a[144:147], v[144:147], v[88:91]\n"
"v_cvt_pk_bf16_f32 v189, v206, v207\n"
"v_mfma_f32_16x16x32_bf16 v[88:91], a[148:151], v[148:151], v[88:91]\n"
"v_exp_f32 v192, v192\n"
"v_mfma_f32_16x16x32_bf16 v[196:199], a[120:123], a[24:27], v[212:215]\n"
"v_mfma_f32_16x16x32_bf16 v[196:199], a[124:127], a[28:31], v[196:199]\n"
"v_mfma_f32_16x16x32_bf16 v[196:199], a[128:131], a[32:35], v[196:199]\n"
"v_exp_f32 v193, v193\n"
"v_mfma_f32_16x16x32_bf16 v[196:199], a[132:135], a[36:39], v[196:199]\n"
"v_mfma_f32_16x16x32_bf16 v[196:199], a[136:139], a[40:43], v[196:199]\n"
"v_cvt_pk_bf16_f32 v166, v192, v193\n"
"v_mfma_f32_16x16x32_bf16 v[196:199], a[140:143], a[44:47], v[196:199]\n"
"v_exp_f32 v194, v194\n"
"v_mfma_f32_16x16x32_bf16 v[92:95], a[144:147], v[152:155], v[92:95]\n"
"v_mfma_f32_16x16x32_bf16 v[92:95], a[148:151], v[156:159], v[92:95]\n"
"v_mfma_f32_16x16x32_bf16 v[96:99], a[152:155], v[128:131], v[96:99]\n"
"v_exp_f32 v195, v195\n"
"v_mfma_f32_16x16x32_bf16 v[96:99], a[156:159], v[132:135], v[96:99]\n"
"s_waitcnt vmcnt(1) lgkmcnt(0)\n"
"s_barrier\n"
"v_mfma_f32_16x16x32_bf16 v[100:103], a[152:155], v[136:139], v[100:103]\n"
"v_cvt_pk_bf16_f32 v167, v194, v195\n"
"v_mfma_f32_16x16x32_bf16 v[100:103], a[156:159], v[140:143], v[100:103]\n"
"v_exp_f32 v196, v196\n"
"v_mfma_f32_16x16x32_bf16 v[200:203], a[120:123], a[48:51], v[216:219]\n"
"ds_read_b64_tr_b16 a[144:145], v229 offset:24576\n"
"v_mfma_f32_16x16x32_bf16 v[200:203], a[124:127], a[52:55], v[200:203]\n"
"ds_read_b64_tr_b16 a[146:147], v229 offset:30720\n"
"v_mfma_f32_16x16x32_bf16 v[200:203], a[128:131], a[56:59], v[200:203]\n"
"v_exp_f32 v197, v197\n"
"v_mfma_f32_16x16x32_bf16 v[200:203], a[132:135], a[60:63], v[200:203]\n"
"ds_read_b64_tr_b16 a[148:149], v229 offset:36864\n"
"v_mfma_f32_16x16x32_bf16 v[200:203], a[136:139], a[64:67], v[200:203]\n"
"v_cvt_pk_bf16_f32 v174, v196, v197\n"
"v_mfma_f32_16x16x32_bf16 v[200:203], a[140:143], a[68:71], v[200:203]\n"
"v_exp_f32 v198, v198\n"
"v_mfma_f32_16x16x32_bf16 v[104:107], a[152:155], v[144:147], v[104:107]\n"
"ds_read_b64_tr_b16 a[150:151], v229 offset:43008\n"
"v_mfma_f32_16x16x32_bf16 v[104:107], a[156:159], v[148:151], v[104:107]\n"
"v_mfma_f32_16x16x32_bf16 v[108:111], a[152:155], v[152:155], v[108:111]\n"
"v_exp_f32 v199, v199\n"
"v_mfma_f32_16x16x32_bf16 v[108:111], a[156:159], v[156:159], v[108:111]\n"
"ds_read_b64_tr_b16 a[152:153], v230 offset:24576\n"
"v_mfma_f32_16x16x32_bf16 v[112:115], a[160:163], v[128:131], v[112:115]\n"
"v_cvt_pk_bf16_f32 v175, v198, v199\n"
"v_mfma_f32_16x16x32_bf16 v[112:115], a[164:167], v[132:135], v[112:115]\n"
"v_exp_f32 v200, v200\n"
"v_smfmac_f32_16x16x64_bf16 a[184:187], a[168:171], v[152:159], v233\n"
"ds_read_b64_tr_b16 a[154:155], v230 offset:30720\n"
"v_mfma_f32_16x16x32_bf16 v[204:207], a[120:123], a[72:75], v[220:223]\n"
"ds_read_b64_tr_b16 a[156:157], v230 offset:36864\n"
"v_mfma_f32_16x16x32_bf16 v[204:207], a[124:127], a[76:79], v[204:207]\n"
"ds_read_b64_tr_b16 a[158:159], v230 offset:43008\n"
"v_mfma_f32_16x16x32_bf16 v[204:207], a[128:131], a[80:83], v[204:207]\n"
"v_exp_f32 v201, v201\n"
"v_mfma_f32_16x16x32_bf16 v[204:207], a[132:135], a[84:87], v[204:207]\n"
"v_mfma_f32_16x16x32_bf16 v[204:207], a[136:139], a[88:91], v[204:207]\n"
"v_cvt_pk_bf16_f32 v182, v200, v201\n"
"v_mfma_f32_16x16x32_bf16 v[204:207], a[140:143], a[92:95], v[204:207]\n"
"v_exp_f32 v202, v202\n"
"v_mfma_f32_16x16x32_bf16 v[116:119], a[160:163], v[136:139], v[116:119]\n"
"v_mfma_f32_16x16x32_bf16 v[116:119], a[164:167], v[140:143], v[116:119]\n"
"v_mfma_f32_16x16x32_bf16 v[120:123], a[160:163], v[144:147], v[120:123]\n"
"v_exp_f32 v203, v203\n"
"v_mfma_f32_16x16x32_bf16 v[120:123], a[164:167], v[148:151], v[120:123]\n"
"v_mfma_f32_16x16x32_bf16 v[124:127], a[160:163], v[152:155], v[124:127]\n"
"v_cvt_pk_bf16_f32 v183, v202, v203\n"
"v_mfma_f32_16x16x32_bf16 v[124:127], a[164:167], v[156:159], v[124:127]\n"
"v_exp_f32 v204, v204\n"
"s_waitcnt lgkmcnt(0)\n"
"v_mfma_f32_16x16x32_bf16 a[192:195], a[144:147], v[160:163], a[192:195]\n"
"v_exp_f32 v205, v205\n"
"v_mfma_f32_16x16x32_bf16 a[192:195], a[148:151], v[164:167], a[192:195]\n"
"v_exp_f32 v206, v206\n"
"v_mfma_f32_16x16x32_bf16 a[196:199], a[144:147], v[168:171], a[196:199]\n"
"v_exp_f32 v207, v207\n"
"v_mfma_f32_16x16x32_bf16 a[196:199], a[148:151], v[172:175], a[196:199]\n"
"v_cvt_pk_bf16_f32 v190, v204, v205\n"
"v_cvt_pk_bf16_f32 v191, v206, v207\n"
"v_mfma_f32_16x16x32_bf16 a[200:203], a[144:147], v[176:179], a[200:203]\n"
"ds_read_b64_tr_b16 a[160:161], v231 offset:24576\n"
"v_mfma_f32_16x16x32_bf16 a[200:203], a[148:151], v[180:183], a[200:203]\n"
"ds_read_b64_tr_b16 a[162:163], v231 offset:30720\n"
"v_smfmac_f32_16x16x64_bf16 a[172:175], a[168:171], v[160:167], v233\n"
"ds_read_b64_tr_b16 a[164:165], v231 offset:36864\n"
"v_smfmac_f32_16x16x64_bf16 a[176:179], a[168:171], v[168:175], v233\n"
"ds_read_b64_tr_b16 a[166:167], v231 offset:43008\n"
"v_smfmac_f32_16x16x64_bf16 a[180:183], a[168:171], v[176:183], v233\n"
"v_smfmac_f32_16x16x64_bf16 a[184:187], a[168:171], v[184:191], v233\n"
"v_mfma_f32_16x16x32_bf16 a[204:207], a[144:147], v[184:187], a[204:207]\n"
"v_mfma_f32_16x16x32_bf16 a[204:207], a[148:151], v[188:191], a[204:207]\n"
"ds_read_b64_tr_b16 a[144:145], v232 offset:24576\n"
"v_mfma_f32_16x16x32_bf16 a[208:211], a[152:155], v[160:163], a[208:211]\n"
"ds_read_b64_tr_b16 a[146:147], v232 offset:30720\n"
"v_mfma_f32_16x16x32_bf16 a[208:211], a[156:159], v[164:167], a[208:211]\n"
"ds_read_b64_tr_b16 a[148:149], v232 offset:36864\n"
"v_mfma_f32_16x16x32_bf16 a[212:215], a[152:155], v[168:171], a[212:215]\n"
"ds_read_b64_tr_b16 a[150:151], v232 offset:43008\n"
"v_mfma_f32_16x16x32_bf16 a[212:215], a[156:159], v[172:175], a[212:215]\n"
"v_mfma_f32_16x16x32_bf16 a[216:219], a[152:155], v[176:179], a[216:219]\n"
"v_mfma_f32_16x16x32_bf16 a[216:219], a[156:159], v[180:183], a[216:219]\n"
"v_accvgpr_read_b32 v192, a172\n"
"v_accvgpr_read_b32 v193, a176\n"
"v_accvgpr_read_b32 v194, a180\n"
"v_accvgpr_read_b32 v195, a184\n"
"v_mfma_f32_16x16x32_bf16 a[220:223], a[152:155], v[184:187], a[220:223]\n"
"v_accvgpr_read_b32 v196, a173\n"
"v_accvgpr_read_b32 v197, a177\n"
"v_accvgpr_read_b32 v198, a181\n"
"v_accvgpr_read_b32 v199, a185\n"
"v_mfma_f32_16x16x32_bf16 a[220:223], a[156:159], v[188:191], a[220:223]\n"
"ds_read_b64_tr_b16 a[152:153], v229 offset:25600\n"
"v_add_f32 v192, v192, v196\n"
"v_add_f32 v193, v193, v197\n"
"v_add_f32 v194, v194, v198\n"
"s_waitcnt lgkmcnt(2)\n"
"v_mfma_f32_16x16x32_bf16 a[224:227], a[160:163], v[160:163], a[224:227]\n"
"ds_read_b64_tr_b16 a[154:155], v229 offset:31744\n"
"v_add_f32 v195, v195, v199\n"
"v_add_f32 v196, v192, v193\n"
"v_add_f32 v197, v194, v195\n"
"v_mfma_f32_16x16x32_bf16 a[224:227], a[164:167], v[164:167], a[224:227]\n"
"ds_read_b64_tr_b16 a[156:157], v229 offset:37888\n"
"v_add_f32 v196, v196, v197\n"
"v_cmp_ngt_f32 vcc, 0x4f800000, v196\n"
"s_lshl_b32 s88, s52, 2\n"
"v_mfma_f32_16x16x32_bf16 a[228:231], a[160:163], v[168:171], a[228:231]\n"
"ds_read_b64_tr_b16 a[158:159], v229 offset:44032\n"
"s_add_u32 s88, s88, 65536\n"
"s_or_b32 s89, vcc_lo, vcc_hi\n"
"v_mov_b32 v197, s88\n"
"v_mfma_f32_16x16x32_bf16 a[228:231], a[164:167], v[172:175], a[228:231]\n"
"v_mov_b32 v196, s89\n"
"ds_write_b32 v197, v196\n"
"v_mov_b32 v197, 65536\n"
"v_mfma_f32_16x16x32_bf16 a[232:235], a[160:163], v[176:179], a[232:235]\n"
"v_rcp_f32 v192, v192\n"
"v_rcp_f32 v193, v193\n"
"v_mfma_f32_16x16x32_bf16 a[232:235], a[164:167], v[180:183], a[232:235]\n"
"v_rcp_f32 v194, v194\n"
"v_rcp_f32 v195, v195\n"
"v_mfma_f32_16x16x32_bf16 a[236:239], a[160:163], v[184:187], a[236:239]\n"
"s_waitcnt lgkmcnt(0)\n"
"s_barrier\n"
"ds_read_b128 v[204:207], v197\n"
"v_mfma_f32_16x16x32_bf16 a[236:239], a[164:167], v[188:191], a[236:239]\n"
"ds_read_b64_tr_b16 a[160:161], v230 offset:25600\n"
"v_accvgpr_read_b32 v196, a192\n"
"v_accvgpr_read_b32 v197, a193\n"
"v_accvgpr_read_b32 v198, a194\n"
"v_mfma_f32_16x16x32_bf16 a[240:243], a[144:147], v[160:163], a[240:243]\n"
"ds_read_b64_tr_b16 a[162:163], v230 offset:31744\n"
"v_accvgpr_read_b32 v199, a195\n"
"v_accvgpr_read_b32 v200, a208\n"
"v_accvgpr_read_b32 v201, a209\n"
"v_mfma_f32_16x16x32_bf16 a[240:243], a[148:151], v[164:167], a[240:243]\n"
"ds_read_b64_tr_b16 a[164:165], v230 offset:37888\n"
"s_waitcnt lgkmcnt(3)\n"
"v_or3_b32 v204, v204, v205, v206\n"
"v_or_b32 v204, v204, v207\n"
"v_cmp_ne_u32 vcc, 0, v204\n"
"s_cbranch_vccnz L_fb_%=\n"
"L_nofb_%=:\n"
"v_cmp_gt_u32 vcc, 32, v249\n"
"s_cbranch_vccnz L_spin_pre_%=\n"
"L_back_pre_%=:\n"
"v_mfma_f32_16x16x32_bf16 a[244:247], a[144:147], v[168:171], a[244:247]\n"
"ds_read_b64_tr_b16 a[166:167], v230 offset:44032\n"
"v_accvgpr_read_b32 v202, a210\n"
"v_accvgpr_read_b32 v203, a211\n"
"v_mul_f32 v196, v196, v192\n"
"s_add_u32 s71, s5, 5259264\n"
"buffer_load_dwordx4 a[0:3], v250, s[8:11], s71 offen offset:0 sc1\n"
"v_mfma_f32_16x16x32_bf16 a[244:247], a[148:151], v[172:175], a[244:247]\n"
"v_mul_f32 v197, v197, v192\n"
"v_mul_f32 v198, v198, v192\n"
"v_mul_f32 v199, v199, v192\n"
"v_mul_f32 v200, v200, v192\n"
"v_mfma_f32_16x16x32_bf16 a[248:251], a[144:147], v[176:179], a[248:251]\n"
"v_mul_f32 v201, v201, v192\n"
"v_mul_f32 v202, v202, v192\n"
"v_mul_f32 v203, v203, v192\n"
"v_cvt_pk_bf16_f32 v128, v196, v197\n"
"buffer_load_dwordx4 a[4:7], v250, s[8:11], s71 offen offset:1024 sc1\n"
"v_mfma_f32_16x16x32_bf16 a[248:251], a[148:151], v[180:183], a[248:251]\n"
"v_cvt_pk_bf16_f32 v129, v198, v199\n"
"v_cvt_pk_bf16_f32 v130, v200, v201\n"
"v_cvt_pk_bf16_f32 v131, v202, v203\n"
"v_accvgpr_read_b32 v196, a196\n"
"v_mfma_f32_16x16x32_bf16 a[252:255], a[144:147], v[184:187], a[252:255]\n"
"v_accvgpr_read_b32 v197, a197\n"
"v_accvgpr_read_b32 v198, a198\n"
"v_accvgpr_read_b32 v199, a199\n"
"v_accvgpr_read_b32 v200, a212\n"
"buffer_store_dwordx4 v[128:131], v244, s[8:11], s12 offen offset:0 sc1\n"
"v_mfma_f32_16x16x32_bf16 a[252:255], a[148:151], v[188:191], a[252:255]\n"
"ds_read_b64_tr_b16 a[144:145], v231 offset:25600\n"
"v_accvgpr_read_b32 v201, a213\n"
"v_accvgpr_read_b32 v202, a214\n"
"v_accvgpr_read_b32 v203, a215\n"
"v_mfma_f32_16x16x32_bf16 v[0:3], a[152:155], v[160:163], v[0:3]\n"
"ds_read_b64_tr_b16 a[146:147], v231 offset:31744\n"
"v_mul_f32 v196, v196, v193\n"
"v_mul_f32 v197, v197, v193\n"
"v_mul_f32 v198, v198, v193\n"
"buffer_load_dwordx4 a[8:11], v250, s[8:11], s71 offen offset:2048 sc1\n"
"v_mfma_f32_16x16x32_bf16 v[0:3], a[156:159], v[164:167], v[0:3]\n"
"ds_read_b64_tr_b16 a[148:149], v231 offset:37888\n"
"v_mul_f32 v199, v199, v193\n"
"v_mul_f32 v200, v200, v193\n"
"v_mul_f32 v201, v201, v193\n"
"v_mfma_f32_16x16x32_bf16 v[4:7], a[152:155], v[168:171], v[4:7]\n"
"ds_read_b64_tr_b16 a[150:151], v231 offset:44032\n"
"v_mul_f32 v202, v202, v193\n"
"v_mul_f32 v203, v203, v193\n"
"v_cvt_pk_bf16_f32 v132, v196, v197\n"
"buffer_load_dwordx4 a[12:15], v250, s[8:11], s71 offen offset:3072 sc1\n"
"v_mfma_f32_16x16x32_bf16 v[4:7], a[156:159], v[172:175], v[4:7]\n"
"v_cvt_pk_bf16_f32 v133, v198, v199\n"
"v_cvt_pk_bf16_f32 v134, v200, v201\n"
"v_cvt_pk_bf16_f32 v135, v202, v203\n"
"v_accvgpr_read_b32 v196, a200\n"
"v_mfma_f32_16x16x32_bf16 v[8:11], a[152:155], v[176:179], v[8:11]\n"
"v_accvgpr_read_b32 v197, a201\n"
"v_accvgpr_read_b32 v198, a202\n"
"v_accvgpr_read_b32 v199, a203\n"
"v_accvgpr_read_b32 v200, a216\n"
"s_add_u32 s70, s5, 5267456\n"
"buffer_load_dwordx4 a[16:19], v244, s[8:11], s70 offen offset:0 sc1\n"
"v_mfma_f32_16x16x32_bf16 v[8:11], a[156:159], v[180:183], v[8:11]\n"
"v_accvgpr_read_b32 v201, a217\n"
"v_accvgpr_read_b32 v202, a218\n"
"v_accvgpr_read_b32 v203, a219\n"
"v_mul_f32 v196, v196, v194\n"
"v_mfma_f32_16x16x32_bf16 v[12:15], a[152:155], v[184:187], v[12:15]\n"
"v_mul_f32 v197, v197, v194\n"
"v_mul_f32 v198, v198, v194\n"
"v_mul_f32 v199, v199, v194\n"
"v_mul_f32 v200, v200, v194\n"
"buffer_store_dwordx4 v[132:135], v244, s[8:11], s13 offen offset:0 sc1\n"
"v_mfma_f32_16x16x32_bf16 v[12:15], a[156:159], v[188:191], v[12:15]\n"
"ds_read_b64_tr_b16 a[152:153], v232 offset:25600\n"
"v_mul_f32 v201, v201, v194\n"
"v_mul_f32 v202, v202, v194\n"
"v_mul_f32 v203, v203, v194\n"
"s_waitcnt lgkmcnt(2)\n"
"v_mfma_f32_16x16x32_bf16 v[16:19], a[160:163], v[160:163], v[16:19]\n"
"ds_read_b64_tr_b16 a[154:155], v232 offset:31744\n"
"v_cvt_pk_bf16_f32 v136, v196, v197\n"
"v_cvt_pk_bf16_f32 v137, v198, v199\n"
"v_cvt_pk_bf16_f32 v138, v200, v201\n"
"buffer_load_dwordx4 a[20:23], v244, s[8:11], s70 offen offset:1024 sc1\n"
"v_mfma_f32_16x16x32_bf16 v[16:19], a[164:167], v[164:167], v[16:19]\n"
"ds_read_b64_tr_b16 a[156:157], v232 offset:37888\n"
"v_cvt_pk_bf16_f32 v139, v202, v203\n"
"v_accvgpr_read_b32 v196, a204\n"
"v_accvgpr_read_b32 v197, a205\n"
"v_mfma_f32_16x16x32_bf16 v[20:23], a[160:163], v[168:171], v[20:23]\n"
"ds_read_b64_tr_b16 a[158:159], v232 offset:44032\n"
"v_accvgpr_read_b32 v198, a206\n"
"v_accvgpr_read_b32 v199, a207\n"
"v_accvgpr_read_b32 v200, a220\n"
"buffer_load_dwordx4 a[24:27], v244, s[8:11], s70 offen offset:2048 sc1\n"
"v_mfma_f32_16x16x32_bf16 v[20:23], a[164:167], v[172:175], v[20:23]\n"
"v_accvgpr_read_b32 v201, a221\n"
"v_accvgpr_read_b32 v202, a222\n"
"v_accvgpr_read_b32 v203, a223\n"
"v_mul_f32 v196, v196, v195\n"
"v_mfma_f32_16x16x32_bf16 v[24:27], a[160:163], v[176:179], v[24:27]\n"
"v_mul_f32 v197, v197, v195\n"
"v_mul_f32 v198, v198, v195\n"
"v_mul_f32 v199, v199, v195\n"
"v_mul_f32 v200, v200, v195\n"
"buffer_load_dwordx4 a[28:31], v244, s[8:11], s70 offen offset:3072 sc1\n"
"v_mfma_f32_16x16x32_bf16 v[24:27], a[164:167], v[180:183], v[24:27]\n"
"v_mul_f32 v201, v201, v195\n"
"v_mul_f32 v202, v202, v195\n"
"v_mul_f32 v203, v203, v195\n"
"v_cvt_pk_bf16_f32 v140, v196, v197\n"
"v_mfma_f32_16x16x32_bf16 v[28:31], a[160:163], v[184:187], v[28:31]\n"
"v_cvt_pk_bf16_f32 v141, v198, v199\n"
"v_cvt_pk_bf16_f32 v142, v200, v201\n"
"v_cvt_pk_bf16_f32 v143, v202, v203\n"
"buffer_store_dwordx4 v[136:139], v244, s[8:11], s14 offen offset:0 sc1\n"
"v_mfma_f32_16x16x32_bf16 v[28:31], a[164:167], v[188:191], v[28:31]\n"
"ds_read_b64_tr_b16 a[160:161], v229 offset:26624\n"
"v_accvgpr_read_b32 v196, a224\n"
"v_accvgpr_read_b32 v197, a225\n"
"v_accvgpr_read_b32 v198, a226\n"
"v_mfma_f32_16x16x32_bf16 v[32:35], a[144:147], v[160:163], v[32:35]\n"
"ds_read_b64_tr_b16 a[162:163], v229 offset:32768\n"
"v_accvgpr_read_b32 v199, a227\n"
"v_accvgpr_read_b32 v200, a240\n"
"v_accvgpr_read_b32 v201, a241\n"
"buffer_load_dwordx4 a[32:35], v250, s[8:11], s70 offen offset:0 sc1\n"
"s_waitcnt lgkmcnt(2)\n"
"v_mfma_f32_16x16x32_bf16 v[32:35], a[148:151], v[164:167], v[32:35]\n"
"ds_read_b64_tr_b16 a[164:165], v229 offset:38912\n"
"v_accvgpr_read_b32 v202, a242\n"
"v_accvgpr_read_b32 v203, a243\n"
"v_mul_f32 v196, v196, v192\n"
"v_mfma_f32_16x16x32_bf16 v[36:39], a[144:147], v[168:171], v[36:39]\n"
"ds_read_b64_tr_b16 a[166:167], v229 offset:45056\n"
"v_mul_f32 v197, v197, v192\n"
"v_mul_f32 v198, v198, v192\n"
"v_mul_f32 v199, v199, v192\n"
"buffer_load_dwordx4 a[36:39], v250, s[8:11], s70 offen offset:1024 sc1\n"
"v_mfma_f32_16x16x32_bf16 v[36:39], a[148:151], v[172:175], v[36:39]\n"
"v_mul_f32 v200, v200, v192\n"
"v_mul_f32 v201, v201, v192\n"
"v_mul_f32 v202, v202, v192\n"
"v_mul_f32 v203, v203, v192\n"
"v_mfma_f32_16x16x32_bf16 v[40:43], a[144:147], v[176:179], v[40:43]\n"
"v_cvt_pk_bf16_f32 v144, v196, v197\n"
"v_cvt_pk_bf16_f32 v145, v198, v199\n"
"v_cvt_pk_bf16_f32 v146, v200, v201\n"
"v_cvt_pk_bf16_f32 v147, v202, v203\n"
"buffer_load_dwordx4 a[40:43], v250, s[8:11], s70 offen offset:2048 sc1\n"
"v_mfma_f32_16x16x32_bf16 v[40:43], a[148:151], v[180:183], v[40:43]\n"
"v_accvgpr_read_b32 v196, a228\n"
"v_accvgpr_read_b32 v197, a229\n"
"v_accvgpr_read_b32 v198, a230\n"
"v_accvgpr_read_b32 v199, a231\n"
"v_mfma_f32_16x16x32_bf16 v[44:47], a[144:147], v[184:187], v[44:47]\n"
"v_accvgpr_read_b32 v200, a244\n"
"v_accvgpr_read_b32 v201, a245\n"
"v_accvgpr_read_b32 v202, a246\n"
"v_accvgpr_read_b32 v203, a247\n"
"buffer_store_dwordx4 v[140:143], v244, s[8:11], s15 offen offset:0 sc1\n"
"v_mfma_f32_16x16x32_bf16 v[44:47], a[148:151], v[188:191], v[44:47]\n"
"ds_read_b64_tr_b16 a[144:145], v230 offset:26624\n"
"v_mul_f32 v196, v196, v193\n"
"v_mul_f32 v197, v197, v193\n"
"v_mul_f32 v198, v198, v193\n"
"v_mfma_f32_16x16x32_bf16 v[48:51], a[152:155], v[160:163], v[48:51]\n"
"ds_read_b64_tr_b16 a[146:147], v230 offset:32768\n"
"v_mul_f32 v199, v199, v193\n"
"v_mul_f32 v200, v200, v193\n"
"v_mul_f32 v201, v201, v193\n"
"buffer_load_dwordx4 a[44:47], v250, s[8:11], s70 offen offset:3072 sc1\n"
"v_mfma_f32_16x16x32_bf16 v[48:51], a[156:159], v[164:167], v[48:51]\n"
"ds_read_b64_tr_b16 a[148:149], v230 offset:38912\n"
"v_mul_f32 v202, v202, v193\n"
"v_mul_f32 v203, v203, v193\n"
"v_cvt_pk_bf16_f32 v148, v196, v197\n"
"v_mfma_f32_16x16x32_bf16 v[52:55], a[152:155], v[168:171], v[52:55]\n"
"ds_read_b64_tr_b16 a[150:151], v230 offset:45056\n"
"v_cvt_pk_bf16_f32 v149, v198, v199\n"
"v_cvt_pk_bf16_f32 v150, v200, v201\n"
"v_cvt_pk_bf16_f32 v151, v202, v203\n"
"s_add_u32 s71, s5, 5275648\n"
"buffer_load_dwordx4 a[48:51], v244, s[8:11], s71 offen offset:0 sc1\n"
"v_mfma_f32_16x16x32_bf16 v[52:55], a[156:159], v[172:175], v[52:55]\n"
"v_accvgpr_read_b32 v196, a232\n"
"v_accvgpr_read_b32 v197, a233\n"
"v_accvgpr_read_b32 v198, a234\n"
"v_accvgpr_read_b32 v199, a235\n"
"v_mfma_f32_16x16x32_bf16 v[56:59], a[152:155], v[176:179], v[56:59]\n"
"v_accvgpr_read_b32 v200, a248\n"
"v_accvgpr_read_b32 v201, a249\n"
"v_accvgpr_read_b32 v202, a250\n"
"v_accvgpr_read_b32 v203, a251\n"
"buffer_load_dwordx4 a[52:55], v244, s[8:11], s71 offen offset:1024 sc1\n"
"v_mfma_f32_16x16x32_bf16 v[56:59], a[156:159], v[180:183], v[56:59]\n"
"v_mul_f32 v196, v196, v194\n"
"v_mul_f32 v197, v197, v194\n"
"v_mul_f32 v198, v198, v194\n"
"v_mul_f32 v199, v199, v194\n"
"v_mfma_f32_16x16x32_bf16 v[60:63], a[152:155], v[184:187], v[60:63]\n"
"v_mul_f32 v200, v200, v194\n"
"v_mul_f32 v201, v201, v194\n"
"v_mul_f32 v202, v202, v194\n"
"v_mul_f32 v203, v203, v194\n"
"buffer_store_dwordx4 v[144:147], v244, s[8:11], s12 offen offset:1024 sc1\n"
"v_mfma_f32_16x16x32_bf16 v[60:63], a[156:159], v[188:191], v[60:63]\n"
"ds_read_b64_tr_b16 a[152:153], v231 offset:26624\n"
"v_cvt_pk_bf16_f32 v152, v196, v197\n"
"v_cvt_pk_bf16_f32 v153, v198, v199\n"
"v_cvt_pk_bf16_f32 v154, v200, v201\n"
"s_waitcnt lgkmcnt(2)\n"
"v_mfma_f32_16x16x32_bf16 v[64:67], a[160:163], v[160:163], v[64:67]\n"
"ds_read_b64_tr_b16 a[154:155], v231 offset:32768\n"
"v_cvt_pk_bf16_f32 v155, v202, v203\n"
"v_accvgpr_read_b32 v196, a236\n"
"v_accvgpr_read_b32 v197, a237\n"
"buffer_load_dwordx4 a[56:59], v244, s[8:11], s71 offen offset:2048 sc1\n"
"v_mfma_f32_16x16x32_bf16 v[64:67], a[164:167], v[164:167], v[64:67]\n"
"ds_read_b64_tr_b16 a[156:157], v231 offset:38912\n"
"v_accvgpr_read_b32 v198, a238\n"
"v_accvgpr_read_b32 v199, a239\n"
"v_accvgpr_read_b32 v200, a252\n"
"v_mfma_f32_16x16x32_bf16 v[68:71], a[160:163], v[168:171], v[68:71]\n"
"ds_read_b64_tr_b16 a[158:159], v231 offset:45056\n"
"v_accvgpr_read_b32 v201, a253\n"
"v_accvgpr_read_b32 v202, a254\n"
"v_accvgpr_read_b32 v203, a255\n"
"buffer_load_dwordx4 a[60:63], v244, s[8:11], s71 offen offset:3072 sc1\n"
"v_mfma_f32_16x16x32_bf16 v[68:71], a[164:167], v[172:175], v[68:71]\n"
"v_mul_f32 v196, v196, v195\n"
"v_mul_f32 v197, v197, v195\n"
"v_mul_f32 v198, v198, v195\n"
"v_mul_f32 v199, v199, v195\n"
"v_mfma_f32_16x16x32_bf16 v[72:75], a[160:163], v[176:179], v[72:75]\n"
"v_mul_f32 v200, v200, v195\n"
"v_mul_f32 v201, v201, v195\n"
"v_mul_f32 v202, v202, v195\n"
"v_mul_f32 v203, v203, v195\n"
"s_add_u32 s72, s4, 655360\n"
"s_add_u32 m0, s6, 20480\n"
"v_mfma_f32_16x16x32_bf16 v[72:75], a[164:167], v[180:183], v[72:75]\n"
"v_cvt_pk_bf16_f32 v156, v196, v197\n"
"v_cvt_pk_bf16_f32 v157, v198, v199\n"
"v_cvt_pk_bf16_f32 v158, v200, v201\n"
"v_cvt_pk_bf16_f32 v159, v202, v203\n"
"v_mfma_f32_16x16x32_bf16 v[76:79], a[160:163], v[184:187], v[76:79]\n"
"v_mul_f32 v0, v0, v192\n"
"v_mul_f32 v1, v1, v192\n"
"v_mul_f32 v2, v2, v192\n"
"buffer_load_dwordx4 v244, s[8:11], s72 offen offset:0 sc1 lds\n"
"v_mfma_f32_16x16x32_bf16 v[76:79], a[164:167], v[188:191], v[76:79]\n"
"ds_read_b64_tr_b16 a[160:161], v232 offset:26624\n"
"v_mul_f32 v3, v3, v192\n"
"v_mul_f32 v16, v16, v192\n"
"v_mfma_f32_16x16x32_bf16 v[80:83], a[144:147], v[160:163], v[80:83]\n"
"ds_read_b64_tr_b16 a[162:163], v232 offset:32768\n"
"v_mul_f32 v17, v17, v192\n"
"v_mul_f32 v18, v18, v192\n"
"buffer_store_dwordx4 v[148:151], v244, s[8:11], s13 offen offset:1024 sc1\n"
"s_waitcnt lgkmcnt(2)\n"
"v_mfma_f32_16x16x32_bf16 v[80:83], a[148:151], v[164:167], v[80:83]\n"
"ds_read_b64_tr_b16 a[164:165], v232 offset:38912\n"
"v_mul_f32 v19, v19, v192\n"
"v_cvt_pk_bf16_f32 v0, v0, v1\n"
"v_mfma_f32_16x16x32_bf16 v[84:87], a[144:147], v[168:171], v[84:87]\n"
"ds_read_b64_tr_b16 a[166:167], v232 offset:45056\n"
"v_cvt_pk_bf16_f32 v1, v2, v3\n"
"v_cvt_pk_bf16_f32 v2, v16, v17\n"
"buffer_load_dwordx4 v244, s[8:11], s72 offen offset:1024 sc1 lds\n"
"v_mfma_f32_16x16x32_bf16 v[84:87], a[148:151], v[172:175], v[84:87]\n"
"v_cvt_pk_bf16_f32 v3, v18, v19\n"
"v_mul_f32 v4, v4, v193\n"
"v_mul_f32 v5, v5, v193\n"
"v_mfma_f32_16x16x32_bf16 v[88:91], a[144:147], v[176:179], v[88:91]\n"
"v_mul_f32 v6, v6, v193\n"
"v_mul_f32 v7, v7, v193\n"
"v_mul_f32 v20, v20, v193\n"
"buffer_load_dwordx4 v244, s[8:11], s72 offen offset:2048 sc1 lds\n"
"v_mfma_f32_16x16x32_bf16 v[88:91], a[148:151], v[180:183], v[88:91]\n"
"v_mul_f32 v21, v21, v193\n"
"v_mul_f32 v22, v22, v193\n"
"v_mul_f32 v23, v23, v193\n"
"v_mfma_f32_16x16x32_bf16 v[92:95], a[144:147], v[184:187], v[92:95]\n"
"v_cvt_pk_bf16_f32 v4, v4, v5\n"
"v_cvt_pk_bf16_f32 v5, v6, v7\n"
"v_cvt_pk_bf16_f32 v6, v20, v21\n"
"buffer_load_dwordx4 v244, s[8:11], s72 offen offset:3072 sc1 lds\n"
"v_mfma_f32_16x16x32_bf16 v[92:95], a[148:151], v[188:191], v[92:95]\n"
"v_cvt_pk_bf16_f32 v7, v22, v23\n"
"v_mul_f32 v8, v8, v194\n"
"v_mul_f32 v9, v9, v194\n"
"v_mfma_f32_16x16x32_bf16 v[96:99], a[152:155], v[160:163], v[96:99]\n"
"v_mul_f32 v10, v10, v194\n"
"v_mul_f32 v11, v11, v194\n"
"v_mul_f32 v24, v24, v194\n"
"buffer_store_dwordx4 v[152:155], v244, s[8:11], s14 offen offset:1024 sc1\n"
"v_mfma_f32_16x16x32_bf16 v[96:99], a[156:159], v[164:167], v[96:99]\n"
"v_mul_f32 v25, v25, v194\n"
"v_mul_f32 v26, v26, v194\n"
"v_mul_f32 v27, v27, v194\n"
"v_mfma_f32_16x16x32_bf16 v[100:103], a[152:155], v[168:171], v[100:103]\n"
"v_cvt_pk_bf16_f32 v8, v8, v9\n"
"v_cvt_pk_bf16_f32 v9, v10, v11\n"
"v_cvt_pk_bf16_f32 v10, v24, v25\n"
"s_add_u32 m0, s6, 24576\n"
"v_mfma_f32_16x16x32_bf16 v[100:103], a[156:159], v[172:175], v[100:103]\n"
"v_cvt_pk_bf16_f32 v11, v26, v27\n"
"v_mul_f32 v12, v12, v195\n"
"v_mul_f32 v13, v13, v195\n"
"buffer_load_dwordx4 v250, s[8:11], s72 offen offset:0 sc1 lds\n"
"v_mfma_f32_16x16x32_bf16 v[104:107], a[152:155], v[176:179], v[104:107]\n"
"ds_read_b128 a[96:99], v251 offset:36864\n"
"v_mul_f32 v14, v14, v195\n"
"v_mul_f32 v15, v15, v195\n"
"v_mfma_f32_16x16x32_bf16 v[104:107], a[156:159], v[180:183], v[104:107]\n"
"ds_read_b128 a[100:103], v251 offset:41984\n"
"v_mul_f32 v28, v28, v195\n"
"v_mul_f32 v29, v29, v195\n"
"s_add_u32 s88, s87, 1048576\n"
"buffer_load_dwordx2 v[208:209], v244, s[8:11], s88 offen offset:0 sc1\n"
"v_mfma_f32_16x16x32_bf16 v[108:111], a[152:155], v[184:187], v[108:111]\n"
"ds_read_b128 a[104:107], v251 offset:47104\n"
"v_mul_f32 v30, v30, v195\n"
"v_mul_f32 v31, v31, v195\n"
"v_mfma_f32_16x16x32_bf16 v[108:111], a[156:159], v[188:191], v[108:111]\n"
"ds_read_b128 a[108:111], v251 offset:52224\n"
"v_cvt_pk_bf16_f32 v12, v12, v13\n"
"v_cvt_pk_bf16_f32 v13, v14, v15\n"
"s_add_u32 s88, s30, 1048576\n"
"buffer_load_dwordx2 v[210:211], v244, s[8:11], s88 offen offset:0 sc1\n"
"s_waitcnt lgkmcnt(4)\n"
"v_mfma_f32_16x16x32_bf16 v[112:115], a[160:163], v[160:163], v[112:115]\n"
"ds_read_b128 a[112:115], v251 offset:37888\n"
"v_cvt_pk_bf16_f32 v14, v28, v29\n"
"v_cvt_pk_bf16_f32 v15, v30, v31\n"
"v_mfma_f32_16x16x32_bf16 v[112:115], a[164:167], v[164:167], v[112:115]\n"
"ds_read_b128 a[116:119], v251 offset:43008\n"
"v_mul_f32 v32, v32, v192\n"
"v_mul_f32 v33, v33, v192\n"
"buffer_store_dwordx4 v[156:159], v244, s[8:11], s15 offen offset:1024 sc1\n"
"v_mfma_f32_16x16x32_bf16 v[116:119], a[160:163], v[168:171], v[116:119]\n"
"ds_read_b128 a[120:123], v251 offset:48128\n"
"v_mul_f32 v34, v34, v192\n"
"v_mul_f32 v35, v35, v192\n"
"v_mfma_f32_16x16x32_bf16 v[116:119], a[164:167], v[172:175], v[116:119]\n"
"ds_read_b128 a[124:127], v251 offset:53248\n"
"v_mul_f32 v48, v48, v192\n"
"v_mul_f32 v49, v49, v192\n"
"s_add_u32 s88, s87, 1048576\n"
"buffer_load_dwordx2 v[212:213], v244, s[8:11], s88 offen offset:1024 sc1\n"
"v_mfma_f32_16x16x32_bf16 v[120:123], a[160:163], v[176:179], v[120:123]\n"
"ds_read_b128 a[128:131], v251 offset:38912\n"
"v_mul_f32 v50, v50, v192\n"
"v_mul_f32 v51, v51, v192\n"
"v_mfma_f32_16x16x32_bf16 v[120:123], a[164:167], v[180:183], v[120:123]\n"
"ds_read_b128 a[132:135], v251 offset:44032\n"
"v_cvt_pk_bf16_f32 v32, v32, v33\n"
"v_cvt_pk_bf16_f32 v33, v34, v35\n"
"s_add_u32 s88, s30, 1048576\n"
"buffer_load_dwordx2 v[214:215], v244, s[8:11], s88 offen offset:1024 sc1\n"
"v_mfma_f32_16x16x32_bf16 v[124:127], a[160:163], v[184:187], v[124:127]\n"
"ds_read_b128 a[136:139], v251 offset:49152\n"
"v_cvt_pk_bf16_f32 v34, v48, v49\n"
"v_cvt_pk_bf16_f32 v35, v50, v51\n"
"v_mfma_f32_16x16x32_bf16 v[124:127], a[164:167], v[188:191], v[124:127]\n"
"ds_read_b128 a[140:143], v251 offset:54272\n"
"v_mul_f32 v36, v36, v193\n"
"v_mul_f32 v37, v37, v193\n"
"s_add_u32 s88, s87, 1048576\n"
"buffer_load_dwordx2 v[216:217], v244, s[8:11], s88 offen offset:2048 sc1\n"
"s_waitcnt vmcnt(29)\n"
"s_waitcnt lgkmcnt(6)\n"
"v_mfma_f32_16x16x32_bf16 v[224:227], a[96:99], a[0:3], 0\n"
"ds_read_b128 a[96:99], v251 offset:39936\n"
"v_mul_f32 v38, v38, v193\n"
"v_mul_f32 v39, v39, v193\n"
"v_mfma_f32_16x16x32_bf16 v[224:227], a[100:103], a[4:7], v[224:227]\n"
"ds_read_b128 a[100:103], v251 offset:45056\n"
"v_mul_f32 v52, v52, v193\n"
"v_mul_f32 v53, v53, v193\n"
"buffer_store_dwordx4 v[0:3], v244, s[8:11], s12 offen offset:2048 sc1\n"
"v_mfma_f32_16x16x32_bf16 v[224:227], a[104:107], a[8:11], v[224:227]\n"
"ds_read_b128 a[104:107], v251 offset:50176\n"
"v_mul_f32 v54, v54, v193\n"
"v_mul_f32 v55, v55, v193\n"
"v_mfma_f32_16x16x32_bf16 v[224:227], a[108:111], a[12:15], v[224:227]\n"
"ds_read_b128 a[108:111], v251 offset:55296\n"
"v_cvt_pk_bf16_f32 v36, v36, v37\n"
"v_cvt_pk_bf16_f32 v37, v38, v39\n"
"buffer_load_dwordx4 a[64:67], v250, s[8:11], s71 offen offset:0 sc1\n"
"v_mfma_f32_16x16x32_bf16 v[228:231], a[112:115], a[0:3], 0\n"
"ds_read_b128 a[112:115], v251 offset:40960\n"
"v_cvt_pk_bf16_f32 v38, v52, v53\n"
"v_cvt_pk_bf16_f32 v39, v54, v55\n"
"v_mfma_f32_16x16x32_bf16 v[228:231], a[116:119], a[4:7], v[228:231]\n"
"ds_read_b128 a[116:119], v251 offset:46080\n"
"v_mul_f32 v40, v40, v194\n"
"v_mul_f32 v41, v41, v194\n"
"buffer_load_dwordx4 a[68:71], v250, s[8:11], s71 offen offset:1024 sc1\n"
"s_waitcnt lgkmcnt(6)\n"
"v_mfma_f32_16x16x32_bf16 v[228:231], a[120:123], a[8:11], v[228:231]\n"
"ds_read_b128 a[120:123], v251 offset:51200\n"
"v_mul_f32 v42, v42, v194\n"
"v_mul_f32 v43, v43, v194\n"
"v_mfma_f32_16x16x32_bf16 v[228:231], a[124:127], a[12:15], v[228:231]\n"
"ds_read_b128 a[124:127], v251 offset:56320\n"
"v_mul_f32 v56, v56, v194\n"
"v_mul_f32 v57, v57, v194\n"
"buffer_load_dwordx4 a[72:75], v250, s[8:11], s71 offen offset:2048 sc1\n"
"v_mfma_f32_16x16x32_bf16 v[232:235], a[128:131], a[0:3], 0\n"
"ds_read_b128 a[128:131], v251 offset:57344\n"
"v_mul_f32 v58, v58, v194\n"
"v_mul_f32 v59, v59, v194\n"
"v_mfma_f32_16x16x32_bf16 v[232:235], a[132:135], a[4:7], v[232:235]\n"
"ds_read_b128 a[132:135], v251 offset:62464\n"
"v_cvt_pk_bf16_f32 v40, v40, v41\n"
"v_cvt_pk_bf16_f32 v41, v42, v43\n"
"buffer_load_dwordx4 a[76:79], v250, s[8:11], s71 offen offset:3072 sc1\n"
"v_mfma_f32_16x16x32_bf16 v[232:235], a[136:139], a[8:11], v[232:235]\n"
"ds_read_b128 a[136:139], v252 offset:2048\n"
"v_cvt_pk_bf16_f32 v42, v56, v57\n"
"v_cvt_pk_bf16_f32 v43, v58, v59\n"
"v_mfma_f32_16x16x32_bf16 v[232:235], a[140:143], a[12:15], v[232:235]\n"
"ds_read_b128 a[140:143], v252 offset:7168\n"
"v_mul_f32 v44, v44, v195\n"
"v_mul_f32 v45, v45, v195\n"
"buffer_store_dwordx4 v[4:7], v244, s[8:11], s13 offen offset:2048 sc1\n"
"s_waitcnt lgkmcnt(6)\n"
"v_mfma_f32_16x16x32_bf16 v[236:239], a[96:99], a[0:3], 0\n"
"ds_read_b128 a[96:99], v251 offset:58368\n"
"v_mul_f32 v46, v46, v195\n"
"v_mul_f32 v47, v47, v195\n"
"v_mfma_f32_16x16x32_bf16 v[236:239], a[100:103], a[4:7], v[236:239]\n"
"ds_read_b128 a[100:103], v251 offset:63488\n"
"v_mul_f32 v60, v60, v195\n"
"v_mul_f32 v61, v61, v195\n"
"s_add_u32 s72, s80, 0\n"
"s_add_u32 m0, s6, 40960\n"
"v_mfma_f32_16x16x32_bf16 v[236:239], a[104:107], a[8:11], v[236:239]\n"
"ds_read_b128 a[104:107], v252 offset:3072\n"
"v_mul_f32 v62, v62, v195\n"
"v_mul_f32 v63, v63, v195\n"
"buffer_load_dwordx4 v244, s[8:11], s72 offen offset:0 sc1 lds\n"
"v_mfma_f32_16x16x32_bf16 v[236:239], a[108:111], a[12:15], v[236:239]\n"
"ds_read_b128 a[108:111], v252 offset:8192\n"
"v_cvt_pk_bf16_f32 v44, v44, v45\n"
"v_cvt_pk_bf16_f32 v45, v46, v47\n"
"v_mfma_f32_16x16x32_bf16 v[240:243], a[112:115], a[0:3], 0\n"
"ds_read_b128 a[112:115], v251 offset:59392\n"
"v_cvt_pk_bf16_f32 v46, v60, v61\n"
"v_cvt_pk_bf16_f32 v47, v62, v63\n"
"buffer_load_dwordx4 v244, s[8:11], s72 offen offset:1024 sc1 lds\n"
"v_mfma_f32_16x16x32_bf16 v[240:243], a[116:119], a[4:7], v[240:243]\n"
"ds_read_b128 a[116:119], v251 offset:64512\n"
"v_mul_f32 v64, v64, v192\n"
"v_mul_f32 v65, v65, v192\n"
"s_waitcnt lgkmcnt(6)\n"
"v_mfma_f32_16x16x32_bf16 v[240:243], a[120:123], a[8:11], v[240:243]\n"
"ds_read_b128 a[120:123], v252 offset:4096\n"
"v_mul_f32 v66, v66, v192\n"
"v_mul_f32 v67, v67, v192\n"
"buffer_load_dwordx4 v244, s[8:11], s72 offen offset:2048 sc1 lds\n"
"v_mfma_f32_16x16x32_bf16 v[240:243], a[124:127], a[12:15], v[240:243]\n"
"ds_read_b128 a[124:127], v252 offset:9216\n"
"v_mul_f32 v80, v80, v192\n"
"v_mul_f32 v81, v81, v192\n"
"s_waitcnt vmcnt(33)\n"
"v_mfma_f32_16x16x32_bf16 v[224:227], a[128:131], a[16:19], v[224:227]\n"
"ds_read_b128 a[128:131], v251 offset:60416\n"
"v_mul_f32 v82, v82, v192\n"
"v_mul_f32 v83, v83, v192\n"
"buffer_load_dwordx4 v244, s[8:11], s72 offen offset:3072 sc1 lds\n"
"v_mfma_f32_16x16x32_bf16 v[224:227], a[132:135], a[20:23], v[224:227]\n"
"ds_read_b128 a[132:135], v252 offset:0\n"
"v_cvt_pk_bf16_f32 v64, v64, v65\n"
"v_cvt_pk_bf16_f32 v65, v66, v67\n"
"v_mfma_f32_16x16x32_bf16 v[224:227], a[136:139], a[24:27], v[224:227]\n"
"ds_read_b128 a[136:139], v252 offset:5120\n"
"v_cvt_pk_bf16_f32 v66, v80, v81\n"
"v_cvt_pk_bf16_f32 v67, v82, v83\n"
"buffer_store_dwordx4 v[8:11], v244, s[8:11], s14 offen offset:2048 sc1\n"
"v_mfma_f32_16x16x32_bf16 v[224:227], a[140:143], a[28:31], v[224:227]\n"
"ds_read_b128 a[140:143], v252 offset:10240\n"
"v_mul_f32 v68, v68, v193\n"
"v_mul_f32 v69, v69, v193\n"
"s_waitcnt lgkmcnt(6)\n"
"v_mfma_f32_16x16x32_bf16 v[228:231], a[96:99], a[16:19], v[228:231]\n"
"ds_read_b128 a[96:99], v251 offset:61440\n"
"v_mul_f32 v70, v70, v193\n"
"v_mul_f32 v71, v71, v193\n"
"s_add_u32 m0, s6, 45056\n"
"v_mfma_f32_16x16x32_bf16 v[228:231], a[100:103], a[20:23], v[228:231]\n"
"ds_read_b128 a[100:103], v252 offset:1024\n"
"v_mul_f32 v84, v84, v193\n"
"v_mul_f32 v85, v85, v193\n"
"buffer_load_dwordx4 v250, s[8:11], s72 offen offset:0 sc1 lds\n"
"v_mfma_f32_16x16x32_bf16 v[228:231], a[104:107], a[24:27], v[228:231]\n"
"ds_read_b128 a[104:107], v252 offset:6144\n"
"v_mul_f32 v86, v86, v193\n"
"v_mul_f32 v87, v87, v193\n"
"v_mfma_f32_16x16x32_bf16 v[228:231], a[108:111], a[28:31], v[228:231]\n"
"ds_read_b128 a[108:111], v252 offset:11264\n"
"v_cvt_pk_bf16_f32 v68, v68, v69\n"
"v_cvt_pk_bf16_f32 v69, v70, v71\n"
"v_mfma_f32_16x16x32_bf16 v[232:235], a[112:115], a[16:19], v[232:235]\n"
"ds_read_b128 a[112:115], v252 offset:12288\n"
"v_cvt_pk_bf16_f32 v70, v84, v85\n"
"v_cvt_pk_bf16_f32 v71, v86, v87\n"
"v_mfma_f32_16x16x32_bf16 v[232:235], a[116:119], a[20:23], v[232:235]\n"
"ds_read_b128 a[116:119], v252 offset:17408\n"
"v_mul_f32 v72, v72, v194\n"
"v_mul_f32 v73, v73, v194\n"
"s_waitcnt lgkmcnt(6)\n"
"v_mfma_f32_16x16x32_bf16 v[232:235], a[120:123], a[24:27], v[232:235]\n"
"ds_read_b128 a[120:123], v252 offset:22528\n"
"v_mul_f32 v74, v74, v194\n"
"v_mul_f32 v75, v75, v194\n"
"v_mfma_f32_16x16x32_bf16 v[232:235], a[124:127], a[28:31], v[232:235]\n"
"ds_read_b128 a[124:127], v252 offset:27648\n"
"v_mul_f32 v88, v88, v194\n"
"v_mul_f32 v89, v89, v194\n"
"v_mfma_f32_16x16x32_bf16 v[236:239], a[128:131], a[16:19], v[236:239]\n"
"ds_read_b128 a[128:131], v252 offset:13312\n"
"v_mul_f32 v90, v90, v194\n"
"v_mul_f32 v91, v91, v194\n"
"buffer_store_dwordx4 v[12:15], v244, s[8:11], s15 offen offset:2048 sc1\n"
"v_mfma_f32_16x16x32_bf16 v[236:239], a[132:135], a[20:23], v[236:239]\n"
"ds_read_b128 a[132:135], v252 offset:18432\n"
"v_cvt_pk_bf16_f32 v72, v72, v73\n"
"v_cvt_pk_bf16_f32 v73, v74, v75\n"
"v_mfma_f32_16x16x32_bf16 v[236:239], a[136:139], a[24:27], v[236:239]\n"
"ds_read_b128 a[136:139], v252 offset:23552\n"
"v_cvt_pk_bf16_f32 v74, v88, v89\n"
"v_cvt_pk_bf16_f32 v75, v90, v91\n"
"v_mfma_f32_16x16x32_bf16 v[236:239], a[140:143], a[28:31], v[236:239]\n"
"ds_read_b128 a[140:143], v252 offset:28672\n"
"v_mul_f32 v76, v76, v195\n"
"v_mul_f32 v77, v77, v195\n"
"s_waitcnt lgkmcnt(6)\n"
"v_mfma_f32_16x16x32_bf16 v[240:243], a[96:99], a[16:19], v[240:243]\n"
"ds_read_b128 a[96:99], v252 offset:14336\n"
"v_mul_f32 v78, v78, v195\n"
"v_mul_f32 v79, v79, v195\n"
"v_mfma_f32_16x16x32_bf16 v[240:243], a[100:103], a[20:23], v[240:243]\n"
"ds_read_b128 a[100:103], v252 offset:19456\n"
"v_mul_f32 v92, v92, v195\n"
"v_mul_f32 v93, v93, v195\n"
"v_mfma_f32_16x16x32_bf16 v[240:243], a[104:107], a[24:27], v[240:243]\n"
"ds_read_b128 a[104:107], v252 offset:24576\n"
"v_mul_f32 v94, v94, v195\n"
"v_mul_f32 v95, v95, v195\n"
"v_mfma_f32_16x16x32_bf16 v[240:243], a[108:111], a[28:31], v[240:243]\n"
"ds_read_b128 a[108:111], v252 offset:29696\n"
"v_cvt_pk_bf16_f32 v76, v76, v77\n"
"v_cvt_pk_bf16_f32 v77, v78, v79\n"
"s_waitcnt vmcnt(31)\n"
"v_mfma_f32_16x16x32_bf16 v[224:227], a[112:115], a[32:35], v[224:227]\n"
"ds_read_b128 a[112:115], v252 offset:15360\n"
"v_cvt_pk_bf16_f32 v78, v92, v93\n"
"v_cvt_pk_bf16_f32 v79, v94, v95\n"
"v_mfma_f32_16x16x32_bf16 v[224:227], a[116:119], a[36:39], v[224:227]\n"
"ds_read_b128 a[116:119], v252 offset:20480\n"
"v_mul_f32 v96, v96, v192\n"
"v_mul_f32 v97, v97, v192\n"
"s_waitcnt lgkmcnt(6)\n"
"v_mfma_f32_16x16x32_bf16 v[224:227], a[120:123], a[40:43], v[224:227]\n"
"ds_read_b128 a[120:123], v252 offset:25600\n"
"v_mul_f32 v98, v98, v192\n"
"v_mul_f32 v99, v99, v192\n"
"buffer_store_dwordx4 v[32:35], v244, s[8:11], s12 offen offset:3072 sc1\n"
"v_mfma_f32_16x16x32_bf16 v[224:227], a[124:127], a[44:47], v[224:227]\n"
"ds_read_b128 a[124:127], v252 offset:30720\n"
"v_mul_f32 v112, v112, v192\n"
"v_mul_f32 v113, v113, v192\n"
"v_mfma_f32_16x16x32_bf16 v[228:231], a[128:131], a[32:35], v[228:231]\n"
"ds_read_b128 a[128:131], v252 offset:16384\n"
"v_mul_f32 v114, v114, v192\n"
"v_mul_f32 v115, v115, v192\n"
"v_mfma_f32_16x16x32_bf16 v[228:231], a[132:135], a[36:39], v[228:231]\n"
"ds_read_b128 a[132:135], v252 offset:21504\n"
"v_cvt_pk_bf16_f32 v96, v96, v97\n"
"v_cvt_pk_bf16_f32 v97, v98, v99\n"
"v_mfma_f32_16x16x32_bf16 v[228:231], a[136:139], a[40:43], v[228:231]\n"
"ds_read_b128 a[136:139], v252 offset:26624\n"
"v_cvt_pk_bf16_f32 v98, v112, v113\n"
"v_cvt_pk_bf16_f32 v99, v114, v115\n"
"v_mfma_f32_16x16x32_bf16 v[228:231], a[140:143], a[44:47], v[228:231]\n"
"s_waitcnt vmcnt(17)\n"
"s_barrier\n"
"v_mul_f32 v100, v100, v193\n"
"s_add_u32 s59, s7, 0\n"
"buffer_atomic_add v246, v245, s[8:11], s59 offen sc1\n"
"s_waitcnt lgkmcnt(5)\n"
"v_mfma_f32_16x16x32_bf16 v[232:235], a[96:99], a[32:35], v[232:235]\n"
"ds_read_b128 a[140:143], v252 offset:31744\n"
"v_mul_f32 v101, v101, v193\n"
"v_mul_f32 v102, v102, v193\n"
"s_add_u32 s72, s80, 81920\n"
"s_add_u32 m0, s6, 61440\n"
"v_mfma_f32_16x16x32_bf16 v[232:235], a[100:103], a[36:39], v[232:235]\n"
"ds_read_b128 a[96:99], v244 offset:0\n"
"v_mul_f32 v103, v103, v193\n"
"v_mul_f32 v116, v116, v193\n"
"buffer_load_dwordx4 v244, s[8:11], s72 offen offset:0 sc1 lds\n"
"v_mfma_f32_16x16x32_bf16 v[232:235], a[104:107], a[40:43], v[232:235]\n"
"ds_read_b128 a[100:103], v244 offset:5120\n"
"v_mul_f32 v117, v117, v193\n"
"v_mul_f32 v118, v118, v193\n"
"v_mfma_f32_16x16x32_bf16 v[232:235], a[108:111], a[44:47], v[232:235]\n"
"ds_read_b128 a[104:107], v244 offset:10240\n"
"v_mul_f32 v119, v119, v193\n"
"v_cvt_pk_bf16_f32 v100, v100, v101\n"
"buffer_load_dwordx4 v244, s[8:11], s72 offen offset:1024 sc1 lds\n"
"v_mfma_f32_16x16x32_bf16 v[236:239], a[112:115], a[32:35], v[236:239]\n"
"ds_read_b128 a[108:111], v244 offset:15360\n"
"v_cvt_pk_bf16_f32 v101, v102, v103\n"
"v_cvt_pk_bf16_f32 v102, v116, v117\n"
"v_mfma_f32_16x16x32_bf16 v[236:239], a[116:119], a[36:39], v[236:239]\n"
"ds_read_b128 a[112:115], v244 offset:1024\n"
"v_cvt_pk_bf16_f32 v103, v118, v119\n"
"v_mul_f32 v104, v104, v194\n"
"buffer_store_dwordx4 v[36:39], v244, s[8:11], s13 offen offset:3072 sc1\n"
"s_waitcnt lgkmcnt(6)\n"
"v_mfma_f32_16x16x32_bf16 v[236:239], a[120:123], a[40:43], v[236:239]\n"
"ds_read_b128 a[116:119], v244 offset:6144\n"
"v_mul_f32 v105, v105, v194\n"
"v_mul_f32 v106, v106, v194\n"
"v_mfma_f32_16x16x32_bf16 v[236:239], a[124:127], a[44:47], v[236:239]\n"
"ds_read_b128 a[120:123], v244 offset:11264\n"
"v_mul_f32 v107, v107, v194\n"
"v_mul_f32 v120, v120, v194\n"
"buffer_load_dwordx4 v244, s[8:11], s72 offen offset:2048 sc1 lds\n"
"v_mfma_f32_16x16x32_bf16 v[240:243], a[128:131], a[32:35], v[240:243]\n"
"ds_read_b128 a[124:127], v244 offset:16384\n"
"v_mul_f32 v121, v121, v194\n"
"v_mul_f32 v122, v122, v194\n"
"v_mfma_f32_16x16x32_bf16 v[240:243], a[132:135], a[36:39], v[240:243]\n"
"ds_read_b128 a[128:131], v244 offset:2048\n"
"v_mul_f32 v123, v123, v194\n"
"v_cvt_pk_bf16_f32 v104, v104, v105\n"
"buffer_load_dwordx4 v244, s[8:11], s72 offen offset:3072 sc1 lds\n"
"v_mfma_f32_16x16x32_bf16 v[240:243], a[136:139], a[40:43], v[240:243]\n"
"ds_read_b128 a[132:135], v244 offset:7168\n"
"v_cvt_pk_bf16_f32 v105, v106, v107\n"
"v_cvt_pk_bf16_f32 v106, v120, v121\n"
"s_waitcnt lgkmcnt(6)\n"
"v_mfma_f32_16x16x32_bf16 v[240:243], a[140:143], a[44:47], v[240:243]\n"
"ds_read_b128 a[136:139], v244 offset:12288\n"
"v_cvt_pk_bf16_f32 v107, v122, v123\n"
"v_mul_f32 v108, v108, v195\n"
"s_add_u32 m0, s6, 65536\n"
"v_mfma_f32_16x16x32_bf16 v[224:227], a[96:99], a[48:51], v[224:227]\n"
"ds_read_b128 a[140:143], v244 offset:17408\n"
"v_mul_f32 v109, v109, v195\n"
"v_mul_f32 v110, v110, v195\n"
"buffer_load_dwordx4 v250, s[8:11], s72 offen offset:0 sc1 lds\n"
"v_mfma_f32_16x16x32_bf16 v[224:227], a[100:103], a[52:55], v[224:227]\n"
"ds_read_b128 a[96:99], v244 offset:3072\n"
"v_mul_f32 v111, v111, v195\n"
"v_mul_f32 v124, v124, v195\n"
"v_mfma_f32_16x16x32_bf16 v[224:227], a[104:107], a[56:59], v[224:227]\n"
"ds_read_b128 a[100:103], v244 offset:8192\n"
"v_mul_f32 v125, v125, v195\n"
"v_mul_f32 v126, v126, v195\n"
"s_add_u32 s72, s80, 163840\n"
"s_add_u32 m0, s6, 81920\n"
"v_mfma_f32_16x16x32_bf16 v[224:227], a[108:111], a[60:63], v[224:227]\n"
"ds_read_b128 a[104:107], v244 offset:13312\n"
"v_mul_f32 v127, v127, v195\n"
"v_cvt_pk_bf16_f32 v108, v108, v109\n"
"buffer_load_dwordx4 v244, s[8:11], s72 offen offset:0 sc1 lds\n"
"s_waitcnt lgkmcnt(6)\n"
"v_mfma_f32_16x16x32_bf16 v[228:231], a[112:115], a[48:51], v[228:231]\n"
"ds_read_b128 a[108:111], v244 offset:18432\n"
"v_cvt_pk_bf16_f32 v109, v110, v111\n"
"v_cvt_pk_bf16_f32 v110, v124, v125\n"
"v_mfma_f32_16x16x32_bf16 v[228:231], a[116:119], a[52:55], v[228:231]\n"
"ds_read_b128 a[112:115], v244 offset:4096\n"
"v_cvt_pk_bf16_f32 v111, v126, v127\n"
"v_mfma_f32_16x16x32_bf16 v[228:231], a[120:123], a[56:59], v[228:231]\n"
"ds_read_b128 a[116:119], v244 offset:9216\n"
"buffer_store_dwordx4 v[40:43], v244, s[8:11], s14 offen offset:3072 sc1\n"
"v_mfma_f32_16x16x32_bf16 v[228:231], a[124:127], a[60:63], v[228:231]\n"
"s_barrier\n"
"v_mfma_f32_16x16x32_bf16 v[232:235], a[128:131], a[48:51], v[232:235]\n"
"ds_read_b128 a[120:123], v244 offset:14336\n"
"buffer_load_dwordx4 v244, s[8:11], s72 offen offset:1024 sc1 lds\n"
"s_waitcnt lgkmcnt(5)\n"
"v_mfma_f32_16x16x32_bf16 v[232:235], a[132:135], a[52:55], v[232:235]\n"
"ds_read_b128 a[124:127], v244 offset:19456\n"
"v_mfma_f32_16x16x32_bf16 v[232:235], a[136:139], a[56:59], v[232:235]\n"
"ds_read_b128 a[128:131], v244 offset:20480\n"
"buffer_load_dwordx4 v244, s[8:11], s72 offen offset:2048 sc1 lds\n"
"v_mfma_f32_16x16x32_bf16 v[232:235], a[140:143], a[60:63], v[232:235]\n"
"ds_read_b128 a[132:135], v244 offset:25600\n"
"v_mfma_f32_16x16x32_bf16 v[236:239], a[96:99], a[48:51], v[236:239]\n"
"ds_read_b128 a[136:139], v244 offset:30720\n"
"buffer_load_dwordx4 v244, s[8:11], s72 offen offset:3072 sc1 lds\n"
"v_mfma_f32_16x16x32_bf16 v[236:239], a[100:103], a[52:55], v[236:239]\n"
"ds_read_b128 a[140:143], v244 offset:35840\n"
"s_waitcnt lgkmcnt(6)\n"
"v_mfma_f32_16x16x32_bf16 v[236:239], a[104:107], a[56:59], v[236:239]\n"
"ds_read_b128 a[96:99], v244 offset:21504\n"
"s_add_u32 m0, s6, 86016\n"
"v_mfma_f32_16x16x32_bf16 v[236:239], a[108:111], a[60:63], v[236:239]\n"
"ds_read_b128 a[100:103], v244 offset:26624\n"
"buffer_load_dwordx4 v250, s[8:11], s72 offen offset:0 sc1 lds\n"
"v_mfma_f32_16x16x32_bf16 v[240:243], a[112:115], a[48:51], v[240:243]\n"
"ds_read_b128 a[104:107], v244 offset:31744\n"
"v_mfma_f32_16x16x32_bf16 v[240:243], a[116:119], a[52:55], v[240:243]\n"
"ds_read_b128 a[108:111], v244 offset:36864\n"
"buffer_store_dwordx4 v[44:47], v244, s[8:11], s15 offen offset:3072 sc1\n"
"s_waitcnt lgkmcnt(6)\n"
"v_mfma_f32_16x16x32_bf16 v[240:243], a[120:123], a[56:59], v[240:243]\n"
"ds_read_b128 a[112:115], v244 offset:22528\n"
"v_mfma_f32_16x16x32_bf16 v[240:243], a[124:127], a[60:63], v[240:243]\n"
"ds_read_b128 a[116:119], v244 offset:27648\n"
"s_add_u32 s72, s81, 0\n"
"s_add_u32 m0, s6, 102400\n"
"s_waitcnt vmcnt(23)\n"
"v_mfma_f32_16x16x32_bf16 v[224:227], a[128:131], a[64:67], v[224:227]\n"
"ds_read_b128 a[120:123], v244 offset:32768\n"
"buffer_load_dwordx4 v244, s[8:11], s72 offen offset:0 sc1 lds\n"
"v_mfma_f32_16x16x32_bf16 v[224:227], a[132:135], a[68:71], v[224:227]\n"
"ds_read_b128 a[124:127], v244 offset:37888\n"
"s_waitcnt lgkmcnt(6)\n"
"v_mfma_f32_16x16x32_bf16 v[224:227], a[136:139], a[72:75], v[224:227]\n"
"ds_read_b128 a[128:131], v244 offset:23552\n"
"buffer_load_dwordx4 v244, s[8:11], s72 offen offset:1024 sc1 lds\n"
"v_mfma_f32_16x16x32_bf16 v[224:227], a[140:143], a[76:79], v[224:227]\n"
"ds_read_b128 a[132:135], v244 offset:28672\n"
"v_mfma_f32_16x16x32_bf16 v[228:231], a[96:99], a[64:67], v[228:231]\n"
"ds_read_b128 a[136:139], v244 offset:33792\n"
"buffer_load_dwordx4 v244, s[8:11], s72 offen offset:2048 sc1 lds\n"
"v_mfma_f32_16x16x32_bf16 v[228:231], a[100:103], a[68:71], v[228:231]\n"
"ds_read_b128 a[140:143], v244 offset:38912\n"
"s_waitcnt lgkmcnt(6)\n"
"v_mfma_f32_16x16x32_bf16 v[228:231], a[104:107], a[72:75], v[228:231]\n"
"ds_read_b128 a[96:99], v244 offset:24576\n"
"buffer_load_dwordx4 v244, s[8:11], s72 offen offset:3072 sc1 lds\n"
"v_mfma_f32_16x16x32_bf16 v[228:231], a[108:111], a[76:79], v[228:231]\n"
"s_waitcnt vmcnt(20)\n"
"s_barrier\n"
"v_mfma_f32_16x16x32_bf16 v[232:235], a[112:115], a[64:67], v[232:235]\n"
"ds_read_b128 a[100:103], v244 offset:29696\n"
"buffer_store_dwordx4 v[64:67], v250, s[8:11], s12 offen offset:0 sc1\n"
"v_mfma_f32_16x16x32_bf16 v[232:235], a[116:119], a[68:71], v[232:235]\n"
"ds_read_b128 a[104:107], v244 offset:34816\n"
"s_waitcnt lgkmcnt(5)\n"
"v_mfma_f32_16x16x32_bf16 v[232:235], a[120:123], a[72:75], v[232:235]\n"
"ds_read_b128 a[108:111], v244 offset:39936\n"
"s_add_u32 m0, s6, 106496\n"
"v_mfma_f32_16x16x32_bf16 v[232:235], a[124:127], a[76:79], v[232:235]\n"
"ds_read_b128 a[112:115], v244 offset:40960\n"
"buffer_load_dwordx4 v250, s[8:11], s72 offen offset:0 sc1 lds\n"
"v_mfma_f32_16x16x32_bf16 v[236:239], a[128:131], a[64:67], v[236:239]\n"
"ds_read_b128 a[116:119], v244 offset:51200\n"
"v_mfma_f32_16x16x32_bf16 v[236:239], a[132:135], a[68:71], v[236:239]\n"
"ds_read_b128 a[120:123], v244 offset:41984\n"
"s_add_u32 s72, s82, 0\n"
"s_add_u32 m0, s6, 122880\n"
"s_waitcnt lgkmcnt(6)\n"
"v_mfma_f32_16x16x32_bf16 v[236:239], a[136:139], a[72:75], v[236:239]\n"
"ds_read_b128 a[124:127], v244 offset:52224\n"
"buffer_load_dwordx4 v244, s[8:11], s72 offen offset:0 sc1 lds\n"
"v_mfma_f32_16x16x32_bf16 v[236:239], a[140:143], a[76:79], v[236:239]\n"
"ds_read_b128 a[128:131], v244 offset:43008\n"
"v_mfma_f32_16x16x32_bf16 v[240:243], a[96:99], a[64:67], v[240:243]\n"
"ds_read_b128 a[132:135], v244 offset:53248\n"
"buffer_load_dwordx4 v244, s[8:11], s72 offen offset:1024 sc1 lds\n"
"s_waitcnt lgkmcnt(6)\n"
"v_mfma_f32_16x16x32_bf16 v[240:243], a[100:103], a[68:71], v[240:243]\n"
"ds_read_b128 a[136:139], v244 offset:44032\n"
"v_mfma_f32_16x16x32_bf16 v[240:243], a[104:107], a[72:75], v[240:243]\n"
"ds_read_b128 a[140:143], v244 offset:54272\n"
"v_mfma_f32_16x16x32_bf16 v[240:243], a[108:111], a[76:79], v[240:243]\n"
"buffer_store_dwordx4 v[68:71], v250, s[8:11], s13 offen offset:0 sc1\n"
"s_waitcnt lgkmcnt(5)\n"
"v_mfma_f32_16x16x32_bf16 v[16:19], a[112:115], v[128:131], 0\n"
"v_mfma_f32_16x16x32_bf16 v[16:19], a[116:119], v[144:147], v[16:19]\n"
"buffer_load_dwordx4 v244, s[8:11], s72 offen offset:2048 sc1 lds\n"
"v_mfma_f32_16x16x32_bf16 v[20:23], a[112:115], v[132:135], 0\n"
"v_mfma_f32_16x16x32_bf16 v[20:23], a[116:119], v[148:151], v[20:23]\n"
"v_mfma_f32_16x16x32_bf16 v[24:27], a[112:115], v[136:139], 0\n"
"ds_read_b128 a[96:99], v244 offset:45056\n"
"s_add_u32 s87, s87, 8388608\n"
"v_mfma_f32_16x16x32_bf16 v[24:27], a[116:119], v[152:155], v[24:27]\n"
"ds_read_b128 a[100:103], v244 offset:55296\n"
"s_add_u32 s30, s30, 8388608\n"
"buffer_load_dwordx4 v244, s[8:11], s72 offen offset:3072 sc1 lds\n"
"v_mfma_f32_16x16x32_bf16 v[28:31], a[112:115], v[140:143], 0\n"
"v_lshlrev_b32 v218, 16, v208\n"
"v_and_b32 v219, 0xffff0000, v208\n"
"v_mfma_f32_16x16x32_bf16 v[28:31], a[116:119], v[156:159], v[28:31]\n"
"v_fmamk_f32 v224, v218, 0x4119ac27, v224\n"
"v_fmamk_f32 v225, v219, 0x4119ac27, v225\n"
"v_mfma_f32_16x16x32_bf16 v[48:51], a[120:123], v[128:131], 0\n"
"v_cvt_pk_bf16_f32 v208, v224, v225\n"
"v_lshlrev_b32 v218, 16, v209\n"
"s_waitcnt lgkmcnt(2)\n"
"v_mfma_f32_16x16x32_bf16 v[48:51], a[124:127], v[144:147], v[48:51]\n"
"v_and_b32 v219, 0xffff0000, v209\n"
"v_fmamk_f32 v226, v218, 0x4119ac27, v226\n"
"buffer_store_dwordx4 v[72:75], v250, s[8:11], s14 offen offset:0 sc1\n"
"v_mfma_f32_16x16x32_bf16 v[52:55], a[120:123], v[132:135], 0\n"
"v_fmamk_f32 v227, v219, 0x4119ac27, v227\n"
"v_cvt_pk_bf16_f32 v209, v226, v227\n"
"v_mfma_f32_16x16x32_bf16 v[52:55], a[124:127], v[148:151], v[52:55]\n"
"v_lshlrev_b32 v218, 16, v210\n"
"v_and_b32 v219, 0xffff0000, v210\n"
"v_mfma_f32_16x16x32_bf16 v[56:59], a[120:123], v[136:139], 0\n"
"ds_read_b128 a[104:107], v244 offset:46080\n"
"v_fmamk_f32 v228, v218, 0x4119ac27, v228\n"
"v_mfma_f32_16x16x32_bf16 v[56:59], a[124:127], v[152:155], v[56:59]\n"
"ds_read_b128 a[108:111], v244 offset:56320\n"
"v_fmamk_f32 v229, v219, 0x4119ac27, v229\n"
"s_add_u32 m0, s6, 126976\n"
"v_mfma_f32_16x16x32_bf16 v[60:63], a[120:123], v[140:143], 0\n"
"v_cvt_pk_bf16_f32 v210, v228, v229\n"
"v_lshlrev_b32 v218, 16, v211\n"
"buffer_load_dwordx4 v250, s[8:11], s72 offen offset:0 sc1 lds\n"
"v_mfma_f32_16x16x32_bf16 v[60:63], a[124:127], v[156:159], v[60:63]\n"
"v_and_b32 v219, 0xffff0000, v211\n"
"v_fmamk_f32 v230, v218, 0x4119ac27, v230\n"
"v_mfma_f32_16x16x32_bf16 v[80:83], a[128:131], v[128:131], 0\n"
"v_fmamk_f32 v231, v219, 0x4119ac27, v231\n"
"v_cvt_pk_bf16_f32 v211, v230, v231\n"
"v_mfma_f32_16x16x32_bf16 v[80:83], a[132:135], v[144:147], v[80:83]\n"
"v_lshlrev_b32 v218, 16, v212\n"
"v_and_b32 v219, 0xffff0000, v212\n"
"v_mfma_f32_16x16x32_bf16 v[84:87], a[128:131], v[132:135], 0\n"
"v_fmamk_f32 v232, v218, 0x4119ac27, v232\n"
"v_fmamk_f32 v233, v219, 0x4119ac27, v233\n"
"s_add_u32 s72, s83, 0\n"
"s_add_u32 m0, s6, 143360\n"
"v_mfma_f32_16x16x32_bf16 v[84:87], a[132:135], v[148:151], v[84:87]\n"
"v_cvt_pk_bf16_f32 v212, v232, v233\n"
"v_lshlrev_b32 v218, 16, v213\n"
"buffer_load_dwordx4 v244, s[8:11], s72 offen offset:0 sc1 lds\n"
"v_mfma_f32_16x16x32_bf16 v[88:91], a[128:131], v[136:139], 0\n"
"ds_read_b128 a[112:115], v244 offset:47104\n"
"v_and_b32 v219, 0xffff0000, v213\n"
"v_mfma_f32_16x16x32_bf16 v[88:91], a[132:135], v[152:155], v[88:91]\n"
"ds_read_b128 a[116:119], v244 offset:57344\n"
"v_fmamk_f32 v234, v218, 0x4119ac27, v234\n"
"v_mfma_f32_16x16x32_bf16 v[92:95], a[128:131], v[140:143], 0\n"
"v_fmamk_f32 v235, v219, 0x4119ac27, v235\n"
"v_cvt_pk_bf16_f32 v213, v234, v235\n"
"v_mfma_f32_16x16x32_bf16 v[92:95], a[132:135], v[156:159], v[92:95]\n"
"v_lshlrev_b32 v218, 16, v214\n"
"v_and_b32 v219, 0xffff0000, v214\n"
"buffer_load_dwordx4 v244, s[8:11], s72 offen offset:1024 sc1 lds\n"
"v_mfma_f32_16x16x32_bf16 v[112:115], a[136:139], v[128:131], 0\n"
"v_fmamk_f32 v236, v218, 0x4119ac27, v236\n"
"v_fmamk_f32 v237, v219, 0x4119ac27, v237\n"
"v_mfma_f32_16x16x32_bf16 v[112:115], a[140:143], v[144:147], v[112:115]\n"
"v_cvt_pk_bf16_f32 v214, v236, v237\n"
"v_lshlrev_b32 v218, 16, v215\n"
"v_mfma_f32_16x16x32_bf16 v[116:119], a[136:139], v[132:135], 0\n"
"v_and_b32 v219, 0xffff0000, v215\n"
"v_fmamk_f32 v238, v218, 0x4119ac27, v238\n"
"v_mfma_f32_16x16x32_bf16 v[116:119], a[140:143], v[148:151], v[116:119]\n"
"v_fmamk_f32 v239, v219, 0x4119ac27, v239\n"
"v_cvt_pk_bf16_f32 v215, v238, v239\n"
"buffer_store_dwordx2 v[208:209], v244, s[8:11], s87 offen offset:0 sc1\n"
"v_mfma_f32_16x16x32_bf16 v[120:123], a[136:139], v[136:139], 0\n"
"ds_read_b128 a[120:123], v244 offset:48128\n"
"v_lshlrev_b32 v218, 16, v216\n"
"v_mfma_f32_16x16x32_bf16 v[120:123], a[140:143], v[152:155], v[120:123]\n"
"ds_read_b128 a[124:127], v244 offset:58368\n"
"v_and_b32 v219, 0xffff0000, v216\n"
"v_mfma_f32_16x16x32_bf16 v[124:127], a[136:139], v[140:143], 0\n"
"v_fmamk_f32 v240, v218, 0x4119ac27, v240\n"
"v_fmamk_f32 v241, v219, 0x4119ac27, v241\n"
"v_mfma_f32_16x16x32_bf16 v[124:127], a[140:143], v[156:159], v[124:127]\n"
"v_cvt_pk_bf16_f32 v216, v240, v241\n"
"v_lshlrev_b32 v218, 16, v217\n"
"buffer_load_dwordx4 v244, s[8:11], s72 offen offset:2048 sc1 lds\n"
"s_waitcnt lgkmcnt(2)\n"
"v_mfma_f32_16x16x32_bf16 v[160:163], a[96:99], v[128:131], 0\n"
"v_and_b32 v219, 0xffff0000, v217\n"
"v_fmamk_f32 v242, v218, 0x4119ac27, v242\n"
"v_mfma_f32_16x16x32_bf16 v[160:163], a[100:103], v[144:147], v[160:163]\n"
"v_fmamk_f32 v243, v219, 0x4119ac27, v243\n"
"v_cvt_pk_bf16_f32 v217, v242, v243\n"
"v_mfma_f32_16x16x32_bf16 v[164:167], a[96:99], v[132:135], 0\n"
"v_lshlrev_b32 v224, 16, v208\n"
"v_and_b32 v225, 0xffff0000, v208\n"
"v_mfma_f32_16x16x32_bf16 v[164:167], a[100:103], v[148:151], v[164:167]\n"
"v_lshlrev_b32 v226, 16, v209\n"
"v_and_b32 v227, 0xffff0000, v209\n"
"buffer_store_dwordx2 v[210:211], v244, s[8:11], s30 offen offset:0 sc1\n"
"v_mfma_f32_16x16x32_bf16 v[168:171], a[96:99], v[136:139], 0\n"
"ds_read_b128 a[128:131], v244 offset:49152\n"
"v_lshlrev_b32 v228, 16, v210\n"
"v_mfma_f32_16x16x32_bf16 v[168:171], a[100:103], v[152:155], v[168:171]\n"
"ds_read_b128 a[132:135], v244 offset:59392\n"
"v_and_b32 v229, 0xffff0000, v210\n"
"v_mfma_f32_16x16x32_bf16 v[172:175], a[96:99], v[140:143], 0\n"
"v_lshlrev_b32 v230, 16, v211\n"
"v_and_b32 v231, 0xffff0000, v211\n"
"v_mfma_f32_16x16x32_bf16 v[172:175], a[100:103], v[156:159], v[172:175]\n"
"v_lshlrev_b32 v232, 16, v212\n"
"v_and_b32 v233, 0xffff0000, v212\n"
"buffer_load_dwordx4 v244, s[8:11], s72 offen offset:3072 sc1 lds\n"
"v_mfma_f32_16x16x32_bf16 v[176:179], a[104:107], v[128:131], 0\n"
"v_lshlrev_b32 v234, 16, v213\n"
"v_and_b32 v235, 0xffff0000, v213\n"
"v_mfma_f32_16x16x32_bf16 v[176:179], a[108:111], v[144:147], v[176:179]\n"
"v_lshlrev_b32 v236, 16, v214\n"
"v_and_b32 v237, 0xffff0000, v214\n"
"v_mfma_f32_16x16x32_bf16 v[180:183], a[104:107], v[132:135], 0\n"
"v_lshlrev_b32 v238, 16, v215\n"
"v_and_b32 v239, 0xffff0000, v215\n"
"v_mfma_f32_16x16x32_bf16 v[180:183], a[108:111], v[148:151], v[180:183]\n"
"v_lshlrev_b32 v240, 16, v216\n"
"v_and_b32 v241, 0xffff0000, v216\n"
"buffer_store_dwordx2 v[212:213], v244, s[8:11], s87 offen offset:1024 sc1\n"
"v_mfma_f32_16x16x32_bf16 v[184:187], a[104:107], v[136:139], 0\n"
"ds_read_b128 a[136:139], v244 offset:50176\n"
"v_lshlrev_b32 v242, 16, v217\n"
"v_mfma_f32_16x16x32_bf16 v[184:187], a[108:111], v[152:155], v[184:187]\n"
"ds_read_b128 a[140:143], v244 offset:60416\n"
"v_and_b32 v243, 0xffff0000, v217\n"
"v_mfma_f32_16x16x32_bf16 v[188:191], a[104:107], v[140:143], 0\n"
"v_add_f32 v220, v224, v225\n"
"v_add_f32 v221, v226, v227\n"
"v_mfma_f32_16x16x32_bf16 v[188:191], a[108:111], v[156:159], v[188:191]\n"
"v_add_f32 v220, v220, v228\n"
"v_add_f32 v221, v221, v229\n"
"s_add_u32 m0, s6, 147456\n"
"v_mfma_f32_16x16x32_bf16 v[192:195], a[112:115], v[128:131], 0\n"
"v_add_f32 v221, v221, v230\n"
"v_add_f32 v220, v220, v231\n"
"buffer_load_dwordx4 v250, s[8:11], s72 offen offset:0 sc1 lds\n"
"v_mfma_f32_16x16x32_bf16 v[192:195], a[116:119], v[144:147], v[192:195]\n"
"v_add_f32 v220, v220, v232\n"
"v_add_f32 v221, v221, v233\n"
"v_mfma_f32_16x16x32_bf16 v[196:199], a[112:115], v[132:135], 0\n"
"v_add_f32 v221, v221, v234\n"
"v_add_f32 v220, v220, v235\n"
"v_mfma_f32_16x16x32_bf16 v[196:199], a[116:119], v[148:151], v[196:199]\n"
"v_add_f32 v220, v220, v236\n"
"v_add_f32 v221, v221, v237\n"
"v_mfma_f32_16x16x32_bf16 v[200:203], a[112:115], v[136:139], 0\n"
"v_add_f32 v221, v221, v238\n"
"v_add_f32 v220, v220, v239\n"
"buffer_store_dwordx2 v[214:215], v244, s[8:11], s30 offen offset:1024 sc1\n"
"v_mfma_f32_16x16x32_bf16 v[200:203], a[116:119], v[152:155], v[200:203]\n"
"v_add_f32 v220, v220, v240\n"
"v_add_f32 v221, v221, v241\n"
"v_mfma_f32_16x16x32_bf16 v[204:207], a[112:115], v[140:143], 0\n"
"v_add_f32 v221, v221, v242\n"
"v_add_f32 v220, v220, v243\n"
"v_mfma_f32_16x16x32_bf16 v[204:207], a[116:119], v[156:159], v[204:207]\n"
"v_add_f32 v220, v220, v221\n"
"v_mov_b32 v221, v220\n"
"s_waitcnt lgkmcnt(0)\n"
"v_mfma_f32_16x16x32_bf16 a[208:211], a[120:123], v[128:131], 0\n"
"v_permlane32_swap_b32 v220, v221\n"
"v_add_f32 v220, v220, v221\n"
"v_mfma_f32_16x16x32_bf16 a[208:211], a[124:127], v[144:147], a[208:211]\n"
"v_mov_b32 v221, v220\n"
"s_nop 1\n"
"v_permlane16_swap_b32 v220, v221\n"
"v_mfma_f32_16x16x32_bf16 a[212:215], a[120:123], v[132:135], 0\n"
"v_add_f32 v220, v220, v221\n"
"v_mul_f32 v220, 0x3c4ccccd, v220\n"
"v_mfma_f32_16x16x32_bf16 a[212:215], a[124:127], v[148:151], a[212:215]\n"
"v_sub_f32 v218, v224, v220\n"
"v_mul_f32 v222, v218, v218\n"
"v_mfma_f32_16x16x32_bf16 a[216:219], a[120:123], v[136:139], 0\n"
"v_sub_f32 v219, v225, v220\n"
"v_mul_f32 v223, v219, v219\n"
"buffer_store_dwordx2 v[216:217], v244, s[8:11], s87 offen offset:2048 sc1\n"
"v_mfma_f32_16x16x32_bf16 a[216:219], a[124:127], v[152:155], a[216:219]\n"
"v_sub_f32 v218, v226, v220\n"
"v_fmac_f32 v222, v218, v218\n"
"v_mfma_f32_16x16x32_bf16 a[220:223], a[120:123], v[140:143], 0\n"
"v_sub_f32 v219, v227, v220\n"
"v_fmac_f32 v223, v219, v219\n"
"v_mfma_f32_16x16x32_bf16 a[220:223], a[124:127], v[156:159], a[220:223]\n"
"v_sub_f32 v218, v228, v220\n"
"v_fmac_f32 v222, v218, v218\n"
"v_mfma_f32_16x16x32_bf16 a[224:227], a[128:131], v[128:131], 0\n"
"v_sub_f32 v219, v229, v220\n"
"v_fmac_f32 v223, v219, v219\n"
"v_mfma_f32_16x16x32_bf16 a[224:227], a[132:135], v[144:147], a[224:227]\n"
"v_sub_f32 v218, v230, v220\n"
"v_fmac_f32 v222, v218, v218\n"
"v_mfma_f32_16x16x32_bf16 a[228:231], a[128:131], v[132:135], 0\n"
"v_sub_f32 v219, v231, v220\n"
"v_fmac_f32 v223, v219, v219\n"
"v_mfma_f32_16x16x32_bf16 a[228:231], a[132:135], v[148:151], a[228:231]\n"
"s_waitcnt vmcnt(30)\n"
"s_barrier\n"
"v_mfma_f32_16x16x32_bf16 a[232:235], a[128:131], v[136:139], 0\n"
"ds_read_b128 a[96:99], v244 offset:61440\n"
"v_sub_f32 v218, v232, v220\n"
"buffer_store_dwordx4 v[76:79], v250, s[8:11], s15 offen offset:0 sc1\n"
"v_mfma_f32_16x16x32_bf16 a[232:235], a[132:135], v[152:155], a[232:235]\n"
"ds_read_b128 a[100:103], v251 offset:6144\n"
"v_fmac_f32 v222, v218, v218\n"
"v_mfma_f32_16x16x32_bf16 a[236:239], a[128:131], v[140:143], 0\n"
"ds_read_b128 a[104:107], v244 offset:62464\n"
"v_sub_f32 v219, v233, v220\n"
"s_add_u32 s59, s7, 0\n"
"buffer_load_dword v249, off, s[8:11], s59 sc1\n"
"v_mfma_f32_16x16x32_bf16 a[236:239], a[132:135], v[156:159], a[236:239]\n"
"ds_read_b128 a[108:111], v251 offset:7168\n"
"v_fmac_f32 v223, v219, v219\n"
"v_mfma_f32_16x16x32_bf16 a[240:243], a[136:139], v[128:131], 0\n"
"ds_read_b128 a[112:115], v244 offset:63488\n"
"v_sub_f32 v218, v234, v220\n"
"s_add_u32 s72, s40, 0\n"
"s_add_u32 m0, s57, 0\n"
"v_mfma_f32_16x16x32_bf16 a[240:243], a[140:143], v[144:147], a[240:243]\n"
"ds_read_b128 a[116:119], v251 offset:8192\n"
"v_fmac_f32 v222, v218, v218\n"
"buffer_load_dwordx4 v244, s[8:11], s72 offen offset:0 sc1 lds\n"
"v_mfma_f32_16x16x32_bf16 a[244:247], a[136:139], v[132:135], 0\n"
"v_sub_f32 v219, v235, v220\n"
"v_fmac_f32 v223, v219, v219\n"
"v_mfma_f32_16x16x32_bf16 a[244:247], a[140:143], v[148:151], a[244:247]\n"
"v_sub_f32 v218, v236, v220\n"
"v_fmac_f32 v222, v218, v218\n"
"v_mfma_f32_16x16x32_bf16 a[248:251], a[136:139], v[136:139], 0\n"
"ds_read_b128 a[120:123], v244 offset:64512\n"
"v_sub_f32 v219, v237, v220\n"
"v_mfma_f32_16x16x32_bf16 a[248:251], a[140:143], v[152:155], a[248:251]\n"
"ds_read_b128 a[124:127], v251 offset:9216\n"
"v_fmac_f32 v223, v219, v219\n"
"buffer_load_dwordx4 v244, s[8:11], s72 offen offset:1024 sc1 lds\n"
"v_mfma_f32_16x16x32_bf16 a[252:255], a[136:139], v[140:143], 0\n"
"v_sub_f32 v218, v238, v220\n"
"v_fmac_f32 v222, v218, v218\n"
"v_mfma_f32_16x16x32_bf16 a[252:255], a[140:143], v[156:159], a[252:255]\n"
"v_sub_f32 v219, v239, v220\n"
"v_fmac_f32 v223, v219, v219\n"
"s_waitcnt lgkmcnt(2)\n"
"v_mfma_f32_16x16x32_bf16 v[16:19], a[96:99], v[0:3], v[16:19]\n"
"v_sub_f32 v218, v240, v220\n"
"v_fmac_f32 v222, v218, v218\n"
"v_mfma_f32_16x16x32_bf16 v[16:19], a[100:103], v[32:35], v[16:19]\n"
"v_sub_f32 v219, v241, v220\n"
"v_fmac_f32 v223, v219, v219\n"
"buffer_load_dwordx4 v244, s[8:11], s72 offen offset:2048 sc1 lds\n"
"v_mfma_f32_16x16x32_bf16 v[20:23], a[96:99], v[4:7], v[20:23]\n"
"v_sub_f32 v218, v242, v220\n"
"v_fmac_f32 v222, v218, v218\n"
"v_mfma_f32_16x16x32_bf16 v[20:23], a[100:103], v[36:39], v[20:23]\n"
"v_sub_f32 v219, v243, v220\n"
"v_fmac_f32 v223, v219, v219\n"
"v_mfma_f32_16x16x32_bf16 v[24:27], a[96:99], v[8:11], v[24:27]\n"
"ds_read_b128 a[128:131], v251 offset:0\n"
"v_add_f32 v221, v222, v223\n"
"v_mfma_f32_16x16x32_bf16 v[24:27], a[100:103], v[40:43], v[24:27]\n"
"ds_read_b128 a[132:135], v251 offset:10240\n"
"v_mov_b32 v222, v221\n"
"buffer_store_dwordx4 v[96:99], v250, s[8:11], s12 offen offset:1024 sc1\n"
"v_mfma_f32_16x16x32_bf16 v[28:31], a[96:99], v[12:15], v[28:31]\n"
"v_permlane32_swap_b32 v221, v222\n"
"v_add_f32 v221, v221, v222\n"
"v_mfma_f32_16x16x32_bf16 v[28:31], a[100:103], v[44:47], v[28:31]\n"
"v_mov_b32 v222, v221\n"
"s_nop 1\n"
"v_permlane16_swap_b32 v221, v222\n"
"v_mfma_f32_16x16x32_bf16 v[48:51], a[104:107], v[0:3], v[48:51]\n"
"v_add_f32 v221, v221, v222\n"
"v_lshrrev_b32 v218, 4, v248\n"
"v_mfma_f32_16x16x32_bf16 v[48:51], a[108:111], v[32:35], v[48:51]\n"
"v_lshlrev_b32 v218, 28, v218\n"
"v_add_u32 v218, v244, v218\n"
"buffer_load_dwordx4 v244, s[8:11], s72 offen offset:3072 sc1 lds\n"
"v_mfma_f32_16x16x32_bf16 v[52:55], a[104:107], v[4:7], v[52:55]\n"
"s_add_u32 s61, s4, 3145728\n"
"s_lshr_b32 s88, s56, 3\n"
"v_mfma_f32_16x16x32_bf16 v[52:55], a[108:111], v[36:39], v[52:55]\n"
"s_lshl2_add_u32 s88, s55, s88\n"
"s_lshl2_add_u32 s89, s88, s52\n"
"v_mfma_f32_16x16x32_bf16 v[56:59], a[104:107], v[8:11], v[56:59]\n"
"ds_read_b128 a[136:139], v251 offset:1024\n"
"s_and_b32 s90, s56, 7\n"
"v_mfma_f32_16x16x32_bf16 v[56:59], a[108:111], v[40:43], v[56:59]\n"
"ds_read_b128 a[140:143], v251 offset:11264\n"
"s_mul_i32 s62, s89, 49152\n"
"s_add_u32 m0, s57, 4096\n"
"v_mfma_f32_16x16x32_bf16 v[60:63], a[104:107], v[12:15], v[60:63]\n"
"s_add_u32 s62, s62, 11534336\n"
"s_mul_i32 s88, s90, 160\n"
"buffer_load_dwordx4 v250, s[8:11], s72 offen offset:0 sc1 lds\n"
"v_mfma_f32_16x16x32_bf16 v[60:63], a[108:111], v[44:47], v[60:63]\n"
"s_add_u32 s68, s5, s88\n"
"s_lshl3_add_u32 s88, s89, s90\n"
"v_mfma_f32_16x16x32_bf16 v[80:83], a[112:115], v[0:3], v[80:83]\n"
"s_mul_i32 s88, s88, 2560\n"
"s_add_u32 s67, s88, 58720256\n"
"v_mfma_f32_16x16x32_bf16 v[80:83], a[116:119], v[32:35], v[80:83]\n"
"s_lshr_b32 s90, s90, 1\n"
"s_lshl2_add_u32 s88, s55, s90\n"
"v_mfma_f32_16x16x32_bf16 v[84:87], a[112:115], v[4:7], v[84:87]\n"
"s_lshl3_add_u32 s88, s88, s55\n"
"s_lshl_b32 s66, s88, 10\n"
"buffer_load_dwordx4 v250, s[8:11], s72 offen offset:1024 sc1 lds\n"
"v_mfma_f32_16x16x32_bf16 v[84:87], a[116:119], v[36:39], v[84:87]\n"
"s_lshr_b32 s88, s56, 4\n"
"s_lshl1_add_u32 s88, s55, s88\n"
"v_mfma_f32_16x16x32_bf16 v[88:91], a[112:115], v[8:11], v[88:91]\n"
"ds_read_b128 a[96:99], v251 offset:2048\n"
"s_lshl2_add_u32 s88, s88, s52\n"
"v_mfma_f32_16x16x32_bf16 v[88:91], a[116:119], v[40:43], v[88:91]\n"
"ds_read_b128 a[100:103], v251 offset:12288\n"
"s_lshl_b32 s89, s88, 11\n"
"v_mfma_f32_16x16x32_bf16 v[92:95], a[112:115], v[12:15], v[92:95]\n"
"s_add_u32 s63, s89, 262144\n"
"s_and_b32 s89, s56, 15\n"
"buffer_store_dwordx2 v[220:221], v218, s[8:11], s31 offen sc1\n"
"v_mfma_f32_16x16x32_bf16 v[92:95], a[116:119], v[44:47], v[92:95]\n"
"s_lshl_b32 s88, s88, 5\n"
"s_add_u32 s88, s88, s89\n"
"s_waitcnt lgkmcnt(2)\n"
"v_mfma_f32_16x16x32_bf16 v[112:115], a[120:123], v[0:3], v[112:115]\n"
"s_mul_i32 s88, s88, 3072\n"
"s_add_u32 s64, s88, 11534336\n"
"v_mfma_f32_16x16x32_bf16 v[112:115], a[124:127], v[32:35], v[112:115]\n"
"s_and_b32 s88, s53, 3\n"
"s_lshl3_add_u32 s90, s56, s54\n"
"v_mfma_f32_16x16x32_bf16 v[116:119], a[120:123], v[4:7], v[116:119]\n"
"s_lshl_b32 s65, s90, 10\n"
"s_lshl_b32 s89, s55, 10\n"
"v_mfma_f32_16x16x32_bf16 v[116:119], a[124:127], v[36:39], v[116:119]\n"
"s_lshl1_add_u32 s88, s88, s89\n"
"s_lshl3_add_u32 s89, s76, s88\n"
"v_mfma_f32_16x16x32_bf16 v[120:123], a[120:123], v[8:11], v[120:123]\n"
"ds_read_b128 a[104:107], v251 offset:3072\n"
"s_mul_i32 s89, s89, 2560\n"
"v_mfma_f32_16x16x32_bf16 v[120:123], a[124:127], v[40:43], v[120:123]\n"
"ds_read_b128 a[108:111], v251 offset:13312\n"
"s_add_u32 s76, s89, 58720256\n"
"v_mfma_f32_16x16x32_bf16 v[124:127], a[120:123], v[12:15], v[124:127]\n"
"s_lshl3_add_u32 s89, s77, s88\n"
"s_mul_i32 s89, s89, 2560\n"
"s_waitcnt vmcnt(8)\n"
"v_cmp_gt_u32 vcc, 4, v249\n"
"s_cbranch_vccnz L_spin_o0_%=\n"
"L_back_o0_%=:\n"
"v_mfma_f32_16x16x32_bf16 v[124:127], a[124:127], v[44:47], v[124:127]\n"
"s_add_u32 s77, s89, 58720256\n"
"s_lshl3_add_u32 s89, s78, s88\n"
"buffer_store_dwordx4 v[100:103], v250, s[8:11], s13 offen offset:1024 sc1\n"
"v_mfma_f32_16x16x32_bf16 v[160:163], a[128:131], v[0:3], v[160:163]\n"
"s_mul_i32 s89, s89, 2560\n"
"s_add_u32 s78, s89, 58720256\n"
"v_mfma_f32_16x16x32_bf16 v[160:163], a[132:135], v[32:35], v[160:163]\n"
"s_lshl3_add_u32 s89, s79, s88\n"
"s_mul_i32 s89, s89, 2560\n"
"v_mfma_f32_16x16x32_bf16 v[164:167], a[128:131], v[4:7], v[164:167]\n"
"s_add_u32 s79, s89, 58720256\n"
"s_mov_b64 s[16:17], s[38:39]\n"
"v_mfma_f32_16x16x32_bf16 v[164:167], a[132:135], v[36:39], v[164:167]\n"
"s_and_b32 s17, s17, 0xffff\n"
"s_mov_b32 s18, 2621440\n"
"s_add_u32 s70, s12, s84\n"
"buffer_load_dwordx4 a[80:83], v244, s[8:11], s70 offen offset:0 sc1\n"
"v_mfma_f32_16x16x32_bf16 v[168:171], a[128:131], v[8:11], v[168:171]\n"
"ds_read_b128 a[112:115], v251 offset:4096\n"
"s_mov_b32 s19, 0x20000\n"
"v_mfma_f32_16x16x32_bf16 v[168:171], a[132:135], v[40:43], v[168:171]\n"
"ds_read_b128 a[116:119], v251 offset:14336\n"
"s_mov_b64 s[20:21], s[24:25]\n"
"v_mfma_f32_16x16x32_bf16 v[172:175], a[128:131], v[12:15], v[172:175]\n"
"s_and_b32 s21, s21, 0xffff\n"
"s_mov_b32 s22, 2621440\n"
"v_mfma_f32_16x16x32_bf16 v[172:175], a[132:135], v[44:47], v[172:175]\n"
"s_mov_b32 s23, 0x20000\n"
"v_mfma_f32_16x16x32_bf16 v[176:179], a[136:139], v[0:3], v[176:179]\n"
"buffer_load_dwordx4 a[84:87], v244, s[8:11], s70 offen offset:1024 sc1\n"
"v_mfma_f32_16x16x32_bf16 v[176:179], a[140:143], v[32:35], v[176:179]\n"
"v_mfma_f32_16x16x32_bf16 v[180:183], a[136:139], v[4:7], v[180:183]\n"
"v_mfma_f32_16x16x32_bf16 v[180:183], a[140:143], v[36:39], v[180:183]\n"
"v_mfma_f32_16x16x32_bf16 v[184:187], a[136:139], v[8:11], v[184:187]\n"
"ds_read_b128 a[120:123], v251 offset:5120\n"
"s_add_u32 s71, s13, s84\n"
"buffer_load_dwordx4 a[88:91], v244, s[8:11], s71 offen offset:0 sc1\n"
"v_mfma_f32_16x16x32_bf16 v[184:187], a[140:143], v[40:43], v[184:187]\n"
"ds_read_b128 a[124:127], v251 offset:15360\n"
"v_mfma_f32_16x16x32_bf16 v[188:191], a[136:139], v[12:15], v[188:191]\n"
"v_mfma_f32_16x16x32_bf16 v[188:191], a[140:143], v[44:47], v[188:191]\n"
"s_waitcnt lgkmcnt(2)\n"
"v_mfma_f32_16x16x32_bf16 v[192:195], a[96:99], v[0:3], v[192:195]\n"
"buffer_store_dwordx4 v[104:107], v250, s[8:11], s14 offen offset:1024 sc1\n"
"v_mfma_f32_16x16x32_bf16 v[192:195], a[100:103], v[32:35], v[192:195]\n"
"v_mfma_f32_16x16x32_bf16 v[196:199], a[96:99], v[4:7], v[196:199]\n"
"v_mfma_f32_16x16x32_bf16 v[196:199], a[100:103], v[36:39], v[196:199]\n"
"v_mfma_f32_16x16x32_bf16 v[200:203], a[96:99], v[8:11], v[200:203]\n"
"buffer_load_dwordx4 a[92:95], v244, s[8:11], s71 offen offset:1024 sc1\n"
"v_mfma_f32_16x16x32_bf16 v[200:203], a[100:103], v[40:43], v[200:203]\n"
"v_mfma_f32_16x16x32_bf16 v[204:207], a[96:99], v[12:15], v[204:207]\n"
"v_mfma_f32_16x16x32_bf16 v[204:207], a[100:103], v[44:47], v[204:207]\n"
"v_mfma_f32_16x16x32_bf16 a[208:211], a[104:107], v[0:3], a[208:211]\n"
"s_add_u32 s70, s14, s84\n"
"buffer_load_dwordx4 a[144:147], v244, s[8:11], s70 offen offset:0 sc1\n"
"v_mfma_f32_16x16x32_bf16 a[208:211], a[108:111], v[32:35], a[208:211]\n"
"v_mfma_f32_16x16x32_bf16 a[212:215], a[104:107], v[4:7], a[212:215]\n"
"v_mfma_f32_16x16x32_bf16 a[212:215], a[108:111], v[36:39], a[212:215]\n"
"v_mfma_f32_16x16x32_bf16 a[216:219], a[104:107], v[8:11], a[216:219]\n"
"buffer_load_dwordx4 a[148:151], v244, s[8:11], s70 offen offset:1024 sc1\n"
"v_mfma_f32_16x16x32_bf16 a[216:219], a[108:111], v[40:43], a[216:219]\n"
"v_mfma_f32_16x16x32_bf16 a[220:223], a[104:107], v[12:15], a[220:223]\n"
"v_mfma_f32_16x16x32_bf16 a[220:223], a[108:111], v[44:47], a[220:223]\n"
"v_mfma_f32_16x16x32_bf16 a[224:227], a[112:115], v[0:3], a[224:227]\n"
"buffer_store_dwordx4 v[108:111], v250, s[8:11], s15 offen offset:1024 sc1\n"
"v_mfma_f32_16x16x32_bf16 a[224:227], a[116:119], v[32:35], a[224:227]\n"
"v_mfma_f32_16x16x32_bf16 a[228:231], a[112:115], v[4:7], a[228:231]\n"
"v_mfma_f32_16x16x32_bf16 a[228:231], a[116:119], v[36:39], a[228:231]\n"
"s_waitcnt vmcnt(9)\n"
"s_barrier\n"
"s_add_u32 s59, s33, 896\n"
"buffer_atomic_add v246, v245, s[8:11], s59 offen sc1\n"
"v_mfma_f32_16x16x32_bf16 a[232:235], a[112:115], v[8:11], a[232:235]\n"
"ds_read_b128 a[128:131], v251 offset:16384\n"
"s_add_u32 s71, s15, s84\n"
"buffer_load_dwordx4 a[152:155], v244, s[8:11], s71 offen offset:0 sc1\n"
"v_mfma_f32_16x16x32_bf16 a[232:235], a[116:119], v[40:43], a[232:235]\n"
"ds_read_b128 a[132:135], v251 offset:26624\n"
"v_mfma_f32_16x16x32_bf16 a[236:239], a[112:115], v[12:15], a[236:239]\n"
"ds_read_b128 a[136:139], v251 offset:17408\n"
"v_mfma_f32_16x16x32_bf16 a[236:239], a[116:119], v[44:47], a[236:239]\n"
"ds_read_b128 a[140:143], v251 offset:27648\n"
"s_waitcnt lgkmcnt(4)\n"
"v_mfma_f32_16x16x32_bf16 a[240:243], a[120:123], v[0:3], a[240:243]\n"
"ds_read_b128 a[96:99], v251 offset:18432\n"
"buffer_load_dwordx4 a[156:159], v244, s[8:11], s71 offen offset:1024 sc1\n"
"v_mfma_f32_16x16x32_bf16 a[240:243], a[124:127], v[32:35], a[240:243]\n"
"ds_read_b128 a[100:103], v251 offset:28672\n"
"v_mfma_f32_16x16x32_bf16 a[244:247], a[120:123], v[4:7], a[244:247]\n"
"v_mfma_f32_16x16x32_bf16 a[244:247], a[124:127], v[36:39], a[244:247]\n"
"v_mfma_f32_16x16x32_bf16 a[248:251], a[120:123], v[8:11], a[248:251]\n"
"ds_read_b128 a[104:107], v251 offset:19456\n"
"s_add_u32 s70, s12, s85\n"
"buffer_load_dwordx4 a[160:163], v244, s[8:11], s70 offen offset:0 sc1\n"
"v_mfma_f32_16x16x32_bf16 a[248:251], a[124:127], v[40:43], a[248:251]\n"
"ds_read_b128 a[108:111], v251 offset:29696\n"
"v_mfma_f32_16x16x32_bf16 a[252:255], a[120:123], v[12:15], a[252:255]\n"
"v_mfma_f32_16x16x32_bf16 a[252:255], a[124:127], v[44:47], a[252:255]\n"
"s_waitcnt lgkmcnt(2)\n"
"v_mfma_f32_16x16x32_bf16 v[16:19], a[128:131], v[64:67], v[16:19]\n"
"buffer_load_dwordx4 a[164:167], v244, s[8:11], s70 offen offset:1024 sc1\n"
"v_mfma_f32_16x16x32_bf16 v[16:19], a[132:135], v[96:99], v[16:19]\n"
"v_mfma_f32_16x16x32_bf16 v[20:23], a[128:131], v[68:71], v[20:23]\n"
"v_mfma_f32_16x16x32_bf16 v[20:23], a[132:135], v[100:103], v[20:23]\n"
"v_mfma_f32_16x16x32_bf16 v[24:27], a[128:131], v[72:75], v[24:27]\n"
"ds_read_b128 a[112:115], v251 offset:20480\n"
"s_add_u32 s71, s13, s85\n"
"buffer_load_dwordx4 a[168:171], v244, s[8:11], s71 offen offset:0 sc1\n"
"v_mfma_f32_16x16x32_bf16 v[24:27], a[132:135], v[104:107], v[24:27]\n"
"ds_read_b128 a[116:119], v251 offset:30720\n"
"v_mfma_f32_16x16x32_bf16 v[28:31], a[128:131], v[76:79], v[28:31]\n"
"v_mfma_f32_16x16x32_bf16 v[28:31], a[132:135], v[108:111], v[28:31]\n"
"v_mfma_f32_16x16x32_bf16 v[48:51], a[136:139], v[64:67], v[48:51]\n"
"buffer_load_dwordx4 a[172:175], v244, s[8:11], s71 offen offset:1024 sc1\n"
"v_mfma_f32_16x16x32_bf16 v[48:51], a[140:143], v[96:99], v[48:51]\n"
"v_mfma_f32_16x16x32_bf16 v[52:55], a[136:139], v[68:71], v[52:55]\n"
"v_mfma_f32_16x16x32_bf16 v[52:55], a[140:143], v[100:103], v[52:55]\n"
"v_mfma_f32_16x16x32_bf16 v[56:59], a[136:139], v[72:75], v[56:59]\n"
"ds_read_b128 a[120:123], v251 offset:21504\n"
"s_add_u32 s70, s14, s85\n"
"buffer_load_dwordx4 a[176:179], v244, s[8:11], s70 offen offset:0 sc1\n"
"v_mfma_f32_16x16x32_bf16 v[56:59], a[140:143], v[104:107], v[56:59]\n"
"ds_read_b128 a[124:127], v251 offset:31744\n"
"v_mfma_f32_16x16x32_bf16 v[60:63], a[136:139], v[76:79], v[60:63]\n"
"v_mfma_f32_16x16x32_bf16 v[60:63], a[140:143], v[108:111], v[60:63]\n"
"v_mfma_f32_16x16x32_bf16 v[80:83], a[96:99], v[64:67], v[80:83]\n"
"buffer_load_dwordx4 a[180:183], v244, s[8:11], s70 offen offset:1024 sc1\n"
"v_mfma_f32_16x16x32_bf16 v[80:83], a[100:103], v[96:99], v[80:83]\n"
"v_mfma_f32_16x16x32_bf16 v[84:87], a[96:99], v[68:71], v[84:87]\n"
"v_mfma_f32_16x16x32_bf16 v[84:87], a[100:103], v[100:103], v[84:87]\n"
"v_mfma_f32_16x16x32_bf16 v[88:91], a[96:99], v[72:75], v[88:91]\n"
"ds_read_b128 a[128:131], v251 offset:22528\n"
"s_add_u32 s71, s15, s85\n"
"buffer_load_dwordx4 a[184:187], v244, s[8:11], s71 offen offset:0 sc1\n"
"v_mfma_f32_16x16x32_bf16 v[88:91], a[100:103], v[104:107], v[88:91]\n"
"ds_read_b128 a[132:135], v251 offset:32768\n"
"v_mfma_f32_16x16x32_bf16 v[92:95], a[96:99], v[76:79], v[92:95]\n"
"v_mfma_f32_16x16x32_bf16 v[92:95], a[100:103], v[108:111], v[92:95]\n"
"s_waitcnt lgkmcnt(2)\n"
"v_mfma_f32_16x16x32_bf16 v[112:115], a[104:107], v[64:67], v[112:115]\n"
"buffer_load_dwordx4 a[188:191], v244, s[8:11], s71 offen offset:1024 sc1\n"
"v_mfma_f32_16x16x32_bf16 v[112:115], a[108:111], v[96:99], v[112:115]\n"
"v_mfma_f32_16x16x32_bf16 v[116:119], a[104:107], v[68:71], v[116:119]\n"
"v_mfma_f32_16x16x32_bf16 v[116:119], a[108:111], v[100:103], v[116:119]\n"
"v_mfma_f32_16x16x32_bf16 v[120:123], a[104:107], v[72:75], v[120:123]\n"
"ds_read_b128 a[136:139], v251 offset:23552\n"
"s_add_u32 s70, s12, s86\n"
"buffer_load_dwordx4 a[192:195], v244, s[8:11], s70 offen offset:0 sc1\n"
"v_mfma_f32_16x16x32_bf16 v[120:123], a[108:111], v[104:107], v[120:123]\n"
"ds_read_b128 a[140:143], v251 offset:33792\n"
"v_mfma_f32_16x16x32_bf16 v[124:127], a[104:107], v[76:79], v[124:127]\n"
"v_mfma_f32_16x16x32_bf16 v[124:127], a[108:111], v[108:111], v[124:127]\n"
"v_mfma_f32_16x16x32_bf16 v[160:163], a[112:115], v[64:67], v[160:163]\n"
"buffer_load_dwordx4 a[196:199], v244, s[8:11], s70 offen offset:1024 sc1\n"
"v_mfma_f32_16x16x32_bf16 v[160:163], a[116:119], v[96:99], v[160:163]\n"
"v_mfma_f32_16x16x32_bf16 v[164:167], a[112:115], v[68:71], v[164:167]\n"
"v_mfma_f32_16x16x32_bf16 v[164:167], a[116:119], v[100:103], v[164:167]\n"
"v_mfma_f32_16x16x32_bf16 v[168:171], a[112:115], v[72:75], v[168:171]\n"
"ds_read_b128 a[96:99], v251 offset:24576\n"
"s_add_u32 s71, s13, s86\n"
"buffer_load_dwordx4 a[200:203], v244, s[8:11], s71 offen offset:0 sc1\n"
"v_mfma_f32_16x16x32_bf16 v[168:171], a[116:119], v[104:107], v[168:171]\n"
"ds_read_b128 a[100:103], v251 offset:34816\n"
"v_mfma_f32_16x16x32_bf16 v[172:175], a[112:115], v[76:79], v[172:175]\n"
"v_mfma_f32_16x16x32_bf16 v[172:175], a[116:119], v[108:111], v[172:175]\n"
"v_mfma_f32_16x16x32_bf16 v[176:179], a[120:123], v[64:67], v[176:179]\n"
"buffer_load_dwordx4 a[204:207], v244, s[8:11], s71 offen offset:1024 sc1\n"
"v_mfma_f32_16x16x32_bf16 v[176:179], a[124:127], v[96:99], v[176:179]\n"
"v_mfma_f32_16x16x32_bf16 v[180:183], a[120:123], v[68:71], v[180:183]\n"
"v_mfma_f32_16x16x32_bf16 v[180:183], a[124:127], v[100:103], v[180:183]\n"
"v_mfma_f32_16x16x32_bf16 v[184:187], a[120:123], v[72:75], v[184:187]\n"
"ds_read_b128 a[104:107], v251 offset:25600\n"
"s_add_u32 s70, s14, s86\n"
"buffer_load_dwordx4 a[0:3], v244, s[8:11], s70 offen offset:0 sc1\n"
"v_mfma_f32_16x16x32_bf16 v[184:187], a[124:127], v[104:107], v[184:187]\n"
"ds_read_b128 a[108:111], v251 offset:35840\n"
"v_mfma_f32_16x16x32_bf16 v[188:191], a[120:123], v[76:79], v[188:191]\n"
"v_mfma_f32_16x16x32_bf16 v[188:191], a[124:127], v[108:111], v[188:191]\n"
"s_waitcnt lgkmcnt(2)\n"
"v_mfma_f32_16x16x32_bf16 v[192:195], a[128:131], v[64:67], v[192:195]\n"
"buffer_load_dwordx4 a[4:7], v244, s[8:11], s70 offen offset:1024 sc1\n"
"v_mfma_f32_16x16x32_bf16 v[192:195], a[132:135], v[96:99], v[192:195]\n"
"v_mfma_f32_16x16x32_bf16 v[196:199], a[128:131], v[68:71], v[196:199]\n"
"v_mfma_f32_16x16x32_bf16 v[196:199], a[132:135], v[100:103], v[196:199]\n"
"v_mfma_f32_16x16x32_bf16 v[200:203], a[128:131], v[72:75], v[200:203]\n"
"s_add_u32 s71, s15, s86\n"
"buffer_load_dwordx4 a[8:11], v244, s[8:11], s71 offen offset:0 sc1\n"
"v_mfma_f32_16x16x32_bf16 v[200:203], a[132:135], v[104:107], v[200:203]\n"
"v_mfma_f32_16x16x32_bf16 v[204:207], a[128:131], v[76:79], v[204:207]\n"
"v_mfma_f32_16x16x32_bf16 v[204:207], a[132:135], v[108:111], v[204:207]\n"
"v_mfma_f32_16x16x32_bf16 a[208:211], a[136:139], v[64:67], a[208:211]\n"
"buffer_load_dwordx4 a[12:15], v244, s[8:11], s71 offen offset:1024 sc1\n"
"v_mfma_f32_16x16x32_bf16 a[208:211], a[140:143], v[96:99], a[208:211]\n"
"v_mfma_f32_16x16x32_bf16 a[212:215], a[136:139], v[68:71], a[212:215]\n"
"v_mfma_f32_16x16x32_bf16 a[212:215], a[140:143], v[100:103], a[212:215]\n"
"v_mfma_f32_16x16x32_bf16 a[216:219], a[136:139], v[72:75], a[216:219]\n"
"s_add_u32 s72, s40, 393216\n"
"s_add_u32 m0, s57, 24576\n"
"v_mfma_f32_16x16x32_bf16 a[216:219], a[140:143], v[104:107], a[216:219]\n"
"buffer_load_dwordx4 v244, s[8:11], s72 offen offset:0 sc1 lds\n"
"v_mfma_f32_16x16x32_bf16 a[220:223], a[136:139], v[76:79], a[220:223]\n"
"v_mfma_f32_16x16x32_bf16 a[220:223], a[140:143], v[108:111], a[220:223]\n"
"v_mfma_f32_16x16x32_bf16 a[224:227], a[96:99], v[64:67], a[224:227]\n"
"v_mfma_f32_16x16x32_bf16 a[224:227], a[100:103], v[96:99], a[224:227]\n"
"buffer_load_dwordx4 v244, s[8:11], s72 offen offset:1024 sc1 lds\n"
"v_mfma_f32_16x16x32_bf16 a[228:231], a[96:99], v[68:71], a[228:231]\n"
"v_mfma_f32_16x16x32_bf16 a[228:231], a[100:103], v[100:103], a[228:231]\n"
"s_waitcnt vmcnt(21)\n"
"s_barrier\n"
"s_add_u32 s59, s7, 128\n"
"buffer_atomic_add v246, v245, s[8:11], s59 offen sc1\n"
"v_mfma_f32_16x16x32_bf16 a[232:235], a[96:99], v[72:75], a[232:235]\n"
"ds_read_b128 a[112:115], v251 offset:36864\n"
"v_mfma_f32_16x16x32_bf16 a[232:235], a[100:103], v[104:107], a[232:235]\n"
"ds_read_b128 a[116:119], v251 offset:47104\n"
"buffer_load_dwordx4 v244, s[8:11], s72 offen offset:2048 sc1 lds\n"
"v_mfma_f32_16x16x32_bf16 a[236:239], a[96:99], v[76:79], a[236:239]\n"
"ds_read_b128 a[120:123], v251 offset:37888\n"
"v_mfma_f32_16x16x32_bf16 a[236:239], a[100:103], v[108:111], a[236:239]\n"
"ds_read_b128 a[124:127], v251 offset:48128\n"
"s_waitcnt lgkmcnt(4)\n"
"v_mfma_f32_16x16x32_bf16 a[240:243], a[104:107], v[64:67], a[240:243]\n"
"ds_read_b128 a[128:131], v251 offset:38912\n"
"v_mfma_f32_16x16x32_bf16 a[240:243], a[108:111], v[96:99], a[240:243]\n"
"ds_read_b128 a[132:135], v251 offset:49152\n"
"v_mfma_f32_16x16x32_bf16 a[244:247], a[104:107], v[68:71], a[244:247]\n"
"buffer_load_dwordx4 v244, s[8:11], s72 offen offset:3072 sc1 lds\n"
"v_mfma_f32_16x16x32_bf16 a[244:247], a[108:111], v[100:103], a[244:247]\n"
"v_mfma_f32_16x16x32_bf16 a[248:251], a[104:107], v[72:75], a[248:251]\n"
"ds_read_b128 a[136:139], v251 offset:39936\n"
"v_mfma_f32_16x16x32_bf16 a[248:251], a[108:111], v[104:107], a[248:251]\n"
"ds_read_b128 a[140:143], v251 offset:50176\n"
"v_mfma_f32_16x16x32_bf16 a[252:255], a[104:107], v[76:79], a[252:255]\n"
"s_add_u32 m0, s57, 28672\n"
"v_mfma_f32_16x16x32_bf16 a[252:255], a[108:111], v[108:111], a[252:255]\n"
"buffer_load_dwordx4 v250, s[8:11], s72 offen offset:0 sc1 lds\n"
"s_waitcnt lgkmcnt(2)\n"
"v_mfma_f32_16x16x32_bf16 v[16:19], a[112:115], a[80:83], v[16:19]\n"
"v_mfma_f32_16x16x32_bf16 v[16:19], a[116:119], a[84:87], v[16:19]\n"
"v_mfma_f32_16x16x32_bf16 v[20:23], a[112:115], a[88:91], v[20:23]\n"
"v_mfma_f32_16x16x32_bf16 v[20:23], a[116:119], a[92:95], v[20:23]\n"
"buffer_load_dwordx4 v250, s[8:11], s72 offen offset:1024 sc1 lds\n"
"v_mfma_f32_16x16x32_bf16 v[24:27], a[112:115], a[144:147], v[24:27]\n"
"ds_read_b128 a[96:99], v251 offset:40960\n"
"v_mfma_f32_16x16x32_bf16 v[24:27], a[116:119], a[148:151], v[24:27]\n"
"ds_read_b128 a[100:103], v251 offset:51200\n"
"s_waitcnt vmcnt(23)\n"
"v_mfma_f32_16x16x32_bf16 v[28:31], a[112:115], a[152:155], v[28:31]\n"
"v_mfma_f32_16x16x32_bf16 v[28:31], a[116:119], a[156:159], v[28:31]\n"
"s_add_u32 s72, s40, 786432\n"
"s_add_u32 m0, s57, 49152\n"
"v_mfma_f32_16x16x32_bf16 v[48:51], a[120:123], a[80:83], v[48:51]\n"
"buffer_load_dwordx4 v244, s[8:11], s72 offen offset:0 sc1 lds\n"
"v_mfma_f32_16x16x32_bf16 v[48:51], a[124:127], a[84:87], v[48:51]\n"
"v_mfma_f32_16x16x32_bf16 v[52:55], a[120:123], a[88:91], v[52:55]\n"
"v_mfma_f32_16x16x32_bf16 v[52:55], a[124:127], a[92:95], v[52:55]\n"
"v_mfma_f32_16x16x32_bf16 v[56:59], a[120:123], a[144:147], v[56:59]\n"
"ds_read_b128 a[104:107], v251 offset:41984\n"
"buffer_load_dwordx4 v244, s[8:11], s72 offen offset:1024 sc1 lds\n"
"v_mfma_f32_16x16x32_bf16 v[56:59], a[124:127], a[148:151], v[56:59]\n"
"ds_read_b128 a[108:111], v251 offset:52224\n"
"v_mfma_f32_16x16x32_bf16 v[60:63], a[120:123], a[152:155], v[60:63]\n"
"v_mfma_f32_16x16x32_bf16 v[60:63], a[124:127], a[156:159], v[60:63]\n"
"v_mfma_f32_16x16x32_bf16 v[80:83], a[128:131], a[80:83], v[80:83]\n"
"buffer_load_dwordx4 v244, s[8:11], s72 offen offset:2048 sc1 lds\n"
"v_mfma_f32_16x16x32_bf16 v[80:83], a[132:135], a[84:87], v[80:83]\n"
"v_mfma_f32_16x16x32_bf16 v[84:87], a[128:131], a[88:91], v[84:87]\n"
"v_mfma_f32_16x16x32_bf16 v[84:87], a[132:135], a[92:95], v[84:87]\n"
"v_mfma_f32_16x16x32_bf16 v[88:91], a[128:131], a[144:147], v[88:91]\n"
"ds_read_b128 a[112:115], v251 offset:43008\n"
"buffer_load_dwordx4 v244, s[8:11], s72 offen offset:3072 sc1 lds\n"
"v_mfma_f32_16x16x32_bf16 v[88:91], a[132:135], a[148:151], v[88:91]\n"
"ds_read_b128 a[116:119], v251 offset:53248\n"
"v_mfma_f32_16x16x32_bf16 v[92:95], a[128:131], a[152:155], v[92:95]\n"
"v_mfma_f32_16x16x32_bf16 v[92:95], a[132:135], a[156:159], v[92:95]\n"
"s_waitcnt lgkmcnt(2)\n"
"v_mfma_f32_16x16x32_bf16 v[112:115], a[136:139], a[80:83], v[112:115]\n"
"s_add_u32 m0, s57, 53248\n"
"v_mfma_f32_16x16x32_bf16 v[112:115], a[140:143], a[84:87], v[112:115]\n"
"buffer_load_dwordx4 v250, s[8:11], s72 offen offset:0 sc1 lds\n"
"v_mfma_f32_16x16x32_bf16 v[116:119], a[136:139], a[88:91], v[116:119]\n"
"v_mfma_f32_16x16x32_bf16 v[116:119], a[140:143], a[92:95], v[116:119]\n"
"v_mfma_f32_16x16x32_bf16 v[120:123], a[136:139], a[144:147], v[120:123]\n"
"ds_read_b128 a[120:123], v251 offset:44032\n"
"v_mfma_f32_16x16x32_bf16 v[120:123], a[140:143], a[148:151], v[120:123]\n"
"ds_read_b128 a[124:127], v251 offset:54272\n"
"v_mfma_f32_16x16x32_bf16 v[124:127], a[136:139], a[152:155], v[124:127]\n"
"buffer_load_dwordx4 v250, s[8:11], s72 offen offset:1024 sc1 lds\n"
"v_mfma_f32_16x16x32_bf16 v[124:127], a[140:143], a[156:159], v[124:127]\n"
"v_mfma_f32_16x16x32_bf16 v[160:163], a[96:99], a[80:83], v[160:163]\n"
"v_mfma_f32_16x16x32_bf16 v[160:163], a[100:103], a[84:87], v[160:163]\n"
"v_mfma_f32_16x16x32_bf16 v[164:167], a[96:99], a[88:91], v[164:167]\n"
"v_mfma_f32_16x16x32_bf16 v[164:167], a[100:103], a[92:95], v[164:167]\n"
"v_mfma_f32_16x16x32_bf16 v[168:171], a[96:99], a[144:147], v[168:171]\n"
"ds_read_b128 a[128:131], v251 offset:45056\n"
"v_mfma_f32_16x16x32_bf16 v[168:171], a[100:103], a[148:151], v[168:171]\n"
"ds_read_b128 a[132:135], v251 offset:55296\n"
"v_mfma_f32_16x16x32_bf16 v[172:175], a[96:99], a[152:155], v[172:175]\n"
"v_mfma_f32_16x16x32_bf16 v[172:175], a[100:103], a[156:159], v[172:175]\n"
"v_mfma_f32_16x16x32_bf16 v[176:179], a[104:107], a[80:83], v[176:179]\n"
"v_mfma_f32_16x16x32_bf16 v[176:179], a[108:111], a[84:87], v[176:179]\n"
"v_mfma_f32_16x16x32_bf16 v[180:183], a[104:107], a[88:91], v[180:183]\n"
"v_mfma_f32_16x16x32_bf16 v[180:183], a[108:111], a[92:95], v[180:183]\n"
"v_mfma_f32_16x16x32_bf16 v[184:187], a[104:107], a[144:147], v[184:187]\n"
"ds_read_b128 a[136:139], v251 offset:46080\n"
"v_mfma_f32_16x16x32_bf16 v[184:187], a[108:111], a[148:151], v[184:187]\n"
"ds_read_b128 a[140:143], v251 offset:56320\n"
"v_mfma_f32_16x16x32_bf16 v[188:191], a[104:107], a[152:155], v[188:191]\n"
"v_mfma_f32_16x16x32_bf16 v[188:191], a[108:111], a[156:159], v[188:191]\n"
"s_waitcnt lgkmcnt(2)\n"
"v_mfma_f32_16x16x32_bf16 v[192:195], a[112:115], a[80:83], v[192:195]\n"
"v_mfma_f32_16x16x32_bf16 v[192:195], a[116:119], a[84:87], v[192:195]\n"
"v_mfma_f32_16x16x32_bf16 v[196:199], a[112:115], a[88:91], v[196:199]\n"
"v_mfma_f32_16x16x32_bf16 v[196:199], a[116:119], a[92:95], v[196:199]\n"
"v_mfma_f32_16x16x32_bf16 v[200:203], a[112:115], a[144:147], v[200:203]\n"
"v_mfma_f32_16x16x32_bf16 v[200:203], a[116:119], a[148:151], v[200:203]\n"
"v_mfma_f32_16x16x32_bf16 v[204:207], a[112:115], a[152:155], v[204:207]\n"
"v_mfma_f32_16x16x32_bf16 v[204:207], a[116:119], a[156:159], v[204:207]\n"
"v_mfma_f32_16x16x32_bf16 a[208:211], a[120:123], a[80:83], a[208:211]\n"
"v_mfma_f32_16x16x32_bf16 a[208:211], a[124:127], a[84:87], a[208:211]\n"
"v_mfma_f32_16x16x32_bf16 a[212:215], a[120:123], a[88:91], a[212:215]\n"
"v_mfma_f32_16x16x32_bf16 a[212:215], a[124:127], a[92:95], a[212:215]\n"
"v_mfma_f32_16x16x32_bf16 a[216:219], a[120:123], a[144:147], a[216:219]\n"
"v_mfma_f32_16x16x32_bf16 a[216:219], a[124:127], a[148:151], a[216:219]\n"
"v_mfma_f32_16x16x32_bf16 a[220:223], a[120:123], a[152:155], a[220:223]\n"
"v_mfma_f32_16x16x32_bf16 a[220:223], a[124:127], a[156:159], a[220:223]\n"
"v_mfma_f32_16x16x32_bf16 a[224:227], a[128:131], a[80:83], a[224:227]\n"
"v_mfma_f32_16x16x32_bf16 a[224:227], a[132:135], a[84:87], a[224:227]\n"
"v_mfma_f32_16x16x32_bf16 a[228:231], a[128:131], a[88:91], a[228:231]\n"
"v_mfma_f32_16x16x32_bf16 a[228:231], a[132:135], a[92:95], a[228:231]\n"
"s_barrier\n"
"v_mfma_f32_16x16x32_bf16 a[232:235], a[128:131], a[144:147], a[232:235]\n"
"ds_read_b128 a[96:99], v251 offset:57344\n"
"s_add_u32 s72, s40, 1179648\n"
"s_add_u32 m0, s57, 73728\n"
"v_mfma_f32_16x16x32_bf16 a[232:235], a[132:135], a[148:151], a[232:235]\n"
"ds_read_b128 a[100:103], v252 offset:2048\n"
"buffer_load_dwordx4 v244, s[8:11], s72 offen offset:0 sc1 lds\n"
"v_mfma_f32_16x16x32_bf16 a[236:239], a[128:131], a[152:155], a[236:239]\n"
"ds_read_b128 a[104:107], v251 offset:58368\n"
"s_add_u32 s59, s33, 896\n"
"buffer_load_dword v249, off, s[8:11], s59 sc1\n"
"v_mfma_f32_16x16x32_bf16 a[236:239], a[132:135], a[156:159], a[236:239]\n"
"ds_read_b128 a[108:111], v252 offset:3072\n"
"s_waitcnt lgkmcnt(4)\n"
"v_mfma_f32_16x16x32_bf16 a[240:243], a[136:139], a[80:83], a[240:243]\n"
"ds_read_b128 a[112:115], v251 offset:59392\n"
"v_mfma_f32_16x16x32_bf16 a[240:243], a[140:143], a[84:87], a[240:243]\n"
"ds_read_b128 a[116:119], v252 offset:4096\n"
"v_mfma_f32_16x16x32_bf16 a[244:247], a[136:139], a[88:91], a[244:247]\n"
"buffer_load_dwordx4 v244, s[8:11], s72 offen offset:1024 sc1 lds\n"
"v_mfma_f32_16x16x32_bf16 a[244:247], a[140:143], a[92:95], a[244:247]\n"
"v_mfma_f32_16x16x32_bf16 a[248:251], a[136:139], a[144:147], a[248:251]\n"
"ds_read_b128 a[120:123], v251 offset:60416\n"
"v_mfma_f32_16x16x32_bf16 a[248:251], a[140:143], a[148:151], a[248:251]\n"
"ds_read_b128 a[124:127], v252 offset:5120\n"
"v_mfma_f32_16x16x32_bf16 a[252:255], a[136:139], a[152:155], a[252:255]\n"
"buffer_load_dwordx4 v244, s[8:11], s72 offen offset:2048 sc1 lds\n"
"v_mfma_f32_16x16x32_bf16 a[252:255], a[140:143], a[156:159], a[252:255]\n"
"s_waitcnt vmcnt(25)\n"
"s_waitcnt lgkmcnt(2)\n"
"v_mfma_f32_16x16x32_bf16 v[16:19], a[96:99], a[160:163], v[16:19]\n"
"v_mfma_f32_16x16x32_bf16 v[16:19], a[100:103], a[164:167], v[16:19]\n"
"v_mfma_f32_16x16x32_bf16 v[20:23], a[96:99], a[168:171], v[20:23]\n"
"buffer_load_dwordx4 v244, s[8:11], s72 offen offset:3072 sc1 lds\n"
"v_mfma_f32_16x16x32_bf16 v[20:23], a[100:103], a[172:175], v[20:23]\n"
"v_mfma_f32_16x16x32_bf16 v[24:27], a[96:99], a[176:179], v[24:27]\n"
"ds_read_b128 a[128:131], v251 offset:61440\n"
"v_mfma_f32_16x16x32_bf16 v[24:27], a[100:103], a[180:183], v[24:27]\n"
"ds_read_b128 a[132:135], v252 offset:6144\n"
"v_mfma_f32_16x16x32_bf16 v[28:31], a[96:99], a[184:187], v[28:31]\n"
"s_add_u32 m0, s57, 77824\n"
"v_mfma_f32_16x16x32_bf16 v[28:31], a[100:103], a[188:191], v[28:31]\n"
"buffer_load_dwordx4 v250, s[8:11], s72 offen offset:0 sc1 lds\n"
"v_mfma_f32_16x16x32_bf16 v[48:51], a[104:107], a[160:163], v[48:51]\n"
"v_mfma_f32_16x16x32_bf16 v[48:51], a[108:111], a[164:167], v[48:51]\n"
"v_mfma_f32_16x16x32_bf16 v[52:55], a[104:107], a[168:171], v[52:55]\n"
"v_mfma_f32_16x16x32_bf16 v[52:55], a[108:111], a[172:175], v[52:55]\n"
"buffer_load_dwordx4 v250, s[8:11], s72 offen offset:1024 sc1 lds\n"
"v_mfma_f32_16x16x32_bf16 v[56:59], a[104:107], a[176:179], v[56:59]\n"
"ds_read_b128 a[136:139], v251 offset:62464\n"
"v_mfma_f32_16x16x32_bf16 v[56:59], a[108:111], a[180:183], v[56:59]\n"
"ds_read_b128 a[140:143], v252 offset:7168\n"
"v_mfma_f32_16x16x32_bf16 v[60:63], a[104:107], a[184:187], v[60:63]\n"
"v_mfma_f32_16x16x32_bf16 v[60:63], a[108:111], a[188:191], v[60:63]\n"
"v_mfma_f32_16x16x32_bf16 v[80:83], a[112:115], a[160:163], v[80:83]\n"
"v_mfma_f32_16x16x32_bf16 v[80:83], a[116:119], a[164:167], v[80:83]\n"
"v_mfma_f32_16x16x32_bf16 v[84:87], a[112:115], a[168:171], v[84:87]\n"
"v_mfma_f32_16x16x32_bf16 v[84:87], a[116:119], a[172:175], v[84:87]\n"
"v_mfma_f32_16x16x32_bf16 v[88:91], a[112:115], a[176:179], v[88:91]\n"
"ds_read_b128 a[96:99], v251 offset:63488\n"
"v_mfma_f32_16x16x32_bf16 v[88:91], a[116:119], a[180:183], v[88:91]\n"
"ds_read_b128 a[100:103], v252 offset:8192\n"
"v_mfma_f32_16x16x32_bf16 v[92:95], a[112:115], a[184:187], v[92:95]\n"
"v_mfma_f32_16x16x32_bf16 v[92:95], a[116:119], a[188:191], v[92:95]\n"
"s_waitcnt lgkmcnt(2)\n"
"v_mfma_f32_16x16x32_bf16 v[112:115], a[120:123], a[160:163], v[112:115]\n"
"v_mfma_f32_16x16x32_bf16 v[112:115], a[124:127], a[164:167], v[112:115]\n"
"v_mfma_f32_16x16x32_bf16 v[116:119], a[120:123], a[168:171], v[116:119]\n"
"v_mfma_f32_16x16x32_bf16 v[116:119], a[124:127], a[172:175], v[116:119]\n"
"v_mfma_f32_16x16x32_bf16 v[120:123], a[120:123], a[176:179], v[120:123]\n"
"ds_read_b128 a[104:107], v251 offset:64512\n"
"v_mfma_f32_16x16x32_bf16 v[120:123], a[124:127], a[180:183], v[120:123]\n"
"ds_read_b128 a[108:111], v252 offset:9216\n"
"v_mfma_f32_16x16x32_bf16 v[124:127], a[120:123], a[184:187], v[124:127]\n"
"s_waitcnt vmcnt(5)\n"
"v_cmp_gt_u32 vcc, 16, v249\n"
"s_cbranch_vccnz L_spin_x_%=\n"
"L_back_x_%=:\n"
"v_mfma_f32_16x16x32_bf16 v[124:127], a[124:127], a[188:191], v[124:127]\n"
"s_add_u32 s70, s41, 0\n"
"buffer_load_dwordx4 a[16:19], v244, s[8:11], s70 offen offset:0 sc1\n"
"v_mfma_f32_16x16x32_bf16 v[160:163], a[128:131], a[160:163], v[160:163]\n"
"v_mfma_f32_16x16x32_bf16 v[160:163], a[132:135], a[164:167], v[160:163]\n"
"v_mfma_f32_16x16x32_bf16 v[164:167], a[128:131], a[168:171], v[164:167]\n"
"v_mfma_f32_16x16x32_bf16 v[164:167], a[132:135], a[172:175], v[164:167]\n"
"buffer_load_dwordx4 a[20:23], v244, s[8:11], s70 offen offset:1024 sc1\n"
"v_mfma_f32_16x16x32_bf16 v[168:171], a[128:131], a[176:179], v[168:171]\n"
"ds_read_b128 a[112:115], v252 offset:0\n"
"v_mfma_f32_16x16x32_bf16 v[168:171], a[132:135], a[180:183], v[168:171]\n"
"ds_read_b128 a[116:119], v252 offset:10240\n"
"v_mfma_f32_16x16x32_bf16 v[172:175], a[128:131], a[184:187], v[172:175]\n"
"v_mfma_f32_16x16x32_bf16 v[172:175], a[132:135], a[188:191], v[172:175]\n"
"buffer_load_dwordx4 a[24:27], v244, s[8:11], s70 offen offset:2048 sc1\n"
"v_mfma_f32_16x16x32_bf16 v[176:179], a[136:139], a[160:163], v[176:179]\n"
"v_mfma_f32_16x16x32_bf16 v[176:179], a[140:143], a[164:167], v[176:179]\n"
"v_mfma_f32_16x16x32_bf16 v[180:183], a[136:139], a[168:171], v[180:183]\n"
"v_mfma_f32_16x16x32_bf16 v[180:183], a[140:143], a[172:175], v[180:183]\n"
"buffer_load_dwordx4 a[28:31], v244, s[8:11], s70 offen offset:3072 sc1\n"
"v_mfma_f32_16x16x32_bf16 v[184:187], a[136:139], a[176:179], v[184:187]\n"
"ds_read_b128 a[120:123], v252 offset:1024\n"
"v_mfma_f32_16x16x32_bf16 v[184:187], a[140:143], a[180:183], v[184:187]\n"
"ds_read_b128 a[124:127], v252 offset:11264\n"
"v_mfma_f32_16x16x32_bf16 v[188:191], a[136:139], a[184:187], v[188:191]\n"
"v_mfma_f32_16x16x32_bf16 v[188:191], a[140:143], a[188:191], v[188:191]\n"
"s_add_u32 s71, s41, 16384\n"
"buffer_load_dwordx4 a[32:35], v250, s[8:11], s71 offen offset:0 sc1\n"
"s_waitcnt lgkmcnt(2)\n"
"v_mfma_f32_16x16x32_bf16 v[192:195], a[96:99], a[160:163], v[192:195]\n"
"v_mfma_f32_16x16x32_bf16 v[192:195], a[100:103], a[164:167], v[192:195]\n"
"v_mfma_f32_16x16x32_bf16 v[196:199], a[96:99], a[168:171], v[196:199]\n"
"v_mfma_f32_16x16x32_bf16 v[196:199], a[100:103], a[172:175], v[196:199]\n"
"buffer_load_dwordx4 a[36:39], v250, s[8:11], s71 offen offset:1024 sc1\n"
"v_mfma_f32_16x16x32_bf16 v[200:203], a[96:99], a[176:179], v[200:203]\n"
"v_mfma_f32_16x16x32_bf16 v[200:203], a[100:103], a[180:183], v[200:203]\n"
"v_mfma_f32_16x16x32_bf16 v[204:207], a[96:99], a[184:187], v[204:207]\n"
"v_mfma_f32_16x16x32_bf16 v[204:207], a[100:103], a[188:191], v[204:207]\n"
"buffer_load_dwordx4 a[40:43], v250, s[8:11], s71 offen offset:2048 sc1\n"
"v_mfma_f32_16x16x32_bf16 a[208:211], a[104:107], a[160:163], a[208:211]\n"
"v_mfma_f32_16x16x32_bf16 a[208:211], a[108:111], a[164:167], a[208:211]\n"
"v_mfma_f32_16x16x32_bf16 a[212:215], a[104:107], a[168:171], a[212:215]\n"
"v_mfma_f32_16x16x32_bf16 a[212:215], a[108:111], a[172:175], a[212:215]\n"
"buffer_load_dwordx4 a[44:47], v250, s[8:11], s71 offen offset:3072 sc1\n"
"v_mfma_f32_16x16x32_bf16 a[216:219], a[104:107], a[176:179], a[216:219]\n"
"v_mfma_f32_16x16x32_bf16 a[216:219], a[108:111], a[180:183], a[216:219]\n"
"v_mfma_f32_16x16x32_bf16 a[220:223], a[104:107], a[184:187], a[220:223]\n"
"v_mfma_f32_16x16x32_bf16 a[220:223], a[108:111], a[188:191], a[220:223]\n"
"s_add_u32 s70, s41, 0\n"
"buffer_load_dwordx4 a[48:51], v250, s[8:11], s70 offen offset:0 sc1\n"
"v_mfma_f32_16x16x32_bf16 a[224:227], a[112:115], a[160:163], a[224:227]\n"
"v_mfma_f32_16x16x32_bf16 a[224:227], a[116:119], a[164:167], a[224:227]\n"
"v_mfma_f32_16x16x32_bf16 a[228:231], a[112:115], a[168:171], a[228:231]\n"
"v_mfma_f32_16x16x32_bf16 a[228:231], a[116:119], a[172:175], a[228:231]\n"
"s_barrier\n"
"v_mfma_f32_16x16x32_bf16 a[232:235], a[112:115], a[176:179], a[232:235]\n"
"ds_read_b128 a[128:131], v252 offset:12288\n"
"buffer_load_dwordx4 a[52:55], v250, s[8:11], s70 offen offset:1024 sc1\n"
"v_mfma_f32_16x16x32_bf16 a[232:235], a[116:119], a[180:183], a[232:235]\n"
"ds_read_b128 a[132:135], v252 offset:22528\n"
"v_mfma_f32_16x16x32_bf16 a[236:239], a[112:115], a[184:187], a[236:239]\n"
"ds_read_b128 a[136:139], v252 offset:13312\n"
"v_mfma_f32_16x16x32_bf16 a[236:239], a[116:119], a[188:191], a[236:239]\n"
"ds_read_b128 a[140:143], v252 offset:23552\n"
"s_waitcnt lgkmcnt(4)\n"
"v_mfma_f32_16x16x32_bf16 a[240:243], a[120:123], a[160:163], a[240:243]\n"
"ds_read_b128 a[96:99], v252 offset:14336\n"
"buffer_load_dwordx4 a[56:59], v250, s[8:11], s70 offen offset:2048 sc1\n"
"v_mfma_f32_16x16x32_bf16 a[240:243], a[124:127], a[164:167], a[240:243]\n"
"ds_read_b128 a[100:103], v252 offset:24576\n"
"v_mfma_f32_16x16x32_bf16 a[244:247], a[120:123], a[168:171], a[244:247]\n"
"v_mfma_f32_16x16x32_bf16 a[244:247], a[124:127], a[172:175], a[244:247]\n"
"v_mfma_f32_16x16x32_bf16 a[248:251], a[120:123], a[176:179], a[248:251]\n"
"ds_read_b128 a[104:107], v252 offset:15360\n"
"buffer_load_dwordx4 a[60:63], v250, s[8:11], s70 offen offset:3072 sc1\n"
"v_mfma_f32_16x16x32_bf16 a[248:251], a[124:127], a[180:183], a[248:251]\n"
"ds_read_b128 a[108:111], v252 offset:25600\n"
"v_mfma_f32_16x16x32_bf16 a[252:255], a[120:123], a[184:187], a[252:255]\n"
"v_mfma_f32_16x16x32_bf16 a[252:255], a[124:127], a[188:191], a[252:255]\n"
"s_waitcnt lgkmcnt(2)\n"
"v_mfma_f32_16x16x32_bf16 v[16:19], a[128:131], a[192:195], v[16:19]\n"
"s_add_u32 s71, s41, 24576\n"
"buffer_load_dwordx4 a[64:67], v244, s[8:11], s71 offen offset:0 sc1\n"
"v_mfma_f32_16x16x32_bf16 v[16:19], a[132:135], a[196:199], v[16:19]\n"
"v_mfma_f32_16x16x32_bf16 v[20:23], a[128:131], a[200:203], v[20:23]\n"
"v_mfma_f32_16x16x32_bf16 v[20:23], a[132:135], a[204:207], v[20:23]\n"
"v_mfma_f32_16x16x32_bf16 v[24:27], a[128:131], a[0:3], v[24:27]\n"
"ds_read_b128 a[112:115], v252 offset:16384\n"
"buffer_load_dwordx4 a[68:71], v244, s[8:11], s71 offen offset:1024 sc1\n"
"v_mfma_f32_16x16x32_bf16 v[24:27], a[132:135], a[4:7], v[24:27]\n"
"ds_read_b128 a[116:119], v252 offset:26624\n"
"v_mfma_f32_16x16x32_bf16 v[28:31], a[128:131], a[8:11], v[28:31]\n"
"v_mfma_f32_16x16x32_bf16 v[28:31], a[132:135], a[12:15], v[28:31]\n"
"v_mfma_f32_16x16x32_bf16 v[48:51], a[136:139], a[192:195], v[48:51]\n"
"buffer_load_dwordx4 a[72:75], v244, s[8:11], s71 offen offset:2048 sc1\n"
"v_mfma_f32_16x16x32_bf16 v[48:51], a[140:143], a[196:199], v[48:51]\n"
"v_mfma_f32_16x16x32_bf16 v[52:55], a[136:139], a[200:203], v[52:55]\n"
"v_mfma_f32_16x16x32_bf16 v[52:55], a[140:143], a[204:207], v[52:55]\n"
"v_mfma_f32_16x16x32_bf16 v[56:59], a[136:139], a[0:3], v[56:59]\n"
"ds_read_b128 a[120:123], v252 offset:17408\n"
"buffer_load_dwordx4 a[76:79], v244, s[8:11], s71 offen offset:3072 sc1\n"
"v_mfma_f32_16x16x32_bf16 v[56:59], a[140:143], a[4:7], v[56:59]\n"
"ds_read_b128 a[124:127], v252 offset:27648\n"
"v_mfma_f32_16x16x32_bf16 v[60:63], a[136:139], a[8:11], v[60:63]\n"
"v_mfma_f32_16x16x32_bf16 v[60:63], a[140:143], a[12:15], v[60:63]\n"
"v_mfma_f32_16x16x32_bf16 v[80:83], a[96:99], a[192:195], v[80:83]\n"
"s_add_u32 s70, s41, 8192\n"
"buffer_load_dwordx4 a[80:83], v244, s[8:11], s70 offen offset:0 sc1\n"
"v_mfma_f32_16x16x32_bf16 v[80:83], a[100:103], a[196:199], v[80:83]\n"
"v_mfma_f32_16x16x32_bf16 v[84:87], a[96:99], a[200:203], v[84:87]\n"
"v_mfma_f32_16x16x32_bf16 v[84:87], a[100:103], a[204:207], v[84:87]\n"
"v_mfma_f32_16x16x32_bf16 v[88:91], a[96:99], a[0:3], v[88:91]\n"
"ds_read_b128 a[128:131], v252 offset:18432\n"
"buffer_load_dwordx4 a[84:87], v244, s[8:11], s70 offen offset:1024 sc1\n"
"v_mfma_f32_16x16x32_bf16 v[88:91], a[100:103], a[4:7], v[88:91]\n"
"ds_read_b128 a[132:135], v252 offset:28672\n"
"v_mfma_f32_16x16x32_bf16 v[92:95], a[96:99], a[8:11], v[92:95]\n"
"v_mfma_f32_16x16x32_bf16 v[92:95], a[100:103], a[12:15], v[92:95]\n"
"s_waitcnt lgkmcnt(2)\n"
"v_mfma_f32_16x16x32_bf16 v[112:115], a[104:107], a[192:195], v[112:115]\n"
"buffer_load_dwordx4 a[88:91], v244, s[8:11], s70 offen offset:2048 sc1\n"
"v_mfma_f32_16x16x32_bf16 v[112:115], a[108:111], a[196:199], v[112:115]\n"
"v_mfma_f32_16x16x32_bf16 v[116:119], a[104:107], a[200:203], v[116:119]\n"
"v_mfma_f32_16x16x32_bf16 v[116:119], a[108:111], a[204:207], v[116:119]\n"
"v_mfma_f32_16x16x32_bf16 v[120:123], a[104:107], a[0:3], v[120:123]\n"
"ds_read_b128 a[136:139], v252 offset:19456\n"
"buffer_load_dwordx4 a[92:95], v244, s[8:11], s70 offen offset:3072 sc1\n"
"v_mfma_f32_16x16x32_bf16 v[120:123], a[108:111], a[4:7], v[120:123]\n"
"ds_read_b128 a[140:143], v252 offset:29696\n"
"v_mfma_f32_16x16x32_bf16 v[124:127], a[104:107], a[8:11], v[124:127]\n"
"v_mfma_f32_16x16x32_bf16 v[124:127], a[108:111], a[12:15], v[124:127]\n"
"v_mfma_f32_16x16x32_bf16 v[160:163], a[112:115], a[192:195], v[160:163]\n"
"s_add_u32 s71, s41, 24576\n"
"buffer_load_dwordx4 a[144:147], v250, s[8:11], s71 offen offset:0 sc1\n"
"v_mfma_f32_16x16x32_bf16 v[160:163], a[116:119], a[196:199], v[160:163]\n"
"v_mfma_f32_16x16x32_bf16 v[164:167], a[112:115], a[200:203], v[164:167]\n"
"v_mfma_f32_16x16x32_bf16 v[164:167], a[116:119], a[204:207], v[164:167]\n"
"v_mfma_f32_16x16x32_bf16 v[168:171], a[112:115], a[0:3], v[168:171]\n"
"ds_read_b128 a[96:99], v252 offset:20480\n"
"buffer_load_dwordx4 a[148:151], v250, s[8:11], s71 offen offset:1024 sc1\n"
"v_mfma_f32_16x16x32_bf16 v[168:171], a[116:119], a[4:7], v[168:171]\n"
"ds_read_b128 a[100:103], v252 offset:30720\n"
"v_mfma_f32_16x16x32_bf16 v[172:175], a[112:115], a[8:11], v[172:175]\n"
"v_mfma_f32_16x16x32_bf16 v[172:175], a[116:119], a[12:15], v[172:175]\n"
"v_mfma_f32_16x16x32_bf16 v[176:179], a[120:123], a[192:195], v[176:179]\n"
"buffer_load_dwordx4 a[152:155], v250, s[8:11], s71 offen offset:2048 sc1\n"
"v_mfma_f32_16x16x32_bf16 v[176:179], a[124:127], a[196:199], v[176:179]\n"
"v_mfma_f32_16x16x32_bf16 v[180:183], a[120:123], a[200:203], v[180:183]\n"
"v_mfma_f32_16x16x32_bf16 v[180:183], a[124:127], a[204:207], v[180:183]\n"
"v_mfma_f32_16x16x32_bf16 v[184:187], a[120:123], a[0:3], v[184:187]\n"
"ds_read_b128 a[104:107], v252 offset:21504\n"
"buffer_load_dwordx4 a[156:159], v250, s[8:11], s71 offen offset:3072 sc1\n"
"v_mfma_f32_16x16x32_bf16 v[184:187], a[124:127], a[4:7], v[184:187]\n"
"ds_read_b128 a[108:111], v252 offset:31744\n"
"v_mfma_f32_16x16x32_bf16 v[188:191], a[120:123], a[8:11], v[188:191]\n"
"v_mfma_f32_16x16x32_bf16 v[188:191], a[124:127], a[12:15], v[188:191]\n"
"s_waitcnt lgkmcnt(2)\n"
"v_mfma_f32_16x16x32_bf16 v[192:195], a[128:131], a[192:195], v[192:195]\n"
"s_add_u32 s70, s41, 8192\n"
"buffer_load_dwordx4 a[160:163], v250, s[8:11], s70 offen offset:0 sc1\n"
"v_mfma_f32_16x16x32_bf16 v[192:195], a[132:135], a[196:199], v[192:195]\n"
"v_mfma_f32_16x16x32_bf16 v[196:199], a[128:131], a[200:203], v[196:199]\n"
"v_mfma_f32_16x16x32_bf16 v[196:199], a[132:135], a[204:207], v[196:199]\n"
"v_mfma_f32_16x16x32_bf16 v[200:203], a[128:131], a[0:3], v[200:203]\n"
"buffer_load_dwordx4 a[164:167], v250, s[8:11], s70 offen offset:1024 sc1\n"
"v_mfma_f32_16x16x32_bf16 v[200:203], a[132:135], a[4:7], v[200:203]\n"
"v_mfma_f32_16x16x32_bf16 v[204:207], a[128:131], a[8:11], v[204:207]\n"
"v_mfma_f32_16x16x32_bf16 v[204:207], a[132:135], a[12:15], v[204:207]\n"
"v_mfma_f32_16x16x32_bf16 a[208:211], a[136:139], a[192:195], a[208:211]\n"
"buffer_load_dwordx4 a[168:171], v250, s[8:11], s70 offen offset:2048 sc1\n"
"v_mfma_f32_16x16x32_bf16 a[208:211], a[140:143], a[196:199], a[208:211]\n"
"v_mfma_f32_16x16x32_bf16 a[212:215], a[136:139], a[200:203], a[212:215]\n"
"v_mfma_f32_16x16x32_bf16 a[212:215], a[140:143], a[204:207], a[212:215]\n"
"v_mfma_f32_16x16x32_bf16 a[216:219], a[136:139], a[0:3], a[216:219]\n"
"buffer_load_dwordx4 a[172:175], v250, s[8:11], s70 offen offset:3072 sc1\n"
"v_mfma_f32_16x16x32_bf16 a[216:219], a[140:143], a[4:7], a[216:219]\n"
"v_mfma_f32_16x16x32_bf16 a[220:223], a[136:139], a[8:11], a[220:223]\n"
"v_mfma_f32_16x16x32_bf16 a[220:223], a[140:143], a[12:15], a[220:223]\n"
"v_mfma_f32_16x16x32_bf16 a[224:227], a[96:99], a[192:195], a[224:227]\n"
"s_add_u32 s71, s41, 32768\n"
"buffer_load_dwordx4 a[176:179], v244, s[8:11], s71 offen offset:0 sc1\n"
"v_mfma_f32_16x16x32_bf16 a[224:227], a[100:103], a[196:199], a[224:227]\n"
"v_mov_b32 v152, 0x3f803f80\n"
"v_mov_b32 v153, 0x3f803f80\n"
"v_mfma_f32_16x16x32_bf16 a[228:231], a[96:99], a[200:203], a[228:231]\n"
"v_mov_b32 v154, 0x3f803f80\n"
"v_mov_b32 v155, 0x3f803f80\n"
"v_mfma_f32_16x16x32_bf16 a[228:231], a[100:103], a[204:207], a[228:231]\n"
"s_barrier\n"
"v_mfma_f32_16x16x32_bf16 a[232:235], a[96:99], a[0:3], a[232:235]\n"
"ds_read_b128 a[112:115], v244 offset:0\n"
"buffer_load_dwordx4 a[180:183], v244, s[8:11], s71 offen offset:1024 sc1\n"
"v_mfma_f32_16x16x32_bf16 a[232:235], a[100:103], a[4:7], a[232:235]\n"
"ds_read_b128 a[116:119], v244 offset:6144\n"
"v_mfma_f32_16x16x32_bf16 a[236:239], a[96:99], a[8:11], a[236:239]\n"
"ds_read_b128 a[120:123], v244 offset:12288\n"
"v_mfma_f32_16x16x32_bf16 a[236:239], a[100:103], a[12:15], a[236:239]\n"
"ds_read_b128 a[124:127], v244 offset:18432\n"
"s_waitcnt lgkmcnt(4)\n"
"v_mfma_f32_16x16x32_bf16 a[240:243], a[104:107], a[192:195], a[240:243]\n"
"ds_read_b128 a[128:131], v244 offset:1024\n"
"buffer_load_dwordx4 a[184:187], v244, s[8:11], s71 offen offset:2048 sc1\n"
"v_mfma_f32_16x16x32_bf16 a[240:243], a[108:111], a[196:199], a[240:243]\n"
"ds_read_b128 a[132:135], v244 offset:7168\n"
"v_mfma_f32_16x16x32_bf16 a[244:247], a[104:107], a[200:203], a[244:247]\n"
"ds_read_b128 a[136:139], v244 offset:13312\n"
"v_mfma_f32_16x16x32_bf16 a[244:247], a[108:111], a[204:207], a[244:247]\n"
"ds_read_b128 a[140:143], v244 offset:19456\n"
"v_mfma_f32_16x16x32_bf16 a[248:251], a[104:107], a[0:3], a[248:251]\n"
"ds_read_b128 a[96:99], v244 offset:2048\n"
"buffer_load_dwordx4 a[188:191], v244, s[8:11], s71 offen offset:3072 sc1\n"
"v_mfma_f32_16x16x32_bf16 a[248:251], a[108:111], a[4:7], a[248:251]\n"
"ds_read_b128 a[100:103], v244 offset:8192\n"
"v_mfma_f32_16x16x32_bf16 a[252:255], a[104:107], a[8:11], a[252:255]\n"
"ds_read_b128 a[104:107], v244 offset:14336\n"
"v_mfma_f32_16x16x32_bf16 a[252:255], a[108:111], a[12:15], a[252:255]\n"
"ds_read_b128 a[108:111], v244 offset:20480\n"
"s_waitcnt vmcnt(24)\n"
"s_waitcnt lgkmcnt(6)\n"
"v_mfma_f32_16x16x32_bf16 v[0:3], a[112:115], a[16:19], 0\n"
"buffer_load_dwordx4 v[208:211], v244, s[8:11], s63 offen offset:0 sc1\n"
"v_mfma_f32_16x16x32_bf16 v[0:3], a[116:119], a[20:23], v[0:3]\n"
"v_mfma_f32_16x16x32_bf16 v[0:3], a[120:123], a[24:27], v[0:3]\n"
"v_mfma_f32_16x16x32_bf16 v[0:3], a[124:127], a[28:31], v[0:3]\n"
"v_mfma_f32_16x16x32_bf16 v[4:7], a[112:115], a[32:35], 0\n"
"buffer_load_dwordx4 v[212:215], v244, s[8:11], s63 offen offset:1024 sc1\n"
"v_mfma_f32_16x16x32_bf16 v[4:7], a[116:119], a[36:39], v[4:7]\n"
"v_mfma_f32_16x16x32_bf16 v[4:7], a[120:123], a[40:43], v[4:7]\n"
"v_mfma_f32_16x16x32_bf16 v[4:7], a[124:127], a[44:47], v[4:7]\n"
"v_mfma_f32_16x16x32_bf16 v[128:131], a[112:115], v[152:155], 0\n"
"ds_read_b128 a[112:115], v244 offset:3072\n"
"s_add_u32 s72, s40, 1572864\n"
"s_add_u32 m0, s57, 98304\n"
"v_mfma_f32_16x16x32_bf16 v[128:131], a[116:119], v[152:155], v[128:131]\n"
"ds_read_b128 a[116:119], v244 offset:9216\n"
"buffer_load_dwordx4 v244, s[8:11], s72 offen offset:0 sc1 lds\n"
"v_mfma_f32_16x16x32_bf16 v[128:131], a[120:123], v[152:155], v[128:131]\n"
"ds_read_b128 a[120:123], v244 offset:15360\n"
"v_mfma_f32_16x16x32_bf16 v[128:131], a[124:127], v[152:155], v[128:131]\n"
"ds_read_b128 a[124:127], v244 offset:21504\n"
"v_mfma_f32_16x16x32_bf16 v[8:11], a[128:131], a[16:19], 0\n"
"v_mfma_f32_16x16x32_bf16 v[8:11], a[132:135], a[20:23], v[8:11]\n"
"buffer_load_dwordx4 v244, s[8:11], s72 offen offset:1024 sc1 lds\n"
"s_waitcnt lgkmcnt(4)\n"
"v_mfma_f32_16x16x32_bf16 v[8:11], a[136:139], a[24:27], v[8:11]\n"
"v_mfma_f32_16x16x32_bf16 v[8:11], a[140:143], a[28:31], v[8:11]\n"
"v_mfma_f32_16x16x32_bf16 v[12:15], a[128:131], a[32:35], 0\n"
"v_mfma_f32_16x16x32_bf16 v[12:15], a[132:135], a[36:39], v[12:15]\n"
"buffer_load_dwordx4 v244, s[8:11], s72 offen offset:2048 sc1 lds\n"
"v_mfma_f32_16x16x32_bf16 v[12:15], a[136:139], a[40:43], v[12:15]\n"
"v_mfma_f32_16x16x32_bf16 v[12:15], a[140:143], a[44:47], v[12:15]\n"
"v_mfma_f32_16x16x32_bf16 v[132:135], a[128:131], v[152:155], 0\n"
"ds_read_b128 a[128:131], v244 offset:4096\n"
"v_mfma_f32_16x16x32_bf16 v[132:135], a[132:135], v[152:155], v[132:135]\n"
"ds_read_b128 a[132:135], v244 offset:10240\n"
"buffer_load_dwordx4 v244, s[8:11], s72 offen offset:3072 sc1 lds\n"
"v_mfma_f32_16x16x32_bf16 v[132:135], a[136:139], v[152:155], v[132:135]\n"
"ds_read_b128 a[136:139], v244 offset:16384\n"
"v_mfma_f32_16x16x32_bf16 v[132:135], a[140:143], v[152:155], v[132:135]\n"
"ds_read_b128 a[140:143], v244 offset:22528\n"
"v_mfma_f32_16x16x32_bf16 v[32:35], a[96:99], a[16:19], 0\n"
"v_mfma_f32_16x16x32_bf16 v[32:35], a[100:103], a[20:23], v[32:35]\n"
"s_add_u32 m0, s57, 102400\n"
"v_mfma_f32_16x16x32_bf16 v[32:35], a[104:107], a[24:27], v[32:35]\n"
"buffer_load_dwordx4 v250, s[8:11], s72 offen offset:0 sc1 lds\n"
"v_mfma_f32_16x16x32_bf16 v[32:35], a[108:111], a[28:31], v[32:35]\n"
"v_mfma_f32_16x16x32_bf16 v[36:39], a[96:99], a[32:35], 0\n"
"v_mfma_f32_16x16x32_bf16 v[36:39], a[100:103], a[36:39], v[36:39]\n"
"v_mfma_f32_16x16x32_bf16 v[36:39], a[104:107], a[40:43], v[36:39]\n"
"buffer_load_dwordx4 v250, s[8:11], s72 offen offset:1024 sc1 lds\n"
"v_mfma_f32_16x16x32_bf16 v[36:39], a[108:111], a[44:47], v[36:39]\n"
"v_mfma_f32_16x16x32_bf16 v[136:139], a[96:99], v[152:155], 0\n"
"ds_read_b128 a[96:99], v244 offset:5120\n"
"v_mfma_f32_16x16x32_bf16 v[136:139], a[100:103], v[152:155], v[136:139]\n"
"ds_read_b128 a[100:103], v244 offset:11264\n"
"v_mfma_f32_16x16x32_bf16 v[136:139], a[104:107], v[152:155], v[136:139]\n"
"ds_read_b128 a[104:107], v244 offset:17408\n"
"s_add_u32 s70, s41, 16384\n"
"buffer_load_dwordx4 a[192:195], v244, s[8:11], s70 offen offset:0 sc1\n"
"v_mfma_f32_16x16x32_bf16 v[136:139], a[108:111], v[152:155], v[136:139]\n"
"ds_read_b128 a[108:111], v244 offset:23552\n"
"s_waitcnt lgkmcnt(4)\n"
"v_mfma_f32_16x16x32_bf16 v[40:43], a[112:115], a[16:19], 0\n"
"v_mfma_f32_16x16x32_bf16 v[40:43], a[116:119], a[20:23], v[40:43]\n"
"v_mfma_f32_16x16x32_bf16 v[40:43], a[120:123], a[24:27], v[40:43]\n"
"buffer_load_dwordx4 a[196:199], v244, s[8:11], s70 offen offset:1024 sc1\n"
"v_mfma_f32_16x16x32_bf16 v[40:43], a[124:127], a[28:31], v[40:43]\n"
"v_mfma_f32_16x16x32_bf16 v[44:47], a[112:115], a[32:35], 0\n"
"v_mfma_f32_16x16x32_bf16 v[44:47], a[116:119], a[36:39], v[44:47]\n"
"v_mfma_f32_16x16x32_bf16 v[44:47], a[120:123], a[40:43], v[44:47]\n"
"buffer_load_dwordx4 a[200:203], v244, s[8:11], s70 offen offset:2048 sc1\n"
"v_mfma_f32_16x16x32_bf16 v[44:47], a[124:127], a[44:47], v[44:47]\n"
"v_mfma_f32_16x16x32_bf16 v[140:143], a[112:115], v[152:155], 0\n"
"v_mfma_f32_16x16x32_bf16 v[140:143], a[116:119], v[152:155], v[140:143]\n"
"v_mfma_f32_16x16x32_bf16 v[140:143], a[120:123], v[152:155], v[140:143]\n"
"buffer_load_dwordx4 a[204:207], v244, s[8:11], s70 offen offset:3072 sc1\n"
"v_mfma_f32_16x16x32_bf16 v[140:143], a[124:127], v[152:155], v[140:143]\n"
"v_mfma_f32_16x16x32_bf16 v[64:67], a[128:131], a[16:19], 0\n"
"v_mfma_f32_16x16x32_bf16 v[64:67], a[132:135], a[20:23], v[64:67]\n"
"v_mfma_f32_16x16x32_bf16 v[64:67], a[136:139], a[24:27], v[64:67]\n"
"s_add_u32 s71, s41, 32768\n"
"buffer_load_dwordx4 a[0:3], v250, s[8:11], s71 offen offset:0 sc1\n"
"v_mfma_f32_16x16x32_bf16 v[64:67], a[140:143], a[28:31], v[64:67]\n"
"v_mfma_f32_16x16x32_bf16 v[68:71], a[128:131], a[32:35], 0\n"
"v_mfma_f32_16x16x32_bf16 v[68:71], a[132:135], a[36:39], v[68:71]\n"
"v_mfma_f32_16x16x32_bf16 v[68:71], a[136:139], a[40:43], v[68:71]\n"
"buffer_load_dwordx4 a[4:7], v250, s[8:11], s71 offen offset:1024 sc1\n"
"v_mfma_f32_16x16x32_bf16 v[68:71], a[140:143], a[44:47], v[68:71]\n"
"v_mfma_f32_16x16x32_bf16 v[144:147], a[128:131], v[152:155], 0\n"
"v_mfma_f32_16x16x32_bf16 v[144:147], a[132:135], v[152:155], v[144:147]\n"
"v_mfma_f32_16x16x32_bf16 v[144:147], a[136:139], v[152:155], v[144:147]\n"
"buffer_load_dwordx4 a[8:11], v250, s[8:11], s71 offen offset:2048 sc1\n"
"v_mfma_f32_16x16x32_bf16 v[144:147], a[140:143], v[152:155], v[144:147]\n"
"s_barrier\n"
"s_waitcnt lgkmcnt(0)\n"
"v_mfma_f32_16x16x32_bf16 v[72:75], a[96:99], a[16:19], 0\n"
"ds_read_b128 a[112:115], v244 offset:24576\n"
"v_mfma_f32_16x16x32_bf16 v[72:75], a[100:103], a[20:23], v[72:75]\n"
"ds_read_b128 a[116:119], v244 offset:30720\n"
"v_mfma_f32_16x16x32_bf16 v[72:75], a[104:107], a[24:27], v[72:75]\n"
"ds_read_b128 a[120:123], v244 offset:36864\n"
"buffer_load_dwordx4 a[12:15], v250, s[8:11], s71 offen offset:3072 sc1\n"
"v_mfma_f32_16x16x32_bf16 v[72:75], a[108:111], a[28:31], v[72:75]\n"
"ds_read_b128 a[124:127], v244 offset:43008\n"
"v_mfma_f32_16x16x32_bf16 v[76:79], a[96:99], a[32:35], 0\n"
"ds_read_b128 a[128:131], v244 offset:25600\n"
"v_mfma_f32_16x16x32_bf16 v[76:79], a[100:103], a[36:39], v[76:79]\n"
"ds_read_b128 a[132:135], v244 offset:31744\n"
"v_mfma_f32_16x16x32_bf16 v[76:79], a[104:107], a[40:43], v[76:79]\n"
"ds_read_b128 a[136:139], v244 offset:37888\n"
"s_add_u32 s72, s81, 81920\n"
"s_add_u32 m0, s6, 122880\n"
"v_mfma_f32_16x16x32_bf16 v[76:79], a[108:111], a[44:47], v[76:79]\n"
"ds_read_b128 a[140:143], v244 offset:44032\n"
"buffer_load_dwordx4 v244, s[8:11], s72 offen offset:0 sc1 lds\n"
"v_mfma_f32_16x16x32_bf16 v[148:151], a[96:99], v[152:155], 0\n"
"ds_read_b128 a[96:99], v244 offset:26624\n"
"v_mfma_f32_16x16x32_bf16 v[148:151], a[100:103], v[152:155], v[148:151]\n"
"ds_read_b128 a[100:103], v244 offset:32768\n"
"v_mfma_f32_16x16x32_bf16 v[148:151], a[104:107], v[152:155], v[148:151]\n"
"ds_read_b128 a[104:107], v244 offset:38912\n"
"v_mfma_f32_16x16x32_bf16 v[148:151], a[108:111], v[152:155], v[148:151]\n"
"ds_read_b128 a[108:111], v244 offset:45056\n"
"s_waitcnt vmcnt(33)\n"
"s_waitcnt lgkmcnt(6)\n"
"v_mfma_f32_16x16x32_bf16 v[0:3], a[112:115], a[48:51], v[0:3]\n"
"buffer_load_dwordx4 v244, s[8:11], s72 offen offset:1024 sc1 lds\n"
"v_mfma_f32_16x16x32_bf16 v[0:3], a[116:119], a[52:55], v[0:3]\n"
"v_mfma_f32_16x16x32_bf16 v[0:3], a[120:123], a[56:59], v[0:3]\n"
"v_mfma_f32_16x16x32_bf16 v[0:3], a[124:127], a[60:63], v[0:3]\n"
"v_mfma_f32_16x16x32_bf16 v[4:7], a[112:115], a[64:67], v[4:7]\n"
"buffer_load_dwordx4 v244, s[8:11], s72 offen offset:2048 sc1 lds\n"
"v_mfma_f32_16x16x32_bf16 v[4:7], a[116:119], a[68:71], v[4:7]\n"
"v_mfma_f32_16x16x32_bf16 v[4:7], a[120:123], a[72:75], v[4:7]\n"
"v_mfma_f32_16x16x32_bf16 v[4:7], a[124:127], a[76:79], v[4:7]\n"
"v_mfma_f32_16x16x32_bf16 v[128:131], a[112:115], v[152:155], v[128:131]\n"
"ds_read_b128 a[112:115], v244 offset:27648\n"
"buffer_load_dwordx4 v244, s[8:11], s72 offen offset:3072 sc1 lds\n"
"v_mfma_f32_16x16x32_bf16 v[128:131], a[116:119], v[152:155], v[128:131]\n"
"ds_read_b128 a[116:119], v244 offset:33792\n"
"v_mfma_f32_16x16x32_bf16 v[128:131], a[120:123], v[152:155], v[128:131]\n"
"ds_read_b128 a[120:123], v244 offset:39936\n"
"v_mfma_f32_16x16x32_bf16 v[128:131], a[124:127], v[152:155], v[128:131]\n"
"ds_read_b128 a[124:127], v244 offset:46080\n"
"v_mfma_f32_16x16x32_bf16 v[8:11], a[128:131], a[48:51], v[8:11]\n"
"s_add_u32 m0, s6, 126976\n"
"v_mfma_f32_16x16x32_bf16 v[8:11], a[132:135], a[52:55], v[8:11]\n"
"buffer_load_dwordx4 v250, s[8:11], s72 offen offset:0 sc1 lds\n"
"s_waitcnt lgkmcnt(4)\n"
"v_mfma_f32_16x16x32_bf16 v[8:11], a[136:139], a[56:59], v[8:11]\n"
"v_mfma_f32_16x16x32_bf16 v[8:11], a[140:143], a[60:63], v[8:11]\n"
"v_mfma_f32_16x16x32_bf16 v[12:15], a[128:131], a[64:67], v[12:15]\n"
"v_mfma_f32_16x16x32_bf16 v[12:15], a[132:135], a[68:71], v[12:15]\n"
"s_add_u32 s72, s82, 81920\n"
"s_add_u32 m0, s6, 143360\n"
"v_mfma_f32_16x16x32_bf16 v[12:15], a[136:139], a[72:75], v[12:15]\n"
"buffer_load_dwordx4 v244, s[8:11], s72 offen offset:0 sc1 lds\n"
"v_mfma_f32_16x16x32_bf16 v[12:15], a[140:143], a[76:79], v[12:15]\n"
"v_mfma_f32_16x16x32_bf16 v[132:135], a[128:131], v[152:155], v[132:135]\n"
"ds_read_b128 a[128:131], v244 offset:28672\n"
"v_mfma_f32_16x16x32_bf16 v[132:135], a[132:135], v[152:155], v[132:135]\n"
"ds_read_b128 a[132:135], v244 offset:34816\n"
"v_mfma_f32_16x16x32_bf16 v[132:135], a[136:139], v[152:155], v[132:135]\n"
"ds_read_b128 a[136:139], v244 offset:40960\n"
"buffer_load_dwordx4 v244, s[8:11], s72 offen offset:1024 sc1 lds\n"
"v_mfma_f32_16x16x32_bf16 v[132:135], a[140:143], v[152:155], v[132:135]\n"
"ds_read_b128 a[140:143], v244 offset:47104\n"
"v_mfma_f32_16x16x32_bf16 v[32:35], a[96:99], a[48:51], v[32:35]\n"
"v_mfma_f32_16x16x32_bf16 v[32:35], a[100:103], a[52:55], v[32:35]\n"
"v_mfma_f32_16x16x32_bf16 v[32:35], a[104:107], a[56:59], v[32:35]\n"
"buffer_load_dwordx4 v244, s[8:11], s72 offen offset:2048 sc1 lds\n"
"v_mfma_f32_16x16x32_bf16 v[32:35], a[108:111], a[60:63], v[32:35]\n"
"v_mfma_f32_16x16x32_bf16 v[36:39], a[96:99], a[64:67], v[36:39]\n"
"v_mfma_f32_16x16x32_bf16 v[36:39], a[100:103], a[68:71], v[36:39]\n"
"v_mfma_f32_16x16x32_bf16 v[36:39], a[104:107], a[72:75], v[36:39]\n"
"buffer_load_dwordx4 v244, s[8:11], s72 offen offset:3072 sc1 lds\n"
"v_mfma_f32_16x16x32_bf16 v[36:39], a[108:111], a[76:79], v[36:39]\n"
"v_mfma_f32_16x16x32_bf16 v[136:139], a[96:99], v[152:155], v[136:139]\n"
"ds_read_b128 a[96:99], v244 offset:29696\n"
"v_mfma_f32_16x16x32_bf16 v[136:139], a[100:103], v[152:155], v[136:139]\n"
"ds_read_b128 a[100:103], v244 offset:35840\n"
"v_mfma_f32_16x16x32_bf16 v[136:139], a[104:107], v[152:155], v[136:139]\n"
"ds_read_b128 a[104:107], v244 offset:41984\n"
"s_add_u32 m0, s6, 147456\n"
"v_mfma_f32_16x16x32_bf16 v[136:139], a[108:111], v[152:155], v[136:139]\n"
"ds_read_b128 a[108:111], v244 offset:48128\n"
"s_waitcnt lgkmcnt(4)\n"
"v_mfma_f32_16x16x32_bf16 v[40:43], a[112:115], a[48:51], v[40:43]\n"
"buffer_load_dwordx4 v250, s[8:11], s72 offen offset:0 sc1 lds\n"
"v_mfma_f32_16x16x32_bf16 v[40:43], a[116:119], a[52:55], v[40:43]\n"
"v_mfma_f32_16x16x32_bf16 v[40:43], a[120:123], a[56:59], v[40:43]\n"
"v_mfma_f32_16x16x32_bf16 v[40:43], a[124:127], a[60:63], v[40:43]\n"
"v_mfma_f32_16x16x32_bf16 v[44:47], a[112:115], a[64:67], v[44:47]\n"
"v_mfma_f32_16x16x32_bf16 v[44:47], a[116:119], a[68:71], v[44:47]\n"
"v_mfma_f32_16x16x32_bf16 v[44:47], a[120:123], a[72:75], v[44:47]\n"
"v_mfma_f32_16x16x32_bf16 v[44:47], a[124:127], a[76:79], v[44:47]\n"
"v_mfma_f32_16x16x32_bf16 v[140:143], a[112:115], v[152:155], v[140:143]\n"
"v_mfma_f32_16x16x32_bf16 v[140:143], a[116:119], v[152:155], v[140:143]\n"
"v_mfma_f32_16x16x32_bf16 v[140:143], a[120:123], v[152:155], v[140:143]\n"
"v_mfma_f32_16x16x32_bf16 v[140:143], a[124:127], v[152:155], v[140:143]\n"
"v_mfma_f32_16x16x32_bf16 v[64:67], a[128:131], a[48:51], v[64:67]\n"
"v_mfma_f32_16x16x32_bf16 v[64:67], a[132:135], a[52:55], v[64:67]\n"
"v_mfma_f32_16x16x32_bf16 v[64:67], a[136:139], a[56:59], v[64:67]\n"
"v_mfma_f32_16x16x32_bf16 v[64:67], a[140:143], a[60:63], v[64:67]\n"
"v_mfma_f32_16x16x32_bf16 v[68:71], a[128:131], a[64:67], v[68:71]\n"
"v_mfma_f32_16x16x32_bf16 v[68:71], a[132:135], a[68:71], v[68:71]\n"
"v_mfma_f32_16x16x32_bf16 v[68:71], a[136:139], a[72:75], v[68:71]\n"
"v_mfma_f32_16x16x32_bf16 v[68:71], a[140:143], a[76:79], v[68:71]\n"
"v_mfma_f32_16x16x32_bf16 v[144:147], a[128:131], v[152:155], v[144:147]\n"
"v_mfma_f32_16x16x32_bf16 v[144:147], a[132:135], v[152:155], v[144:147]\n"
"v_mfma_f32_16x16x32_bf16 v[144:147], a[136:139], v[152:155], v[144:147]\n"
"v_mfma_f32_16x16x32_bf16 v[144:147], a[140:143], v[152:155], v[144:147]\n"
"s_barrier\n"
"s_waitcnt lgkmcnt(0)\n"
"v_mfma_f32_16x16x32_bf16 v[72:75], a[96:99], a[48:51], v[72:75]\n"
"ds_read_b128 a[112:115], v244 offset:49152\n"
"s_add_u32 s72, s83, 81920\n"
"s_add_u32 m0, s6, 0\n"
"v_mfma_f32_16x16x32_bf16 v[72:75], a[100:103], a[52:55], v[72:75]\n"
"ds_read_b128 a[116:119], v244 offset:55296\n"
"buffer_load_dwordx4 v244, s[8:11], s72 offen offset:0 sc1 lds\n"
"v_mfma_f32_16x16x32_bf16 v[72:75], a[104:107], a[56:59], v[72:75]\n"
"ds_read_b128 a[120:123], v244 offset:61440\n"
"v_mfma_f32_16x16x32_bf16 v[72:75], a[108:111], a[60:63], v[72:75]\n"
"ds_read_b128 a[124:127], v251 offset:2048\n"
"v_mfma_f32_16x16x32_bf16 v[76:79], a[96:99], a[64:67], v[76:79]\n"
"ds_read_b128 a[128:131], v244 offset:50176\n"
"v_mfma_f32_16x16x32_bf16 v[76:79], a[100:103], a[68:71], v[76:79]\n"
"ds_read_b128 a[132:135], v244 offset:56320\n"
"buffer_load_dwordx4 v244, s[8:11], s72 offen offset:1024 sc1 lds\n"
"v_mfma_f32_16x16x32_bf16 v[76:79], a[104:107], a[72:75], v[76:79]\n"
"ds_read_b128 a[136:139], v244 offset:62464\n"
"v_mfma_f32_16x16x32_bf16 v[76:79], a[108:111], a[76:79], v[76:79]\n"
"ds_read_b128 a[140:143], v251 offset:3072\n"
"v_mfma_f32_16x16x32_bf16 v[148:151], a[96:99], v[152:155], v[148:151]\n"
"ds_read_b128 a[96:99], v244 offset:51200\n"
"v_mfma_f32_16x16x32_bf16 v[148:151], a[100:103], v[152:155], v[148:151]\n"
"ds_read_b128 a[100:103], v244 offset:57344\n"
"buffer_load_dwordx4 v244, s[8:11], s72 offen offset:2048 sc1 lds\n"
"v_mfma_f32_16x16x32_bf16 v[148:151], a[104:107], v[152:155], v[148:151]\n"
"ds_read_b128 a[104:107], v244 offset:63488\n"
"v_mfma_f32_16x16x32_bf16 v[148:151], a[108:111], v[152:155], v[148:151]\n"
"ds_read_b128 a[108:111], v251 offset:4096\n"
"s_waitcnt vmcnt(37)\n"
"s_waitcnt lgkmcnt(6)\n"
"v_mfma_f32_16x16x32_bf16 v[0:3], a[112:115], a[80:83], v[0:3]\n"
"v_mfma_f32_16x16x32_bf16 v[0:3], a[116:119], a[84:87], v[0:3]\n"
"buffer_load_dwordx4 v244, s[8:11], s72 offen offset:3072 sc1 lds\n"
"v_mfma_f32_16x16x32_bf16 v[0:3], a[120:123], a[88:91], v[0:3]\n"
"v_mfma_f32_16x16x32_bf16 v[0:3], a[124:127], a[92:95], v[0:3]\n"
"v_mfma_f32_16x16x32_bf16 v[4:7], a[112:115], a[144:147], v[4:7]\n"
"v_mfma_f32_16x16x32_bf16 v[4:7], a[116:119], a[148:151], v[4:7]\n"
"s_add_u32 m0, s6, 4096\n"
"v_mfma_f32_16x16x32_bf16 v[4:7], a[120:123], a[152:155], v[4:7]\n"
"s_add_u32 s59, s7, 128\n"
"buffer_load_dword v249, off, s[8:11], s59 sc1\n"
"v_mfma_f32_16x16x32_bf16 v[4:7], a[124:127], a[156:159], v[4:7]\n"
"s_waitcnt vmcnt(29)\n"
"v_add_f32 v216, v208, v210\n"
"v_add_f32 v217, v212, v214\n"
"buffer_load_dwordx4 v250, s[8:11], s72 offen offset:0 sc1 lds\n"
"v_mfma_f32_16x16x32_bf16 v[128:131], a[112:115], v[152:155], v[128:131]\n"
"ds_read_b128 a[112:115], v244 offset:52224\n"
"v_mov_b32 v220, v216\n"
"v_mfma_f32_16x16x32_bf16 v[128:131], a[116:119], v[152:155], v[128:131]\n"
"ds_read_b128 a[116:119], v244 offset:58368\n"
"v_mov_b32 v221, v217\n"
"v_mfma_f32_16x16x32_bf16 v[128:131], a[120:123], v[152:155], v[128:131]\n"
"ds_read_b128 a[120:123], v244 offset:64512\n"
"v_permlane32_swap_b32 v216, v220\n"
"v_mfma_f32_16x16x32_bf16 v[128:131], a[124:127], v[152:155], v[128:131]\n"
"ds_read_b128 a[124:127], v251 offset:5120\n"
"v_permlane32_swap_b32 v217, v221\n"
"v_mfma_f32_16x16x32_bf16 v[8:11], a[128:131], a[80:83], v[8:11]\n"
"v_add_f32 v216, v216, v220\n"
"v_add_f32 v217, v217, v221\n"
"v_mfma_f32_16x16x32_bf16 v[8:11], a[132:135], a[84:87], v[8:11]\n"
"v_mov_b32 v220, v216\n"
"v_mov_b32 v221, v217\n"
"s_waitcnt lgkmcnt(4)\n"
"v_mfma_f32_16x16x32_bf16 v[8:11], a[136:139], a[88:91], v[8:11]\n"
"v_permlane16_swap_b32 v216, v220\n"
"v_permlane16_swap_b32 v217, v221\n"
"v_mfma_f32_16x16x32_bf16 v[8:11], a[140:143], a[92:95], v[8:11]\n"
"v_add_f32 v216, v216, v220\n"
"v_add_f32 v217, v217, v221\n"
"v_mfma_f32_16x16x32_bf16 v[12:15], a[128:131], a[144:147], v[12:15]\n"
"v_mul_f32 v216, 0x3e000000, v216\n"
"v_mul_f32 v217, 0x3e000000, v217\n"
"v_mfma_f32_16x16x32_bf16 v[12:15], a[132:135], a[148:151], v[12:15]\n"
"v_sub_f32 v208, v208, v216\n"
"v_sub_f32 v210, v210, v216\n"
"v_mfma_f32_16x16x32_bf16 v[12:15], a[136:139], a[152:155], v[12:15]\n"
"v_add_f32 v218, v209, v211\n"
"v_mul_f32 v208, v208, v208\n"
"v_mfma_f32_16x16x32_bf16 v[12:15], a[140:143], a[156:159], v[12:15]\n"
"v_fmac_f32 v208, v210, v210\n"
"v_fmamk_f32 v218, v208, 0x42a00000, v218\n"
"v_mfma_f32_16x16x32_bf16 v[132:135], a[128:131], v[152:155], v[132:135]\n"
"ds_read_b128 a[128:131], v244 offset:53248\n"
"v_sub_f32 v212, v212, v217\n"
"v_mfma_f32_16x16x32_bf16 v[132:135], a[132:135], v[152:155], v[132:135]\n"
"ds_read_b128 a[132:135], v244 offset:59392\n"
"v_sub_f32 v214, v214, v217\n"
"v_mfma_f32_16x16x32_bf16 v[132:135], a[136:139], v[152:155], v[132:135]\n"
"ds_read_b128 a[136:139], v251 offset:0\n"
"v_add_f32 v219, v213, v215\n"
"v_mfma_f32_16x16x32_bf16 v[132:135], a[140:143], v[152:155], v[132:135]\n"
"ds_read_b128 a[140:143], v251 offset:6144\n"
"v_mul_f32 v212, v212, v212\n"
"v_mfma_f32_16x16x32_bf16 v[32:35], a[96:99], a[80:83], v[32:35]\n"
"v_fmac_f32 v212, v214, v214\n"
"v_fmamk_f32 v219, v212, 0x42a00000, v219\n"
"v_mfma_f32_16x16x32_bf16 v[32:35], a[100:103], a[84:87], v[32:35]\n"
"v_mov_b32 v220, v218\n"
"v_mov_b32 v221, v219\n"
"v_mfma_f32_16x16x32_bf16 v[32:35], a[104:107], a[88:91], v[32:35]\n"
"v_permlane32_swap_b32 v218, v220\n"
"v_permlane32_swap_b32 v219, v221\n"
"v_mfma_f32_16x16x32_bf16 v[32:35], a[108:111], a[92:95], v[32:35]\n"
"v_add_f32 v218, v218, v220\n"
"v_add_f32 v219, v219, v221\n"
"v_mfma_f32_16x16x32_bf16 v[36:39], a[96:99], a[144:147], v[36:39]\n"
"v_mov_b32 v220, v218\n"
"v_mov_b32 v221, v219\n"
"v_mfma_f32_16x16x32_bf16 v[36:39], a[100:103], a[148:151], v[36:39]\n"
"v_permlane16_swap_b32 v218, v220\n"
"v_permlane16_swap_b32 v219, v221\n"
"v_mfma_f32_16x16x32_bf16 v[36:39], a[104:107], a[152:155], v[36:39]\n"
"v_add_f32 v218, v218, v220\n"
"v_add_f32 v219, v219, v221\n"
"v_mfma_f32_16x16x32_bf16 v[36:39], a[108:111], a[156:159], v[36:39]\n"
"v_mov_b32 v220, 0x3727c5ac\n"
"v_fmamk_f32 v218, v218, 0x3acccccd, v220\n"
"v_mfma_f32_16x16x32_bf16 v[136:139], a[96:99], v[152:155], v[136:139]\n"
"ds_read_b128 a[96:99], v244 offset:54272\n"
"v_fmamk_f32 v219, v219, 0x3acccccd, v220\n"
"v_mfma_f32_16x16x32_bf16 v[136:139], a[100:103], v[152:155], v[136:139]\n"
"ds_read_b128 a[100:103], v244 offset:60416\n"
"v_mfma_f32_16x16x32_bf16 v[136:139], a[104:107], v[152:155], v[136:139]\n"
"ds_read_b128 a[104:107], v251 offset:1024\n"
"v_mfma_f32_16x16x32_bf16 v[136:139], a[108:111], v[152:155], v[136:139]\n"
"ds_read_b128 a[108:111], v251 offset:7168\n"
"s_waitcnt lgkmcnt(4)\n"
"v_mfma_f32_16x16x32_bf16 v[40:43], a[112:115], a[80:83], v[40:43]\n"
"v_rsq_f32 v218, v218\n"
"v_mfma_f32_16x16x32_bf16 v[40:43], a[116:119], a[84:87], v[40:43]\n"
"v_rsq_f32 v219, v219\n"
"v_mfma_f32_16x16x32_bf16 v[40:43], a[120:123], a[88:91], v[40:43]\n"
"s_add_u32 s63, s64, 49152\n"
"v_mfma_f32_16x16x32_bf16 v[40:43], a[124:127], a[92:95], v[40:43]\n"
"v_mfma_f32_16x16x32_bf16 v[44:47], a[112:115], a[144:147], v[44:47]\n"
"v_mfma_f32_16x16x32_bf16 v[44:47], a[116:119], a[148:151], v[44:47]\n"
"v_mfma_f32_16x16x32_bf16 v[44:47], a[120:123], a[152:155], v[44:47]\n"
"v_mfma_f32_16x16x32_bf16 v[44:47], a[124:127], a[156:159], v[44:47]\n"
"v_mfma_f32_16x16x32_bf16 v[140:143], a[112:115], v[152:155], v[140:143]\n"
"v_mfma_f32_16x16x32_bf16 v[140:143], a[116:119], v[152:155], v[140:143]\n"
"v_mfma_f32_16x16x32_bf16 v[140:143], a[120:123], v[152:155], v[140:143]\n"
"s_waitcnt vmcnt(1)\n"
"v_cmp_gt_u32 vcc, 4, v249\n"
"s_cbranch_vccnz L_spin_o1_%=\n"
"L_back_o1_%=:\n"
"v_mfma_f32_16x16x32_bf16 v[140:143], a[124:127], v[152:155], v[140:143]\n"
"s_add_u32 s70, s12, s84\n"
"buffer_load_dwordx4 a[16:19], v244, s[8:11], s70 offen offset:2048 sc1\n"
"v_mfma_f32_16x16x32_bf16 v[64:67], a[128:131], a[80:83], v[64:67]\n"
"v_mfma_f32_16x16x32_bf16 v[64:67], a[132:135], a[84:87], v[64:67]\n"
"v_mfma_f32_16x16x32_bf16 v[64:67], a[136:139], a[88:91], v[64:67]\n"
"v_mfma_f32_16x16x32_bf16 v[64:67], a[140:143], a[92:95], v[64:67]\n"
"buffer_load_dwordx4 a[20:23], v244, s[8:11], s70 offen offset:3072 sc1\n"
"v_mfma_f32_16x16x32_bf16 v[68:71], a[128:131], a[144:147], v[68:71]\n"
"v_mfma_f32_16x16x32_bf16 v[68:71], a[132:135], a[148:151], v[68:71]\n"
"v_mfma_f32_16x16x32_bf16 v[68:71], a[136:139], a[152:155], v[68:71]\n"
"v_mfma_f32_16x16x32_bf16 v[68:71], a[140:143], a[156:159], v[68:71]\n"
"s_add_u32 s71, s13, s84\n"
"buffer_load_dwordx4 a[24:27], v244, s[8:11], s71 offen offset:2048 sc1\n"
"v_mfma_f32_16x16x32_bf16 v[144:147], a[128:131], v[152:155], v[144:147]\n"
"v_mfma_f32_16x16x32_bf16 v[144:147], a[132:135], v[152:155], v[144:147]\n"
"v_mfma_f32_16x16x32_bf16 v[144:147], a[136:139], v[152:155], v[144:147]\n"
"v_mfma_f32_16x16x32_bf16 v[144:147], a[140:143], v[152:155], v[144:147]\n"
"s_barrier\n"
"s_waitcnt lgkmcnt(0)\n"
"v_mfma_f32_16x16x32_bf16 v[72:75], a[96:99], a[80:83], v[72:75]\n"
"ds_read_b128 a[112:115], v251 offset:8192\n"
"buffer_load_dwordx4 a[28:31], v244, s[8:11], s71 offen offset:3072 sc1\n"
"v_mfma_f32_16x16x32_bf16 v[72:75], a[100:103], a[84:87], v[72:75]\n"
"ds_read_b128 a[116:119], v251 offset:14336\n"
"v_mfma_f32_16x16x32_bf16 v[72:75], a[104:107], a[88:91], v[72:75]\n"
"ds_read_b128 a[120:123], v251 offset:20480\n"
"v_mfma_f32_16x16x32_bf16 v[72:75], a[108:111], a[92:95], v[72:75]\n"
"ds_read_b128 a[124:127], v251 offset:26624\n"
"v_mfma_f32_16x16x32_bf16 v[76:79], a[96:99], a[144:147], v[76:79]\n"
"ds_read_b128 a[128:131], v251 offset:9216\n"
"s_add_u32 s70, s14, s84\n"
"buffer_load_dwordx4 a[32:35], v244, s[8:11], s70 offen offset:2048 sc1\n"
"v_mfma_f32_16x16x32_bf16 v[76:79], a[100:103], a[148:151], v[76:79]\n"
"ds_read_b128 a[132:135], v251 offset:15360\n"
"v_mfma_f32_16x16x32_bf16 v[76:79], a[104:107], a[152:155], v[76:79]\n"
"ds_read_b128 a[136:139], v251 offset:21504\n"
"v_mfma_f32_16x16x32_bf16 v[76:79], a[108:111], a[156:159], v[76:79]\n"
"ds_read_b128 a[140:143], v251 offset:27648\n"
"v_mfma_f32_16x16x32_bf16 v[148:151], a[96:99], v[152:155], v[148:151]\n"
"ds_read_b128 a[96:99], v251 offset:10240\n"
"buffer_load_dwordx4 a[36:39], v244, s[8:11], s70 offen offset:3072 sc1\n"
"v_mfma_f32_16x16x32_bf16 v[148:151], a[100:103], v[152:155], v[148:151]\n"
"ds_read_b128 a[100:103], v251 offset:16384\n"
"v_mfma_f32_16x16x32_bf16 v[148:151], a[104:107], v[152:155], v[148:151]\n"
"ds_read_b128 a[104:107], v251 offset:22528\n"
"v_mfma_f32_16x16x32_bf16 v[148:151], a[108:111], v[152:155], v[148:151]\n"
"ds_read_b128 a[108:111], v251 offset:28672\n"
"s_waitcnt lgkmcnt(6)\n"
"v_mfma_f32_16x16x32_bf16 v[0:3], a[112:115], a[160:163], v[0:3]\n"
"s_add_u32 s71, s15, s84\n"
"buffer_load_dwordx4 a[40:43], v244, s[8:11], s71 offen offset:2048 sc1\n"
"v_mfma_f32_16x16x32_bf16 v[0:3], a[116:119], a[164:167], v[0:3]\n"
"v_mfma_f32_16x16x32_bf16 v[0:3], a[120:123], a[168:171], v[0:3]\n"
"v_mfma_f32_16x16x32_bf16 v[0:3], a[124:127], a[172:175], v[0:3]\n"
"v_mfma_f32_16x16x32_bf16 v[4:7], a[112:115], a[176:179], v[4:7]\n"
"buffer_load_dwordx4 a[44:47], v244, s[8:11], s71 offen offset:3072 sc1\n"
"v_mfma_f32_16x16x32_bf16 v[4:7], a[116:119], a[180:183], v[4:7]\n"
"v_mfma_f32_16x16x32_bf16 v[4:7], a[120:123], a[184:187], v[4:7]\n"
"v_mfma_f32_16x16x32_bf16 v[4:7], a[124:127], a[188:191], v[4:7]\n"
"v_mfma_f32_16x16x32_bf16 v[128:131], a[112:115], v[152:155], v[128:131]\n"
"ds_read_b128 a[112:115], v251 offset:11264\n"
"s_add_u32 s70, s12, s85\n"
"buffer_load_dwordx4 a[48:51], v244, s[8:11], s70 offen offset:2048 sc1\n"
"v_mfma_f32_16x16x32_bf16 v[128:131], a[116:119], v[152:155], v[128:131]\n"
"ds_read_b128 a[116:119], v251 offset:17408\n"
"v_mfma_f32_16x16x32_bf16 v[128:131], a[120:123], v[152:155], v[128:131]\n"
"ds_read_b128 a[120:123], v251 offset:23552\n"
"v_mfma_f32_16x16x32_bf16 v[128:131], a[124:127], v[152:155], v[128:131]\n"
"ds_read_b128 a[124:127], v251 offset:29696\n"
"v_mfma_f32_16x16x32_bf16 v[8:11], a[128:131], a[160:163], v[8:11]\n"
"buffer_load_dwordx4 a[52:55], v244, s[8:11], s70 offen offset:3072 sc1\n"
"v_mfma_f32_16x16x32_bf16 v[8:11], a[132:135], a[164:167], v[8:11]\n"
"s_waitcnt lgkmcnt(4)\n"
"v_mfma_f32_16x16x32_bf16 v[8:11], a[136:139], a[168:171], v[8:11]\n"
"v_mfma_f32_16x16x32_bf16 v[8:11], a[140:143], a[172:175], v[8:11]\n"
"v_mfma_f32_16x16x32_bf16 v[12:15], a[128:131], a[176:179], v[12:15]\n"
"s_add_u32 s71, s13, s85\n"
"buffer_load_dwordx4 a[56:59], v244, s[8:11], s71 offen offset:2048 sc1\n"
"v_mfma_f32_16x16x32_bf16 v[12:15], a[132:135], a[180:183], v[12:15]\n"
"v_mfma_f32_16x16x32_bf16 v[12:15], a[136:139], a[184:187], v[12:15]\n"
"v_mfma_f32_16x16x32_bf16 v[12:15], a[140:143], a[188:191], v[12:15]\n"
"v_mfma_f32_16x16x32_bf16 v[132:135], a[128:131], v[152:155], v[132:135]\n"
"ds_read_b128 a[128:131], v251 offset:12288\n"
"buffer_load_dwordx4 a[60:63], v244, s[8:11], s71 offen offset:3072 sc1\n"
"v_mfma_f32_16x16x32_bf16 v[132:135], a[132:135], v[152:155], v[132:135]\n"
"ds_read_b128 a[132:135], v251 offset:18432\n"
"v_mfma_f32_16x16x32_bf16 v[132:135], a[136:139], v[152:155], v[132:135]\n"
"ds_read_b128 a[136:139], v251 offset:24576\n"
"v_mfma_f32_16x16x32_bf16 v[132:135], a[140:143], v[152:155], v[132:135]\n"
"ds_read_b128 a[140:143], v251 offset:30720\n"
"v_mfma_f32_16x16x32_bf16 v[32:35], a[96:99], a[160:163], v[32:35]\n"
"s_add_u32 s70, s14, s85\n"
"buffer_load_dwordx4 a[64:67], v244, s[8:11], s70 offen offset:2048 sc1\n"
"v_mfma_f32_16x16x32_bf16 v[32:35], a[100:103], a[164:167], v[32:35]\n"
"v_mfma_f32_16x16x32_bf16 v[32:35], a[104:107], a[168:171], v[32:35]\n"
"v_mfma_f32_16x16x32_bf16 v[32:35], a[108:111], a[172:175], v[32:35]\n"
"v_mfma_f32_16x16x32_bf16 v[36:39], a[96:99], a[176:179], v[36:39]\n"
"buffer_load_dwordx4 a[68:71], v244, s[8:11], s70 offen offset:3072 sc1\n"
"v_mfma_f32_16x16x32_bf16 v[36:39], a[100:103], a[180:183], v[36:39]\n"
"v_mfma_f32_16x16x32_bf16 v[36:39], a[104:107], a[184:187], v[36:39]\n"
"v_mfma_f32_16x16x32_bf16 v[36:39], a[108:111], a[188:191], v[36:39]\n"
"v_mfma_f32_16x16x32_bf16 v[136:139], a[96:99], v[152:155], v[136:139]\n"
"ds_read_b128 a[96:99], v251 offset:13312\n"
"s_add_u32 s71, s15, s85\n"
"buffer_load_dwordx4 a[72:75], v244, s[8:11], s71 offen offset:2048 sc1\n"
"v_mfma_f32_16x16x32_bf16 v[136:139], a[100:103], v[152:155], v[136:139]\n"
"ds_read_b128 a[100:103], v251 offset:19456\n"
"v_mfma_f32_16x16x32_bf16 v[136:139], a[104:107], v[152:155], v[136:139]\n"
"ds_read_b128 a[104:107], v251 offset:25600\n"
"v_mfma_f32_16x16x32_bf16 v[136:139], a[108:111], v[152:155], v[136:139]\n"
"ds_read_b128 a[108:111], v251 offset:31744\n"
"s_waitcnt lgkmcnt(4)\n"
"v_mfma_f32_16x16x32_bf16 v[40:43], a[112:115], a[160:163], v[40:43]\n"
"buffer_load_dwordx4 a[76:79], v244, s[8:11], s71 offen offset:3072 sc1\n"
"v_mfma_f32_16x16x32_bf16 v[40:43], a[116:119], a[164:167], v[40:43]\n"
"v_mfma_f32_16x16x32_bf16 v[40:43], a[120:123], a[168:171], v[40:43]\n"
"v_mfma_f32_16x16x32_bf16 v[40:43], a[124:127], a[172:175], v[40:43]\n"
"v_mfma_f32_16x16x32_bf16 v[44:47], a[112:115], a[176:179], v[44:47]\n"
"s_add_u32 s70, s12, s86\n"
"buffer_load_dwordx4 a[80:83], v244, s[8:11], s70 offen offset:2048 sc1\n"
"v_mfma_f32_16x16x32_bf16 v[44:47], a[116:119], a[180:183], v[44:47]\n"
"v_mfma_f32_16x16x32_bf16 v[44:47], a[120:123], a[184:187], v[44:47]\n"
"v_mfma_f32_16x16x32_bf16 v[44:47], a[124:127], a[188:191], v[44:47]\n"
"v_mfma_f32_16x16x32_bf16 v[140:143], a[112:115], v[152:155], v[140:143]\n"
"buffer_load_dwordx4 a[84:87], v244, s[8:11], s70 offen offset:3072 sc1\n"
"v_mfma_f32_16x16x32_bf16 v[140:143], a[116:119], v[152:155], v[140:143]\n"
"v_mfma_f32_16x16x32_bf16 v[140:143], a[120:123], v[152:155], v[140:143]\n"
"v_mfma_f32_16x16x32_bf16 v[140:143], a[124:127], v[152:155], v[140:143]\n"
"v_mfma_f32_16x16x32_bf16 v[64:67], a[128:131], a[160:163], v[64:67]\n"
"s_add_u32 s71, s13, s86\n"
"buffer_load_dwordx4 a[88:91], v244, s[8:11], s71 offen offset:2048 sc1\n"
"v_mfma_f32_16x16x32_bf16 v[64:67], a[132:135], a[164:167], v[64:67]\n"
"v_mfma_f32_16x16x32_bf16 v[64:67], a[136:139], a[168:171], v[64:67]\n"
"v_mfma_f32_16x16x32_bf16 v[64:67], a[140:143], a[172:175], v[64:67]\n"
"v_mfma_f32_16x16x32_bf16 v[68:71], a[128:131], a[176:179], v[68:71]\n"
"buffer_load_dwordx4 a[92:95], v244, s[8:11], s71 offen offset:3072 sc1\n"
"v_mfma_f32_16x16x32_bf16 v[68:71], a[132:135], a[180:183], v[68:71]\n"
"v_mfma_f32_16x16x32_bf16 v[68:71], a[136:139], a[184:187], v[68:71]\n"
"v_mfma_f32_16x16x32_bf16 v[68:71], a[140:143], a[188:191], v[68:71]\n"
"v_mfma_f32_16x16x32_bf16 v[144:147], a[128:131], v[152:155], v[144:147]\n"
"s_add_u32 s70, s14, s86\n"
"buffer_load_dwordx4 a[144:147], v244, s[8:11], s70 offen offset:2048 sc1\n"
"v_mfma_f32_16x16x32_bf16 v[144:147], a[132:135], v[152:155], v[144:147]\n"
"v_mfma_f32_16x16x32_bf16 v[144:147], a[136:139], v[152:155], v[144:147]\n"
"v_mfma_f32_16x16x32_bf16 v[144:147], a[140:143], v[152:155], v[144:147]\n"
"s_barrier\n"
"s_waitcnt lgkmcnt(0)\n"
"v_mfma_f32_16x16x32_bf16 v[72:75], a[96:99], a[160:163], v[72:75]\n"
"ds_read_b128 a[112:115], v251 offset:32768\n"
"buffer_load_dwordx4 a[148:151], v244, s[8:11], s70 offen offset:3072 sc1\n"
"v_mfma_f32_16x16x32_bf16 v[72:75], a[100:103], a[164:167], v[72:75]\n"
"ds_read_b128 a[116:119], v251 offset:38912\n"
"v_mfma_f32_16x16x32_bf16 v[72:75], a[104:107], a[168:171], v[72:75]\n"
"ds_read_b128 a[120:123], v251 offset:45056\n"
"v_mfma_f32_16x16x32_bf16 v[72:75], a[108:111], a[172:175], v[72:75]\n"
"ds_read_b128 a[124:127], v251 offset:51200\n"
"v_mfma_f32_16x16x32_bf16 v[76:79], a[96:99], a[176:179], v[76:79]\n"
"ds_read_b128 a[128:131], v251 offset:33792\n"
"s_add_u32 s71, s15, s86\n"
"buffer_load_dwordx4 a[152:155], v244, s[8:11], s71 offen offset:2048 sc1\n"
"v_mfma_f32_16x16x32_bf16 v[76:79], a[100:103], a[180:183], v[76:79]\n"
"ds_read_b128 a[132:135], v251 offset:39936\n"
"v_mfma_f32_16x16x32_bf16 v[76:79], a[104:107], a[184:187], v[76:79]\n"
"ds_read_b128 a[136:139], v251 offset:46080\n"
"v_mfma_f32_16x16x32_bf16 v[76:79], a[108:111], a[188:191], v[76:79]\n"
"ds_read_b128 a[140:143], v251 offset:52224\n"
"v_mfma_f32_16x16x32_bf16 v[148:151], a[96:99], v[152:155], v[148:151]\n"
"ds_read_b128 a[96:99], v251 offset:34816\n"
"buffer_load_dwordx4 a[156:159], v244, s[8:11], s71 offen offset:3072 sc1\n"
"v_mfma_f32_16x16x32_bf16 v[148:151], a[100:103], v[152:155], v[148:151]\n"
"ds_read_b128 a[100:103], v251 offset:40960\n"
"v_mfma_f32_16x16x32_bf16 v[148:151], a[104:107], v[152:155], v[148:151]\n"
"ds_read_b128 a[104:107], v251 offset:47104\n"
"v_mfma_f32_16x16x32_bf16 v[148:151], a[108:111], v[152:155], v[148:151]\n"
"ds_read_b128 a[108:111], v251 offset:53248\n"
"s_waitcnt lgkmcnt(6)\n"
"v_mfma_f32_16x16x32_bf16 v[0:3], a[112:115], a[192:195], v[0:3]\n"
"s_add_u32 s72, s81, 163840\n"
"s_add_u32 m0, s6, 20480\n"
"v_mfma_f32_16x16x32_bf16 v[0:3], a[116:119], a[196:199], v[0:3]\n"
"buffer_load_dwordx4 v244, s[8:11], s72 offen offset:0 sc1 lds\n"
"v_mfma_f32_16x16x32_bf16 v[0:3], a[120:123], a[200:203], v[0:3]\n"
"v_mfma_f32_16x16x32_bf16 v[0:3], a[124:127], a[204:207], v[0:3]\n"
"v_mfma_f32_16x16x32_bf16 v[4:7], a[112:115], a[0:3], v[4:7]\n"
"v_mfma_f32_16x16x32_bf16 v[4:7], a[116:119], a[4:7], v[4:7]\n"
"buffer_load_dwordx4 v244, s[8:11], s72 offen offset:1024 sc1 lds\n"
"v_mfma_f32_16x16x32_bf16 v[4:7], a[120:123], a[8:11], v[4:7]\n"
"v_mfma_f32_16x16x32_bf16 v[4:7], a[124:127], a[12:15], v[4:7]\n"
"v_mfma_f32_16x16x32_bf16 v[128:131], a[112:115], v[152:155], v[128:131]\n"
"ds_read_b128 a[112:115], v251 offset:35840\n"
"v_mfma_f32_16x16x32_bf16 v[128:131], a[116:119], v[152:155], v[128:131]\n"
"ds_read_b128 a[116:119], v251 offset:41984\n"
"buffer_load_dwordx4 v244, s[8:11], s72 offen offset:2048 sc1 lds\n"
"v_mfma_f32_16x16x32_bf16 v[128:131], a[120:123], v[152:155], v[128:131]\n"
"ds_read_b128 a[120:123], v251 offset:48128\n"
"v_mfma_f32_16x16x32_bf16 v[128:131], a[124:127], v[152:155], v[128:131]\n"
"ds_read_b128 a[124:127], v251 offset:54272\n"
"v_mfma_f32_16x16x32_bf16 v[8:11], a[128:131], a[192:195], v[8:11]\n"
"v_mfma_f32_16x16x32_bf16 v[8:11], a[132:135], a[196:199], v[8:11]\n"
"buffer_load_dwordx4 v244, s[8:11], s72 offen offset:3072 sc1 lds\n"
"s_waitcnt lgkmcnt(4)\n"
"v_mfma_f32_16x16x32_bf16 v[8:11], a[136:139], a[200:203], v[8:11]\n"
"v_mfma_f32_16x16x32_bf16 v[8:11], a[140:143], a[204:207], v[8:11]\n"
"v_mfma_f32_16x16x32_bf16 v[12:15], a[128:131], a[0:3], v[12:15]\n"
"v_mfma_f32_16x16x32_bf16 v[12:15], a[132:135], a[4:7], v[12:15]\n"
"s_add_u32 m0, s6, 24576\n"
"v_mfma_f32_16x16x32_bf16 v[12:15], a[136:139], a[8:11], v[12:15]\n"
"buffer_load_dwordx4 v250, s[8:11], s72 offen offset:0 sc1 lds\n"
"v_mfma_f32_16x16x32_bf16 v[12:15], a[140:143], a[12:15], v[12:15]\n"
"v_mfma_f32_16x16x32_bf16 v[132:135], a[128:131], v[152:155], v[132:135]\n"
"ds_read_b128 a[128:131], v251 offset:36864\n"
"v_mfma_f32_16x16x32_bf16 v[132:135], a[132:135], v[152:155], v[132:135]\n"
"ds_read_b128 a[132:135], v251 offset:43008\n"
"v_mfma_f32_16x16x32_bf16 v[132:135], a[136:139], v[152:155], v[132:135]\n"
"ds_read_b128 a[136:139], v251 offset:49152\n"
"s_add_u32 s72, s82, 163840\n"
"s_add_u32 m0, s6, 40960\n"
"v_mfma_f32_16x16x32_bf16 v[132:135], a[140:143], v[152:155], v[132:135]\n"
"ds_read_b128 a[140:143], v251 offset:55296\n"
"v_mfma_f32_16x16x32_bf16 v[32:35], a[96:99], a[192:195], v[32:35]\n"
"buffer_load_dwordx4 v244, s[8:11], s72 offen offset:0 sc1 lds\n"
"v_mfma_f32_16x16x32_bf16 v[32:35], a[100:103], a[196:199], v[32:35]\n"
"v_mfma_f32_16x16x32_bf16 v[32:35], a[104:107], a[200:203], v[32:35]\n"
"v_mfma_f32_16x16x32_bf16 v[32:35], a[108:111], a[204:207], v[32:35]\n"
"v_mfma_f32_16x16x32_bf16 v[36:39], a[96:99], a[0:3], v[36:39]\n"
"buffer_load_dwordx4 v244, s[8:11], s72 offen offset:1024 sc1 lds\n"
"v_mfma_f32_16x16x32_bf16 v[36:39], a[100:103], a[4:7], v[36:39]\n"
"v_mfma_f32_16x16x32_bf16 v[36:39], a[104:107], a[8:11], v[36:39]\n"
"v_mfma_f32_16x16x32_bf16 v[36:39], a[108:111], a[12:15], v[36:39]\n"
"v_mfma_f32_16x16x32_bf16 v[136:139], a[96:99], v[152:155], v[136:139]\n"
"ds_read_b128 a[96:99], v251 offset:37888\n"
"buffer_load_dwordx4 v244, s[8:11], s72 offen offset:2048 sc1 lds\n"
"v_mfma_f32_16x16x32_bf16 v[136:139], a[100:103], v[152:155], v[136:139]\n"
"ds_read_b128 a[100:103], v251 offset:44032\n"
"v_mfma_f32_16x16x32_bf16 v[136:139], a[104:107], v[152:155], v[136:139]\n"
"ds_read_b128 a[104:107], v251 offset:50176\n"
"v_mfma_f32_16x16x32_bf16 v[136:139], a[108:111], v[152:155], v[136:139]\n"
"ds_read_b128 a[108:111], v251 offset:56320\n"
"s_waitcnt lgkmcnt(4)\n"
"v_mfma_f32_16x16x32_bf16 v[40:43], a[112:115], a[192:195], v[40:43]\n"
"buffer_load_dwordx4 v244, s[8:11], s72 offen offset:3072 sc1 lds\n"
"v_mfma_f32_16x16x32_bf16 v[40:43], a[116:119], a[196:199], v[40:43]\n"
"v_mfma_f32_16x16x32_bf16 v[40:43], a[120:123], a[200:203], v[40:43]\n"
"v_mfma_f32_16x16x32_bf16 v[40:43], a[124:127], a[204:207], v[40:43]\n"
"v_mfma_f32_16x16x32_bf16 v[44:47], a[112:115], a[0:3], v[44:47]\n"
"s_add_u32 m0, s6, 45056\n"
"v_mfma_f32_16x16x32_bf16 v[44:47], a[116:119], a[4:7], v[44:47]\n"
"buffer_load_dwordx4 v250, s[8:11], s72 offen offset:0 sc1 lds\n"
"v_mfma_f32_16x16x32_bf16 v[44:47], a[120:123], a[8:11], v[44:47]\n"
"v_mfma_f32_16x16x32_bf16 v[44:47], a[124:127], a[12:15], v[44:47]\n"
"v_mfma_f32_16x16x32_bf16 v[140:143], a[112:115], v[152:155], v[140:143]\n"
"v_mfma_f32_16x16x32_bf16 v[140:143], a[116:119], v[152:155], v[140:143]\n"
"s_add_u32 s70, s12, s84\n"
"buffer_load_dwordx4 a[160:163], v250, s[8:11], s70 offen offset:0 sc1\n"
"v_mfma_f32_16x16x32_bf16 v[140:143], a[120:123], v[152:155], v[140:143]\n"
"v_mfma_f32_16x16x32_bf16 v[140:143], a[124:127], v[152:155], v[140:143]\n"
"v_mfma_f32_16x16x32_bf16 v[64:67], a[128:131], a[192:195], v[64:67]\n"
"v_mfma_f32_16x16x32_bf16 v[64:67], a[132:135], a[196:199], v[64:67]\n"
"buffer_load_dwordx4 a[164:167], v250, s[8:11], s70 offen offset:1024 sc1\n"
"v_mfma_f32_16x16x32_bf16 v[64:67], a[136:139], a[200:203], v[64:67]\n"
"v_mfma_f32_16x16x32_bf16 v[64:67], a[140:143], a[204:207], v[64:67]\n"
"v_mfma_f32_16x16x32_bf16 v[68:71], a[128:131], a[0:3], v[68:71]\n"
"v_mfma_f32_16x16x32_bf16 v[68:71], a[132:135], a[4:7], v[68:71]\n"
"s_add_u32 s71, s12, s85\n"
"buffer_load_dwordx4 a[168:171], v250, s[8:11], s71 offen offset:0 sc1\n"
"v_mfma_f32_16x16x32_bf16 v[68:71], a[136:139], a[8:11], v[68:71]\n"
"v_mfma_f32_16x16x32_bf16 v[68:71], a[140:143], a[12:15], v[68:71]\n"
"v_mfma_f32_16x16x32_bf16 v[144:147], a[128:131], v[152:155], v[144:147]\n"
"v_mfma_f32_16x16x32_bf16 v[144:147], a[132:135], v[152:155], v[144:147]\n"
"buffer_load_dwordx4 a[172:175], v250, s[8:11], s71 offen offset:1024 sc1\n"
"v_mfma_f32_16x16x32_bf16 v[144:147], a[136:139], v[152:155], v[144:147]\n"
"v_mfma_f32_16x16x32_bf16 v[144:147], a[140:143], v[152:155], v[144:147]\n"
"s_barrier\n"
"s_waitcnt lgkmcnt(0)\n"
"v_mfma_f32_16x16x32_bf16 v[72:75], a[96:99], a[192:195], v[72:75]\n"
"ds_read_b128 a[112:115], v251 offset:57344\n"
"v_mfma_f32_16x16x32_bf16 v[72:75], a[100:103], a[196:199], v[72:75]\n"
"ds_read_b128 a[116:119], v252 offset:2048\n"
"s_add_u32 s72, s83, 163840\n"
"s_add_u32 m0, s6, 61440\n"
"v_mfma_f32_16x16x32_bf16 v[72:75], a[104:107], a[200:203], v[72:75]\n"
"ds_read_b128 a[120:123], v251 offset:58368\n"
"buffer_load_dwordx4 v244, s[8:11], s72 offen offset:0 sc1 lds\n"
"v_mfma_f32_16x16x32_bf16 v[72:75], a[108:111], a[204:207], v[72:75]\n"
"ds_read_b128 a[124:127], v252 offset:3072\n"
"v_mfma_f32_16x16x32_bf16 v[76:79], a[96:99], a[0:3], v[76:79]\n"
"ds_read_b128 a[128:131], v251 offset:59392\n"
"v_mfma_f32_16x16x32_bf16 v[76:79], a[100:103], a[4:7], v[76:79]\n"
"ds_read_b128 a[132:135], v252 offset:4096\n"
"v_mfma_f32_16x16x32_bf16 v[76:79], a[104:107], a[8:11], v[76:79]\n"
"buffer_load_dwordx4 v244, s[8:11], s72 offen offset:1024 sc1 lds\n"
"v_mfma_f32_16x16x32_bf16 v[76:79], a[108:111], a[12:15], v[76:79]\n"
"v_mfma_f32_16x16x32_bf16 v[148:151], a[96:99], v[152:155], v[148:151]\n"
"ds_read_b128 a[136:139], v251 offset:60416\n"
"v_mfma_f32_16x16x32_bf16 v[148:151], a[100:103], v[152:155], v[148:151]\n"
"ds_read_b128 a[140:143], v252 offset:5120\n"
"v_mfma_f32_16x16x32_bf16 v[148:151], a[104:107], v[152:155], v[148:151]\n"
"buffer_load_dwordx4 v244, s[8:11], s72 offen offset:2048 sc1 lds\n"
"v_mfma_f32_16x16x32_bf16 v[148:151], a[108:111], v[152:155], v[148:151]\n"
"s_waitcnt vmcnt(33)\n"
"s_waitcnt lgkmcnt(2)\n"
"v_mfma_f32_16x16x32_bf16 v[16:19], a[112:115], a[16:19], v[16:19]\n"
"v_mfma_f32_16x16x32_bf16 v[16:19], a[116:119], a[20:23], v[16:19]\n"
"v_mfma_f32_16x16x32_bf16 v[20:23], a[112:115], a[24:27], v[20:23]\n"
"buffer_load_dwordx4 v244, s[8:11], s72 offen offset:3072 sc1 lds\n"
"v_mfma_f32_16x16x32_bf16 v[20:23], a[116:119], a[28:31], v[20:23]\n"
"v_mfma_f32_16x16x32_bf16 v[24:27], a[112:115], a[32:35], v[24:27]\n"
"ds_read_b128 a[96:99], v251 offset:61440\n"
"v_fma_f32 v0, -v216, v128, v0\n"
"v_fma_f32 v1, -v216, v129, v1\n"
"v_mfma_f32_16x16x32_bf16 v[24:27], a[116:119], a[36:39], v[24:27]\n"
"ds_read_b128 a[100:103], v252 offset:6144\n"
"v_fma_f32 v2, -v216, v130, v2\n"
"v_fma_f32 v3, -v216, v131, v3\n"
"s_add_u32 m0, s6, 65536\n"
"v_mfma_f32_16x16x32_bf16 v[28:31], a[112:115], a[40:43], v[28:31]\n"
"v_mul_f32 v0, v0, v218\n"
"v_mul_f32 v1, v1, v218\n"
"v_mul_f32 v2, v2, v218\n"
"buffer_load_dwordx4 v250, s[8:11], s72 offen offset:0 sc1 lds\n"
"v_mfma_f32_16x16x32_bf16 v[28:31], a[116:119], a[44:47], v[28:31]\n"
"v_mul_f32 v3, v3, v218\n"
"v_mul_f32 v208, v0, v0\n"
"v_mul_f32 v209, v1, v1\n"
"v_mfma_f32_16x16x32_bf16 v[48:51], a[120:123], a[16:19], v[48:51]\n"
"v_mul_f32 v210, v2, v2\n"
"v_mul_f32 v211, v3, v3\n"
"v_fmaak_f32 v208, v247, v208, 0xc0135761\n"
"v_mfma_f32_16x16x32_bf16 v[48:51], a[124:127], a[20:23], v[48:51]\n"
"v_fmaak_f32 v209, v247, v209, 0xc0135761\n"
"v_fmaak_f32 v210, v247, v210, 0xc0135761\n"
"v_fmaak_f32 v211, v247, v211, 0xc0135761\n"
"s_add_u32 s70, s12, s86\n"
"buffer_load_dwordx4 a[176:179], v250, s[8:11], s70 offen offset:0 sc1\n"
"v_mfma_f32_16x16x32_bf16 v[52:55], a[120:123], a[24:27], v[52:55]\n"
"v_mul_f32 v208, v208, v0\n"
"v_mul_f32 v209, v209, v1\n"
"v_mul_f32 v210, v210, v2\n"
"v_mfma_f32_16x16x32_bf16 v[52:55], a[124:127], a[28:31], v[52:55]\n"
"v_mul_f32 v211, v211, v3\n"
"v_exp_f32 v208, v208\n"
"v_mfma_f32_16x16x32_bf16 v[56:59], a[120:123], a[32:35], v[56:59]\n"
"ds_read_b128 a[104:107], v251 offset:62464\n"
"v_exp_f32 v209, v209\n"
"buffer_load_dwordx4 a[180:183], v250, s[8:11], s70 offen offset:1024 sc1\n"
"v_mfma_f32_16x16x32_bf16 v[56:59], a[124:127], a[36:39], v[56:59]\n"
"ds_read_b128 a[108:111], v252 offset:7168\n"
"v_exp_f32 v210, v210\n"
"v_mfma_f32_16x16x32_bf16 v[60:63], a[120:123], a[40:43], v[60:63]\n"
"v_exp_f32 v211, v211\n"
"v_add_f32 v208, 1.0, v208\n"
"v_mfma_f32_16x16x32_bf16 v[60:63], a[124:127], a[44:47], v[60:63]\n"
"v_add_f32 v209, 1.0, v209\n"
"v_add_f32 v210, 1.0, v210\n"
"v_add_f32 v211, 1.0, v211\n"
"v_mfma_f32_16x16x32_bf16 v[80:83], a[128:131], a[16:19], v[80:83]\n"
"v_rcp_f32 v208, v208\n"
"s_add_u32 s71, s13, s84\n"
"buffer_load_dwordx4 a[184:187], v250, s[8:11], s71 offen offset:0 sc1\n"
"v_mfma_f32_16x16x32_bf16 v[80:83], a[132:135], a[20:23], v[80:83]\n"
"v_rcp_f32 v209, v209\n"
"v_mfma_f32_16x16x32_bf16 v[84:87], a[128:131], a[24:27], v[84:87]\n"
"v_rcp_f32 v210, v210\n"
"v_mfma_f32_16x16x32_bf16 v[84:87], a[132:135], a[28:31], v[84:87]\n"
"v_rcp_f32 v211, v211\n"
"v_mul_f32 v0, v0, v208\n"
"buffer_load_dwordx4 a[188:191], v250, s[8:11], s71 offen offset:1024 sc1\n"
"v_mfma_f32_16x16x32_bf16 v[88:91], a[128:131], a[32:35], v[88:91]\n"
"ds_read_b128 a[112:115], v251 offset:63488\n"
"v_mul_f32 v1, v1, v209\n"
"v_mul_f32 v2, v2, v210\n"
"v_mfma_f32_16x16x32_bf16 v[88:91], a[132:135], a[36:39], v[88:91]\n"
"ds_read_b128 a[116:119], v252 offset:8192\n"
"v_mul_f32 v3, v3, v211\n"
"v_fma_f32 v8, -v216, v132, v8\n"
"v_mfma_f32_16x16x32_bf16 v[92:95], a[128:131], a[40:43], v[92:95]\n"
"v_fma_f32 v9, -v216, v133, v9\n"
"v_fma_f32 v10, -v216, v134, v10\n"
"v_fma_f32 v11, -v216, v135, v11\n"
"s_add_u32 s70, s13, s85\n"
"buffer_load_dwordx4 a[192:195], v250, s[8:11], s70 offen offset:0 sc1\n"
"v_mfma_f32_16x16x32_bf16 v[92:95], a[132:135], a[44:47], v[92:95]\n"
"v_mul_f32 v8, v8, v218\n"
"v_mul_f32 v9, v9, v218\n"
"v_mul_f32 v10, v10, v218\n"
"s_waitcnt lgkmcnt(2)\n"
"v_mfma_f32_16x16x32_bf16 v[112:115], a[136:139], a[16:19], v[112:115]\n"
"v_mul_f32 v11, v11, v218\n"
"v_mul_f32 v212, v8, v8\n"
"v_mul_f32 v213, v9, v9\n"
"v_mfma_f32_16x16x32_bf16 v[112:115], a[140:143], a[20:23], v[112:115]\n"
"v_mul_f32 v214, v10, v10\n"
"v_mul_f32 v215, v11, v11\n"
"v_fmaak_f32 v212, v247, v212, 0xc0135761\n"
"buffer_load_dwordx4 a[196:199], v250, s[8:11], s70 offen offset:1024 sc1\n"
"v_mfma_f32_16x16x32_bf16 v[116:119], a[136:139], a[24:27], v[116:119]\n"
"v_fmaak_f32 v213, v247, v213, 0xc0135761\n"
"v_fmaak_f32 v214, v247, v214, 0xc0135761\n"
"v_fmaak_f32 v215, v247, v215, 0xc0135761\n"
"v_mfma_f32_16x16x32_bf16 v[116:119], a[140:143], a[28:31], v[116:119]\n"
"v_mul_f32 v212, v212, v8\n"
"v_mul_f32 v213, v213, v9\n"
"v_mul_f32 v214, v214, v10\n"
"v_mfma_f32_16x16x32_bf16 v[120:123], a[136:139], a[32:35], v[120:123]\n"
"ds_read_b128 a[120:123], v251 offset:64512\n"
"v_mul_f32 v215, v215, v11\n"
"s_add_u32 s71, s13, s86\n"
"buffer_load_dwordx4 a[200:203], v250, s[8:11], s71 offen offset:0 sc1\n"
"v_mfma_f32_16x16x32_bf16 v[120:123], a[140:143], a[36:39], v[120:123]\n"
"ds_read_b128 a[124:127], v252 offset:9216\n"
"v_exp_f32 v212, v212\n"
"v_mfma_f32_16x16x32_bf16 v[124:127], a[136:139], a[40:43], v[124:127]\n"
"v_exp_f32 v213, v213\n"
"v_mfma_f32_16x16x32_bf16 v[124:127], a[140:143], a[44:47], v[124:127]\n"
"v_exp_f32 v214, v214\n"
"buffer_load_dwordx4 a[204:207], v250, s[8:11], s71 offen offset:1024 sc1\n"
"v_mfma_f32_16x16x32_bf16 v[160:163], a[96:99], a[16:19], v[160:163]\n"
"v_exp_f32 v215, v215\n"
"v_add_f32 v212, 1.0, v212\n"
"v_mfma_f32_16x16x32_bf16 v[160:163], a[100:103], a[20:23], v[160:163]\n"
"v_add_f32 v213, 1.0, v213\n"
"v_add_f32 v214, 1.0, v214\n"
"v_add_f32 v215, 1.0, v215\n"
"v_mfma_f32_16x16x32_bf16 v[164:167], a[96:99], a[24:27], v[164:167]\n"
"v_rcp_f32 v212, v212\n"
"s_add_u32 s70, s14, s84\n"
"buffer_load_dwordx4 a[0:3], v250, s[8:11], s70 offen offset:0 sc1\n"
"v_mfma_f32_16x16x32_bf16 v[164:167], a[100:103], a[28:31], v[164:167]\n"
"v_rcp_f32 v213, v213\n"
"v_mfma_f32_16x16x32_bf16 v[168:171], a[96:99], a[32:35], v[168:171]\n"
"ds_read_b128 a[128:131], v252 offset:0\n"
"v_rcp_f32 v214, v214\n"
"v_mfma_f32_16x16x32_bf16 v[168:171], a[100:103], a[36:39], v[168:171]\n"
"ds_read_b128 a[132:135], v252 offset:10240\n"
"v_rcp_f32 v215, v215\n"
"buffer_load_dwordx4 a[4:7], v250, s[8:11], s70 offen offset:1024 sc1\n"
"v_mfma_f32_16x16x32_bf16 v[172:175], a[96:99], a[40:43], v[172:175]\n"
"v_mul_f32 v8, v8, v212\n"
"v_mul_f32 v9, v9, v213\n"
"v_mul_f32 v10, v10, v214\n"
"v_mfma_f32_16x16x32_bf16 v[172:175], a[100:103], a[44:47], v[172:175]\n"
"v_mul_f32 v11, v11, v215\n"
"v_cvt_pk_bf16_f32 v0, v0, v1\n"
"v_cvt_pk_bf16_f32 v1, v2, v3\n"
"v_mfma_f32_16x16x32_bf16 v[176:179], a[104:107], a[16:19], v[176:179]\n"
"v_cvt_pk_bf16_f32 v2, v8, v9\n"
"v_cvt_pk_bf16_f32 v3, v10, v11\n"
"v_fma_f32 v4, -v217, v128, v4\n"
"s_add_u32 s71, s14, s85\n"
"buffer_load_dwordx4 a[8:11], v250, s[8:11], s71 offen offset:0 sc1\n"
"v_mfma_f32_16x16x32_bf16 v[176:179], a[108:111], a[20:23], v[176:179]\n"
"v_fma_f32 v5, -v217, v129, v5\n"
"v_fma_f32 v6, -v217, v130, v6\n"
"v_fma_f32 v7, -v217, v131, v7\n"
"v_mfma_f32_16x16x32_bf16 v[180:183], a[104:107], a[24:27], v[180:183]\n"
"v_mul_f32 v4, v4, v219\n"
"v_mul_f32 v5, v5, v219\n"
"v_mul_f32 v6, v6, v219\n"
"v_mfma_f32_16x16x32_bf16 v[180:183], a[108:111], a[28:31], v[180:183]\n"
"v_mul_f32 v7, v7, v219\n"
"v_mul_f32 v208, v4, v4\n"
"v_mul_f32 v209, v5, v5\n"
"buffer_store_dwordx4 v[0:3], v244, s[8:11], s64 offen offset:0 sc1\n"
"v_mfma_f32_16x16x32_bf16 v[184:187], a[104:107], a[32:35], v[184:187]\n"
"ds_read_b128 a[136:139], v252 offset:1024\n"
"v_mul_f32 v210, v6, v6\n"
"v_mul_f32 v211, v7, v7\n"
"v_mfma_f32_16x16x32_bf16 v[184:187], a[108:111], a[36:39], v[184:187]\n"
"ds_read_b128 a[140:143], v252 offset:11264\n"
"v_fmaak_f32 v208, v247, v208, 0xc0135761\n"
"v_fmaak_f32 v209, v247, v209, 0xc0135761\n"
"v_mfma_f32_16x16x32_bf16 v[188:191], a[104:107], a[40:43], v[188:191]\n"
"v_fmaak_f32 v210, v247, v210, 0xc0135761\n"
"v_fmaak_f32 v211, v247, v211, 0xc0135761\n"
"v_mul_f32 v208, v208, v4\n"
"buffer_load_dwordx4 a[12:15], v250, s[8:11], s71 offen offset:1024 sc1\n"
"v_mfma_f32_16x16x32_bf16 v[188:191], a[108:111], a[44:47], v[188:191]\n"
"v_mul_f32 v209, v209, v5\n"
"v_mul_f32 v210, v210, v6\n"
"v_mul_f32 v211, v211, v7\n"
"s_waitcnt lgkmcnt(2)\n"
"v_mfma_f32_16x16x32_bf16 v[192:195], a[112:115], a[16:19], v[192:195]\n"
"v_exp_f32 v208, v208\n"
"v_mfma_f32_16x16x32_bf16 v[192:195], a[116:119], a[20:23], v[192:195]\n"
"v_exp_f32 v209, v209\n"
"v_mfma_f32_16x16x32_bf16 v[196:199], a[112:115], a[24:27], v[196:199]\n"
"v_exp_f32 v210, v210\n"
"v_mfma_f32_16x16x32_bf16 v[196:199], a[116:119], a[28:31], v[196:199]\n"
"v_exp_f32 v211, v211\n"
"v_add_f32 v208, 1.0, v208\n"
"v_mfma_f32_16x16x32_bf16 v[200:203], a[112:115], a[32:35], v[200:203]\n"
"v_add_f32 v209, 1.0, v209\n"
"v_add_f32 v210, 1.0, v210\n"
"v_add_f32 v211, 1.0, v211\n"
"v_mfma_f32_16x16x32_bf16 v[200:203], a[116:119], a[36:39], v[200:203]\n"
"v_rcp_f32 v208, v208\n"
"v_mfma_f32_16x16x32_bf16 v[204:207], a[112:115], a[40:43], v[204:207]\n"
"v_rcp_f32 v209, v209\n"
"v_mfma_f32_16x16x32_bf16 v[204:207], a[116:119], a[44:47], v[204:207]\n"
"v_rcp_f32 v210, v210\n"
"v_mfma_f32_16x16x32_bf16 a[208:211], a[120:123], a[16:19], a[208:211]\n"
"v_rcp_f32 v211, v211\n"
"v_mul_f32 v4, v4, v208\n"
"v_mfma_f32_16x16x32_bf16 a[208:211], a[124:127], a[20:23], a[208:211]\n"
"v_mul_f32 v5, v5, v209\n"
"v_mul_f32 v6, v6, v210\n"
"v_mul_f32 v7, v7, v211\n"
"v_mfma_f32_16x16x32_bf16 a[212:215], a[120:123], a[24:27], a[212:215]\n"
"v_fma_f32 v12, -v217, v132, v12\n"
"v_fma_f32 v13, -v217, v133, v13\n"
"v_fma_f32 v14, -v217, v134, v14\n"
"v_mfma_f32_16x16x32_bf16 a[212:215], a[124:127], a[28:31], a[212:215]\n"
"v_fma_f32 v15, -v217, v135, v15\n"
"v_mul_f32 v12, v12, v219\n"
"v_mul_f32 v13, v13, v219\n"
"v_mfma_f32_16x16x32_bf16 a[216:219], a[120:123], a[32:35], a[216:219]\n"
"v_mul_f32 v14, v14, v219\n"
"v_mul_f32 v15, v15, v219\n"
"v_mul_f32 v212, v12, v12\n"
"v_mfma_f32_16x16x32_bf16 a[216:219], a[124:127], a[36:39], a[216:219]\n"
"v_mul_f32 v213, v13, v13\n"
"v_mul_f32 v214, v14, v14\n"
"v_mul_f32 v215, v15, v15\n"
"v_mfma_f32_16x16x32_bf16 a[220:223], a[120:123], a[40:43], a[220:223]\n"
"v_fmaak_f32 v212, v247, v212, 0xc0135761\n"
"v_fmaak_f32 v213, v247, v213, 0xc0135761\n"
"v_fmaak_f32 v214, v247, v214, 0xc0135761\n"
"v_mfma_f32_16x16x32_bf16 a[220:223], a[124:127], a[44:47], a[220:223]\n"
"v_fmaak_f32 v215, v247, v215, 0xc0135761\n"
"v_mul_f32 v212, v212, v12\n"
"v_mul_f32 v213, v213, v13\n"
"v_mfma_f32_16x16x32_bf16 a[224:227], a[128:131], a[16:19], a[224:227]\n"
"v_mul_f32 v214, v214, v14\n"
"v_mul_f32 v215, v215, v15\n"
"v_mfma_f32_16x16x32_bf16 a[224:227], a[132:135], a[20:23], a[224:227]\n"
"v_exp_f32 v212, v212\n"
"v_mfma_f32_16x16x32_bf16 a[228:231], a[128:131], a[24:27], a[228:231]\n"
"v_exp_f32 v213, v213\n"
"v_mfma_f32_16x16x32_bf16 a[228:231], a[132:135], a[28:31], a[228:231]\n"
"s_barrier\n"
"v_mfma_f32_16x16x32_bf16 a[232:235], a[128:131], a[32:35], a[232:235]\n"
"ds_read_b128 a[96:99], v252 offset:12288\n"
"v_exp_f32 v214, v214\n"
"s_add_u32 s72, s61, 0\n"
"s_add_u32 m0, s6, 81920\n"
"v_mfma_f32_16x16x32_bf16 a[232:235], a[132:135], a[36:39], a[232:235]\n"
"ds_read_b128 a[100:103], v252 offset:22528\n"
"v_exp_f32 v215, v215\n"
"buffer_load_dwordx4 v244, s[8:11], s72 offen offset:0 sc1 lds\n"
"v_mfma_f32_16x16x32_bf16 a[236:239], a[128:131], a[40:43], a[236:239]\n"
"ds_read_b128 a[104:107], v252 offset:13312\n"
"v_add_f32 v212, 1.0, v212\n"
"v_add_f32 v213, 1.0, v213\n"
"v_mfma_f32_16x16x32_bf16 a[236:239], a[132:135], a[44:47], a[236:239]\n"
"ds_read_b128 a[108:111], v252 offset:23552\n"
"v_add_f32 v214, 1.0, v214\n"
"v_add_f32 v215, 1.0, v215\n"
"s_waitcnt lgkmcnt(4)\n"
"v_mfma_f32_16x16x32_bf16 a[240:243], a[136:139], a[16:19], a[240:243]\n"
"ds_read_b128 a[112:115], v252 offset:14336\n"
"v_rcp_f32 v212, v212\n"
"s_add_u32 s70, s14, s86\n"
"buffer_load_dwordx4 a[16:19], v250, s[8:11], s70 offen offset:0 sc1\n"
"v_mfma_f32_16x16x32_bf16 a[240:243], a[140:143], a[20:23], a[240:243]\n"
"ds_read_b128 a[116:119], v252 offset:24576\n"
"v_rcp_f32 v213, v213\n"
"v_mfma_f32_16x16x32_bf16 a[244:247], a[136:139], a[24:27], a[244:247]\n"
"v_rcp_f32 v214, v214\n"
"v_mfma_f32_16x16x32_bf16 a[244:247], a[140:143], a[28:31], a[244:247]\n"
"v_rcp_f32 v215, v215\n"
"v_mul_f32 v12, v12, v212\n"
"buffer_load_dwordx4 a[20:23], v250, s[8:11], s70 offen offset:1024 sc1\n"
"v_mfma_f32_16x16x32_bf16 a[248:251], a[136:139], a[32:35], a[248:251]\n"
"ds_read_b128 a[120:123], v252 offset:15360\n"
"v_mul_f32 v13, v13, v213\n"
"v_mul_f32 v14, v14, v214\n"
"v_mfma_f32_16x16x32_bf16 a[248:251], a[140:143], a[36:39], a[248:251]\n"
"ds_read_b128 a[124:127], v252 offset:25600\n"
"v_mul_f32 v15, v15, v215\n"
"v_cvt_pk_bf16_f32 v4, v4, v5\n"
"v_mfma_f32_16x16x32_bf16 a[252:255], a[136:139], a[40:43], a[252:255]\n"
"v_cvt_pk_bf16_f32 v5, v6, v7\n"
"v_cvt_pk_bf16_f32 v6, v12, v13\n"
"v_cvt_pk_bf16_f32 v7, v14, v15\n"
"s_add_u32 s71, s15, s84\n"
"buffer_load_dwordx4 a[24:27], v250, s[8:11], s71 offen offset:0 sc1\n"
"v_mfma_f32_16x16x32_bf16 a[252:255], a[140:143], a[44:47], a[252:255]\n"
"v_fma_f32 v32, -v216, v136, v32\n"
"v_fma_f32 v33, -v216, v137, v33\n"
"v_fma_f32 v34, -v216, v138, v34\n"
"s_waitcnt vmcnt(44)\n"
"s_waitcnt lgkmcnt(2)\n"
"v_mfma_f32_16x16x32_bf16 v[16:19], a[96:99], a[48:51], v[16:19]\n"
"v_fma_f32 v35, -v216, v139, v35\n"
"v_mul_f32 v32, v32, v218\n"
"v_mul_f32 v33, v33, v218\n"
"v_mfma_f32_16x16x32_bf16 v[16:19], a[100:103], a[52:55], v[16:19]\n"
"v_mul_f32 v34, v34, v218\n"
"v_mul_f32 v35, v35, v218\n"
"v_mul_f32 v208, v32, v32\n"
"buffer_store_dwordx4 v[4:7], v244, s[8:11], s63 offen offset:0 sc1\n"
"v_mfma_f32_16x16x32_bf16 v[20:23], a[96:99], a[56:59], v[20:23]\n"
"v_mul_f32 v209, v33, v33\n"
"v_mul_f32 v210, v34, v34\n"
"v_mul_f32 v211, v35, v35\n"
"v_mfma_f32_16x16x32_bf16 v[20:23], a[100:103], a[60:63], v[20:23]\n"
"v_fmaak_f32 v208, v247, v208, 0xc0135761\n"
"v_fmaak_f32 v209, v247, v209, 0xc0135761\n"
"v_fmaak_f32 v210, v247, v210, 0xc0135761\n"
"v_mfma_f32_16x16x32_bf16 v[24:27], a[96:99], a[64:67], v[24:27]\n"
"ds_read_b128 a[128:131], v252 offset:16384\n"
"v_fmaak_f32 v211, v247, v211, 0xc0135761\n"
"v_mul_f32 v208, v208, v32\n"
"buffer_load_dwordx4 a[28:31], v250, s[8:11], s71 offen offset:1024 sc1\n"
"v_mfma_f32_16x16x32_bf16 v[24:27], a[100:103], a[68:71], v[24:27]\n"
"ds_read_b128 a[132:135], v252 offset:26624\n"
"v_mul_f32 v209, v209, v33\n"
"v_mul_f32 v210, v210, v34\n"
"v_mfma_f32_16x16x32_bf16 v[28:31], a[96:99], a[72:75], v[28:31]\n"
"v_mul_f32 v211, v211, v35\n"
"v_exp_f32 v208, v208\n"
"v_mfma_f32_16x16x32_bf16 v[28:31], a[100:103], a[76:79], v[28:31]\n"
"v_exp_f32 v209, v209\n"
"s_add_u32 s70, s15, s85\n"
"buffer_load_dwordx4 a[32:35], v250, s[8:11], s70 offen offset:0 sc1\n"
"v_mfma_f32_16x16x32_bf16 v[48:51], a[104:107], a[48:51], v[48:51]\n"
"v_exp_f32 v210, v210\n"
"v_mfma_f32_16x16x32_bf16 v[48:51], a[108:111], a[52:55], v[48:51]\n"
"v_exp_f32 v211, v211\n"
"v_add_f32 v208, 1.0, v208\n"
"v_mfma_f32_16x16x32_bf16 v[52:55], a[104:107], a[56:59], v[52:55]\n"
"v_add_f32 v209, 1.0, v209\n"
"v_add_f32 v210, 1.0, v210\n"
"v_add_f32 v211, 1.0, v211\n"
"v_mfma_f32_16x16x32_bf16 v[52:55], a[108:111], a[60:63], v[52:55]\n"
"v_rcp_f32 v208, v208\n"
"buffer_load_dwordx4 a[36:39], v250, s[8:11], s70 offen offset:1024 sc1\n"
"v_mfma_f32_16x16x32_bf16 v[56:59], a[104:107], a[64:67], v[56:59]\n"
"ds_read_b128 a[136:139], v252 offset:17408\n"
"v_rcp_f32 v209, v209\n"
"v_mfma_f32_16x16x32_bf16 v[56:59], a[108:111], a[68:71], v[56:59]\n"
"ds_read_b128 a[140:143], v252 offset:27648\n"
"v_rcp_f32 v210, v210\n"
"v_mfma_f32_16x16x32_bf16 v[60:63], a[104:107], a[72:75], v[60:63]\n"
"v_rcp_f32 v211, v211\n"
"v_mul_f32 v32, v32, v208\n"
"s_add_u32 s71, s15, s86\n"
"buffer_load_dwordx4 a[40:43], v250, s[8:11], s71 offen offset:0 sc1\n"
"v_mfma_f32_16x16x32_bf16 v[60:63], a[108:111], a[76:79], v[60:63]\n"
"v_mul_f32 v33, v33, v209\n"
"v_mul_f32 v34, v34, v210\n"
"v_mul_f32 v35, v35, v211\n"
"v_mfma_f32_16x16x32_bf16 v[80:83], a[112:115], a[48:51], v[80:83]\n"
"v_fma_f32 v40, -v216, v140, v40\n"
"v_fma_f32 v41, -v216, v141, v41\n"
"v_fma_f32 v42, -v216, v142, v42\n"
"v_mfma_f32_16x16x32_bf16 v[80:83], a[116:119], a[52:55], v[80:83]\n"
"v_fma_f32 v43, -v216, v143, v43\n"
"v_mul_f32 v40, v40, v218\n"
"v_mul_f32 v41, v41, v218\n"
"buffer_load_dwordx4 a[44:47], v250, s[8:11], s71 offen offset:1024 sc1\n"
"v_mfma_f32_16x16x32_bf16 v[84:87], a[112:115], a[56:59], v[84:87]\n"
"v_mul_f32 v42, v42, v218\n"
"v_mul_f32 v43, v43, v218\n"
"v_mul_f32 v212, v40, v40\n"
"v_mfma_f32_16x16x32_bf16 v[84:87], a[116:119], a[60:63], v[84:87]\n"
"v_mul_f32 v213, v41, v41\n"
"v_mul_f32 v214, v42, v42\n"
"v_mul_f32 v215, v43, v43\n"
"v_mfma_f32_16x16x32_bf16 v[88:91], a[112:115], a[64:67], v[88:91]\n"
"ds_read_b128 a[96:99], v252 offset:18432\n"
"v_fmaak_f32 v212, v247, v212, 0xc0135761\n"
"v_fmaak_f32 v213, v247, v213, 0xc0135761\n"
"buffer_load_dwordx4 v244, s[8:11], s72 offen offset:1024 sc1 lds\n"
"v_mfma_f32_16x16x32_bf16 v[88:91], a[116:119], a[68:71], v[88:91]\n"
"ds_read_b128 a[100:103], v252 offset:28672\n"
"v_fmaak_f32 v214, v247, v214, 0xc0135761\n"
"v_fmaak_f32 v215, v247, v215, 0xc0135761\n"
"v_mfma_f32_16x16x32_bf16 v[92:95], a[112:115], a[72:75], v[92:95]\n"
"v_mul_f32 v212, v212, v40\n"
"v_mul_f32 v213, v213, v41\n"
"v_mul_f32 v214, v214, v42\n"
"v_mfma_f32_16x16x32_bf16 v[92:95], a[116:119], a[76:79], v[92:95]\n"
"v_mul_f32 v215, v215, v43\n"
"v_exp_f32 v212, v212\n"
"s_waitcnt lgkmcnt(2)\n"
"v_mfma_f32_16x16x32_bf16 v[112:115], a[120:123], a[48:51], v[112:115]\n"
"v_exp_f32 v213, v213\n"
"buffer_load_dwordx4 v244, s[8:11], s72 offen offset:2048 sc1 lds\n"
"v_mfma_f32_16x16x32_bf16 v[112:115], a[124:127], a[52:55], v[112:115]\n"
"v_exp_f32 v214, v214\n"
"v_mfma_f32_16x16x32_bf16 v[116:119], a[120:123], a[56:59], v[116:119]\n"
"v_exp_f32 v215, v215\n"
"v_add_f32 v212, 1.0, v212\n"
"v_mfma_f32_16x16x32_bf16 v[116:119], a[124:127], a[60:63], v[116:119]\n"
"v_add_f32 v213, 1.0, v213\n"
"v_add_f32 v214, 1.0, v214\n"
"v_add_f32 v215, 1.0, v215\n"
"buffer_load_dwordx4 v244, s[8:11], s72 offen offset:3072 sc1 lds\n"
"v_mfma_f32_16x16x32_bf16 v[120:123], a[120:123], a[64:67], v[120:123]\n"
"ds_read_b128 a[104:107], v252 offset:19456\n"
"v_rcp_f32 v212, v212\n"
"v_mfma_f32_16x16x32_bf16 v[120:123], a[124:127], a[68:71], v[120:123]\n"
"ds_read_b128 a[108:111], v252 offset:29696\n"
"v_rcp_f32 v213, v213\n"
"v_mfma_f32_16x16x32_bf16 v[124:127], a[120:123], a[72:75], v[124:127]\n"
"v_rcp_f32 v214, v214\n"
"s_add_u32 m0, s6, 86016\n"
"v_mfma_f32_16x16x32_bf16 v[124:127], a[124:127], a[76:79], v[124:127]\n"
"v_rcp_f32 v215, v215\n"
"v_mul_f32 v40, v40, v212\n"
"buffer_load_dwordx4 v250, s[8:11], s72 offen offset:0 sc1 lds\n"
"v_mfma_f32_16x16x32_bf16 v[160:163], a[128:131], a[48:51], v[160:163]\n"
"v_mul_f32 v41, v41, v213\n"
"v_mul_f32 v42, v42, v214\n"
"v_mul_f32 v43, v43, v215\n"
"v_mfma_f32_16x16x32_bf16 v[160:163], a[132:135], a[52:55], v[160:163]\n"
"v_cvt_pk_bf16_f32 v32, v32, v33\n"
"v_cvt_pk_bf16_f32 v33, v34, v35\n"
"v_cvt_pk_bf16_f32 v34, v40, v41\n"
"v_mfma_f32_16x16x32_bf16 v[164:167], a[128:131], a[56:59], v[164:167]\n"
"v_cvt_pk_bf16_f32 v35, v42, v43\n"
"v_fma_f32 v36, -v217, v136, v36\n"
"v_fma_f32 v37, -v217, v137, v37\n"
"s_add_u32 s72, s61, 163840\n"
"s_add_u32 m0, s6, 102400\n"
"v_mfma_f32_16x16x32_bf16 v[164:167], a[132:135], a[60:63], v[164:167]\n"
"v_fma_f32 v38, -v217, v138, v38\n"
"v_fma_f32 v39, -v217, v139, v39\n"
"v_mul_f32 v36, v36, v219\n"
"buffer_load_dwordx4 v244, s[8:11], s72 offen offset:0 sc1 lds\n"
"v_mfma_f32_16x16x32_bf16 v[168:171], a[128:131], a[64:67], v[168:171]\n"
"ds_read_b128 a[112:115], v252 offset:20480\n"
"v_mul_f32 v37, v37, v219\n"
"v_mul_f32 v38, v38, v219\n"
"v_mfma_f32_16x16x32_bf16 v[168:171], a[132:135], a[68:71], v[168:171]\n"
"ds_read_b128 a[116:119], v252 offset:30720\n"
"v_mul_f32 v39, v39, v219\n"
"v_mul_f32 v208, v36, v36\n"
"v_mfma_f32_16x16x32_bf16 v[172:175], a[128:131], a[72:75], v[172:175]\n"
"v_mul_f32 v209, v37, v37\n"
"v_mul_f32 v210, v38, v38\n"
"v_mul_f32 v211, v39, v39\n"
"buffer_store_dwordx4 v[32:35], v244, s[8:11], s64 offen offset:1024 sc1\n"
"v_mfma_f32_16x16x32_bf16 v[172:175], a[132:135], a[76:79], v[172:175]\n"
"v_fmaak_f32 v208, v247, v208, 0xc0135761\n"
"v_fmaak_f32 v209, v247, v209, 0xc0135761\n"
"v_fmaak_f32 v210, v247, v210, 0xc0135761\n"
"v_mfma_f32_16x16x32_bf16 v[176:179], a[136:139], a[48:51], v[176:179]\n"
"v_fmaak_f32 v211, v247, v211, 0xc0135761\n"
"v_mul_f32 v208, v208, v36\n"
"v_mul_f32 v209, v209, v37\n"
"v_mfma_f32_16x16x32_bf16 v[176:179], a[140:143], a[52:55], v[176:179]\n"
"v_mul_f32 v210, v210, v38\n"
"v_mul_f32 v211, v211, v39\n"
"buffer_load_dwordx4 v244, s[8:11], s72 offen offset:1024 sc1 lds\n"
"v_mfma_f32_16x16x32_bf16 v[180:183], a[136:139], a[56:59], v[180:183]\n"
"v_exp_f32 v208, v208\n"
"v_mfma_f32_16x16x32_bf16 v[180:183], a[140:143], a[60:63], v[180:183]\n"
"v_exp_f32 v209, v209\n"
"v_mfma_f32_16x16x32_bf16 v[184:187], a[136:139], a[64:67], v[184:187]\n"
"ds_read_b128 a[120:123], v252 offset:21504\n"
"v_exp_f32 v210, v210\n"
"buffer_load_dwordx4 v244, s[8:11], s72 offen offset:2048 sc1 lds\n"
"v_mfma_f32_16x16x32_bf16 v[184:187], a[140:143], a[68:71], v[184:187]\n"
"ds_read_b128 a[124:127], v252 offset:31744\n"
"v_exp_f32 v211, v211\n"
"v_mfma_f32_16x16x32_bf16 v[188:191], a[136:139], a[72:75], v[188:191]\n"
"v_add_f32 v208, 1.0, v208\n"
"v_add_f32 v209, 1.0, v209\n"
"v_add_f32 v210, 1.0, v210\n"
"v_mfma_f32_16x16x32_bf16 v[188:191], a[140:143], a[76:79], v[188:191]\n"
"v_add_f32 v211, 1.0, v211\n"
"v_rcp_f32 v208, v208\n"
"s_waitcnt lgkmcnt(2)\n"
"v_mfma_f32_16x16x32_bf16 v[192:195], a[96:99], a[48:51], v[192:195]\n"
"v_rcp_f32 v209, v209\n"
"buffer_load_dwordx4 v244, s[8:11], s72 offen offset:3072 sc1 lds\n"
"v_mfma_f32_16x16x32_bf16 v[192:195], a[100:103], a[52:55], v[192:195]\n"
"v_rcp_f32 v210, v210\n"
"v_mfma_f32_16x16x32_bf16 v[196:199], a[96:99], a[56:59], v[196:199]\n"
"v_rcp_f32 v211, v211\n"
"v_mul_f32 v36, v36, v208\n"
"v_mfma_f32_16x16x32_bf16 v[196:199], a[100:103], a[60:63], v[196:199]\n"
"v_mul_f32 v37, v37, v209\n"
"v_mul_f32 v38, v38, v210\n"
"v_mul_f32 v39, v39, v211\n"
"s_add_u32 m0, s6, 106496\n"
"v_mfma_f32_16x16x32_bf16 v[200:203], a[96:99], a[64:67], v[200:203]\n"
"v_fma_f32 v44, -v217, v140, v44\n"
"v_fma_f32 v45, -v217, v141, v45\n"
"v_fma_f32 v46, -v217, v142, v46\n"
"buffer_load_dwordx4 v250, s[8:11], s72 offen offset:0 sc1 lds\n"
"v_mfma_f32_16x16x32_bf16 v[200:203], a[100:103], a[68:71], v[200:203]\n"
"v_fma_f32 v47, -v217, v143, v47\n"
"v_mul_f32 v44, v44, v219\n"
"v_mul_f32 v45, v45, v219\n"
"v_mfma_f32_16x16x32_bf16 v[204:207], a[96:99], a[72:75], v[204:207]\n"
"v_mul_f32 v46, v46, v219\n"
"v_mul_f32 v47, v47, v219\n"
"v_mul_f32 v212, v44, v44\n"
"v_mfma_f32_16x16x32_bf16 v[204:207], a[100:103], a[76:79], v[204:207]\n"
"v_mul_f32 v213, v45, v45\n"
"v_mul_f32 v214, v46, v46\n"
"v_mul_f32 v215, v47, v47\n"
"v_mfma_f32_16x16x32_bf16 a[208:211], a[104:107], a[48:51], a[208:211]\n"
"v_fmaak_f32 v212, v247, v212, 0xc0135761\n"
"v_fmaak_f32 v213, v247, v213, 0xc0135761\n"
"v_fmaak_f32 v214, v247, v214, 0xc0135761\n"
"v_mfma_f32_16x16x32_bf16 a[208:211], a[108:111], a[52:55], a[208:211]\n"
"v_fmaak_f32 v215, v247, v215, 0xc0135761\n"
"v_mul_f32 v212, v212, v44\n"
"v_mul_f32 v213, v213, v45\n"
"v_mfma_f32_16x16x32_bf16 a[212:215], a[104:107], a[56:59], a[212:215]\n"
"v_mul_f32 v214, v214, v46\n"
"v_mul_f32 v215, v215, v47\n"
"v_mfma_f32_16x16x32_bf16 a[212:215], a[108:111], a[60:63], a[212:215]\n"
"v_exp_f32 v212, v212\n"
"v_mfma_f32_16x16x32_bf16 a[216:219], a[104:107], a[64:67], a[216:219]\n"
"v_exp_f32 v213, v213\n"
"v_mfma_f32_16x16x32_bf16 a[216:219], a[108:111], a[68:71], a[216:219]\n"
"v_exp_f32 v214, v214\n"
"v_mfma_f32_16x16x32_bf16 a[220:223], a[104:107], a[72:75], a[220:223]\n"
"v_exp_f32 v215, v215\n"
"v_add_f32 v212, 1.0, v212\n"
"v_mfma_f32_16x16x32_bf16 a[220:223], a[108:111], a[76:79], a[220:223]\n"
"v_add_f32 v213, 1.0, v213\n"
"v_add_f32 v214, 1.0, v214\n"
"v_add_f32 v215, 1.0, v215\n"
"v_mfma_f32_16x16x32_bf16 a[224:227], a[112:115], a[48:51], a[224:227]\n"
"v_rcp_f32 v212, v212\n"
"v_mfma_f32_16x16x32_bf16 a[224:227], a[116:119], a[52:55], a[224:227]\n"
"v_rcp_f32 v213, v213\n"
"v_mfma_f32_16x16x32_bf16 a[228:231], a[112:115], a[56:59], a[228:231]\n"
"v_rcp_f32 v214, v214\n"
"v_mfma_f32_16x16x32_bf16 a[228:231], a[116:119], a[60:63], a[228:231]\n"
"s_barrier\n"
"v_mfma_f32_16x16x32_bf16 a[232:235], a[112:115], a[64:67], a[232:235]\n"
"ds_read_b128 a[128:131], v244 offset:0\n"
"v_rcp_f32 v215, v215\n"
"s_add_u32 s72, s61, 327680\n"
"s_add_u32 m0, s6, 122880\n"
"v_mfma_f32_16x16x32_bf16 a[232:235], a[116:119], a[68:71], a[232:235]\n"
"ds_read_b128 a[132:135], v244 offset:10240\n"
"v_mul_f32 v44, v44, v212\n"
"v_mul_f32 v45, v45, v213\n"
"buffer_load_dwordx4 v244, s[8:11], s72 offen offset:0 sc1 lds\n"
"v_mfma_f32_16x16x32_bf16 a[236:239], a[112:115], a[72:75], a[236:239]\n"
"ds_read_b128 a[136:139], v244 offset:1024\n"
"v_mul_f32 v46, v46, v214\n"
"v_mul_f32 v47, v47, v215\n"
"v_mfma_f32_16x16x32_bf16 a[236:239], a[116:119], a[76:79], a[236:239]\n"
"ds_read_b128 a[140:143], v244 offset:11264\n"
"v_cvt_pk_bf16_f32 v36, v36, v37\n"
"v_cvt_pk_bf16_f32 v37, v38, v39\n"
"s_waitcnt lgkmcnt(4)\n"
"v_mfma_f32_16x16x32_bf16 a[240:243], a[120:123], a[48:51], a[240:243]\n"
"ds_read_b128 a[96:99], v244 offset:2048\n"
"v_cvt_pk_bf16_f32 v38, v44, v45\n"
"v_cvt_pk_bf16_f32 v39, v46, v47\n"
"buffer_load_dwordx4 v244, s[8:11], s72 offen offset:1024 sc1 lds\n"
"v_mfma_f32_16x16x32_bf16 a[240:243], a[124:127], a[52:55], a[240:243]\n"
"ds_read_b128 a[100:103], v244 offset:12288\n"
"v_fma_f32 v64, -v216, v144, v64\n"
"v_fma_f32 v65, -v216, v145, v65\n"
"v_mfma_f32_16x16x32_bf16 a[244:247], a[120:123], a[56:59], a[244:247]\n"
"v_fma_f32 v66, -v216, v146, v66\n"
"v_fma_f32 v67, -v216, v147, v67\n"
"v_mul_f32 v64, v64, v218\n"
"v_mfma_f32_16x16x32_bf16 a[244:247], a[124:127], a[60:63], a[244:247]\n"
"v_mul_f32 v65, v65, v218\n"
"v_mul_f32 v66, v66, v218\n"
"v_mul_f32 v67, v67, v218\n"
"buffer_store_dwordx4 v[36:39], v244, s[8:11], s63 offen offset:1024 sc1\n"
"v_mfma_f32_16x16x32_bf16 a[248:251], a[120:123], a[64:67], a[248:251]\n"
"ds_read_b128 a[104:107], v244 offset:3072\n"
"v_mul_f32 v208, v64, v64\n"
"v_mul_f32 v209, v65, v65\n"
"v_mfma_f32_16x16x32_bf16 a[248:251], a[124:127], a[68:71], a[248:251]\n"
"ds_read_b128 a[108:111], v244 offset:13312\n"
"v_mul_f32 v210, v66, v66\n"
"v_mul_f32 v211, v67, v67\n"
"v_mfma_f32_16x16x32_bf16 a[252:255], a[120:123], a[72:75], a[252:255]\n"
"v_fmaak_f32 v208, v247, v208, 0xc0135761\n"
"v_fmaak_f32 v209, v247, v209, 0xc0135761\n"
"v_fmaak_f32 v210, v247, v210, 0xc0135761\n"
"buffer_load_dwordx4 v244, s[8:11], s72 offen offset:2048 sc1 lds\n"
"v_mfma_f32_16x16x32_bf16 a[252:255], a[124:127], a[76:79], a[252:255]\n"
"v_fmaak_f32 v211, v247, v211, 0xc0135761\n"
"v_mul_f32 v208, v208, v64\n"
"v_mul_f32 v209, v209, v65\n"
"s_waitcnt vmcnt(56)\n"
"s_waitcnt lgkmcnt(2)\n"
"v_mfma_f32_16x16x32_bf16 v[16:19], a[128:131], a[80:83], v[16:19]\n"
"v_mul_f32 v210, v210, v66\n"
"v_mul_f32 v211, v211, v67\n"
"v_mfma_f32_16x16x32_bf16 v[16:19], a[132:135], a[84:87], v[16:19]\n"
"v_exp_f32 v208, v208\n"
"buffer_load_dwordx4 v244, s[8:11], s72 offen offset:3072 sc1 lds\n"
"v_mfma_f32_16x16x32_bf16 v[20:23], a[128:131], a[88:91], v[20:23]\n"
"v_exp_f32 v209, v209\n"
"v_mfma_f32_16x16x32_bf16 v[20:23], a[132:135], a[92:95], v[20:23]\n"
"v_exp_f32 v210, v210\n"
"v_mfma_f32_16x16x32_bf16 v[24:27], a[128:131], a[144:147], v[24:27]\n"
"ds_read_b128 a[112:115], v244 offset:4096\n"
"v_exp_f32 v211, v211\n"
"s_add_u32 m0, s6, 126976\n"
"v_mfma_f32_16x16x32_bf16 v[24:27], a[132:135], a[148:151], v[24:27]\n"
"ds_read_b128 a[116:119], v244 offset:14336\n"
"v_add_f32 v208, 1.0, v208\n"
"v_add_f32 v209, 1.0, v209\n"
"v_mfma_f32_16x16x32_bf16 v[28:31], a[128:131], a[152:155], v[28:31]\n"
"v_add_f32 v210, 1.0, v210\n"
"v_add_f32 v211, 1.0, v211\n"
"buffer_load_dwordx4 v250, s[8:11], s72 offen offset:0 sc1 lds\n"
"v_mfma_f32_16x16x32_bf16 v[28:31], a[132:135], a[156:159], v[28:31]\n"
"v_rcp_f32 v208, v208\n"
"v_mfma_f32_16x16x32_bf16 v[48:51], a[136:139], a[80:83], v[48:51]\n"
"v_rcp_f32 v209, v209\n"
"v_mfma_f32_16x16x32_bf16 v[48:51], a[140:143], a[84:87], v[48:51]\n"
"v_rcp_f32 v210, v210\n"
"v_mfma_f32_16x16x32_bf16 v[52:55], a[136:139], a[88:91], v[52:55]\n"
"v_rcp_f32 v211, v211\n"
"v_mul_f32 v64, v64, v208\n"
"v_mfma_f32_16x16x32_bf16 v[52:55], a[140:143], a[92:95], v[52:55]\n"
"v_mul_f32 v65, v65, v209\n"
"v_mul_f32 v66, v66, v210\n"
"v_mul_f32 v67, v67, v211\n"
"v_mfma_f32_16x16x32_bf16 v[56:59], a[136:139], a[144:147], v[56:59]\n"
"ds_read_b128 a[120:123], v244 offset:5120\n"
"v_fma_f32 v72, -v216, v148, v72\n"
"v_fma_f32 v73, -v216, v149, v73\n"
"v_mfma_f32_16x16x32_bf16 v[56:59], a[140:143], a[148:151], v[56:59]\n"
"ds_read_b128 a[124:127], v244 offset:15360\n"
"v_fma_f32 v74, -v216, v150, v74\n"
"v_fma_f32 v75, -v216, v151, v75\n"
"v_mfma_f32_16x16x32_bf16 v[60:63], a[136:139], a[152:155], v[60:63]\n"
"v_mul_f32 v72, v72, v218\n"
"v_mul_f32 v73, v73, v218\n"
"v_mul_f32 v74, v74, v218\n"
"v_mfma_f32_16x16x32_bf16 v[60:63], a[140:143], a[156:159], v[60:63]\n"
"v_mul_f32 v75, v75, v218\n"
"v_mul_f32 v212, v72, v72\n"
"v_mul_f32 v213, v73, v73\n"
"v_mfma_f32_16x16x32_bf16 v[80:83], a[96:99], a[80:83], v[80:83]\n"
"v_mul_f32 v214, v74, v74\n"
"v_mul_f32 v215, v75, v75\n"
"v_fmaak_f32 v212, v247, v212, 0xc0135761\n"
"v_mfma_f32_16x16x32_bf16 v[80:83], a[100:103], a[84:87], v[80:83]\n"
"v_fmaak_f32 v213, v247, v213, 0xc0135761\n"
"v_fmaak_f32 v214, v247, v214, 0xc0135761\n"
"v_fmaak_f32 v215, v247, v215, 0xc0135761\n"
"v_mfma_f32_16x16x32_bf16 v[84:87], a[96:99], a[88:91], v[84:87]\n"
"v_mul_f32 v212, v212, v72\n"
"v_mul_f32 v213, v213, v73\n"
"v_mul_f32 v214, v214, v74\n"
"v_mfma_f32_16x16x32_bf16 v[84:87], a[100:103], a[92:95], v[84:87]\n"
"v_mul_f32 v215, v215, v75\n"
"v_exp_f32 v212, v212\n"
"v_mfma_f32_16x16x32_bf16 v[88:91], a[96:99], a[144:147], v[88:91]\n"
"ds_read_b128 a[128:131], v244 offset:6144\n"
"v_exp_f32 v213, v213\n"
"v_mfma_f32_16x16x32_bf16 v[88:91], a[100:103], a[148:151], v[88:91]\n"
"ds_read_b128 a[132:135], v244 offset:16384\n"
"v_exp_f32 v214, v214\n"
"v_mfma_f32_16x16x32_bf16 v[92:95], a[96:99], a[152:155], v[92:95]\n"
"v_exp_f32 v215, v215\n"
"v_add_f32 v212, 1.0, v212\n"
"v_mfma_f32_16x16x32_bf16 v[92:95], a[100:103], a[156:159], v[92:95]\n"
"v_add_f32 v213, 1.0, v213\n"
"v_add_f32 v214, 1.0, v214\n"
"v_add_f32 v215, 1.0, v215\n"
"s_waitcnt lgkmcnt(2)\n"
"v_mfma_f32_16x16x32_bf16 v[112:115], a[104:107], a[80:83], v[112:115]\n"
"v_rcp_f32 v212, v212\n"
"v_mfma_f32_16x16x32_bf16 v[112:115], a[108:111], a[84:87], v[112:115]\n"
"v_rcp_f32 v213, v213\n"
"v_mfma_f32_16x16x32_bf16 v[116:119], a[104:107], a[88:91], v[116:119]\n"
"v_rcp_f32 v214, v214\n"
"v_mfma_f32_16x16x32_bf16 v[116:119], a[108:111], a[92:95], v[116:119]\n"
"v_rcp_f32 v215, v215\n"
"v_mul_f32 v72, v72, v212\n"
"v_mfma_f32_16x16x32_bf16 v[120:123], a[104:107], a[144:147], v[120:123]\n"
"ds_read_b128 a[136:139], v244 offset:7168\n"
"v_mul_f32 v73, v73, v213\n"
"v_mul_f32 v74, v74, v214\n"
"v_mfma_f32_16x16x32_bf16 v[120:123], a[108:111], a[148:151], v[120:123]\n"
"ds_read_b128 a[140:143], v244 offset:17408\n"
"v_mul_f32 v75, v75, v215\n"
"v_cvt_pk_bf16_f32 v64, v64, v65\n"
"v_mfma_f32_16x16x32_bf16 v[124:127], a[104:107], a[152:155], v[124:127]\n"
"v_cvt_pk_bf16_f32 v65, v66, v67\n"
"v_cvt_pk_bf16_f32 v66, v72, v73\n"
"v_cvt_pk_bf16_f32 v67, v74, v75\n"
"v_mfma_f32_16x16x32_bf16 v[124:127], a[108:111], a[156:159], v[124:127]\n"
"v_fma_f32 v68, -v217, v144, v68\n"
"v_fma_f32 v69, -v217, v145, v69\n"
"v_fma_f32 v70, -v217, v146, v70\n"
"buffer_store_dwordx4 v[64:67], v244, s[8:11], s64 offen offset:2048 sc1\n"
"v_mfma_f32_16x16x32_bf16 v[160:163], a[112:115], a[80:83], v[160:163]\n"
"v_fma_f32 v71, -v217, v147, v71\n"
"v_mul_f32 v68, v68, v219\n"
"v_mul_f32 v69, v69, v219\n"
"v_mfma_f32_16x16x32_bf16 v[160:163], a[116:119], a[84:87], v[160:163]\n"
"v_mul_f32 v70, v70, v219\n"
"v_mul_f32 v71, v71, v219\n"
"v_mul_f32 v208, v68, v68\n"
"v_mfma_f32_16x16x32_bf16 v[164:167], a[112:115], a[88:91], v[164:167]\n"
"v_mul_f32 v209, v69, v69\n"
"v_mul_f32 v210, v70, v70\n"
"v_mul_f32 v211, v71, v71\n"
"v_mfma_f32_16x16x32_bf16 v[164:167], a[116:119], a[92:95], v[164:167]\n"
"v_fmaak_f32 v208, v247, v208, 0xc0135761\n"
"v_fmaak_f32 v209, v247, v209, 0xc0135761\n"
"v_fmaak_f32 v210, v247, v210, 0xc0135761\n"
"v_mfma_f32_16x16x32_bf16 v[168:171], a[112:115], a[144:147], v[168:171]\n"
"ds_read_b128 a[96:99], v244 offset:8192\n"
"v_fmaak_f32 v211, v247, v211, 0xc0135761\n"
"v_mul_f32 v208, v208, v68\n"
"v_mfma_f32_16x16x32_bf16 v[168:171], a[116:119], a[148:151], v[168:171]\n"
"ds_read_b128 a[100:103], v244 offset:18432\n"
"v_mul_f32 v209, v209, v69\n"
"v_mul_f32 v210, v210, v70\n"
"v_mfma_f32_16x16x32_bf16 v[172:175], a[112:115], a[152:155], v[172:175]\n"
"v_mul_f32 v211, v211, v71\n"
"v_exp_f32 v208, v208\n"
"v_mfma_f32_16x16x32_bf16 v[172:175], a[116:119], a[156:159], v[172:175]\n"
"v_exp_f32 v209, v209\n"
"v_mfma_f32_16x16x32_bf16 v[176:179], a[120:123], a[80:83], v[176:179]\n"
"v_exp_f32 v210, v210\n"
"v_mfma_f32_16x16x32_bf16 v[176:179], a[124:127], a[84:87], v[176:179]\n"
"v_exp_f32 v211, v211\n"
"v_add_f32 v208, 1.0, v208\n"
"v_mfma_f32_16x16x32_bf16 v[180:183], a[120:123], a[88:91], v[180:183]\n"
"v_add_f32 v209, 1.0, v209\n"
"v_add_f32 v210, 1.0, v210\n"
"v_add_f32 v211, 1.0, v211\n"
"v_mfma_f32_16x16x32_bf16 v[180:183], a[124:127], a[92:95], v[180:183]\n"
"v_rcp_f32 v208, v208\n"
"v_mfma_f32_16x16x32_bf16 v[184:187], a[120:123], a[144:147], v[184:187]\n"
"ds_read_b128 a[104:107], v244 offset:9216\n"
"v_rcp_f32 v209, v209\n"
"v_mfma_f32_16x16x32_bf16 v[184:187], a[124:127], a[148:151], v[184:187]\n"
"ds_read_b128 a[108:111], v244 offset:19456\n"
"v_rcp_f32 v210, v210\n"
"v_mfma_f32_16x16x32_bf16 v[188:191], a[120:123], a[152:155], v[188:191]\n"
"v_rcp_f32 v211, v211\n"
"v_mul_f32 v68, v68, v208\n"
"v_mfma_f32_16x16x32_bf16 v[188:191], a[124:127], a[156:159], v[188:191]\n"
"v_mul_f32 v69, v69, v209\n"
"v_mul_f32 v70, v70, v210\n"
"v_mul_f32 v71, v71, v211\n"
"s_waitcnt lgkmcnt(2)\n"
"v_mfma_f32_16x16x32_bf16 v[192:195], a[128:131], a[80:83], v[192:195]\n"
"v_fma_f32 v76, -v217, v148, v76\n"
"v_fma_f32 v77, -v217, v149, v77\n"
"v_fma_f32 v78, -v217, v150, v78\n"
"v_mfma_f32_16x16x32_bf16 v[192:195], a[132:135], a[84:87], v[192:195]\n"
"v_fma_f32 v79, -v217, v151, v79\n"
"v_mul_f32 v76, v76, v219\n"
"v_mul_f32 v77, v77, v219\n"
"v_mfma_f32_16x16x32_bf16 v[196:199], a[128:131], a[88:91], v[196:199]\n"
"v_mul_f32 v78, v78, v219\n"
"v_mul_f32 v79, v79, v219\n"
"v_mul_f32 v212, v76, v76\n"
"v_mfma_f32_16x16x32_bf16 v[196:199], a[132:135], a[92:95], v[196:199]\n"
"v_mul_f32 v213, v77, v77\n"
"v_mul_f32 v214, v78, v78\n"
"v_mul_f32 v215, v79, v79\n"
"v_mfma_f32_16x16x32_bf16 v[200:203], a[128:131], a[144:147], v[200:203]\n"
"v_fmaak_f32 v212, v247, v212, 0xc0135761\n"
"v_fmaak_f32 v213, v247, v213, 0xc0135761\n"
"v_fmaak_f32 v214, v247, v214, 0xc0135761\n"
"v_mfma_f32_16x16x32_bf16 v[200:203], a[132:135], a[148:151], v[200:203]\n"
"v_fmaak_f32 v215, v247, v215, 0xc0135761\n"
"v_mul_f32 v212, v212, v76\n"
"v_mul_f32 v213, v213, v77\n"
"v_mfma_f32_16x16x32_bf16 v[204:207], a[128:131], a[152:155], v[204:207]\n"
"v_mul_f32 v214, v214, v78\n"
"v_mul_f32 v215, v215, v79\n"
"v_mfma_f32_16x16x32_bf16 v[204:207], a[132:135], a[156:159], v[204:207]\n"
"v_exp_f32 v212, v212\n"
"v_mfma_f32_16x16x32_bf16 a[208:211], a[136:139], a[80:83], a[208:211]\n"
"v_exp_f32 v213, v213\n"
"v_mfma_f32_16x16x32_bf16 a[208:211], a[140:143], a[84:87], a[208:211]\n"
"v_exp_f32 v214, v214\n"
"v_mfma_f32_16x16x32_bf16 a[212:215], a[136:139], a[88:91], a[212:215]\n"
"v_exp_f32 v215, v215\n"
"v_add_f32 v212, 1.0, v212\n"
"v_mfma_f32_16x16x32_bf16 a[212:215], a[140:143], a[92:95], a[212:215]\n"
"v_add_f32 v213, 1.0, v213\n"
"v_add_f32 v214, 1.0, v214\n"
"v_add_f32 v215, 1.0, v215\n"
"v_mfma_f32_16x16x32_bf16 a[216:219], a[136:139], a[144:147], a[216:219]\n"
"v_rcp_f32 v212, v212\n"
"v_mfma_f32_16x16x32_bf16 a[216:219], a[140:143], a[148:151], a[216:219]\n"
"v_rcp_f32 v213, v213\n"
"v_mfma_f32_16x16x32_bf16 a[220:223], a[136:139], a[152:155], a[220:223]\n"
"v_rcp_f32 v214, v214\n"
"v_mfma_f32_16x16x32_bf16 a[220:223], a[140:143], a[156:159], a[220:223]\n"
"v_rcp_f32 v215, v215\n"
"v_mul_f32 v76, v76, v212\n"
"v_mfma_f32_16x16x32_bf16 a[224:227], a[96:99], a[80:83], a[224:227]\n"
"v_mul_f32 v77, v77, v213\n"
"v_mul_f32 v78, v78, v214\n"
"v_mul_f32 v79, v79, v215\n"
"v_mfma_f32_16x16x32_bf16 a[224:227], a[100:103], a[84:87], a[224:227]\n"
"v_cvt_pk_bf16_f32 v68, v68, v69\n"
"v_cvt_pk_bf16_f32 v69, v70, v71\n"
"v_cvt_pk_bf16_f32 v70, v76, v77\n"
"v_mfma_f32_16x16x32_bf16 a[228:231], a[96:99], a[88:91], a[228:231]\n"
"v_cvt_pk_bf16_f32 v71, v78, v79\n"
"v_mfma_f32_16x16x32_bf16 a[228:231], a[100:103], a[92:95], a[228:231]\n"
"s_waitcnt vmcnt(40)\n"
"s_barrier\n"
"v_lshrrev_b32 v253, 1, v244\n"
"buffer_store_dwordx4 v[68:71], v244, s[8:11], s63 offen offset:2048 sc1\n"
"v_mfma_f32_16x16x32_bf16 a[232:235], a[96:99], a[144:147], a[232:235]\n"
"ds_read_b128 a[112:115], v244 offset:20480\n"
"v_add_u32 v254, 2560, v253\n"
"v_and_b32 v209, 15, v248\n"
"v_mfma_f32_16x16x32_bf16 a[232:235], a[100:103], a[148:151], a[232:235]\n"
"ds_read_b128 a[116:119], v244 offset:30720\n"
"v_lshrrev_b32 v210, 4, v248\n"
"v_lshlrev_b32 v212, 4, v209\n"
"v_mfma_f32_16x16x32_bf16 a[236:239], a[96:99], a[152:155], a[236:239]\n"
"ds_read_b128 a[120:123], v244 offset:40960\n"
"v_lshl_add_u32 v212, v210, 2, v212\n"
"v_lshrrev_b32 v208, 2, v248\n"
"s_add_u32 s72, s61, 491520\n"
"s_add_u32 m0, s6, 143360\n"
"v_mfma_f32_16x16x32_bf16 a[236:239], a[100:103], a[156:159], a[236:239]\n"
"ds_read_b128 a[124:127], v244 offset:51200\n"
"v_mul_u32_u24 v208, 1280, v208\n"
"v_and_b32 v211, 3, v248\n"
"buffer_load_dwordx4 v244, s[8:11], s72 offen offset:0 sc1 lds\n"
"s_waitcnt lgkmcnt(4)\n"
"v_mfma_f32_16x16x32_bf16 a[240:243], a[104:107], a[80:83], a[240:243]\n"
"ds_read_b128 a[128:131], v244 offset:61440\n"
"v_lshl_add_u32 v208, v211, 3, v208\n"
"v_lshlrev_b32 v210, 2, v210\n"
"v_mfma_f32_16x16x32_bf16 a[240:243], a[108:111], a[84:87], a[240:243]\n"
"ds_read_b128 a[132:135], v251 offset:6144\n"
"v_sub_u32 v209, v209, v210\n"
"v_and_b32 v210, 1, v209\n"
"v_mfma_f32_16x16x32_bf16 a[244:247], a[104:107], a[88:91], a[244:247]\n"
"v_lshlrev_b32 v210, 4, v210\n"
"v_mov_b32 v211, 0x3f80\n"
"v_lshlrev_b32 v210, v210, v211\n"
"v_mfma_f32_16x16x32_bf16 a[244:247], a[108:111], a[92:95], a[244:247]\n"
"v_lshrrev_b32 v209, 1, v209\n"
"buffer_load_dwordx4 v244, s[8:11], s72 offen offset:1024 sc1 lds\n"
"v_mfma_f32_16x16x32_bf16 a[248:251], a[104:107], a[144:147], a[248:251]\n"
"ds_read_b128 a[136:139], v244 offset:21504\n"
"v_mfma_f32_16x16x32_bf16 a[248:251], a[108:111], a[148:151], a[248:251]\n"
"ds_read_b128 a[140:143], v244 offset:31744\n"
"v_mfma_f32_16x16x32_bf16 a[252:255], a[104:107], a[152:155], a[252:255]\n"
"ds_read_b128 a[96:99], v244 offset:41984\n"
"buffer_load_dwordx4 v244, s[8:11], s72 offen offset:2048 sc1 lds\n"
"v_mfma_f32_16x16x32_bf16 a[252:255], a[108:111], a[156:159], a[252:255]\n"
"ds_read_b128 a[100:103], v244 offset:52224\n"
"s_waitcnt lgkmcnt(4)\n"
"v_mfma_f32_16x16x32_bf16 v[16:19], a[112:115], a[160:163], v[16:19]\n"
"ds_read_b128 a[104:107], v244 offset:62464\n"
"v_mfma_f32_16x16x32_bf16 v[16:19], a[116:119], a[164:167], v[16:19]\n"
"ds_read_b128 a[108:111], v251 offset:7168\n"
"v_mfma_f32_16x16x32_bf16 v[16:19], a[120:123], a[168:171], v[16:19]\n"
"buffer_load_dwordx4 v244, s[8:11], s72 offen offset:3072 sc1 lds\n"
"v_mfma_f32_16x16x32_bf16 v[16:19], a[124:127], a[172:175], v[16:19]\n"
"s_waitcnt vmcnt(43)\n"
"v_mfma_f32_16x16x32_bf16 v[16:19], a[128:131], a[176:179], v[16:19]\n"
"v_mfma_f32_16x16x32_bf16 v[16:19], a[132:135], a[180:183], v[16:19]\n"
"s_add_u32 m0, s6, 147456\n"
"s_waitcnt vmcnt(37)\n"
"v_mfma_f32_16x16x32_bf16 v[20:23], a[112:115], a[184:187], v[20:23]\n"
"buffer_load_dwordx4 v250, s[8:11], s72 offen offset:0 sc1 lds\n"
"v_mfma_f32_16x16x32_bf16 v[20:23], a[116:119], a[188:191], v[20:23]\n"
"v_mfma_f32_16x16x32_bf16 v[20:23], a[120:123], a[192:195], v[20:23]\n"
"v_mfma_f32_16x16x32_bf16 v[20:23], a[124:127], a[196:199], v[20:23]\n"
"v_mfma_f32_16x16x32_bf16 v[20:23], a[128:131], a[200:203], v[20:23]\n"
"v_mfma_f32_16x16x32_bf16 v[20:23], a[132:135], a[204:207], v[20:23]\n"
"s_waitcnt vmcnt(30)\n"
"v_mfma_f32_16x16x32_bf16 v[24:27], a[112:115], a[0:3], v[24:27]\n"
"v_mfma_f32_16x16x32_bf16 v[24:27], a[116:119], a[4:7], v[24:27]\n"
"v_mfma_f32_16x16x32_bf16 v[24:27], a[120:123], a[8:11], v[24:27]\n"
"v_mfma_f32_16x16x32_bf16 v[24:27], a[124:127], a[12:15], v[24:27]\n"
"v_mfma_f32_16x16x32_bf16 v[24:27], a[128:131], a[16:19], v[24:27]\n"
"v_mfma_f32_16x16x32_bf16 v[24:27], a[132:135], a[20:23], v[24:27]\n"
"s_waitcnt vmcnt(23)\n"
"v_mfma_f32_16x16x32_bf16 v[28:31], a[112:115], a[24:27], v[28:31]\n"
"v_mfma_f32_16x16x32_bf16 v[28:31], a[116:119], a[28:31], v[28:31]\n"
"v_mfma_f32_16x16x32_bf16 v[28:31], a[120:123], a[32:35], v[28:31]\n"
"ds_read_b128 a[112:115], v244 offset:25600\n"
"v_mfma_f32_16x16x32_bf16 v[28:31], a[124:127], a[36:39], v[28:31]\n"
"ds_read_b128 a[116:119], v244 offset:35840\n"
"v_mfma_f32_16x16x32_bf16 v[28:31], a[128:131], a[40:43], v[28:31]\n"
"ds_read_b128 a[120:123], v244 offset:46080\n"
"v_mfma_f32_16x16x32_bf16 v[28:31], a[132:135], a[44:47], v[28:31]\n"
"ds_read_b128 a[124:127], v244 offset:56320\n"
"s_waitcnt lgkmcnt(4)\n"
"v_mfma_f32_16x16x32_bf16 v[48:51], a[136:139], a[160:163], v[48:51]\n"
"ds_read_b128 a[128:131], v251 offset:1024\n"
"v_mfma_f32_16x16x32_bf16 v[48:51], a[140:143], a[164:167], v[48:51]\n"
"ds_read_b128 a[132:135], v251 offset:11264\n"
"v_mfma_f32_16x16x32_bf16 v[48:51], a[96:99], a[168:171], v[48:51]\n"
"v_mfma_f32_16x16x32_bf16 v[48:51], a[100:103], a[172:175], v[48:51]\n"
"v_mfma_f32_16x16x32_bf16 v[48:51], a[104:107], a[176:179], v[48:51]\n"
"v_mfma_f32_16x16x32_bf16 v[48:51], a[108:111], a[180:183], v[48:51]\n"
"v_mfma_f32_16x16x32_bf16 v[52:55], a[136:139], a[184:187], v[52:55]\n"
"v_mfma_f32_16x16x32_bf16 v[52:55], a[140:143], a[188:191], v[52:55]\n"
"v_mfma_f32_16x16x32_bf16 v[52:55], a[96:99], a[192:195], v[52:55]\n"
"v_mfma_f32_16x16x32_bf16 v[52:55], a[100:103], a[196:199], v[52:55]\n"
"v_mfma_f32_16x16x32_bf16 v[52:55], a[104:107], a[200:203], v[52:55]\n"
"v_cvt_pk_bf16_f32 v16, v16, v17\n"
"v_cvt_pk_bf16_f32 v17, v18, v19\n"
"v_mfma_f32_16x16x32_bf16 v[52:55], a[108:111], a[204:207], v[52:55]\n"
"v_cvt_pk_bf16_f32 v18, v48, v49\n"
"v_cvt_pk_bf16_f32 v19, v50, v51\n"
"v_mfma_f32_16x16x32_bf16 v[56:59], a[136:139], a[0:3], v[56:59]\n"
"buffer_store_dwordx4 v[16:19], v244, s[8:11], s76 offen offset:0 sc1\n"
"v_mfma_f32_16x16x32_bf16 v[56:59], a[140:143], a[4:7], v[56:59]\n"
"v_mfma_f32_16x16x32_bf16 v[56:59], a[96:99], a[8:11], v[56:59]\n"
"v_mfma_f32_16x16x32_bf16 v[56:59], a[100:103], a[12:15], v[56:59]\n"
"v_mfma_f32_16x16x32_bf16 v[56:59], a[104:107], a[16:19], v[56:59]\n"
"v_cvt_pk_bf16_f32 v20, v20, v21\n"
"v_cvt_pk_bf16_f32 v21, v22, v23\n"
"v_mfma_f32_16x16x32_bf16 v[56:59], a[108:111], a[20:23], v[56:59]\n"
"v_cvt_pk_bf16_f32 v22, v52, v53\n"
"v_cvt_pk_bf16_f32 v23, v54, v55\n"
"v_mfma_f32_16x16x32_bf16 v[60:63], a[136:139], a[24:27], v[60:63]\n"
"v_mfma_f32_16x16x32_bf16 v[60:63], a[140:143], a[28:31], v[60:63]\n"
"v_mfma_f32_16x16x32_bf16 v[60:63], a[96:99], a[32:35], v[60:63]\n"
"ds_read_b128 a[136:139], v244 offset:26624\n"
"v_mfma_f32_16x16x32_bf16 v[60:63], a[100:103], a[36:39], v[60:63]\n"
"ds_read_b128 a[140:143], v244 offset:36864\n"
"v_mfma_f32_16x16x32_bf16 v[60:63], a[104:107], a[40:43], v[60:63]\n"
"ds_read_b128 a[96:99], v244 offset:47104\n"
"v_cvt_pk_bf16_f32 v24, v24, v25\n"
"v_mfma_f32_16x16x32_bf16 v[60:63], a[108:111], a[44:47], v[60:63]\n"
"ds_read_b128 a[100:103], v244 offset:57344\n"
"v_cvt_pk_bf16_f32 v25, v26, v27\n"
"s_waitcnt lgkmcnt(4)\n"
"v_mfma_f32_16x16x32_bf16 v[176:179], a[112:115], a[160:163], v[176:179]\n"
"ds_read_b128 a[104:107], v251 offset:2048\n"
"v_cvt_pk_bf16_f32 v26, v56, v57\n"
"buffer_store_dwordx4 v[20:23], v244, s[8:11], s77 offen offset:0 sc1\n"
"v_mfma_f32_16x16x32_bf16 v[176:179], a[116:119], a[164:167], v[176:179]\n"
"ds_read_b128 a[108:111], v251 offset:12288\n"
"v_cvt_pk_bf16_f32 v27, v58, v59\n"
"v_mfma_f32_16x16x32_bf16 v[176:179], a[120:123], a[168:171], v[176:179]\n"
"v_mfma_f32_16x16x32_bf16 v[176:179], a[124:127], a[172:175], v[176:179]\n"
"v_mfma_f32_16x16x32_bf16 v[176:179], a[128:131], a[176:179], v[176:179]\n"
"v_cvt_pk_bf16_f32 v28, v28, v29\n"
"v_cvt_pk_bf16_f32 v29, v30, v31\n"
"v_mfma_f32_16x16x32_bf16 v[176:179], a[132:135], a[180:183], v[176:179]\n"
"v_cvt_pk_bf16_f32 v30, v60, v61\n"
"v_cvt_pk_bf16_f32 v31, v62, v63\n"
"v_mfma_f32_16x16x32_bf16 v[180:183], a[112:115], a[184:187], v[180:183]\n"
"v_mfma_f32_16x16x32_bf16 v[180:183], a[116:119], a[188:191], v[180:183]\n"
"v_mfma_f32_16x16x32_bf16 v[180:183], a[120:123], a[192:195], v[180:183]\n"
"v_mfma_f32_16x16x32_bf16 v[180:183], a[124:127], a[196:199], v[180:183]\n"
"v_mfma_f32_16x16x32_bf16 v[180:183], a[128:131], a[200:203], v[180:183]\n"
"v_mfma_f32_16x16x32_bf16 v[180:183], a[132:135], a[204:207], v[180:183]\n"
"v_mfma_f32_16x16x32_bf16 v[184:187], a[112:115], a[0:3], v[184:187]\n"
"buffer_store_dwordx4 v[24:27], v244, s[8:11], s78 offen offset:0 sc1\n"
"v_mfma_f32_16x16x32_bf16 v[184:187], a[116:119], a[4:7], v[184:187]\n"
"v_mfma_f32_16x16x32_bf16 v[184:187], a[120:123], a[8:11], v[184:187]\n"
"v_mfma_f32_16x16x32_bf16 v[184:187], a[124:127], a[12:15], v[184:187]\n"
"v_mfma_f32_16x16x32_bf16 v[184:187], a[128:131], a[16:19], v[184:187]\n"
"v_mfma_f32_16x16x32_bf16 v[184:187], a[132:135], a[20:23], v[184:187]\n"
"v_mfma_f32_16x16x32_bf16 v[188:191], a[112:115], a[24:27], v[188:191]\n"
"v_mfma_f32_16x16x32_bf16 v[188:191], a[116:119], a[28:31], v[188:191]\n"
"v_mfma_f32_16x16x32_bf16 v[188:191], a[120:123], a[32:35], v[188:191]\n"
"ds_read_b128 a[112:115], v244 offset:22528\n"
"v_mfma_f32_16x16x32_bf16 v[188:191], a[124:127], a[36:39], v[188:191]\n"
"ds_read_b128 a[116:119], v244 offset:32768\n"
"v_mfma_f32_16x16x32_bf16 v[188:191], a[128:131], a[40:43], v[188:191]\n"
"ds_read_b128 a[120:123], v244 offset:43008\n"
"v_mfma_f32_16x16x32_bf16 v[188:191], a[132:135], a[44:47], v[188:191]\n"
"ds_read_b128 a[124:127], v244 offset:53248\n"
"s_waitcnt lgkmcnt(4)\n"
"v_mfma_f32_16x16x32_bf16 v[192:195], a[136:139], a[160:163], v[192:195]\n"
"s_waitcnt vmcnt(8)\n"
"s_barrier\n"
"s_add_u32 s59, s33, 17280\n"
"buffer_atomic_add v246, v245, s[8:11], s59 offen sc1\n"
"v_mfma_f32_16x16x32_bf16 v[192:195], a[140:143], a[164:167], v[192:195]\n"
"ds_read_b128 a[128:131], v244 offset:63488\n"
"buffer_store_dwordx4 v[28:31], v244, s[8:11], s79 offen offset:0 sc1\n"
"v_mfma_f32_16x16x32_bf16 v[192:195], a[96:99], a[168:171], v[192:195]\n"
"ds_read_b128 a[132:135], v251 offset:8192\n"
"v_mfma_f32_16x16x32_bf16 v[192:195], a[100:103], a[172:175], v[192:195]\n"
"v_mfma_f32_16x16x32_bf16 v[192:195], a[104:107], a[176:179], v[192:195]\n"
"s_add_u32 s72, s61, 655360\n"
"s_add_u32 m0, s6, 0\n"
"v_mfma_f32_16x16x32_bf16 v[192:195], a[108:111], a[180:183], v[192:195]\n"
"buffer_load_dwordx4 v244, s[8:11], s72 offen offset:0 sc1 lds\n"
"v_mfma_f32_16x16x32_bf16 v[196:199], a[136:139], a[184:187], v[196:199]\n"
"v_mfma_f32_16x16x32_bf16 v[196:199], a[140:143], a[188:191], v[196:199]\n"
"v_mfma_f32_16x16x32_bf16 v[196:199], a[96:99], a[192:195], v[196:199]\n"
"buffer_load_dwordx4 v244, s[8:11], s72 offen offset:1024 sc1 lds\n"
"v_mfma_f32_16x16x32_bf16 v[196:199], a[100:103], a[196:199], v[196:199]\n"
"v_mfma_f32_16x16x32_bf16 v[196:199], a[104:107], a[200:203], v[196:199]\n"
"v_cvt_pk_bf16_f32 v176, v176, v177\n"
"v_cvt_pk_bf16_f32 v177, v178, v179\n"
"v_mfma_f32_16x16x32_bf16 v[196:199], a[108:111], a[204:207], v[196:199]\n"
"v_cvt_pk_bf16_f32 v178, v192, v193\n"
"v_cvt_pk_bf16_f32 v179, v194, v195\n"
"v_mfma_f32_16x16x32_bf16 v[200:203], a[136:139], a[0:3], v[200:203]\n"
"buffer_load_dwordx4 v244, s[8:11], s72 offen offset:2048 sc1 lds\n"
"v_mfma_f32_16x16x32_bf16 v[200:203], a[140:143], a[4:7], v[200:203]\n"
"v_mfma_f32_16x16x32_bf16 v[200:203], a[96:99], a[8:11], v[200:203]\n"
"v_mfma_f32_16x16x32_bf16 v[200:203], a[100:103], a[12:15], v[200:203]\n"
"buffer_store_dwordx4 v[176:179], v244, s[8:11], s76 offen offset:2560 sc1\n"
"v_mfma_f32_16x16x32_bf16 v[200:203], a[104:107], a[16:19], v[200:203]\n"
"v_cvt_pk_bf16_f32 v180, v180, v181\n"
"v_cvt_pk_bf16_f32 v181, v182, v183\n"
"v_mfma_f32_16x16x32_bf16 v[200:203], a[108:111], a[20:23], v[200:203]\n"
"v_cvt_pk_bf16_f32 v182, v196, v197\n"
"v_cvt_pk_bf16_f32 v183, v198, v199\n"
"v_mfma_f32_16x16x32_bf16 v[204:207], a[136:139], a[24:27], v[204:207]\n"
"buffer_load_dwordx4 v244, s[8:11], s72 offen offset:3072 sc1 lds\n"
"v_mfma_f32_16x16x32_bf16 v[204:207], a[140:143], a[28:31], v[204:207]\n"
"v_mfma_f32_16x16x32_bf16 v[204:207], a[96:99], a[32:35], v[204:207]\n"
"ds_read_b128 a[136:139], v244 offset:23552\n"
"v_mfma_f32_16x16x32_bf16 v[204:207], a[100:103], a[36:39], v[204:207]\n"
"ds_read_b128 a[140:143], v244 offset:33792\n"
"s_add_u32 m0, s6, 4096\n"
"v_mfma_f32_16x16x32_bf16 v[204:207], a[104:107], a[40:43], v[204:207]\n"
"ds_read_b128 a[96:99], v244 offset:44032\n"
"v_cvt_pk_bf16_f32 v184, v184, v185\n"
"buffer_load_dwordx4 v250, s[8:11], s72 offen offset:0 sc1 lds\n"
"v_mfma_f32_16x16x32_bf16 v[204:207], a[108:111], a[44:47], v[204:207]\n"
"ds_read_b128 a[100:103], v244 offset:54272\n"
"v_cvt_pk_bf16_f32 v185, v186, v187\n"
"s_waitcnt lgkmcnt(4)\n"
"v_mfma_f32_16x16x32_bf16 v[80:83], a[112:115], a[160:163], v[80:83]\n"
"ds_read_b128 a[104:107], v244 offset:64512\n"
"v_cvt_pk_bf16_f32 v186, v200, v201\n"
"v_mfma_f32_16x16x32_bf16 v[80:83], a[116:119], a[164:167], v[80:83]\n"
"ds_read_b128 a[108:111], v251 offset:9216\n"
"v_cvt_pk_bf16_f32 v187, v202, v203\n"
"v_mfma_f32_16x16x32_bf16 v[80:83], a[120:123], a[168:171], v[80:83]\n"
"v_mfma_f32_16x16x32_bf16 v[80:83], a[124:127], a[172:175], v[80:83]\n"
"buffer_store_dwordx4 v[180:183], v244, s[8:11], s77 offen offset:2560 sc1\n"
"v_mfma_f32_16x16x32_bf16 v[80:83], a[128:131], a[176:179], v[80:83]\n"
"v_cvt_pk_bf16_f32 v188, v188, v189\n"
"v_cvt_pk_bf16_f32 v189, v190, v191\n"
"v_mfma_f32_16x16x32_bf16 v[80:83], a[132:135], a[180:183], v[80:83]\n"
"v_cvt_pk_bf16_f32 v190, v204, v205\n"
"v_cvt_pk_bf16_f32 v191, v206, v207\n"
"v_mfma_f32_16x16x32_bf16 v[84:87], a[112:115], a[184:187], v[84:87]\n"
"v_mfma_f32_16x16x32_bf16 v[84:87], a[116:119], a[188:191], v[84:87]\n"
"v_mfma_f32_16x16x32_bf16 v[84:87], a[120:123], a[192:195], v[84:87]\n"
"v_mfma_f32_16x16x32_bf16 v[84:87], a[124:127], a[196:199], v[84:87]\n"
"v_mfma_f32_16x16x32_bf16 v[84:87], a[128:131], a[200:203], v[84:87]\n"
"v_mfma_f32_16x16x32_bf16 v[84:87], a[132:135], a[204:207], v[84:87]\n"
"v_mfma_f32_16x16x32_bf16 v[88:91], a[112:115], a[0:3], v[88:91]\n"
"v_mfma_f32_16x16x32_bf16 v[88:91], a[116:119], a[4:7], v[88:91]\n"
"v_mfma_f32_16x16x32_bf16 v[88:91], a[120:123], a[8:11], v[88:91]\n"
"v_mfma_f32_16x16x32_bf16 v[88:91], a[124:127], a[12:15], v[88:91]\n"
"buffer_store_dwordx4 v[184:187], v244, s[8:11], s78 offen offset:2560 sc1\n"
"v_mfma_f32_16x16x32_bf16 v[88:91], a[128:131], a[16:19], v[88:91]\n"
"v_mfma_f32_16x16x32_bf16 v[88:91], a[132:135], a[20:23], v[88:91]\n"
"v_mfma_f32_16x16x32_bf16 v[92:95], a[112:115], a[24:27], v[92:95]\n"
"v_mfma_f32_16x16x32_bf16 v[92:95], a[116:119], a[28:31], v[92:95]\n"
"v_mfma_f32_16x16x32_bf16 v[92:95], a[120:123], a[32:35], v[92:95]\n"
"ds_read_b128 a[112:115], v244 offset:27648\n"
"v_mfma_f32_16x16x32_bf16 v[92:95], a[124:127], a[36:39], v[92:95]\n"
"ds_read_b128 a[116:119], v244 offset:37888\n"
"v_mfma_f32_16x16x32_bf16 v[92:95], a[128:131], a[40:43], v[92:95]\n"
"ds_read_b128 a[120:123], v244 offset:48128\n"
"v_mfma_f32_16x16x32_bf16 v[92:95], a[132:135], a[44:47], v[92:95]\n"
"ds_read_b128 a[124:127], v244 offset:58368\n"
"s_waitcnt lgkmcnt(4)\n"
"v_mfma_f32_16x16x32_bf16 v[112:115], a[136:139], a[160:163], v[112:115]\n"
"s_barrier\n"
"v_mfma_f32_16x16x32_bf16 v[112:115], a[140:143], a[164:167], v[112:115]\n"
"ds_read_b128 a[128:131], v251 offset:3072\n"
"v_mfma_f32_16x16x32_bf16 v[112:115], a[96:99], a[168:171], v[112:115]\n"
"ds_read_b128 a[132:135], v251 offset:13312\n"
"v_mfma_f32_16x16x32_bf16 v[112:115], a[100:103], a[172:175], v[112:115]\n"
"buffer_store_dwordx4 v[188:191], v244, s[8:11], s79 offen offset:2560 sc1\n"
"v_mfma_f32_16x16x32_bf16 v[112:115], a[104:107], a[176:179], v[112:115]\n"
"v_mfma_f32_16x16x32_bf16 v[112:115], a[108:111], a[180:183], v[112:115]\n"
"v_mfma_f32_16x16x32_bf16 v[116:119], a[136:139], a[184:187], v[116:119]\n"
"v_mfma_f32_16x16x32_bf16 v[116:119], a[140:143], a[188:191], v[116:119]\n"
"v_mfma_f32_16x16x32_bf16 v[116:119], a[96:99], a[192:195], v[116:119]\n"
"v_mfma_f32_16x16x32_bf16 v[116:119], a[100:103], a[196:199], v[116:119]\n"
"v_mfma_f32_16x16x32_bf16 v[116:119], a[104:107], a[200:203], v[116:119]\n"
"s_add_u32 s59, s33, 17280\n"
"buffer_load_dword v249, off, s[8:11], s59 sc1\n"
"v_mfma_f32_16x16x32_bf16 v[116:119], a[108:111], a[204:207], v[116:119]\n"
"v_cvt_pk_bf16_f32 v80, v80, v81\n"
"v_cvt_pk_bf16_f32 v81, v82, v83\n"
"v_mfma_f32_16x16x32_bf16 v[120:123], a[136:139], a[0:3], v[120:123]\n"
"v_cvt_pk_bf16_f32 v82, v112, v113\n"
"v_cvt_pk_bf16_f32 v83, v114, v115\n"
"v_mfma_f32_16x16x32_bf16 v[120:123], a[140:143], a[4:7], v[120:123]\n"
"v_mfma_f32_16x16x32_bf16 v[120:123], a[96:99], a[8:11], v[120:123]\n"
"v_mfma_f32_16x16x32_bf16 v[120:123], a[100:103], a[12:15], v[120:123]\n"
"buffer_store_dwordx4 v[80:83], v244, s[8:11], s76 offen offset:1024 sc1\n"
"v_mfma_f32_16x16x32_bf16 v[120:123], a[104:107], a[16:19], v[120:123]\n"
"v_cvt_pk_bf16_f32 v84, v84, v85\n"
"v_cvt_pk_bf16_f32 v85, v86, v87\n"
"v_mfma_f32_16x16x32_bf16 v[120:123], a[108:111], a[20:23], v[120:123]\n"
"v_cvt_pk_bf16_f32 v86, v116, v117\n"
"v_cvt_pk_bf16_f32 v87, v118, v119\n"
"v_mfma_f32_16x16x32_bf16 v[124:127], a[136:139], a[24:27], v[124:127]\n"
"v_mfma_f32_16x16x32_bf16 v[124:127], a[140:143], a[28:31], v[124:127]\n"
"v_mfma_f32_16x16x32_bf16 v[124:127], a[96:99], a[32:35], v[124:127]\n"
"ds_read_b128 a[136:139], v244 offset:28672\n"
"v_mfma_f32_16x16x32_bf16 v[124:127], a[100:103], a[36:39], v[124:127]\n"
"ds_read_b128 a[140:143], v244 offset:38912\n"
"v_mfma_f32_16x16x32_bf16 v[124:127], a[104:107], a[40:43], v[124:127]\n"
"ds_read_b128 a[96:99], v244 offset:49152\n"
"v_cvt_pk_bf16_f32 v88, v88, v89\n"
"v_mfma_f32_16x16x32_bf16 v[124:127], a[108:111], a[44:47], v[124:127]\n"
"ds_read_b128 a[100:103], v244 offset:59392\n"
"v_cvt_pk_bf16_f32 v89, v90, v91\n"
"s_waitcnt lgkmcnt(4)\n"
"v_mfma_f32_16x16x32_bf16 a[208:211], a[112:115], a[160:163], a[208:211]\n"
"ds_read_b128 a[104:107], v251 offset:4096\n"
"v_cvt_pk_bf16_f32 v90, v120, v121\n"
"v_mfma_f32_16x16x32_bf16 a[208:211], a[116:119], a[164:167], a[208:211]\n"
"ds_read_b128 a[108:111], v251 offset:14336\n"
"v_cvt_pk_bf16_f32 v91, v122, v123\n"
"v_mfma_f32_16x16x32_bf16 a[208:211], a[120:123], a[168:171], a[208:211]\n"
"v_mfma_f32_16x16x32_bf16 a[208:211], a[124:127], a[172:175], a[208:211]\n"
"buffer_store_dwordx4 v[84:87], v244, s[8:11], s77 offen offset:1024 sc1\n"
"v_mfma_f32_16x16x32_bf16 a[208:211], a[128:131], a[176:179], a[208:211]\n"
"v_cvt_pk_bf16_f32 v92, v92, v93\n"
"v_cvt_pk_bf16_f32 v93, v94, v95\n"
"v_mfma_f32_16x16x32_bf16 a[208:211], a[132:135], a[180:183], a[208:211]\n"
"v_cvt_pk_bf16_f32 v94, v124, v125\n"
"v_cvt_pk_bf16_f32 v95, v126, v127\n"
"v_mfma_f32_16x16x32_bf16 a[212:215], a[112:115], a[184:187], a[212:215]\n"
"v_mfma_f32_16x16x32_bf16 a[212:215], a[116:119], a[188:191], a[212:215]\n"
"v_mfma_f32_16x16x32_bf16 a[212:215], a[120:123], a[192:195], a[212:215]\n"
"v_mfma_f32_16x16x32_bf16 a[212:215], a[124:127], a[196:199], a[212:215]\n"
"v_mfma_f32_16x16x32_bf16 a[212:215], a[128:131], a[200:203], a[212:215]\n"
"v_mfma_f32_16x16x32_bf16 a[212:215], a[132:135], a[204:207], a[212:215]\n"
"v_mfma_f32_16x16x32_bf16 a[216:219], a[112:115], a[0:3], a[216:219]\n"
"v_mfma_f32_16x16x32_bf16 a[216:219], a[116:119], a[4:7], a[216:219]\n"
"v_mfma_f32_16x16x32_bf16 a[216:219], a[120:123], a[8:11], a[216:219]\n"
"v_mfma_f32_16x16x32_bf16 a[216:219], a[124:127], a[12:15], a[216:219]\n"
"buffer_store_dwordx4 v[88:91], v244, s[8:11], s78 offen offset:1024 sc1\n"
"v_mfma_f32_16x16x32_bf16 a[216:219], a[128:131], a[16:19], a[216:219]\n"
"v_mfma_f32_16x16x32_bf16 a[216:219], a[132:135], a[20:23], a[216:219]\n"
"v_mfma_f32_16x16x32_bf16 a[220:223], a[112:115], a[24:27], a[220:223]\n"
"v_mfma_f32_16x16x32_bf16 a[220:223], a[116:119], a[28:31], a[220:223]\n"
"v_mfma_f32_16x16x32_bf16 a[220:223], a[120:123], a[32:35], a[220:223]\n"
"ds_read_b128 a[112:115], v244 offset:24576\n"
"v_mfma_f32_16x16x32_bf16 a[220:223], a[124:127], a[36:39], a[220:223]\n"
"ds_read_b128 a[116:119], v244 offset:34816\n"
"v_mfma_f32_16x16x32_bf16 a[220:223], a[128:131], a[40:43], a[220:223]\n"
"ds_read_b128 a[120:123], v244 offset:45056\n"
"v_mfma_f32_16x16x32_bf16 a[220:223], a[132:135], a[44:47], a[220:223]\n"
"ds_read_b128 a[124:127], v244 offset:55296\n"
"s_waitcnt lgkmcnt(4)\n"
"v_mfma_f32_16x16x32_bf16 a[224:227], a[136:139], a[160:163], a[224:227]\n"
"s_waitcnt vmcnt(4)\n"
"s_barrier\n"
"s_add_u32 s59, s65, 512\n"
"buffer_atomic_add v246, v245, s[8:11], s59 offen sc1\n"
"v_mfma_f32_16x16x32_bf16 a[224:227], a[140:143], a[164:167], a[224:227]\n"
"ds_read_b128 a[128:131], v251 offset:0\n"
"v_mfma_f32_16x16x32_bf16 a[224:227], a[96:99], a[168:171], a[224:227]\n"
"ds_read_b128 a[132:135], v251 offset:10240\n"
"s_waitcnt vmcnt(4)\n"
"v_cmp_gt_u32 vcc, 16, v249\n"
"s_cbranch_vccnz L_spin_u_%=\n"
"L_back_u_%=:\n"
"v_mfma_f32_16x16x32_bf16 a[224:227], a[100:103], a[172:175], a[224:227]\n"
"buffer_store_dwordx4 v[92:95], v244, s[8:11], s79 offen offset:1024 sc1\n"
"v_mfma_f32_16x16x32_bf16 a[224:227], a[104:107], a[176:179], a[224:227]\n"
"v_mfma_f32_16x16x32_bf16 a[224:227], a[108:111], a[180:183], a[224:227]\n"
"v_mfma_f32_16x16x32_bf16 a[228:231], a[136:139], a[184:187], a[228:231]\n"
"s_add_u32 s70, s62, 0\n"
"buffer_load_dwordx4 a[48:51], v244, s[8:11], s70 offen offset:0 sc1\n"
"v_mfma_f32_16x16x32_bf16 a[228:231], a[140:143], a[188:191], a[228:231]\n"
"v_mfma_f32_16x16x32_bf16 a[228:231], a[96:99], a[192:195], a[228:231]\n"
"v_mfma_f32_16x16x32_bf16 a[228:231], a[100:103], a[196:199], a[228:231]\n"
"buffer_load_dwordx4 a[52:55], v244, s[8:11], s70 offen offset:1024 sc1\n"
"v_mfma_f32_16x16x32_bf16 a[228:231], a[104:107], a[200:203], a[228:231]\n"
"v_accvgpr_read_b32 v192, a208\n"
"v_accvgpr_read_b32 v193, a209\n"
"v_mfma_f32_16x16x32_bf16 a[228:231], a[108:111], a[204:207], a[228:231]\n"
"v_accvgpr_read_b32 v194, a210\n"
"v_accvgpr_read_b32 v195, a211\n"
"v_mfma_f32_16x16x32_bf16 a[232:235], a[136:139], a[0:3], a[232:235]\n"
"v_cvt_pk_bf16_f32 v48, v192, v193\n"
"v_cvt_pk_bf16_f32 v49, v194, v195\n"
"buffer_load_dwordx4 a[56:59], v244, s[8:11], s70 offen offset:2048 sc1\n"
"v_mfma_f32_16x16x32_bf16 a[232:235], a[140:143], a[4:7], a[232:235]\n"
"v_accvgpr_read_b32 v192, a224\n"
"v_accvgpr_read_b32 v193, a225\n"
"v_mfma_f32_16x16x32_bf16 a[232:235], a[96:99], a[8:11], a[232:235]\n"
"v_accvgpr_read_b32 v194, a226\n"
"v_accvgpr_read_b32 v195, a227\n"
"v_mfma_f32_16x16x32_bf16 a[232:235], a[100:103], a[12:15], a[232:235]\n"
"v_cvt_pk_bf16_f32 v50, v192, v193\n"
"v_cvt_pk_bf16_f32 v51, v194, v195\n"
"buffer_load_dwordx4 a[60:63], v244, s[8:11], s70 offen offset:3072 sc1\n"
"v_mfma_f32_16x16x32_bf16 a[232:235], a[104:107], a[16:19], a[232:235]\n"
"v_accvgpr_read_b32 v196, a212\n"
"v_accvgpr_read_b32 v197, a213\n"
"v_accvgpr_read_b32 v198, a214\n"
"v_mfma_f32_16x16x32_bf16 a[232:235], a[108:111], a[20:23], a[232:235]\n"
"v_accvgpr_read_b32 v199, a215\n"
"v_cvt_pk_bf16_f32 v52, v196, v197\n"
"v_cvt_pk_bf16_f32 v53, v198, v199\n"
"v_mfma_f32_16x16x32_bf16 a[236:239], a[136:139], a[24:27], a[236:239]\n"
"v_accvgpr_read_b32 v196, a228\n"
"v_accvgpr_read_b32 v197, a229\n"
"v_accvgpr_read_b32 v198, a230\n"
"buffer_store_dwordx4 v[48:51], v244, s[8:11], s76 offen offset:3584 sc1\n"
"v_mfma_f32_16x16x32_bf16 a[236:239], a[140:143], a[28:31], a[236:239]\n"
"v_accvgpr_read_b32 v199, a231\n"
"v_cvt_pk_bf16_f32 v54, v196, v197\n"
"v_cvt_pk_bf16_f32 v55, v198, v199\n"
"v_mfma_f32_16x16x32_bf16 a[236:239], a[96:99], a[32:35], a[236:239]\n"
"ds_read_b128 a[136:139], v244 offset:29696\n"
"v_mfma_f32_16x16x32_bf16 a[236:239], a[100:103], a[36:39], a[236:239]\n"
"ds_read_b128 a[140:143], v244 offset:39936\n"
"buffer_load_dwordx4 a[64:67], v250, s[8:11], s70 offen offset:0 sc1\n"
"v_mfma_f32_16x16x32_bf16 a[236:239], a[104:107], a[40:43], a[236:239]\n"
"ds_read_b128 a[96:99], v244 offset:50176\n"
"v_accvgpr_read_b32 v200, a216\n"
"v_accvgpr_read_b32 v201, a217\n"
"v_mfma_f32_16x16x32_bf16 a[236:239], a[108:111], a[44:47], a[236:239]\n"
"ds_read_b128 a[100:103], v244 offset:60416\n"
"v_accvgpr_read_b32 v202, a218\n"
"v_accvgpr_read_b32 v203, a219\n"
"s_waitcnt lgkmcnt(4)\n"
"v_mfma_f32_16x16x32_bf16 v[160:163], a[112:115], a[160:163], v[160:163]\n"
"ds_read_b128 a[104:107], v251 offset:5120\n"
"v_cvt_pk_bf16_f32 v56, v200, v201\n"
"v_cvt_pk_bf16_f32 v57, v202, v203\n"
"buffer_load_dwordx4 a[68:71], v250, s[8:11], s70 offen offset:1024 sc1\n"
"v_mfma_f32_16x16x32_bf16 v[160:163], a[116:119], a[164:167], v[160:163]\n"
"ds_read_b128 a[108:111], v251 offset:15360\n"
"v_accvgpr_read_b32 v200, a232\n"
"v_accvgpr_read_b32 v201, a233\n"
"v_mfma_f32_16x16x32_bf16 v[160:163], a[120:123], a[168:171], v[160:163]\n"
"v_accvgpr_read_b32 v202, a234\n"
"v_accvgpr_read_b32 v203, a235\n"
"v_cvt_pk_bf16_f32 v58, v200, v201\n"
"v_mfma_f32_16x16x32_bf16 v[160:163], a[124:127], a[172:175], v[160:163]\n"
"v_cvt_pk_bf16_f32 v59, v202, v203\n"
"buffer_load_dwordx4 a[72:75], v250, s[8:11], s70 offen offset:2048 sc1\n"
"v_mfma_f32_16x16x32_bf16 v[160:163], a[128:131], a[176:179], v[160:163]\n"
"v_accvgpr_read_b32 v204, a220\n"
"v_accvgpr_read_b32 v205, a221\n"
"v_mfma_f32_16x16x32_bf16 v[160:163], a[132:135], a[180:183], v[160:163]\n"
"v_accvgpr_read_b32 v206, a222\n"
"v_accvgpr_read_b32 v207, a223\n"
"v_mfma_f32_16x16x32_bf16 v[164:167], a[112:115], a[184:187], v[164:167]\n"
"v_cvt_pk_bf16_f32 v60, v204, v205\n"
"v_cvt_pk_bf16_f32 v61, v206, v207\n"
"buffer_store_dwordx4 v[52:55], v244, s[8:11], s77 offen offset:3584 sc1\n"
"v_mfma_f32_16x16x32_bf16 v[164:167], a[116:119], a[188:191], v[164:167]\n"
"v_accvgpr_read_b32 v204, a236\n"
"v_accvgpr_read_b32 v205, a237\n"
"v_mfma_f32_16x16x32_bf16 v[164:167], a[120:123], a[192:195], v[164:167]\n"
"v_accvgpr_read_b32 v206, a238\n"
"v_accvgpr_read_b32 v207, a239\n"
"v_mfma_f32_16x16x32_bf16 v[164:167], a[124:127], a[196:199], v[164:167]\n"
"v_cvt_pk_bf16_f32 v62, v204, v205\n"
"v_cvt_pk_bf16_f32 v63, v206, v207\n"
"buffer_load_dwordx4 a[76:79], v250, s[8:11], s70 offen offset:3072 sc1\n"
"v_mfma_f32_16x16x32_bf16 v[164:167], a[128:131], a[200:203], v[164:167]\n"
"v_cvt_pk_bf16_f32 v160, v160, v161\n"
"v_cvt_pk_bf16_f32 v161, v162, v163\n"
"v_mfma_f32_16x16x32_bf16 v[164:167], a[132:135], a[204:207], v[164:167]\n"
"v_mfma_f32_16x16x32_bf16 v[168:171], a[112:115], a[0:3], v[168:171]\n"
"s_add_u32 s71, s62, 8192\n"
"buffer_load_dwordx4 a[80:83], v244, s[8:11], s71 offen offset:0 sc1\n"
"v_mfma_f32_16x16x32_bf16 v[168:171], a[116:119], a[4:7], v[168:171]\n"
"v_mfma_f32_16x16x32_bf16 v[168:171], a[120:123], a[8:11], v[168:171]\n"
"v_mfma_f32_16x16x32_bf16 v[168:171], a[124:127], a[12:15], v[168:171]\n"
"buffer_load_dwordx4 a[84:87], v244, s[8:11], s71 offen offset:1024 sc1\n"
"v_mfma_f32_16x16x32_bf16 v[168:171], a[128:131], a[16:19], v[168:171]\n"
"v_cvt_pk_bf16_f32 v164, v164, v165\n"
"v_cvt_pk_bf16_f32 v165, v166, v167\n"
"v_mfma_f32_16x16x32_bf16 v[168:171], a[132:135], a[20:23], v[168:171]\n"
"v_mfma_f32_16x16x32_bf16 v[172:175], a[112:115], a[24:27], v[172:175]\n"
"buffer_store_dwordx4 v[56:59], v244, s[8:11], s78 offen offset:3584 sc1\n"
"v_mfma_f32_16x16x32_bf16 v[172:175], a[116:119], a[28:31], v[172:175]\n"
"v_mfma_f32_16x16x32_bf16 v[172:175], a[120:123], a[32:35], v[172:175]\n"
"v_mfma_f32_16x16x32_bf16 v[172:175], a[124:127], a[36:39], v[172:175]\n"
"buffer_load_dwordx4 a[88:91], v244, s[8:11], s71 offen offset:2048 sc1\n"
"v_mfma_f32_16x16x32_bf16 v[172:175], a[128:131], a[40:43], v[172:175]\n"
"s_nop 0\n"
"v_cvt_pk_bf16_f32 v168, v168, v169\n"
"v_cvt_pk_bf16_f32 v169, v170, v171\n"
"v_mfma_f32_16x16x32_bf16 v[172:175], a[132:135], a[44:47], v[172:175]\n"
"s_waitcnt lgkmcnt(0)\n"
"v_mfma_f32_16x16x32_bf16 a[240:243], a[136:139], a[160:163], a[240:243]\n"
"buffer_load_dwordx4 a[92:95], v244, s[8:11], s71 offen offset:3072 sc1\n"
"v_mfma_f32_16x16x32_bf16 a[240:243], a[140:143], a[164:167], a[240:243]\n"
"v_mfma_f32_16x16x32_bf16 a[240:243], a[96:99], a[168:171], a[240:243]\n"
"v_mfma_f32_16x16x32_bf16 a[240:243], a[100:103], a[172:175], a[240:243]\n"
"buffer_load_dwordx4 a[144:147], v250, s[8:11], s71 offen offset:0 sc1\n"
"v_mfma_f32_16x16x32_bf16 a[240:243], a[104:107], a[176:179], a[240:243]\n"
"v_cvt_pk_bf16_f32 v172, v172, v173\n"
"v_cvt_pk_bf16_f32 v173, v174, v175\n"
"v_mfma_f32_16x16x32_bf16 a[240:243], a[108:111], a[180:183], a[240:243]\n"
"v_mfma_f32_16x16x32_bf16 a[244:247], a[136:139], a[184:187], a[244:247]\n"
"buffer_store_dwordx4 v[60:63], v244, s[8:11], s79 offen offset:3584 sc1\n"
"v_mfma_f32_16x16x32_bf16 a[244:247], a[140:143], a[188:191], a[244:247]\n"
"v_mfma_f32_16x16x32_bf16 a[244:247], a[96:99], a[192:195], a[244:247]\n"
"v_mfma_f32_16x16x32_bf16 a[244:247], a[100:103], a[196:199], a[244:247]\n"
"buffer_load_dwordx4 a[148:151], v250, s[8:11], s71 offen offset:1024 sc1\n"
"v_mfma_f32_16x16x32_bf16 a[244:247], a[104:107], a[200:203], a[244:247]\n"
"s_nop 0\n"
"v_accvgpr_read_b32 v112, a240\n"
"v_accvgpr_read_b32 v113, a241\n"
"v_mfma_f32_16x16x32_bf16 a[244:247], a[108:111], a[204:207], a[244:247]\n"
"s_barrier\n"
"v_mfma_f32_16x16x32_bf16 a[248:251], a[136:139], a[0:3], a[248:251]\n"
"ds_read_b128 a[112:115], v251 offset:16384\n"
"v_accvgpr_read_b32 v114, a242\n"
"buffer_load_dwordx4 a[152:155], v250, s[8:11], s71 offen offset:2048 sc1\n"
"v_mfma_f32_16x16x32_bf16 a[248:251], a[140:143], a[4:7], a[248:251]\n"
"ds_read_b128 a[116:119], v251 offset:21504\n"
"v_accvgpr_read_b32 v115, a243\n"
"v_mfma_f32_16x16x32_bf16 a[248:251], a[96:99], a[8:11], a[248:251]\n"
"ds_read_b128 a[120:123], v251 offset:26624\n"
"v_cvt_pk_bf16_f32 v192, v112, v113\n"
"v_mfma_f32_16x16x32_bf16 a[248:251], a[100:103], a[12:15], a[248:251]\n"
"ds_read_b128 a[124:127], v251 offset:31744\n"
"v_cvt_pk_bf16_f32 v193, v114, v115\n"
"buffer_load_dwordx4 a[156:159], v250, s[8:11], s71 offen offset:3072 sc1\n"
"v_mfma_f32_16x16x32_bf16 a[248:251], a[104:107], a[16:19], a[248:251]\n"
"ds_read_b128 a[128:131], v251 offset:17408\n"
"v_accvgpr_read_b32 v116, a244\n"
"v_mfma_f32_16x16x32_bf16 a[248:251], a[108:111], a[20:23], a[248:251]\n"
"ds_read_b128 a[132:135], v251 offset:22528\n"
"v_accvgpr_read_b32 v117, a245\n"
"v_mfma_f32_16x16x32_bf16 a[252:255], a[136:139], a[24:27], a[252:255]\n"
"ds_read_b128 a[136:139], v251 offset:27648\n"
"v_accvgpr_read_b32 v118, a246\n"
"buffer_store_dwordx2 v[160:161], v253, s[8:11], s76 offen offset:2048 sc1\n"
"v_mfma_f32_16x16x32_bf16 a[252:255], a[140:143], a[28:31], a[252:255]\n"
"ds_read_b128 a[140:143], v251 offset:32768\n"
"v_accvgpr_read_b32 v119, a247\n"
"v_mfma_f32_16x16x32_bf16 a[252:255], a[96:99], a[32:35], a[252:255]\n"
"ds_read_b128 a[96:99], v251 offset:18432\n"
"v_cvt_pk_bf16_f32 v196, v116, v117\n"
"v_mfma_f32_16x16x32_bf16 a[252:255], a[100:103], a[36:39], a[252:255]\n"
"ds_read_b128 a[100:103], v251 offset:23552\n"
"v_cvt_pk_bf16_f32 v197, v118, v119\n"
"s_add_u32 s70, s62, 16384\n"
"buffer_load_dwordx4 a[160:163], v244, s[8:11], s70 offen offset:0 sc1\n"
"v_mfma_f32_16x16x32_bf16 a[252:255], a[104:107], a[40:43], a[252:255]\n"
"ds_read_b128 a[104:107], v251 offset:28672\n"
"v_accvgpr_read_b32 v120, a248\n"
"v_mfma_f32_16x16x32_bf16 a[252:255], a[108:111], a[44:47], a[252:255]\n"
"ds_read_b128 a[108:111], v251 offset:33792\n"
"v_accvgpr_read_b32 v121, a249\n"
"s_waitcnt vmcnt(18)\n"
"s_waitcnt lgkmcnt(6)\n"
"v_mfma_f32_16x16x32_bf16 v[96:99], a[112:115], a[48:51], 0\n"
"ds_read_b128 a[112:115], v251 offset:19456\n"
"v_accvgpr_read_b32 v122, a250\n"
"buffer_store_dwordx2 v[164:165], v253, s[8:11], s77 offen offset:2048 sc1\n"
"v_mfma_f32_16x16x32_bf16 v[96:99], a[116:119], a[52:55], v[96:99]\n"
"ds_read_b128 a[116:119], v251 offset:24576\n"
"v_accvgpr_read_b32 v123, a251\n"
"v_mfma_f32_16x16x32_bf16 v[96:99], a[120:123], a[56:59], v[96:99]\n"
"ds_read_b128 a[120:123], v251 offset:29696\n"
"v_cvt_pk_bf16_f32 v200, v120, v121\n"
"buffer_load_dwordx4 a[164:167], v244, s[8:11], s70 offen offset:1024 sc1\n"
"v_mfma_f32_16x16x32_bf16 v[96:99], a[124:127], a[60:63], v[96:99]\n"
"ds_read_b128 a[124:127], v251 offset:34816\n"
"v_cvt_pk_bf16_f32 v201, v122, v123\n"
"v_mfma_f32_16x16x32_bf16 v[100:103], a[128:131], a[48:51], 0\n"
"ds_read_b128 a[128:131], v251 offset:20480\n"
"v_accvgpr_read_b32 v124, a252\n"
"buffer_store_dwordx2 v[168:169], v253, s[8:11], s78 offen offset:2048 sc1\n"
"v_mfma_f32_16x16x32_bf16 v[100:103], a[132:135], a[52:55], v[100:103]\n"
"ds_read_b128 a[132:135], v251 offset:25600\n"
"v_accvgpr_read_b32 v125, a253\n"
"s_waitcnt lgkmcnt(6)\n"
"v_mfma_f32_16x16x32_bf16 v[100:103], a[136:139], a[56:59], v[100:103]\n"
"ds_read_b128 a[136:139], v251 offset:30720\n"
"v_accvgpr_read_b32 v126, a254\n"
"buffer_load_dwordx4 a[168:171], v244, s[8:11], s70 offen offset:2048 sc1\n"
"v_mfma_f32_16x16x32_bf16 v[100:103], a[140:143], a[60:63], v[100:103]\n"
"s_waitcnt vmcnt(9)\n"
"s_barrier\n"
"s_add_u32 s59, s65, 640\n"
"buffer_atomic_add v246, v245, s[8:11], s59 offen sc1\n"
"v_mfma_f32_16x16x32_bf16 v[104:107], a[96:99], a[48:51], 0\n"
"ds_read_b128 a[140:143], v251 offset:35840\n"
"v_accvgpr_read_b32 v127, a255\n"
"buffer_store_dwordx2 v[172:173], v253, s[8:11], s79 offen offset:2048 sc1\n"
"v_mfma_f32_16x16x32_bf16 v[104:107], a[100:103], a[52:55], v[104:107]\n"
"ds_read_b128 a[96:99], v251 offset:36864\n"
"v_cvt_pk_bf16_f32 v204, v124, v125\n"
"v_mfma_f32_16x16x32_bf16 v[104:107], a[104:107], a[56:59], v[104:107]\n"
"ds_read_b128 a[100:103], v251 offset:41984\n"
"v_cvt_pk_bf16_f32 v205, v126, v127\n"
"v_mfma_f32_16x16x32_bf16 v[104:107], a[108:111], a[60:63], v[104:107]\n"
"ds_read_b128 a[104:107], v251 offset:47104\n"
"buffer_load_dwordx4 a[172:175], v244, s[8:11], s70 offen offset:3072 sc1\n"
"s_waitcnt lgkmcnt(5)\n"
"v_mfma_f32_16x16x32_bf16 v[108:111], a[112:115], a[48:51], 0\n"
"ds_read_b128 a[108:111], v251 offset:52224\n"
"v_mfma_f32_16x16x32_bf16 v[108:111], a[116:119], a[52:55], v[108:111]\n"
"ds_read_b128 a[112:115], v251 offset:37888\n"
"buffer_store_dwordx2 v[192:193], v254, s[8:11], s76 offen offset:2048 sc1\n"
"v_mfma_f32_16x16x32_bf16 v[108:111], a[120:123], a[56:59], v[108:111]\n"
"ds_read_b128 a[116:119], v251 offset:43008\n"
"v_mfma_f32_16x16x32_bf16 v[108:111], a[124:127], a[60:63], v[108:111]\n"
"ds_read_b128 a[120:123], v251 offset:48128\n"
"s_add_u32 s72, s61, 819200\n"
"s_add_u32 m0, s6, 20480\n"
"v_mfma_f32_16x16x32_bf16 v[152:155], a[128:131], a[48:51], 0\n"
"ds_read_b128 a[124:127], v251 offset:53248\n"
"buffer_load_dwordx4 v244, s[8:11], s72 offen offset:0 sc1 lds\n"
"v_mfma_f32_16x16x32_bf16 v[152:155], a[132:135], a[52:55], v[152:155]\n"
"ds_read_b128 a[128:131], v251 offset:38912\n"
"s_waitcnt lgkmcnt(6)\n"
"v_mfma_f32_16x16x32_bf16 v[152:155], a[136:139], a[56:59], v[152:155]\n"
"ds_read_b128 a[132:135], v251 offset:44032\n"
"buffer_store_dwordx2 v[196:197], v254, s[8:11], s77 offen offset:2048 sc1\n"
"v_mfma_f32_16x16x32_bf16 v[152:155], a[140:143], a[60:63], v[152:155]\n"
"ds_read_b128 a[136:139], v251 offset:49152\n"
"v_mfma_f32_16x16x32_bf16 v[96:99], a[96:99], a[64:67], v[96:99]\n"
"ds_read_b128 a[140:143], v251 offset:54272\n"
"buffer_load_dwordx4 v244, s[8:11], s72 offen offset:1024 sc1 lds\n"
"v_mfma_f32_16x16x32_bf16 v[96:99], a[100:103], a[68:71], v[96:99]\n"
"ds_read_b128 a[96:99], v251 offset:39936\n"
"v_mfma_f32_16x16x32_bf16 v[96:99], a[104:107], a[72:75], v[96:99]\n"
"ds_read_b128 a[100:103], v251 offset:45056\n"
"buffer_store_dwordx2 v[200:201], v254, s[8:11], s78 offen offset:2048 sc1\n"
"s_waitcnt lgkmcnt(6)\n"
"v_mfma_f32_16x16x32_bf16 v[96:99], a[108:111], a[76:79], v[96:99]\n"
"ds_read_b128 a[104:107], v251 offset:50176\n"
"v_mfma_f32_16x16x32_bf16 v[100:103], a[112:115], a[64:67], v[100:103]\n"
"ds_read_b128 a[108:111], v251 offset:55296\n"
"buffer_load_dwordx4 v244, s[8:11], s72 offen offset:2048 sc1 lds\n"
"v_mfma_f32_16x16x32_bf16 v[100:103], a[116:119], a[68:71], v[100:103]\n"
"ds_read_b128 a[112:115], v251 offset:40960\n"
"v_mfma_f32_16x16x32_bf16 v[100:103], a[120:123], a[72:75], v[100:103]\n"
"ds_read_b128 a[116:119], v251 offset:46080\n"
"buffer_store_dwordx2 v[204:205], v254, s[8:11], s79 offen offset:2048 sc1\n"
"v_mfma_f32_16x16x32_bf16 v[100:103], a[124:127], a[76:79], v[100:103]\n"
"s_barrier\n"
"s_waitcnt lgkmcnt(5)\n"
"v_mfma_f32_16x16x32_bf16 v[104:107], a[128:131], a[64:67], v[104:107]\n"
"ds_read_b128 a[120:123], v251 offset:51200\n"
"buffer_load_dwordx4 v244, s[8:11], s72 offen offset:3072 sc1 lds\n"
"v_mfma_f32_16x16x32_bf16 v[104:107], a[132:135], a[68:71], v[104:107]\n"
"ds_read_b128 a[124:127], v251 offset:56320\n"
"v_mfma_f32_16x16x32_bf16 v[104:107], a[136:139], a[72:75], v[104:107]\n"
"ds_read_b128 a[128:131], v251 offset:57344\n"
"s_add_u32 m0, s6, 24576\n"
"v_mfma_f32_16x16x32_bf16 v[104:107], a[140:143], a[76:79], v[104:107]\n"
"ds_read_b128 a[132:135], v251 offset:62464\n"
"buffer_load_dwordx4 v250, s[8:11], s72 offen offset:0 sc1 lds\n"
"v_mfma_f32_16x16x32_bf16 v[108:111], a[96:99], a[64:67], v[108:111]\n"
"ds_read_b128 a[136:139], v252 offset:2048\n"
"s_waitcnt lgkmcnt(5)\n"
"v_mfma_f32_16x16x32_bf16 v[108:111], a[100:103], a[68:71], v[108:111]\n"
"ds_read_b128 a[140:143], v252 offset:7168\n"
"buffer_load_dwordx4 a[176:179], v250, s[8:11], s70 offen offset:0 sc1\n"
"v_mfma_f32_16x16x32_bf16 v[108:111], a[104:107], a[72:75], v[108:111]\n"
"ds_read_b128 a[96:99], v251 offset:58368\n"
"v_mfma_f32_16x16x32_bf16 v[108:111], a[108:111], a[76:79], v[108:111]\n"
"ds_read_b128 a[100:103], v251 offset:63488\n"
"buffer_load_dwordx4 a[180:183], v250, s[8:11], s70 offen offset:1024 sc1\n"
"v_mfma_f32_16x16x32_bf16 v[152:155], a[112:115], a[64:67], v[152:155]\n"
"ds_read_b128 a[104:107], v252 offset:3072\n"
"s_add_u32 s59, s66, 512\n"
"buffer_load_dword v249, off, s[8:11], s59 sc1\n"
"v_mfma_f32_16x16x32_bf16 v[152:155], a[116:119], a[68:71], v[152:155]\n"
"ds_read_b128 a[108:111], v252 offset:8192\n"
"buffer_load_dwordx4 a[184:187], v250, s[8:11], s70 offen offset:2048 sc1\n"
"s_waitcnt lgkmcnt(6)\n"
"v_mfma_f32_16x16x32_bf16 v[152:155], a[120:123], a[72:75], v[152:155]\n"
"ds_read_b128 a[112:115], v251 offset:59392\n"
"v_mfma_f32_16x16x32_bf16 v[152:155], a[124:127], a[76:79], v[152:155]\n"
"ds_read_b128 a[116:119], v251 offset:64512\n"
"buffer_load_dwordx4 a[188:191], v250, s[8:11], s70 offen offset:3072 sc1\n"
"v_mfma_f32_16x16x32_bf16 v[96:99], a[128:131], a[80:83], v[96:99]\n"
"ds_read_b128 a[120:123], v252 offset:4096\n"
"v_mfma_f32_16x16x32_bf16 v[96:99], a[132:135], a[84:87], v[96:99]\n"
"ds_read_b128 a[124:127], v252 offset:9216\n"
"s_add_u32 s72, s61, 983040\n"
"s_add_u32 m0, s6, 40960\n"
"s_waitcnt lgkmcnt(6)\n"
"v_mfma_f32_16x16x32_bf16 v[96:99], a[136:139], a[88:91], v[96:99]\n"
"ds_read_b128 a[128:131], v251 offset:60416\n"
"buffer_load_dwordx4 v244, s[8:11], s72 offen offset:0 sc1 lds\n"
"v_mfma_f32_16x16x32_bf16 v[96:99], a[140:143], a[92:95], v[96:99]\n"
"ds_read_b128 a[132:135], v252 offset:0\n"
"v_mfma_f32_16x16x32_bf16 v[100:103], a[96:99], a[80:83], v[100:103]\n"
"ds_read_b128 a[136:139], v252 offset:5120\n"
"buffer_load_dwordx4 v244, s[8:11], s72 offen offset:1024 sc1 lds\n"
"v_mfma_f32_16x16x32_bf16 v[100:103], a[100:103], a[84:87], v[100:103]\n"
"ds_read_b128 a[140:143], v252 offset:10240\n"
"s_waitcnt lgkmcnt(6)\n"
"v_mfma_f32_16x16x32_bf16 v[100:103], a[104:107], a[88:91], v[100:103]\n"
"ds_read_b128 a[96:99], v251 offset:61440\n"
"buffer_load_dwordx4 v244, s[8:11], s72 offen offset:2048 sc1 lds\n"
"v_mfma_f32_16x16x32_bf16 v[100:103], a[108:111], a[92:95], v[100:103]\n"
"s_barrier\n"
"v_mfma_f32_16x16x32_bf16 v[104:107], a[112:115], a[80:83], v[104:107]\n"
"ds_read_b128 a[100:103], v252 offset:1024\n"
"buffer_load_dwordx4 v244, s[8:11], s72 offen offset:3072 sc1 lds\n"
"v_mfma_f32_16x16x32_bf16 v[104:107], a[116:119], a[84:87], v[104:107]\n"
"ds_read_b128 a[104:107], v252 offset:6144\n"
"s_waitcnt lgkmcnt(5)\n"
"v_mfma_f32_16x16x32_bf16 v[104:107], a[120:123], a[88:91], v[104:107]\n"
"ds_read_b128 a[108:111], v252 offset:11264\n"
"s_add_u32 m0, s6, 45056\n"
"v_mfma_f32_16x16x32_bf16 v[104:107], a[124:127], a[92:95], v[104:107]\n"
"ds_read_b128 a[112:115], v252 offset:12288\n"
"buffer_load_dwordx4 v250, s[8:11], s72 offen offset:0 sc1 lds\n"
"v_mfma_f32_16x16x32_bf16 v[108:111], a[128:131], a[80:83], v[108:111]\n"
"ds_read_b128 a[116:119], v252 offset:17408\n"
"v_mfma_f32_16x16x32_bf16 v[108:111], a[132:135], a[84:87], v[108:111]\n"
"ds_read_b128 a[120:123], v252 offset:22528\n"
"s_add_u32 s71, s62, 24576\n"
"buffer_load_dwordx4 a[192:195], v244, s[8:11], s71 offen offset:0 sc1\n"
"s_waitcnt lgkmcnt(6)\n"
"v_mfma_f32_16x16x32_bf16 v[108:111], a[136:139], a[88:91], v[108:111]\n"
"ds_read_b128 a[124:127], v252 offset:27648\n"
"v_mfma_f32_16x16x32_bf16 v[108:111], a[140:143], a[92:95], v[108:111]\n"
"ds_read_b128 a[128:131], v252 offset:13312\n"
"buffer_load_dwordx4 a[196:199], v244, s[8:11], s71 offen offset:1024 sc1\n"
"v_mfma_f32_16x16x32_bf16 v[152:155], a[96:99], a[80:83], v[152:155]\n"
"ds_read_b128 a[132:135], v252 offset:18432\n"
"s_waitcnt lgkmcnt(6)\n"
"v_mfma_f32_16x16x32_bf16 v[152:155], a[100:103], a[84:87], v[152:155]\n"
"ds_read_b128 a[136:139], v252 offset:23552\n"
"buffer_load_dwordx4 a[200:203], v244, s[8:11], s71 offen offset:2048 sc1\n"
"v_mfma_f32_16x16x32_bf16 v[152:155], a[104:107], a[88:91], v[152:155]\n"
"ds_read_b128 a[140:143], v252 offset:28672\n"
"v_mfma_f32_16x16x32_bf16 v[152:155], a[108:111], a[92:95], v[152:155]\n"
"ds_read_b128 a[96:99], v252 offset:14336\n"
"buffer_load_dwordx4 a[204:207], v244, s[8:11], s71 offen offset:3072 sc1\n"
"s_waitcnt lgkmcnt(6)\n"
"v_mfma_f32_16x16x32_bf16 v[96:99], a[112:115], a[144:147], v[96:99]\n"
"ds_read_b128 a[100:103], v252 offset:19456\n"
"s_waitcnt vmcnt(11)\n"
"v_cmp_gt_u32 vcc, 8, v249\n"
"s_cbranch_vccnz L_spin_p0_%=\n"
"L_back_p0_%=:\n"
"v_mfma_f32_16x16x32_bf16 v[96:99], a[116:119], a[148:151], v[96:99]\n"
"ds_read_b128 a[104:107], v252 offset:24576\n"
"s_add_u32 s70, s67, 0\n"
"buffer_load_dwordx4 a[0:3], v244, s[8:11], s70 offen offset:0 sc1\n"
"v_mfma_f32_16x16x32_bf16 v[96:99], a[120:123], a[152:155], v[96:99]\n"
"ds_read_b128 a[108:111], v252 offset:29696\n"
"s_waitcnt lgkmcnt(6)\n"
"v_mfma_f32_16x16x32_bf16 v[96:99], a[124:127], a[156:159], v[96:99]\n"
"ds_read_b128 a[112:115], v252 offset:15360\n"
"s_add_u32 s71, s67, 2621440\n"
"buffer_load_dwordx4 a[4:7], v244, s[8:11], s71 offen offset:0 sc1\n"
"v_mfma_f32_16x16x32_bf16 v[100:103], a[128:131], a[144:147], v[100:103]\n"
"ds_read_b128 a[116:119], v252 offset:20480\n"
"v_mfma_f32_16x16x32_bf16 v[100:103], a[132:135], a[148:151], v[100:103]\n"
"ds_read_b128 a[120:123], v252 offset:25600\n"
"s_add_u32 s70, s67, 5242880\n"
"buffer_load_dwordx4 a[8:11], v244, s[8:11], s70 offen offset:0 sc1\n"
"s_waitcnt lgkmcnt(6)\n"
"v_mfma_f32_16x16x32_bf16 v[100:103], a[136:139], a[152:155], v[100:103]\n"
"ds_read_b128 a[124:127], v252 offset:30720\n"
"v_mfma_f32_16x16x32_bf16 v[100:103], a[140:143], a[156:159], v[100:103]\n"
"s_barrier\n"
"s_add_u32 s72, s61, 1146880\n"
"s_add_u32 m0, s6, 61440\n"
"s_add_u32 s59, s65, 768\n"
"buffer_atomic_add v246, v245, s[8:11], s59 offen sc1\n"
"v_mfma_f32_16x16x32_bf16 v[104:107], a[96:99], a[144:147], v[104:107]\n"
"ds_read_b128 a[128:131], v252 offset:16384\n"
"buffer_load_dwordx4 v244, s[8:11], s72 offen offset:0 sc1 lds\n"
"s_waitcnt lgkmcnt(5)\n"
"v_mfma_f32_16x16x32_bf16 v[104:107], a[100:103], a[148:151], v[104:107]\n"
"ds_read_b128 a[132:135], v252 offset:21504\n"
"v_mfma_f32_16x16x32_bf16 v[104:107], a[104:107], a[152:155], v[104:107]\n"
"ds_read_b128 a[136:139], v252 offset:26624\n"
"buffer_load_dwordx4 v244, s[8:11], s72 offen offset:1024 sc1 lds\n"
"v_mfma_f32_16x16x32_bf16 v[104:107], a[108:111], a[156:159], v[104:107]\n"
"ds_read_b128 a[140:143], v252 offset:31744\n"
"s_waitcnt lgkmcnt(5)\n"
"v_mfma_f32_16x16x32_bf16 v[108:111], a[112:115], a[144:147], v[108:111]\n"
"ds_read_b128 a[96:99], v244 offset:0\n"
"buffer_load_dwordx4 v244, s[8:11], s72 offen offset:2048 sc1 lds\n"
"v_mfma_f32_16x16x32_bf16 v[108:111], a[116:119], a[148:151], v[108:111]\n"
"ds_read_b128 a[100:103], v244 offset:5120\n"
"v_mfma_f32_16x16x32_bf16 v[108:111], a[120:123], a[152:155], v[108:111]\n"
"ds_read_b128 a[104:107], v244 offset:10240\n"
"buffer_load_dwordx4 v244, s[8:11], s72 offen offset:3072 sc1 lds\n"
"s_waitcnt lgkmcnt(6)\n"
"v_mfma_f32_16x16x32_bf16 v[108:111], a[124:127], a[156:159], v[108:111]\n"
"ds_read_b128 a[108:111], v244 offset:15360\n"
"v_mfma_f32_16x16x32_bf16 v[152:155], a[128:131], a[144:147], v[152:155]\n"
"ds_read_b128 a[112:115], v244 offset:1024\n"
"s_add_u32 m0, s6, 65536\n"
"s_waitcnt lgkmcnt(6)\n"
"v_mfma_f32_16x16x32_bf16 v[152:155], a[132:135], a[148:151], v[152:155]\n"
"ds_read_b128 a[116:119], v244 offset:6144\n"
"buffer_load_dwordx4 v250, s[8:11], s72 offen offset:0 sc1 lds\n"
"v_mfma_f32_16x16x32_bf16 v[152:155], a[136:139], a[152:155], v[152:155]\n"
"ds_read_b128 a[120:123], v244 offset:11264\n"
"s_waitcnt lgkmcnt(6)\n"
"v_mfma_f32_16x16x32_bf16 v[152:155], a[140:143], a[156:159], v[152:155]\n"
"ds_read_b128 a[124:127], v244 offset:16384\n"
"s_add_u32 s71, s67, 7864320\n"
"buffer_load_dwordx4 a[12:15], v244, s[8:11], s71 offen offset:0 sc1\n"
"v_mfma_f32_16x16x32_bf16 v[96:99], a[96:99], a[160:163], v[96:99]\n"
"ds_read_b128 a[128:131], v244 offset:2048\n"
"s_waitcnt lgkmcnt(6)\n"
"v_mfma_f32_16x16x32_bf16 v[96:99], a[100:103], a[164:167], v[96:99]\n"
"ds_read_b128 a[132:135], v244 offset:7168\n"
"s_add_u32 s70, s67, 10485760\n"
"buffer_load_dwordx4 a[16:19], v244, s[8:11], s70 offen offset:0 sc1\n"
"v_mfma_f32_16x16x32_bf16 v[96:99], a[104:107], a[168:171], v[96:99]\n"
"ds_read_b128 a[136:139], v244 offset:12288\n"
"s_waitcnt lgkmcnt(6)\n"
"v_mfma_f32_16x16x32_bf16 v[96:99], a[108:111], a[172:175], v[96:99]\n"
"ds_read_b128 a[140:143], v244 offset:17408\n"
"s_add_u32 s71, s67, 13107200\n"
"buffer_load_dwordx4 a[20:23], v244, s[8:11], s71 offen offset:0 sc1\n"
"v_mfma_f32_16x16x32_bf16 v[100:103], a[112:115], a[160:163], v[100:103]\n"
"ds_read_b128 a[96:99], v244 offset:3072\n"
"s_waitcnt lgkmcnt(6)\n"
"v_mfma_f32_16x16x32_bf16 v[100:103], a[116:119], a[164:167], v[100:103]\n"
"ds_read_b128 a[100:103], v244 offset:8192\n"
"s_add_u32 s70, s67, 15728640\n"
"buffer_load_dwordx4 a[24:27], v244, s[8:11], s70 offen offset:0 sc1\n"
"v_mfma_f32_16x16x32_bf16 v[100:103], a[120:123], a[168:171], v[100:103]\n"
"ds_read_b128 a[104:107], v244 offset:13312\n"
"s_waitcnt lgkmcnt(6)\n"
"v_mfma_f32_16x16x32_bf16 v[100:103], a[124:127], a[172:175], v[100:103]\n"
"s_barrier\n"
"v_mfma_f32_16x16x32_bf16 v[104:107], a[128:131], a[160:163], v[104:107]\n"
"ds_read_b128 a[108:111], v244 offset:18432\n"
"s_add_u32 s71, s67, 18350080\n"
"buffer_load_dwordx4 a[28:31], v244, s[8:11], s71 offen offset:0 sc1\n"
"s_waitcnt lgkmcnt(5)\n"
"v_mfma_f32_16x16x32_bf16 v[104:107], a[132:135], a[164:167], v[104:107]\n"
"ds_read_b128 a[112:115], v244 offset:4096\n"
"v_mfma_f32_16x16x32_bf16 v[104:107], a[136:139], a[168:171], v[104:107]\n"
"ds_read_b128 a[116:119], v244 offset:9216\n"
"buffer_load_dwordx2 v[128:129], v208, s[20:23], s68 offen offset:0\n"
"s_waitcnt lgkmcnt(5)\n"
"v_mfma_f32_16x16x32_bf16 v[104:107], a[140:143], a[172:175], v[104:107]\n"
"ds_read_b128 a[120:123], v244 offset:14336\n"
"v_mfma_f32_16x16x32_bf16 v[108:111], a[96:99], a[160:163], v[108:111]\n"
"ds_read_b128 a[124:127], v244 offset:19456\n"
"buffer_load_dwordx2 v[130:131], v208, s[20:23], s68 offen offset:32\n"
"s_waitcnt lgkmcnt(5)\n"
"v_mfma_f32_16x16x32_bf16 v[108:111], a[100:103], a[164:167], v[108:111]\n"
"ds_read_b128 a[128:131], v244 offset:20480\n"
"v_mfma_f32_16x16x32_bf16 v[108:111], a[104:107], a[168:171], v[108:111]\n"
"ds_read_b128 a[132:135], v244 offset:25600\n"
"buffer_load_dwordx2 v[132:133], v208, s[20:23], s68 offen offset:64\n"
"s_waitcnt lgkmcnt(6)\n"
"v_mfma_f32_16x16x32_bf16 v[108:111], a[108:111], a[172:175], v[108:111]\n"
"ds_read_b128 a[136:139], v244 offset:30720\n"
"s_waitcnt lgkmcnt(6)\n"
"v_mfma_f32_16x16x32_bf16 v[152:155], a[112:115], a[160:163], v[152:155]\n"
"ds_read_b128 a[140:143], v244 offset:35840\n"
"buffer_load_dwordx2 v[134:135], v208, s[20:23], s68 offen offset:96\n"
"s_waitcnt lgkmcnt(6)\n"
"v_mfma_f32_16x16x32_bf16 v[152:155], a[116:119], a[164:167], v[152:155]\n"
"ds_read_b128 a[96:99], v244 offset:21504\n"
"s_waitcnt lgkmcnt(6)\n"
"v_mfma_f32_16x16x32_bf16 v[152:155], a[120:123], a[168:171], v[152:155]\n"
"ds_read_b128 a[100:103], v244 offset:26624\n"
"buffer_load_dwordx2 v[136:137], v208, s[20:23], s68 offen offset:128\n"
"s_waitcnt lgkmcnt(6)\n"
"v_mfma_f32_16x16x32_bf16 v[152:155], a[124:127], a[172:175], v[152:155]\n"
"ds_read_b128 a[104:107], v244 offset:31744\n"
"s_waitcnt lgkmcnt(6)\n"
"v_mfma_f32_16x16x32_bf16 v[96:99], a[128:131], a[176:179], v[96:99]\n"
"ds_read_b128 a[108:111], v244 offset:36864\n"
"v_cmp_eq_u32 vcc, 0, v209\n"
"s_nop 1\n"
"v_cndmask_b32 v66, 0, v210, vcc\n"
"s_add_u32 s70, s62, 24576\n"
"buffer_load_dwordx4 a[32:35], v250, s[8:11], s70 offen offset:0 sc1\n"
"s_waitcnt lgkmcnt(6)\n"
"v_mfma_f32_16x16x32_bf16 v[96:99], a[132:135], a[180:183], v[96:99]\n"
"ds_read_b128 a[112:115], v244 offset:22528\n"
"v_cmp_eq_u32 vcc, 1, v209\n"
"s_nop 1\n"
"v_cndmask_b32 v67, 0, v210, vcc\n"
"s_waitcnt vmcnt(29)\n"
"s_waitcnt lgkmcnt(6)\n"
"v_mfma_f32_16x16x32_bf16 v[96:99], a[136:139], a[184:187], v[96:99]\n"
"ds_read_b128 a[116:119], v244 offset:27648\n"
"v_mov_b32 v64, 0\n"
"v_mov_b32 v65, 0\n"
"v_mov_b32 v68, 0\n"
"buffer_load_dwordx4 a[36:39], v250, s[8:11], s70 offen offset:1024 sc1\n"
"s_waitcnt lgkmcnt(6)\n"
"v_mfma_f32_16x16x32_bf16 v[96:99], a[140:143], a[188:191], v[96:99]\n"
"ds_read_b128 a[120:123], v244 offset:32768\n"
"v_mov_b32 v69, 0\n"
"v_mov_b32 v72, v66\n"
"v_mov_b32 v73, v67\n"
"s_waitcnt lgkmcnt(6)\n"
"v_mfma_f32_16x16x32_bf16 v[100:103], a[96:99], a[176:179], v[100:103]\n"
"ds_read_b128 a[124:127], v244 offset:37888\n"
"v_mov_b32 v74, v66\n"
"v_mov_b32 v75, v67\n"
"s_waitcnt lgkmcnt(6)\n"
"v_mfma_f32_16x16x32_bf16 v[100:103], a[100:103], a[180:183], v[100:103]\n"
"ds_read_b128 a[128:131], v244 offset:23552\n"
"buffer_load_dwordx4 a[40:43], v250, s[8:11], s70 offen offset:2048 sc1\n"
"s_waitcnt lgkmcnt(6)\n"
"v_mfma_f32_16x16x32_bf16 v[100:103], a[104:107], a[184:187], v[100:103]\n"
"ds_read_b128 a[132:135], v244 offset:28672\n"
"ds_read_b128 a[136:139], v244 offset:33792\n"
"s_add_u32 s59, s66, 640\n"
"buffer_load_dword v249, off, s[8:11], s59 sc1\n"
"s_waitcnt lgkmcnt(7)\n"
"v_mfma_f32_16x16x32_bf16 v[100:103], a[108:111], a[188:191], v[100:103]\n"
"s_waitcnt vmcnt(27)\n"
"s_barrier\n"
"s_waitcnt lgkmcnt(6)\n"
"v_mfma_f32_16x16x32_bf16 v[104:107], a[112:115], a[176:179], v[104:107]\n"
"ds_read_b128 a[140:143], v244 offset:38912\n"
"buffer_load_dwordx4 a[44:47], v250, s[8:11], s70 offen offset:3072 sc1\n"
"s_waitcnt lgkmcnt(6)\n"
"v_mfma_f32_16x16x32_bf16 v[104:107], a[116:119], a[180:183], v[104:107]\n"
"ds_read_b128 a[96:99], v244 offset:24576\n"
"s_waitcnt lgkmcnt(6)\n"
"v_mfma_f32_16x16x32_bf16 v[104:107], a[120:123], a[184:187], v[104:107]\n"
"ds_read_b128 a[100:103], v244 offset:29696\n"
"s_add_u32 s72, s61, 1310720\n"
"s_add_u32 m0, s6, 81920\n"
"s_waitcnt lgkmcnt(6)\n"
"v_mfma_f32_16x16x32_bf16 v[104:107], a[124:127], a[188:191], v[104:107]\n"
"ds_read_b128 a[104:107], v244 offset:34816\n"
"buffer_load_dwordx4 v244, s[8:11], s72 offen offset:0 sc1 lds\n"
"s_waitcnt lgkmcnt(6)\n"
"v_mfma_f32_16x16x32_bf16 v[108:111], a[128:131], a[176:179], v[108:111]\n"
"ds_read_b128 a[108:111], v244 offset:39936\n"
"s_waitcnt lgkmcnt(5)\n"
"v_mfma_f32_16x16x32_bf16 v[108:111], a[132:135], a[180:183], v[108:111]\n"
"ds_read_b128 a[112:115], v244 offset:40960\n"
"buffer_load_dwordx4 v244, s[8:11], s72 offen offset:1024 sc1 lds\n"
"v_mfma_f32_16x16x32_bf16 v[108:111], a[136:139], a[184:187], v[108:111]\n"
"ds_read_b128 a[116:119], v244 offset:46080\n"
"s_waitcnt lgkmcnt(6)\n"
"v_mfma_f32_16x16x32_bf16 v[108:111], a[140:143], a[188:191], v[108:111]\n"
"ds_read_b128 a[120:123], v244 offset:51200\n"
"buffer_load_dwordx4 v244, s[8:11], s72 offen offset:2048 sc1 lds\n"
"s_waitcnt lgkmcnt(6)\n"
"v_mfma_f32_16x16x32_bf16 v[152:155], a[96:99], a[176:179], v[152:155]\n"
"ds_read_b128 a[124:127], v244 offset:56320\n"
"s_waitcnt lgkmcnt(6)\n"
"v_mfma_f32_16x16x32_bf16 v[152:155], a[100:103], a[180:183], v[152:155]\n"
"ds_read_b128 a[128:131], v244 offset:41984\n"
"buffer_load_dwordx4 v244, s[8:11], s72 offen offset:3072 sc1 lds\n"
"s_waitcnt lgkmcnt(6)\n"
"v_mfma_f32_16x16x32_bf16 v[152:155], a[104:107], a[184:187], v[152:155]\n"
"ds_read_b128 a[132:135], v244 offset:47104\n"
"s_waitcnt lgkmcnt(6)\n"
"v_mfma_f32_16x16x32_bf16 v[152:155], a[108:111], a[188:191], v[152:155]\n"
"ds_read_b128 a[136:139], v244 offset:52224\n"
"s_add_u32 m0, s6, 86016\n"
"s_waitcnt vmcnt(28)\n"
"s_waitcnt lgkmcnt(6)\n"
"v_mfma_f32_16x16x32_bf16 v[96:99], a[112:115], a[192:195], v[96:99]\n"
"ds_read_b128 a[140:143], v244 offset:57344\n"
"buffer_load_dwordx4 v250, s[8:11], s72 offen offset:0 sc1 lds\n"
"s_waitcnt lgkmcnt(6)\n"
"v_mfma_f32_16x16x32_bf16 v[96:99], a[116:119], a[196:199], v[96:99]\n"
"ds_read_b128 a[96:99], v244 offset:43008\n"
"s_waitcnt lgkmcnt(6)\n"
"v_mfma_f32_16x16x32_bf16 v[96:99], a[120:123], a[200:203], v[96:99]\n"
"ds_read_b128 a[100:103], v244 offset:48128\n"
"s_add_u32 s71, s62, 32768\n"
"buffer_load_dwordx4 a[48:51], v244, s[8:11], s71 offen offset:0 sc1\n"
"s_waitcnt lgkmcnt(6)\n"
"v_mfma_f32_16x16x32_bf16 v[96:99], a[124:127], a[204:207], v[96:99]\n"
"ds_read_b128 a[104:107], v244 offset:53248\n"
"s_waitcnt lgkmcnt(6)\n"
"v_mfma_f32_16x16x32_bf16 v[100:103], a[128:131], a[192:195], v[100:103]\n"
"ds_read_b128 a[108:111], v244 offset:58368\n"
"buffer_load_dwordx4 a[52:55], v244, s[8:11], s71 offen offset:1024 sc1\n"
"s_waitcnt lgkmcnt(6)\n"
"v_mfma_f32_16x16x32_bf16 v[100:103], a[132:135], a[196:199], v[100:103]\n"
"ds_read_b128 a[112:115], v244 offset:44032\n"
"s_waitcnt lgkmcnt(6)\n"
"v_mfma_f32_16x16x32_bf16 v[100:103], a[136:139], a[200:203], v[100:103]\n"
"ds_read_b128 a[116:119], v244 offset:49152\n"
"buffer_load_dwordx4 a[56:59], v244, s[8:11], s71 offen offset:2048 sc1\n"
"s_waitcnt lgkmcnt(6)\n"
"v_mfma_f32_16x16x32_bf16 v[100:103], a[140:143], a[204:207], v[100:103]\n"
"ds_read_b128 a[120:123], v244 offset:54272\n"
"s_waitcnt lgkmcnt(6)\n"
"v_mfma_f32_16x16x32_bf16 v[104:107], a[96:99], a[192:195], v[104:107]\n"
"ds_read_b128 a[124:127], v244 offset:59392\n"
"buffer_load_dwordx4 a[60:63], v244, s[8:11], s71 offen offset:3072 sc1\n"
"s_waitcnt lgkmcnt(6)\n"
"v_mfma_f32_16x16x32_bf16 v[104:107], a[100:103], a[196:199], v[104:107]\n"
"ds_read_b128 a[128:131], v244 offset:45056\n"
"s_waitcnt lgkmcnt(6)\n"
"v_mfma_f32_16x16x32_bf16 v[104:107], a[104:107], a[200:203], v[104:107]\n"
"ds_read_b128 a[132:135], v244 offset:50176\n"
"s_waitcnt vmcnt(10)\n"
"v_cmp_gt_u32 vcc, 8, v249\n"
"s_cbranch_vccnz L_spin_p1_%=\n"
"L_back_p1_%=:\n"
"s_add_u32 s72, s61, 1474560\n"
"s_add_u32 m0, s6, 102400\n"
"s_waitcnt lgkmcnt(6)\n"
"v_mfma_f32_16x16x32_bf16 v[104:107], a[108:111], a[204:207], v[104:107]\n"
"ds_read_b128 a[136:139], v244 offset:55296\n"
"buffer_load_dwordx4 v244, s[8:11], s72 offen offset:0 sc1 lds\n"
"s_waitcnt lgkmcnt(6)\n"
"v_mfma_f32_16x16x32_bf16 v[108:111], a[112:115], a[192:195], v[108:111]\n"
"ds_read_b128 a[140:143], v244 offset:60416\n"
"s_waitcnt lgkmcnt(6)\n"
"v_mfma_f32_16x16x32_bf16 v[108:111], a[116:119], a[196:199], v[108:111]\n"
"buffer_load_dwordx4 v244, s[8:11], s72 offen offset:1024 sc1 lds\n"
"s_waitcnt lgkmcnt(5)\n"
"v_mfma_f32_16x16x32_bf16 v[108:111], a[120:123], a[200:203], v[108:111]\n"
"s_waitcnt lgkmcnt(4)\n"
"v_mfma_f32_16x16x32_bf16 v[108:111], a[124:127], a[204:207], v[108:111]\n"
"buffer_load_dwordx4 v244, s[8:11], s72 offen offset:2048 sc1 lds\n"
"s_waitcnt lgkmcnt(3)\n"
"v_mfma_f32_16x16x32_bf16 v[152:155], a[128:131], a[192:195], v[152:155]\n"
"s_waitcnt lgkmcnt(2)\n"
"v_mfma_f32_16x16x32_bf16 v[152:155], a[132:135], a[196:199], v[152:155]\n"
"buffer_load_dwordx4 v244, s[8:11], s72 offen offset:3072 sc1 lds\n"
"s_waitcnt lgkmcnt(1)\n"
"v_mfma_f32_16x16x32_bf16 v[152:155], a[136:139], a[200:203], v[152:155]\n"
"s_waitcnt lgkmcnt(0)\n"
"v_mfma_f32_16x16x32_bf16 v[152:155], a[140:143], a[204:207], v[152:155]\n"
"s_add_u32 m0, s6, 106496\n"
"v_mfma_f32_16x16x32_bf16 v[0:3], v[66:69], a[0:3], 0\n"
"buffer_load_dwordx4 v250, s[8:11], s72 offen offset:0 sc1 lds\n"
"v_mfma_f32_16x16x32_bf16 v[0:3], v[66:69], a[4:7], v[0:3]\n"
"v_mfma_f32_16x16x32_bf16 v[0:3], v[66:69], a[8:11], v[0:3]\n"
"buffer_load_dwordx4 a[64:67], v250, s[8:11], s71 offen offset:0 sc1\n"
"v_mfma_f32_16x16x32_bf16 v[0:3], v[66:69], a[12:15], v[0:3]\n"
"s_barrier\n"
"v_mfma_f32_16x16x32_bf16 v[0:3], v[66:69], a[16:19], v[0:3]\n"
"ds_read_b128 a[96:99], v244 offset:61440\n"
"buffer_load_dwordx4 a[68:71], v250, s[8:11], s71 offen offset:1024 sc1\n"
"v_mfma_f32_16x16x32_bf16 v[0:3], v[66:69], a[20:23], v[0:3]\n"
"ds_read_b128 a[100:103], v251 offset:1024\n"
"v_mfma_f32_16x16x32_bf16 v[0:3], v[66:69], a[24:27], v[0:3]\n"
"ds_read_b128 a[104:107], v251 offset:6144\n"
"buffer_load_dwordx4 a[72:75], v250, s[8:11], s71 offen offset:2048 sc1\n"
"v_mfma_f32_16x16x32_bf16 v[0:3], v[66:69], a[28:31], v[0:3]\n"
"ds_read_b128 a[108:111], v251 offset:11264\n"
"v_mfma_f32_16x16x32_bf16 v[4:7], v[64:67], a[0:3], 0\n"
"ds_read_b128 a[112:115], v244 offset:62464\n"
"buffer_load_dwordx4 a[76:79], v250, s[8:11], s71 offen offset:3072 sc1\n"
"v_mfma_f32_16x16x32_bf16 v[4:7], v[64:67], a[4:7], v[4:7]\n"
"ds_read_b128 a[116:119], v251 offset:2048\n"
"v_mfma_f32_16x16x32_bf16 v[4:7], v[64:67], a[8:11], v[4:7]\n"
"ds_read_b128 a[120:123], v251 offset:7168\n"
"s_add_u32 s70, s67, 0\n"
"buffer_load_dwordx4 a[80:83], v244, s[8:11], s70 offen offset:1024 sc1\n"
"v_mfma_f32_16x16x32_bf16 v[4:7], v[64:67], a[12:15], v[4:7]\n"
"ds_read_b128 a[124:127], v251 offset:12288\n"
"v_mfma_f32_16x16x32_bf16 v[4:7], v[64:67], a[16:19], v[4:7]\n"
"ds_read_b128 a[128:131], v244 offset:63488\n"
"s_add_u32 s71, s67, 2621440\n"
"buffer_load_dwordx4 a[84:87], v244, s[8:11], s71 offen offset:1024 sc1\n"
"v_mfma_f32_16x16x32_bf16 v[4:7], v[64:67], a[20:23], v[4:7]\n"
"ds_read_b128 a[132:135], v251 offset:3072\n"
"v_mfma_f32_16x16x32_bf16 v[4:7], v[64:67], a[24:27], v[4:7]\n"
"ds_read_b128 a[136:139], v251 offset:8192\n"
"s_add_u32 s70, s67, 5242880\n"
"buffer_load_dwordx4 a[88:91], v244, s[8:11], s70 offen offset:1024 sc1\n"
"v_mfma_f32_16x16x32_bf16 v[4:7], v[64:67], a[28:31], v[4:7]\n"
"ds_read_b128 a[140:143], v251 offset:13312\n"
"s_waitcnt lgkmcnt(6)\n"
"v_mfma_f32_16x16x32_bf16 v[96:99], a[96:99], a[32:35], v[96:99]\n"
"ds_read_b128 a[96:99], v244 offset:64512\n"
"s_add_u32 s72, s61, 1638400\n"
"s_add_u32 m0, s6, 122880\n"
"v_mfma_f32_16x16x32_bf16 v[96:99], a[100:103], a[36:39], v[96:99]\n"
"ds_read_b128 a[100:103], v251 offset:4096\n"
"buffer_load_dwordx4 v244, s[8:11], s72 offen offset:0 sc1 lds\n"
"v_mfma_f32_16x16x32_bf16 v[96:99], a[104:107], a[40:43], v[96:99]\n"
"ds_read_b128 a[104:107], v251 offset:9216\n"
"s_waitcnt vmcnt(22)\n"
"v_mfma_f32_16x16x32_bf16 v[96:99], a[108:111], a[44:47], v[96:99]\n"
"ds_read_b128 a[108:111], v251 offset:14336\n"
"buffer_load_dwordx4 v244, s[8:11], s72 offen offset:1024 sc1 lds\n"
"v_mfma_f32_16x16x32_bf16 v[100:103], a[112:115], a[32:35], v[100:103]\n"
"ds_read_b128 a[112:115], v251 offset:0\n"
"v_mfma_f32_16x16x32_bf16 v[100:103], a[116:119], a[36:39], v[100:103]\n"
"ds_read_b128 a[116:119], v251 offset:5120\n"
"buffer_load_dwordx4 v244, s[8:11], s72 offen offset:2048 sc1 lds\n"
"s_waitcnt lgkmcnt(6)\n"
"v_mfma_f32_16x16x32_bf16 v[100:103], a[120:123], a[40:43], v[100:103]\n"
"ds_read_b128 a[120:123], v251 offset:10240\n"
"v_mfma_f32_16x16x32_bf16 v[100:103], a[124:127], a[44:47], v[100:103]\n"
"s_waitcnt vmcnt(19)\n"
"s_barrier\n"
"v_mfma_f32_16x16x32_bf16 v[104:107], a[128:131], a[32:35], v[104:107]\n"
"ds_read_b128 a[124:127], v251 offset:15360\n"
"buffer_load_dwordx4 v244, s[8:11], s72 offen offset:3072 sc1 lds\n"
"v_mfma_f32_16x16x32_bf16 v[104:107], a[132:135], a[36:39], v[104:107]\n"
"ds_read_b128 a[128:131], v251 offset:16384\n"
"v_mfma_f32_16x16x32_bf16 v[104:107], a[136:139], a[40:43], v[104:107]\n"
"ds_read_b128 a[132:135], v251 offset:21504\n"
"s_add_u32 m0, s6, 126976\n"
"v_mfma_f32_16x16x32_bf16 v[104:107], a[140:143], a[44:47], v[104:107]\n"
"ds_read_b128 a[136:139], v251 offset:26624\n"
"buffer_load_dwordx4 v250, s[8:11], s72 offen offset:0 sc1 lds\n"
"s_waitcnt lgkmcnt(5)\n"
"v_mfma_f32_16x16x32_bf16 v[108:111], a[96:99], a[32:35], v[108:111]\n"
"ds_read_b128 a[140:143], v251 offset:31744\n"
"v_mfma_f32_16x16x32_bf16 v[108:111], a[100:103], a[36:39], v[108:111]\n"
"ds_read_b128 a[96:99], v251 offset:17408\n"
"s_add_u32 s71, s67, 7864320\n"
"buffer_load_dwordx4 a[92:95], v244, s[8:11], s71 offen offset:1024 sc1\n"
"v_mfma_f32_16x16x32_bf16 v[108:111], a[104:107], a[40:43], v[108:111]\n"
"ds_read_b128 a[100:103], v251 offset:22528\n"
"v_mfma_f32_16x16x32_bf16 v[108:111], a[108:111], a[44:47], v[108:111]\n"
"ds_read_b128 a[104:107], v251 offset:27648\n"
"s_add_u32 s70, s67, 10485760\n"
"buffer_load_dwordx4 a[144:147], v244, s[8:11], s70 offen offset:1024 sc1\n"
"v_mfma_f32_16x16x32_bf16 v[152:155], a[112:115], a[32:35], v[152:155]\n"
"ds_read_b128 a[108:111], v251 offset:32768\n"
"v_mfma_f32_16x16x32_bf16 v[152:155], a[116:119], a[36:39], v[152:155]\n"
"ds_read_b128 a[112:115], v251 offset:18432\n"
"s_add_u32 s71, s67, 13107200\n"
"buffer_load_dwordx4 a[148:151], v244, s[8:11], s71 offen offset:1024 sc1\n"
"s_waitcnt lgkmcnt(6)\n"
"v_mfma_f32_16x16x32_bf16 v[152:155], a[120:123], a[40:43], v[152:155]\n"
"ds_read_b128 a[116:119], v251 offset:23552\n"
"v_mfma_f32_16x16x32_bf16 v[152:155], a[124:127], a[44:47], v[152:155]\n"
"ds_read_b128 a[120:123], v251 offset:28672\n"
"s_add_u32 s70, s67, 15728640\n"
"buffer_load_dwordx4 a[152:155], v244, s[8:11], s70 offen offset:1024 sc1\n"
"s_waitcnt vmcnt(21)\n"
"v_mfma_f32_16x16x32_bf16 v[96:99], a[128:131], a[48:51], v[96:99]\n"
"ds_read_b128 a[124:127], v251 offset:33792\n"
"v_mfma_f32_16x16x32_bf16 v[96:99], a[132:135], a[52:55], v[96:99]\n"
"ds_read_b128 a[128:131], v251 offset:19456\n"
"s_add_u32 s71, s67, 18350080\n"
"buffer_load_dwordx4 a[156:159], v244, s[8:11], s71 offen offset:1024 sc1\n"
"v_mfma_f32_16x16x32_bf16 v[96:99], a[136:139], a[56:59], v[96:99]\n"
"ds_read_b128 a[132:135], v251 offset:24576\n"
"s_waitcnt lgkmcnt(6)\n"
"v_mfma_f32_16x16x32_bf16 v[96:99], a[140:143], a[60:63], v[96:99]\n"
"ds_read_b128 a[136:139], v251 offset:29696\n"
"s_add_u32 s70, s62, 40960\n"
"buffer_load_dwordx4 a[160:163], v244, s[8:11], s70 offen offset:0 sc1\n"
"v_mfma_f32_16x16x32_bf16 v[100:103], a[96:99], a[48:51], v[100:103]\n"
"ds_read_b128 a[140:143], v251 offset:34816\n"
"v_mfma_f32_16x16x32_bf16 v[100:103], a[100:103], a[52:55], v[100:103]\n"
"ds_read_b128 a[96:99], v251 offset:20480\n"
"buffer_load_dwordx4 a[164:167], v244, s[8:11], s70 offen offset:1024 sc1\n"
"v_mfma_f32_16x16x32_bf16 v[100:103], a[104:107], a[56:59], v[100:103]\n"
"ds_read_b128 a[100:103], v251 offset:25600\n"
"v_mfma_f32_16x16x32_bf16 v[100:103], a[108:111], a[60:63], v[100:103]\n"
"s_waitcnt vmcnt(19)\n"
"s_barrier\n"
"s_waitcnt lgkmcnt(5)\n"
"v_mfma_f32_16x16x32_bf16 v[104:107], a[112:115], a[48:51], v[104:107]\n"
"ds_read_b128 a[104:107], v251 offset:30720\n"
"buffer_load_dwordx4 a[168:171], v244, s[8:11], s70 offen offset:2048 sc1\n"
"v_mfma_f32_16x16x32_bf16 v[104:107], a[116:119], a[52:55], v[104:107]\n"
"ds_read_b128 a[108:111], v251 offset:35840\n"
"v_mfma_f32_16x16x32_bf16 v[104:107], a[120:123], a[56:59], v[104:107]\n"
"ds_read_b128 a[112:115], v251 offset:36864\n"
"s_add_u32 s59, s66, 768\n"
"buffer_load_dword v249, off, s[8:11], s59 sc1\n"
"v_mfma_f32_16x16x32_bf16 v[104:107], a[124:127], a[60:63], v[104:107]\n"
"ds_read_b128 a[116:119], v251 offset:41984\n"
"buffer_load_dwordx4 a[172:175], v244, s[8:11], s70 offen offset:3072 sc1\n"
"v_mfma_f32_16x16x32_bf16 v[108:111], a[128:131], a[48:51], v[108:111]\n"
"ds_read_b128 a[120:123], v251 offset:47104\n"
"s_waitcnt lgkmcnt(5)\n"
"v_mfma_f32_16x16x32_bf16 v[108:111], a[132:135], a[52:55], v[108:111]\n"
"ds_read_b128 a[124:127], v251 offset:52224\n"
"s_add_u32 s72, s61, 1802240\n"
"s_add_u32 m0, s6, 143360\n"
"v_mfma_f32_16x16x32_bf16 v[108:111], a[136:139], a[56:59], v[108:111]\n"
"ds_read_b128 a[128:131], v251 offset:37888\n"
"buffer_load_dwordx4 v244, s[8:11], s72 offen offset:0 sc1 lds\n"
"v_mfma_f32_16x16x32_bf16 v[108:111], a[140:143], a[60:63], v[108:111]\n"
"ds_read_b128 a[132:135], v251 offset:43008\n"
"v_mfma_f32_16x16x32_bf16 v[152:155], a[96:99], a[48:51], v[152:155]\n"
"ds_read_b128 a[136:139], v251 offset:48128\n"
"buffer_load_dwordx4 v244, s[8:11], s72 offen offset:1024 sc1 lds\n"
"v_mfma_f32_16x16x32_bf16 v[152:155], a[100:103], a[52:55], v[152:155]\n"
"ds_read_b128 a[140:143], v251 offset:53248\n"
"s_waitcnt lgkmcnt(6)\n"
"v_mfma_f32_16x16x32_bf16 v[152:155], a[104:107], a[56:59], v[152:155]\n"
"ds_read_b128 a[96:99], v251 offset:38912\n"
"buffer_load_dwordx4 v244, s[8:11], s72 offen offset:2048 sc1 lds\n"
"v_mfma_f32_16x16x32_bf16 v[152:155], a[108:111], a[60:63], v[152:155]\n"
"ds_read_b128 a[100:103], v251 offset:44032\n"
"ds_bpermute_b32 v128, v212, v128\n"
"ds_bpermute_b32 v129, v212, v129\n"
"s_waitcnt vmcnt(21)\n"
"v_mfma_f32_16x16x32_bf16 v[96:99], a[112:115], a[64:67], v[96:99]\n"
"ds_read_b128 a[104:107], v251 offset:49152\n"
"ds_bpermute_b32 v130, v212, v130\n"
"ds_bpermute_b32 v131, v212, v131\n"
"buffer_load_dwordx4 v244, s[8:11], s72 offen offset:3072 sc1 lds\n"
"v_mfma_f32_16x16x32_bf16 v[96:99], a[116:119], a[68:71], v[96:99]\n"
"ds_read_b128 a[108:111], v251 offset:54272\n"
"ds_bpermute_b32 v132, v212, v132\n"
"ds_bpermute_b32 v133, v212, v133\n"
"s_waitcnt lgkmcnt(12)\n"
"v_mfma_f32_16x16x32_bf16 v[96:99], a[120:123], a[72:75], v[96:99]\n"
"ds_read_b128 a[112:115], v251 offset:39936\n"
"ds_bpermute_b32 v134, v212, v134\n"
"ds_bpermute_b32 v135, v212, v135\n"
"s_add_u32 m0, s6, 147456\n"
"v_mfma_f32_16x16x32_bf16 v[96:99], a[124:127], a[76:79], v[96:99]\n"
"ds_read_b128 a[116:119], v251 offset:45056\n"
"ds_bpermute_b32 v136, v212, v136\n"
"ds_bpermute_b32 v137, v212, v137\n"
"buffer_load_dwordx4 v250, s[8:11], s72 offen offset:0 sc1 lds\n"
"v_mfma_f32_16x16x32_bf16 v[100:103], a[128:131], a[64:67], v[100:103]\n"
"ds_read_b128 a[120:123], v251 offset:50176\n"
"s_waitcnt lgkmcnt(14)\n"
"v_lshlrev_b32 v176, 16, v128\n"
"v_and_b32 v177, 0xffff0000, v128\n"
"v_mfma_f32_16x16x32_bf16 v[100:103], a[132:135], a[68:71], v[100:103]\n"
"ds_read_b128 a[124:127], v251 offset:55296\n"
"s_waitcnt lgkmcnt(14)\n"
"v_lshlrev_b32 v178, 16, v129\n"
"v_and_b32 v179, 0xffff0000, v129\n"
"buffer_load_dwordx4 a[176:179], v250, s[8:11], s70 offen offset:0 sc1\n"
"v_mfma_f32_16x16x32_bf16 v[100:103], a[136:139], a[72:75], v[100:103]\n"
"ds_read_b128 a[128:131], v251 offset:40960\n"
"s_waitcnt lgkmcnt(12)\n"
"v_lshlrev_b32 v180, 16, v130\n"
"v_and_b32 v181, 0xffff0000, v130\n"
"v_mfma_f32_16x16x32_bf16 v[100:103], a[140:143], a[76:79], v[100:103]\n"
"ds_read_b128 a[132:135], v251 offset:46080\n"
"v_lshlrev_b32 v182, 16, v131\n"
"v_and_b32 v183, 0xffff0000, v131\n"
"buffer_load_dwordx4 a[180:183], v250, s[8:11], s70 offen offset:1024 sc1\n"
"v_mfma_f32_16x16x32_bf16 v[104:107], a[96:99], a[64:67], v[104:107]\n"
"ds_read_b128 a[136:139], v251 offset:51200\n"
"s_waitcnt lgkmcnt(8)\n"
"v_lshlrev_b32 v184, 16, v132\n"
"v_and_b32 v185, 0xffff0000, v132\n"
"v_mfma_f32_16x16x32_bf16 v[104:107], a[100:103], a[68:71], v[104:107]\n"
"ds_read_b128 a[140:143], v251 offset:56320\n"
"v_lshlrev_b32 v186, 16, v133\n"
"v_and_b32 v187, 0xffff0000, v133\n"
"buffer_load_dwordx4 a[184:187], v250, s[8:11], s70 offen offset:2048 sc1\n"
"v_mfma_f32_16x16x32_bf16 v[104:107], a[104:107], a[72:75], v[104:107]\n"
"v_lshlrev_b32 v188, 16, v134\n"
"v_and_b32 v189, 0xffff0000, v134\n"
"v_lshlrev_b32 v190, 16, v135\n"
"v_mfma_f32_16x16x32_bf16 v[104:107], a[108:111], a[76:79], v[104:107]\n"
"v_and_b32 v191, 0xffff0000, v135\n"
"s_waitcnt lgkmcnt(4)\n"
"v_lshlrev_b32 v192, 16, v136\n"
"v_and_b32 v193, 0xffff0000, v136\n"
"v_mfma_f32_16x16x32_bf16 v[108:111], a[112:115], a[64:67], v[108:111]\n"
"v_lshlrev_b32 v194, 16, v137\n"
"v_and_b32 v195, 0xffff0000, v137\n"
"buffer_load_dwordx4 a[188:191], v250, s[8:11], s70 offen offset:3072 sc1\n"
"v_mfma_f32_16x16x32_bf16 v[108:111], a[116:119], a[68:71], v[108:111]\n"
"v_mfma_f32_16x16x32_bf16 v[108:111], a[120:123], a[72:75], v[108:111]\n"
"s_waitcnt vmcnt(10)\n"
"v_cmp_gt_u32 vcc, 8, v249\n"
"s_cbranch_vccnz L_spin_p2_%=\n"
"L_back_p2_%=:\n"
"v_mfma_f32_16x16x32_bf16 v[108:111], a[124:127], a[76:79], v[108:111]\n"
"s_add_u32 s71, s67, 2048\n"
"buffer_load_dwordx2 a[192:193], v253, s[8:11], s71 offen sc1\n"
"s_waitcnt lgkmcnt(0)\n"
"v_mfma_f32_16x16x32_bf16 v[152:155], a[128:131], a[64:67], v[152:155]\n"
"v_mfma_f32_16x16x32_bf16 v[152:155], a[132:135], a[68:71], v[152:155]\n"
"s_add_u32 s70, s67, 2623488\n"
"buffer_load_dwordx2 a[194:195], v253, s[8:11], s70 offen sc1\n"
"v_mfma_f32_16x16x32_bf16 v[152:155], a[136:139], a[72:75], v[152:155]\n"
"v_mfma_f32_16x16x32_bf16 v[152:155], a[140:143], a[76:79], v[152:155]\n"
"s_add_u32 s71, s67, 5244928\n"
"buffer_load_dwordx2 a[196:197], v253, s[8:11], s71 offen sc1\n"
"v_mfma_f32_16x16x32_bf16 v[8:11], v[66:69], a[80:83], 0\n"
"v_mfma_f32_16x16x32_bf16 v[8:11], v[66:69], a[84:87], v[8:11]\n"
"s_add_u32 s70, s67, 7866368\n"
"buffer_load_dwordx2 a[198:199], v253, s[8:11], s70 offen sc1\n"
"v_mfma_f32_16x16x32_bf16 v[8:11], v[66:69], a[88:91], v[8:11]\n"
"v_mfma_f32_16x16x32_bf16 v[8:11], v[66:69], a[92:95], v[8:11]\n"
"s_barrier\n"
"v_mfma_f32_16x16x32_bf16 v[8:11], v[66:69], a[144:147], v[8:11]\n"
"ds_read_b128 a[96:99], v251 offset:57344\n"
"s_add_u32 s71, s67, 10487808\n"
"buffer_load_dwordx2 a[200:201], v253, s[8:11], s71 offen sc1\n"
"v_mfma_f32_16x16x32_bf16 v[8:11], v[66:69], a[148:151], v[8:11]\n"
"ds_read_b128 a[100:103], v251 offset:62464\n"
"v_mfma_f32_16x16x32_bf16 v[8:11], v[66:69], a[152:155], v[8:11]\n"
"ds_read_b128 a[104:107], v252 offset:2048\n"
"s_add_u32 s70, s67, 13109248\n"
"buffer_load_dwordx2 a[202:203], v253, s[8:11], s70 offen sc1\n"
"v_mfma_f32_16x16x32_bf16 v[8:11], v[66:69], a[156:159], v[8:11]\n"
"ds_read_b128 a[108:111], v252 offset:7168\n"
"v_mfma_f32_16x16x32_bf16 v[12:15], v[64:67], a[80:83], 0\n"
"ds_read_b128 a[112:115], v251 offset:58368\n"
"s_add_u32 s71, s67, 15730688\n"
"buffer_load_dwordx2 a[204:205], v253, s[8:11], s71 offen sc1\n"
"v_mfma_f32_16x16x32_bf16 v[12:15], v[64:67], a[84:87], v[12:15]\n"
"ds_read_b128 a[116:119], v251 offset:63488\n"
"v_mfma_f32_16x16x32_bf16 v[12:15], v[64:67], a[88:91], v[12:15]\n"
"ds_read_b128 a[120:123], v252 offset:3072\n"
"s_add_u32 s70, s67, 18352128\n"
"buffer_load_dwordx2 a[206:207], v253, s[8:11], s70 offen sc1\n"
"v_mfma_f32_16x16x32_bf16 v[12:15], v[64:67], a[92:95], v[12:15]\n"
"ds_read_b128 a[124:127], v252 offset:8192\n"
"v_mfma_f32_16x16x32_bf16 v[12:15], v[64:67], a[144:147], v[12:15]\n"
"ds_read_b128 a[128:131], v251 offset:59392\n"
"v_mfma_f32_16x16x32_bf16 v[12:15], v[64:67], a[148:151], v[12:15]\n"
"ds_read_b128 a[132:135], v251 offset:64512\n"
"v_mfma_f32_16x16x32_bf16 v[12:15], v[64:67], a[152:155], v[12:15]\n"
"ds_read_b128 a[136:139], v252 offset:4096\n"
"v_mfma_f32_16x16x32_bf16 v[12:15], v[64:67], a[156:159], v[12:15]\n"
"ds_read_b128 a[140:143], v252 offset:9216\n"
"s_waitcnt lgkmcnt(6)\n"
"v_mfma_f32_16x16x32_bf16 v[96:99], a[96:99], a[160:163], v[96:99]\n"
"ds_read_b128 a[96:99], v251 offset:60416\n"
"v_mfma_f32_16x16x32_bf16 v[96:99], a[100:103], a[164:167], v[96:99]\n"
"ds_read_b128 a[100:103], v252 offset:0\n"
"v_mfma_f32_16x16x32_bf16 v[96:99], a[104:107], a[168:171], v[96:99]\n"
"ds_read_b128 a[104:107], v252 offset:5120\n"
"s_waitcnt vmcnt(17)\n"
"v_mfma_f32_16x16x32_bf16 v[96:99], a[108:111], a[172:175], v[96:99]\n"
"ds_read_b128 a[108:111], v252 offset:10240\n"
"v_mfma_f32_16x16x32_bf16 v[100:103], a[112:115], a[160:163], v[100:103]\n"
"ds_read_b128 a[112:115], v251 offset:61440\n"
"v_mfma_f32_16x16x32_bf16 v[100:103], a[116:119], a[164:167], v[100:103]\n"
"ds_read_b128 a[116:119], v252 offset:1024\n"
"s_waitcnt lgkmcnt(6)\n"
"v_mfma_f32_16x16x32_bf16 v[100:103], a[120:123], a[168:171], v[100:103]\n"
"ds_read_b128 a[120:123], v252 offset:6144\n"
"v_mfma_f32_16x16x32_bf16 v[100:103], a[124:127], a[172:175], v[100:103]\n"
"s_waitcnt vmcnt(12)\n"
"s_barrier\n"
"v_mfma_f32_16x16x32_bf16 v[104:107], a[128:131], a[160:163], v[104:107]\n"
"ds_read_b128 a[124:127], v252 offset:11264\n"
"v_mfma_f32_16x16x32_bf16 v[104:107], a[132:135], a[164:167], v[104:107]\n"
"ds_read_b128 a[128:131], v252 offset:12288\n"
"v_mfma_f32_16x16x32_bf16 v[104:107], a[136:139], a[168:171], v[104:107]\n"
"ds_read_b128 a[132:135], v252 offset:17408\n"
"v_mfma_f32_16x16x32_bf16 v[104:107], a[140:143], a[172:175], v[104:107]\n"
"ds_read_b128 a[136:139], v252 offset:22528\n"
"s_waitcnt lgkmcnt(5)\n"
"v_mfma_f32_16x16x32_bf16 v[108:111], a[96:99], a[160:163], v[108:111]\n"
"ds_read_b128 a[140:143], v252 offset:27648\n"
"v_mfma_f32_16x16x32_bf16 v[108:111], a[100:103], a[164:167], v[108:111]\n"
"ds_read_b128 a[96:99], v252 offset:13312\n"
"v_mfma_f32_16x16x32_bf16 v[108:111], a[104:107], a[168:171], v[108:111]\n"
"ds_read_b128 a[100:103], v252 offset:18432\n"
"v_mfma_f32_16x16x32_bf16 v[108:111], a[108:111], a[172:175], v[108:111]\n"
"ds_read_b128 a[104:107], v252 offset:23552\n"
"v_mfma_f32_16x16x32_bf16 v[152:155], a[112:115], a[160:163], v[152:155]\n"
"ds_read_b128 a[108:111], v252 offset:28672\n"
"v_mfma_f32_16x16x32_bf16 v[152:155], a[116:119], a[164:167], v[152:155]\n"
"ds_read_b128 a[112:115], v252 offset:14336\n"
"s_waitcnt lgkmcnt(6)\n"
"v_mfma_f32_16x16x32_bf16 v[152:155], a[120:123], a[168:171], v[152:155]\n"
"ds_read_b128 a[116:119], v252 offset:19456\n"
"v_mfma_f32_16x16x32_bf16 v[152:155], a[124:127], a[172:175], v[152:155]\n"
"ds_read_b128 a[120:123], v252 offset:24576\n"
"s_waitcnt vmcnt(8)\n"
"v_mfma_f32_16x16x32_bf16 v[96:99], a[128:131], a[176:179], v[96:99]\n"
"ds_read_b128 a[124:127], v252 offset:29696\n"
"v_mfma_f32_16x16x32_bf16 v[96:99], a[132:135], a[180:183], v[96:99]\n"
"ds_read_b128 a[128:131], v252 offset:15360\n"
"v_mfma_f32_16x16x32_bf16 v[96:99], a[136:139], a[184:187], v[96:99]\n"
"ds_read_b128 a[132:135], v252 offset:20480\n"
"s_waitcnt lgkmcnt(6)\n"
"v_mfma_f32_16x16x32_bf16 v[96:99], a[140:143], a[188:191], v[96:99]\n"
"ds_read_b128 a[136:139], v252 offset:25600\n"
"v_mfma_f32_16x16x32_bf16 v[100:103], a[96:99], a[176:179], v[100:103]\n"
"ds_read_b128 a[140:143], v252 offset:30720\n"
"v_mfma_f32_16x16x32_bf16 v[100:103], a[100:103], a[180:183], v[100:103]\n"
"ds_read_b128 a[96:99], v252 offset:16384\n"
"v_mfma_f32_16x16x32_bf16 v[100:103], a[104:107], a[184:187], v[100:103]\n"
"ds_read_b128 a[100:103], v252 offset:21504\n"
"v_mfma_f32_16x16x32_bf16 v[100:103], a[108:111], a[188:191], v[100:103]\n"
"ds_read_b128 a[104:107], v252 offset:26624\n"
"s_waitcnt lgkmcnt(6)\n"
"v_mfma_f32_16x16x32_bf16 v[104:107], a[112:115], a[176:179], v[104:107]\n"
"ds_read_b128 a[108:111], v252 offset:31744\n"
"v_mfma_f32_16x16x32_bf16 v[104:107], a[116:119], a[180:183], v[104:107]\n"
"v_mfma_f32_16x16x32_bf16 v[104:107], a[120:123], a[184:187], v[104:107]\n"
"v_mfma_f32_16x16x32_bf16 v[104:107], a[124:127], a[188:191], v[104:107]\n"
"v_add_f32 v96, v96, v224\n"
"v_add_f32 v97, v97, v225\n"
"v_add_f32 v98, v98, v226\n"
"v_add_f32 v99, v99, v227\n"
"v_mul_f32 v160, 0xbfb8aa3b, v96\n"
"v_mul_f32 v161, 0xbfb8aa3b, v97\n"
"v_mul_f32 v162, 0xbfb8aa3b, v98\n"
"v_mul_f32 v163, 0xbfb8aa3b, v99\n"
"v_exp_f32 v160, v160\n"
"v_exp_f32 v161, v161\n"
"v_mfma_f32_16x16x32_bf16 v[108:111], a[128:131], a[176:179], v[108:111]\n"
"v_exp_f32 v162, v162\n"
"v_exp_f32 v163, v163\n"
"v_add_f32 v160, 1.0, v160\n"
"v_add_f32 v161, 1.0, v161\n"
"v_add_f32 v162, 1.0, v162\n"
"v_add_f32 v163, 1.0, v163\n"
"v_rcp_f32 v160, v160\n"
"v_rcp_f32 v161, v161\n"
"s_waitcnt lgkmcnt(2)\n"
"v_mfma_f32_16x16x32_bf16 v[108:111], a[132:135], a[180:183], v[108:111]\n"
"v_rcp_f32 v162, v162\n"
"v_rcp_f32 v163, v163\n"
"v_mul_f32 v96, v96, v160\n"
"v_mul_f32 v97, v97, v161\n"
"v_mul_f32 v98, v98, v162\n"
"v_mul_f32 v99, v99, v163\n"
"v_add_f32 v100, v100, v228\n"
"v_add_f32 v101, v101, v229\n"
"v_add_f32 v102, v102, v230\n"
"v_add_f32 v103, v103, v231\n"
"v_mul_f32 v164, 0xbfb8aa3b, v100\n"
"v_mfma_f32_16x16x32_bf16 v[108:111], a[136:139], a[184:187], v[108:111]\n"
"v_mul_f32 v165, 0xbfb8aa3b, v101\n"
"v_mul_f32 v166, 0xbfb8aa3b, v102\n"
"v_mul_f32 v167, 0xbfb8aa3b, v103\n"
"v_exp_f32 v164, v164\n"
"v_exp_f32 v165, v165\n"
"v_exp_f32 v166, v166\n"
"v_exp_f32 v167, v167\n"
"v_add_f32 v164, 1.0, v164\n"
"v_add_f32 v165, 1.0, v165\n"
"v_mfma_f32_16x16x32_bf16 v[108:111], a[140:143], a[188:191], v[108:111]\n"
"v_add_f32 v166, 1.0, v166\n"
"v_add_f32 v167, 1.0, v167\n"
"v_rcp_f32 v164, v164\n"
"v_rcp_f32 v165, v165\n"
"v_rcp_f32 v166, v166\n"
"v_rcp_f32 v167, v167\n"
"v_mul_f32 v100, v100, v164\n"
"v_mul_f32 v101, v101, v165\n"
"v_mul_f32 v102, v102, v166\n"
"v_mfma_f32_16x16x32_bf16 v[152:155], a[96:99], a[176:179], v[152:155]\n"
"v_mul_f32 v103, v103, v167\n"
"v_add_f32 v104, v104, v232\n"
"v_add_f32 v105, v105, v233\n"
"v_add_f32 v106, v106, v234\n"
"v_add_f32 v107, v107, v235\n"
"v_mul_f32 v160, 0xbfb8aa3b, v104\n"
"v_mul_f32 v161, 0xbfb8aa3b, v105\n"
"v_mul_f32 v162, 0xbfb8aa3b, v106\n"
"v_mul_f32 v163, 0xbfb8aa3b, v107\n"
"v_exp_f32 v160, v160\n"
"v_exp_f32 v161, v161\n"
"v_mfma_f32_16x16x32_bf16 v[152:155], a[100:103], a[180:183], v[152:155]\n"
"v_exp_f32 v162, v162\n"
"v_exp_f32 v163, v163\n"
"v_add_f32 v160, 1.0, v160\n"
"v_add_f32 v161, 1.0, v161\n"
"v_add_f32 v162, 1.0, v162\n"
"v_add_f32 v163, 1.0, v163\n"
"v_rcp_f32 v160, v160\n"
"v_rcp_f32 v161, v161\n"
"s_waitcnt lgkmcnt(0)\n"
"v_mfma_f32_16x16x32_bf16 v[152:155], a[104:107], a[184:187], v[152:155]\n"
"v_rcp_f32 v162, v162\n"
"v_rcp_f32 v163, v163\n"
"v_mul_f32 v104, v104, v160\n"
"v_mul_f32 v105, v105, v161\n"
"v_mul_f32 v106, v106, v162\n"
"v_mul_f32 v107, v107, v163\n"
"v_add_f32 v108, v108, v236\n"
"v_add_f32 v109, v109, v237\n"
"v_add_f32 v110, v110, v238\n"
"v_add_f32 v111, v111, v239\n"
"v_mul_f32 v164, 0xbfb8aa3b, v108\n"
"v_mfma_f32_16x16x32_bf16 v[152:155], a[108:111], a[188:191], v[152:155]\n"
"v_mul_f32 v165, 0xbfb8aa3b, v109\n"
"v_mul_f32 v166, 0xbfb8aa3b, v110\n"
"v_mul_f32 v167, 0xbfb8aa3b, v111\n"
"v_exp_f32 v164, v164\n"
"v_exp_f32 v165, v165\n"
"v_exp_f32 v166, v166\n"
"v_exp_f32 v167, v167\n"
"v_add_f32 v164, 1.0, v164\n"
"v_add_f32 v165, 1.0, v165\n"
"s_waitcnt vmcnt(0)\n"
"v_mfma_f32_16x16x32_bf16 v[32:35], v[72:75], a[192:195], 0\n"
"v_add_f32 v166, 1.0, v166\n"
"v_add_f32 v167, 1.0, v167\n"
"v_rcp_f32 v164, v164\n"
"v_rcp_f32 v165, v165\n"
"v_rcp_f32 v166, v166\n"
"v_rcp_f32 v167, v167\n"
"v_mul_f32 v108, v108, v164\n"
"v_mul_f32 v109, v109, v165\n"
"v_mul_f32 v110, v110, v166\n"
"v_mfma_f32_16x16x32_bf16 v[32:35], v[72:75], a[196:199], v[32:35]\n"
"v_mul_f32 v111, v111, v167\n"
"v_add_f32 v152, v152, v240\n"
"v_add_f32 v153, v153, v241\n"
"v_add_f32 v154, v154, v242\n"
"v_add_f32 v155, v155, v243\n"
"v_mul_f32 v160, 0xbfb8aa3b, v152\n"
"v_mul_f32 v161, 0xbfb8aa3b, v153\n"
"v_mul_f32 v162, 0xbfb8aa3b, v154\n"
"v_mul_f32 v163, 0xbfb8aa3b, v155\n"
"v_exp_f32 v160, v160\n"
"v_exp_f32 v161, v161\n"
"v_mfma_f32_16x16x32_bf16 v[32:35], v[72:75], a[200:203], v[32:35]\n"
"v_exp_f32 v162, v162\n"
"v_exp_f32 v163, v163\n"
"v_add_f32 v160, 1.0, v160\n"
"v_add_f32 v161, 1.0, v161\n"
"v_add_f32 v162, 1.0, v162\n"
"v_add_f32 v163, 1.0, v163\n"
"v_rcp_f32 v160, v160\n"
"v_rcp_f32 v161, v161\n"
"v_mfma_f32_16x16x32_bf16 v[32:35], v[72:75], a[204:207], v[32:35]\n"
"v_rcp_f32 v162, v162\n"
"v_rcp_f32 v163, v163\n"
"v_mul_f32 v152, v152, v160\n"
"v_mul_f32 v153, v153, v161\n"
"v_mul_f32 v154, v154, v162\n"
"v_mul_f32 v155, v155, v163\n"
"v_fmac_f32 v176, v0, v96\n"
"v_fmac_f32 v177, v1, v97\n"
"v_fmac_f32 v178, v2, v98\n"
"v_fmac_f32 v179, v3, v99\n"
"v_cvt_pk_bf16_f32 v128, v176, v177\n"
"v_cvt_pk_bf16_f32 v129, v178, v179\n"
"ds_permute_b32 v128, v212, v128\n"
"ds_permute_b32 v129, v212, v129\n"
"v_fmac_f32 v180, v4, v100\n"
"v_fmac_f32 v181, v5, v101\n"
"v_fmac_f32 v182, v6, v102\n"
"v_fmac_f32 v183, v7, v103\n"
"v_cvt_pk_bf16_f32 v130, v180, v181\n"
"v_cvt_pk_bf16_f32 v131, v182, v183\n"
"ds_permute_b32 v130, v212, v130\n"
"ds_permute_b32 v131, v212, v131\n"
"v_fmac_f32 v184, v8, v104\n"
"v_fmac_f32 v185, v9, v105\n"
"v_fmac_f32 v186, v10, v106\n"
"v_fmac_f32 v187, v11, v107\n"
"v_cvt_pk_bf16_f32 v132, v184, v185\n"
"v_cvt_pk_bf16_f32 v133, v186, v187\n"
"ds_permute_b32 v132, v212, v132\n"
"ds_permute_b32 v133, v212, v133\n"
"v_fmac_f32 v188, v12, v108\n"
"v_fmac_f32 v189, v13, v109\n"
"v_fmac_f32 v190, v14, v110\n"
"v_fmac_f32 v191, v15, v111\n"
"v_cvt_pk_bf16_f32 v134, v188, v189\n"
"v_cvt_pk_bf16_f32 v135, v190, v191\n"
"ds_permute_b32 v134, v212, v134\n"
"ds_permute_b32 v135, v212, v135\n"
"v_fmac_f32 v192, v32, v152\n"
"v_fmac_f32 v193, v33, v153\n"
"v_fmac_f32 v194, v34, v154\n"
"v_fmac_f32 v195, v35, v155\n"
"v_cvt_pk_bf16_f32 v136, v192, v193\n"
"v_cvt_pk_bf16_f32 v137, v194, v195\n"
"ds_permute_b32 v136, v212, v136\n"
"ds_permute_b32 v137, v212, v137\n"
"s_waitcnt lgkmcnt(8)\n"
"buffer_store_dwordx2 v[128:129], v208, s[16:19], s68 offen offset:0\n"
"s_waitcnt lgkmcnt(6)\n"
"buffer_store_dwordx2 v[130:131], v208, s[16:19], s68 offen offset:32\n"
"s_waitcnt lgkmcnt(4)\n"
"buffer_store_dwordx2 v[132:133], v208, s[16:19], s68 offen offset:64\n"
"s_waitcnt lgkmcnt(2)\n"
"buffer_store_dwordx2 v[134:135], v208, s[16:19], s68 offen offset:96\n"
"s_waitcnt lgkmcnt(0)\n"
"buffer_store_dwordx2 v[136:137], v208, s[16:19], s68 offen offset:128\n"
"s_endpgm\n"
"L_sw_%=:\n"
"s_cmp_lt_u32 s58, 4\n"
"s_cbranch_scc1 L_sw3_%=\n"
"s_mov_b32 s46, 1966080\n"
"s_mov_b32 s50, 1966080\n"
"s_cmp_eq_u32 s58, 4\n"
"s_cbranch_scc1 L_sw5_%=\n"
"s_lshl_b32 s88, s55, 10\n"
"buffer_atomic_add v246, v236, s[8:11], s88 offen offset:256 sc1\n"
"s_mov_b64 s[44:45], s[36:37]\n"
"s_add_u32 s48, s8, 20971520\n"
"s_addc_u32 s49, s9, 0\n"
"s_mov_b32 s71, s92\n"
"s_mov_b32 s72, s93\n"
"s_mov_b32 s59, 4\n"
"s_branch L_loop_%=\n"
"L_sw5_%=:\n"
"s_mov_b64 s[44:45], s[34:35]\n"
"s_add_u32 s48, s8, 18874368\n"
"s_addc_u32 s49, s9, 0\n"
"s_mov_b32 s71, s94\n"
"s_mov_b32 s72, s95\n"
"s_mov_b32 s73, 983040\n"
"s_mov_b32 s74, 983040\n"
"v_mov_b32 v234, v235\n"
"s_mov_b32 s59, 2\n"
"s_branch L_loop_%=\n"
"L_sw3_%=:\n"
"s_cmp_eq_u32 s58, 1\n"
"s_cbranch_scc1 L_sw2_%=\n"
"s_mov_b32 s50, 0\n"
"v_lshlrev_b32 v234, 7, v248\n"
"s_mov_b64 s[44:45], s[8:9]\n"
"s_mul_i32 s88, s55, 983040\n"
"s_add_u32 s88, s88, 23068672\n"
"s_add_u32 s71, s88, s43\n"
"s_add_u32 s46, s88, 983040\n"
"s_mov_b32 s59, 1\n"
"s_branch L_loop_%=\n"
"L_sw2_%=:\n"
"s_lshl_b32 s88, s55, 10\n"
"buffer_atomic_add v246, v236, s[8:11], s88 offen offset:384 sc1\n"
"buffer_load_dword v249, v239, s[8:11], 0 offen offset:256 sc1\n"
"s_mul_i32 s88, s55, 327680\n"
"s_add_u32 s89, s88, 5263360\n"
"s_add_u32 s88, s88, 720896\n"
"s_cmp_lt_u32 s43, 327680\n"
"s_cselect_b32 s88, s89, s88\n"
"s_cmp_lt_u32 s43, 655360\n"
"s_cselect_b32 s88, s88, 17825792\n"
"s_add_u32 s71, s88, s43\n"
"s_mov_b32 s46, 18644992\n"
"s_mov_b32 s59, 0\n"
"s_branch L_loop_%=\n"
"L_spin_pre0_%=:\n"
"s_mov_b32 s58, 0x8000\n"
"L_spinl_pre0_%=:\n"
"buffer_load_dword v249, v239, s[8:11], 0 offen offset:256 sc1\n"
"s_waitcnt vmcnt(0)\n"
"v_cmp_gt_u32 vcc, 32, v249\n"
"s_nop 4\n"
"s_cbranch_vccz L_back_pre0_%=\n"
"s_sleep 1\n"
"s_sub_u32 s58, s58, 1\n"
"s_cmp_lg_u32 s58, 0\n"
"s_cbranch_scc1 L_spinl_pre0_%=\n"
"s_branch L_back_pre0_%=\n"
"L_spin_pre_%=:\n"
"s_mov_b32 s58, 0x8000\n"
"L_spinl_pre_%=:\n"
"buffer_load_dword v249, v239, s[8:11], 0 offen offset:384 sc1\n"
"s_waitcnt vmcnt(0)\n"
"v_cmp_gt_u32 vcc, 32, v249\n"
"s_nop 4\n"
"s_cbranch_vccz L_back_pre_%=\n"
"s_sleep 1\n"
"s_sub_u32 s58, s58, 1\n"
"s_cmp_lg_u32 s58, 0\n"
"s_cbranch_scc1 L_spinl_pre_%=\n"
"s_branch L_back_pre_%=\n"
"L_spin_o0_%=:\n"
"s_mov_b32 s58, 0x8000\n"
"s_add_u32 s59, s7, 0\n"
"L_spinl_o0_%=:\n"
"buffer_load_dword v249, off, s[8:11], s59 sc1\n"
"s_waitcnt vmcnt(0)\n"
"v_cmp_gt_u32 vcc, 4, v249\n"
"s_nop 4\n"
"s_cbranch_vccz L_back_o0_%=\n"
"s_sleep 1\n"
"s_sub_u32 s58, s58, 1\n"
"s_cmp_lg_u32 s58, 0\n"
"s_cbranch_scc1 L_spinl_o0_%=\n"
"s_branch L_back_o0_%=\n"
"L_spin_x_%=:\n"
"s_mov_b32 s58, 0x8000\n"
"s_add_u32 s59, s33, 896\n"
"L_spinl_x_%=:\n"
"buffer_load_dword v249, off, s[8:11], s59 sc1\n"
"s_waitcnt vmcnt(0)\n"
"v_cmp_gt_u32 vcc, 16, v249\n"
"s_nop 4\n"
"s_cbranch_vccz L_back_x_%=\n"
"s_sleep 1\n"
"s_sub_u32 s58, s58, 1\n"
"s_cmp_lg_u32 s58, 0\n"
"s_cbranch_scc1 L_spinl_x_%=\n"
"s_branch L_back_x_%=\n"
"L_spin_o1_%=:\n"
"s_mov_b32 s58, 0x8000\n"
"s_add_u32 s59, s7, 128\n"
"L_spinl_o1_%=:\n"
"buffer_load_dword v249, off, s[8:11], s59 sc1\n"
"s_waitcnt vmcnt(0)\n"
"v_cmp_gt_u32 vcc, 4, v249\n"
"s_nop 4\n"
"s_cbranch_vccz L_back_o1_%=\n"
"s_sleep 1\n"
"s_sub_u32 s58, s58, 1\n"
"s_cmp_lg_u32 s58, 0\n"
"s_cbranch_scc1 L_spinl_o1_%=\n"
"s_branch L_back_o1_%=\n"
"L_spin_u_%=:\n"
"s_mov_b32 s58, 0x8000\n"
"s_add_u32 s59, s33, 17280\n"
"L_spinl_u_%=:\n"
"buffer_load_dword v249, off, s[8:11], s59 sc1\n"
"s_waitcnt vmcnt(0)\n"
"v_cmp_gt_u32 vcc, 16, v249\n"
"s_nop 4\n"
"s_cbranch_vccz L_back_u_%=\n"
"s_sleep 1\n"
"s_sub_u32 s58, s58, 1\n"
"s_cmp_lg_u32 s58, 0\n"
"s_cbranch_scc1 L_spinl_u_%=\n"
"s_branch L_back_u_%=\n"
"L_spin_p0_%=:\n"
"s_mov_b32 s58, 0x8000\n"
"s_add_u32 s59, s66, 512\n"
"L_spinl_p0_%=:\n"
"buffer_load_dword v249, off, s[8:11], s59 sc1\n"
"s_waitcnt vmcnt(0)\n"
"v_cmp_gt_u32 vcc, 8, v249\n"
"s_nop 4\n"
"s_cbranch_vccz L_back_p0_%=\n"
"s_sleep 1\n"
"s_sub_u32 s58, s58, 1\n"
"s_cmp_lg_u32 s58, 0\n"
"s_cbranch_scc1 L_spinl_p0_%=\n"
"s_branch L_back_p0_%=\n"
"L_spin_p1_%=:\n"
"s_mov_b32 s58, 0x8000\n"
"s_add_u32 s59, s66, 640\n"
"L_spinl_p1_%=:\n"
"buffer_load_dword v249, off, s[8:11], s59 sc1\n"
"s_waitcnt vmcnt(0)\n"
"v_cmp_gt_u32 vcc, 8, v249\n"
"s_nop 4\n"
"s_cbranch_vccz L_back_p1_%=\n"
"s_sleep 1\n"
"s_sub_u32 s58, s58, 1\n"
"s_cmp_lg_u32 s58, 0\n"
"s_cbranch_scc1 L_spinl_p1_%=\n"
"s_branch L_back_p1_%=\n"
"L_spin_p2_%=:\n"
"s_mov_b32 s58, 0x8000\n"
"s_add_u32 s59, s66, 768\n"
"L_spinl_p2_%=:\n"
"buffer_load_dword v249, off, s[8:11], s59 sc1\n"
"s_waitcnt vmcnt(0)\n"
"v_cmp_gt_u32 vcc, 8, v249\n"
"s_nop 4\n"
"s_cbranch_vccz L_back_p2_%=\n"
"s_sleep 1\n"
"s_sub_u32 s58, s58, 1\n"
"s_cmp_lg_u32 s58, 0\n"
"s_cbranch_scc1 L_spinl_p2_%=\n"
"s_branch L_back_p2_%=\n"
"fused_code_end:\n"
"L_fb_%=:\n"
"s_cmp_lg_u32 s60, 0\n"
"s_cbranch_scc1 L_nofb_%=\n"
"s_mov_b32 s60, 1073741824\n"
"s_waitcnt vmcnt(0) lgkmcnt(0)\n"
"s_nop 15\n"
"s_mul_i32 s88, s53, 786432\n"
"s_mov_b64 s[16:17], s[26:27]\n"
"s_add_u32 s16, s16, s88\n"
"s_addc_u32 s17, s17, 0\n"
"s_sub_u32 s16, s16, 1792\n"
"s_subb_u32 s17, s17, 0\n"
"s_and_b32 s17, s17, 0xffff\n"
"s_mov_b32 s18, 788224\n"
"s_mov_b32 s19, 0x20000\n"
"s_mov_b64 s[20:21], s[28:29]\n"
"s_add_u32 s20, s20, s88\n"
"s_addc_u32 s21, s21, 0\n"
"s_sub_u32 s20, s20, 1792\n"
"s_subb_u32 s21, s21, 0\n"
"s_and_b32 s21, s21, 0xffff\n"
"s_mov_b32 s22, 788224\n"
"s_mov_b32 s23, 0x20000\n"
"s_mov_b32 s61, s57\n"
"s_add_u32 s62, s57, 3072\n"
"v_mov_b32 v240, 0\n"
"v_mov_b32 v241, 0\n"
"v_mov_b32 v242, 0\n"
"v_mov_b32 v243, 0\n"
"v_mov_b32 v253, 0\n"
"v_mov_b32 v128, 0xff800000\n"
"v_mov_b32 v132, 0xff800000\n"
"v_mov_b32 v136, 0xff800000\n"
"v_mov_b32 v140, 0xff800000\n"
"s_mov_b32 s58, 32\n"
"L_fbl_%=:\n"
"s_barrier\n"
"s_mov_b32 m0, s63\n"
"s_nop 0\n"
"buffer_load_dwordx4 v224, s[16:19], s61 offen offset:0 lds\n"
"buffer_load_dwordx4 v225, s[16:19], s61 offen offset:1024 lds\n"
"buffer_load_dwordx4 v226, s[16:19], s61 offen offset:2048 lds\n"
"s_mov_b32 m0, s64\n"
"s_nop 0\n"
"buffer_load_dwordx4 v224, s[16:19], s62 offen offset:0 lds\n"
"buffer_load_dwordx4 v225, s[16:19], s62 offen offset:1024 lds\n"
"buffer_load_dwordx4 v226, s[16:19], s62 offen offset:2048 lds\n"
"s_add_u32 s61, s61, 24576\n"
"s_add_u32 s62, s62, 24576\n"
"s_waitcnt vmcnt(0)\n"
"s_barrier\n"
"ds_read_b128 a[96:99], v227 offset:0\n"
"ds_read_b128 a[100:103], v228 offset:0\n"
"ds_read_b128 a[104:107], v227 offset:1024\n"
"ds_read_b128 a[108:111], v228 offset:1024\n"
"ds_read_b128 a[112:115], v227 offset:2048\n"
"ds_read_b128 a[116:119], v228 offset:2048\n"
"s_waitcnt lgkmcnt(0)\n"
"v_mfma_f32_16x16x32_bf16 v[192:195], a[96:99], a[0:3], 0\n"
"v_mfma_f32_16x16x32_bf16 v[192:195], a[100:103], a[4:7], v[192:195]\n"
"v_mfma_f32_16x16x32_bf16 v[192:195], a[104:107], a[8:11], v[192:195]\n"
"v_mfma_f32_16x16x32_bf16 v[192:195], a[108:111], a[12:15], v[192:195]\n"
"v_mfma_f32_16x16x32_bf16 v[192:195], a[112:115], a[16:19], v[192:195]\n"
"v_mfma_f32_16x16x32_bf16 v[192:195], a[116:119], a[20:23], v[192:195]\n"
"v_mfma_f32_16x16x32_bf16 v[196:199], a[96:99], a[24:27], 0\n"
"v_mfma_f32_16x16x32_bf16 v[196:199], a[100:103], a[28:31], v[196:199]\n"
"v_mfma_f32_16x16x32_bf16 v[196:199], a[104:107], a[32:35], v[196:199]\n"
"v_mfma_f32_16x16x32_bf16 v[196:199], a[108:111], a[36:39], v[196:199]\n"
"v_mfma_f32_16x16x32_bf16 v[196:199], a[112:115], a[40:43], v[196:199]\n"
"v_mfma_f32_16x16x32_bf16 v[196:199], a[116:119], a[44:47], v[196:199]\n"
"v_mfma_f32_16x16x32_bf16 v[200:203], a[96:99], a[48:51], 0\n"
"v_mfma_f32_16x16x32_bf16 v[200:203], a[100:103], a[52:55], v[200:203]\n"
"v_mfma_f32_16x16x32_bf16 v[200:203], a[104:107], a[56:59], v[200:203]\n"
"v_mfma_f32_16x16x32_bf16 v[200:203], a[108:111], a[60:63], v[200:203]\n"
"v_mfma_f32_16x16x32_bf16 v[200:203], a[112:115], a[64:67], v[200:203]\n"
"v_mfma_f32_16x16x32_bf16 v[200:203], a[116:119], a[68:71], v[200:203]\n"
"v_mfma_f32_16x16x32_bf16 v[204:207], a[96:99], a[72:75], 0\n"
"v_mfma_f32_16x16x32_bf16 v[204:207], a[100:103], a[76:79], v[204:207]\n"
"v_mfma_f32_16x16x32_bf16 v[204:207], a[104:107], a[80:83], v[204:207]\n"
"v_mfma_f32_16x16x32_bf16 v[204:207], a[108:111], a[84:87], v[204:207]\n"
"v_mfma_f32_16x16x32_bf16 v[204:207], a[112:115], a[88:91], v[204:207]\n"
"v_mfma_f32_16x16x32_bf16 v[204:207], a[116:119], a[92:95], v[204:207]\n"
"s_nop 15\n"
"v_max3_f32 v128, v128, v192, v193\n"
"v_max3_f32 v128, v128, v194, v195\n"
"v_max3_f32 v132, v132, v196, v197\n"
"v_max3_f32 v132, v132, v198, v199\n"
"v_max3_f32 v136, v136, v200, v201\n"
"v_max3_f32 v136, v136, v202, v203\n"
"v_max3_f32 v140, v140, v204, v205\n"
"v_max3_f32 v140, v140, v206, v207\n"
"ds_read_b128 a[120:123], v227 offset:6144\n"
"ds_read_b128 a[124:127], v228 offset:6144\n"
"ds_read_b128 a[128:131], v227 offset:7168\n"
"ds_read_b128 a[132:135], v228 offset:7168\n"
"ds_read_b128 a[136:139], v227 offset:8192\n"
"ds_read_b128 a[140:143], v228 offset:8192\n"
"s_waitcnt lgkmcnt(0)\n"
"v_mfma_f32_16x16x32_bf16 v[192:195], a[120:123], a[0:3], 0\n"
"v_mfma_f32_16x16x32_bf16 v[192:195], a[124:127], a[4:7], v[192:195]\n"
"v_mfma_f32_16x16x32_bf16 v[192:195], a[128:131], a[8:11], v[192:195]\n"
"v_mfma_f32_16x16x32_bf16 v[192:195], a[132:135], a[12:15], v[192:195]\n"
"v_mfma_f32_16x16x32_bf16 v[192:195], a[136:139], a[16:19], v[192:195]\n"
"v_mfma_f32_16x16x32_bf16 v[192:195], a[140:143], a[20:23], v[192:195]\n"
"v_mfma_f32_16x16x32_bf16 v[196:199], a[120:123], a[24:27], 0\n"
"v_mfma_f32_16x16x32_bf16 v[196:199], a[124:127], a[28:31], v[196:199]\n"
"v_mfma_f32_16x16x32_bf16 v[196:199], a[128:131], a[32:35], v[196:199]\n"
"v_mfma_f32_16x16x32_bf16 v[196:199], a[132:135], a[36:39], v[196:199]\n"
"v_mfma_f32_16x16x32_bf16 v[196:199], a[136:139], a[40:43], v[196:199]\n"
"v_mfma_f32_16x16x32_bf16 v[196:199], a[140:143], a[44:47], v[196:199]\n"
"v_mfma_f32_16x16x32_bf16 v[200:203], a[120:123], a[48:51], 0\n"
"v_mfma_f32_16x16x32_bf16 v[200:203], a[124:127], a[52:55], v[200:203]\n"
"v_mfma_f32_16x16x32_bf16 v[200:203], a[128:131], a[56:59], v[200:203]\n"
"v_mfma_f32_16x16x32_bf16 v[200:203], a[132:135], a[60:63], v[200:203]\n"
"v_mfma_f32_16x16x32_bf16 v[200:203], a[136:139], a[64:67], v[200:203]\n"
"v_mfma_f32_16x16x32_bf16 v[200:203], a[140:143], a[68:71], v[200:203]\n"
"v_mfma_f32_16x16x32_bf16 v[204:207], a[120:123], a[72:75], 0\n"
"v_mfma_f32_16x16x32_bf16 v[204:207], a[124:127], a[76:79], v[204:207]\n"
"v_mfma_f32_16x16x32_bf16 v[204:207], a[128:131], a[80:83], v[204:207]\n"
"v_mfma_f32_16x16x32_bf16 v[204:207], a[132:135], a[84:87], v[204:207]\n"
"v_mfma_f32_16x16x32_bf16 v[204:207], a[136:139], a[88:91], v[204:207]\n"
"v_mfma_f32_16x16x32_bf16 v[204:207], a[140:143], a[92:95], v[204:207]\n"
"s_nop 15\n"
"v_max3_f32 v128, v128, v192, v193\n"
"v_max3_f32 v128, v128, v194, v195\n"
"v_max3_f32 v132, v132, v196, v197\n"
"v_max3_f32 v132, v132, v198, v199\n"
"v_max3_f32 v136, v136, v200, v201\n"
"v_max3_f32 v136, v136, v202, v203\n"
"v_max3_f32 v140, v140, v204, v205\n"
"v_max3_f32 v140, v140, v206, v207\n"
"ds_read_b128 a[96:99], v227 offset:12288\n"
"ds_read_b128 a[100:103], v228 offset:12288\n"
"ds_read_b128 a[104:107], v227 offset:13312\n"
"ds_read_b128 a[108:111], v228 offset:13312\n"
"ds_read_b128 a[112:115], v227 offset:14336\n"
"ds_read_b128 a[116:119], v228 offset:14336\n"
"s_waitcnt lgkmcnt(0)\n"
"v_mfma_f32_16x16x32_bf16 v[192:195], a[96:99], a[0:3], 0\n"
"v_mfma_f32_16x16x32_bf16 v[192:195], a[100:103], a[4:7], v[192:195]\n"
"v_mfma_f32_16x16x32_bf16 v[192:195], a[104:107], a[8:11], v[192:195]\n"
"v_mfma_f32_16x16x32_bf16 v[192:195], a[108:111], a[12:15], v[192:195]\n"
"v_mfma_f32_16x16x32_bf16 v[192:195], a[112:115], a[16:19], v[192:195]\n"
"v_mfma_f32_16x16x32_bf16 v[192:195], a[116:119], a[20:23], v[192:195]\n"
"v_mfma_f32_16x16x32_bf16 v[196:199], a[96:99], a[24:27], 0\n"
"v_mfma_f32_16x16x32_bf16 v[196:199], a[100:103], a[28:31], v[196:199]\n"
"v_mfma_f32_16x16x32_bf16 v[196:199], a[104:107], a[32:35], v[196:199]\n"
"v_mfma_f32_16x16x32_bf16 v[196:199], a[108:111], a[36:39], v[196:199]\n"
"v_mfma_f32_16x16x32_bf16 v[196:199], a[112:115], a[40:43], v[196:199]\n"
"v_mfma_f32_16x16x32_bf16 v[196:199], a[116:119], a[44:47], v[196:199]\n"
"v_mfma_f32_16x16x32_bf16 v[200:203], a[96:99], a[48:51], 0\n"
"v_mfma_f32_16x16x32_bf16 v[200:203], a[100:103], a[52:55], v[200:203]\n"
"v_mfma_f32_16x16x32_bf16 v[200:203], a[104:107], a[56:59], v[200:203]\n"
"v_mfma_f32_16x16x32_bf16 v[200:203], a[108:111], a[60:63], v[200:203]\n"
"v_mfma_f32_16x16x32_bf16 v[200:203], a[112:115], a[64:67], v[200:203]\n"
"v_mfma_f32_16x16x32_bf16 v[200:203], a[116:119], a[68:71], v[200:203]\n"
"v_mfma_f32_16x16x32_bf16 v[204:207], a[96:99], a[72:75], 0\n"
"v_mfma_f32_16x16x32_bf16 v[204:207], a[100:103], a[76:79], v[204:207]\n"
"v_mfma_f32_16x16x32_bf16 v[204:207], a[104:107], a[80:83], v[204:207]\n"
"v_mfma_f32_16x16x32_bf16 v[204:207], a[108:111], a[84:87], v[204:207]\n"
"v_mfma_f32_16x16x32_bf16 v[204:207], a[112:115], a[88:91], v[204:207]\n"
"v_mfma_f32_16x16x32_bf16 v[204:207], a[116:119], a[92:95], v[204:207]\n"
"s_nop 15\n"
"v_max3_f32 v128, v128, v192, v193\n"
"v_max3_f32 v128, v128, v194, v195\n"
"v_max3_f32 v132, v132, v196, v197\n"
"v_max3_f32 v132, v132, v198, v199\n"
"v_max3_f32 v136, v136, v200, v201\n"
"v_max3_f32 v136, v136, v202, v203\n"
"v_max3_f32 v140, v140, v204, v205\n"
"v_max3_f32 v140, v140, v206, v207\n"
"ds_read_b128 a[120:123], v227 offset:18432\n"
"ds_read_b128 a[124:127], v228 offset:18432\n"
"ds_read_b128 a[128:131], v227 offset:19456\n"
"ds_read_b128 a[132:135], v228 offset:19456\n"
"ds_read_b128 a[136:139], v227 offset:20480\n"
"ds_read_b128 a[140:143], v228 offset:20480\n"
"s_waitcnt lgkmcnt(0)\n"
"v_mfma_f32_16x16x32_bf16 v[192:195], a[120:123], a[0:3], 0\n"
"v_mfma_f32_16x16x32_bf16 v[192:195], a[124:127], a[4:7], v[192:195]\n"
"v_mfma_f32_16x16x32_bf16 v[192:195], a[128:131], a[8:11], v[192:195]\n"
"v_mfma_f32_16x16x32_bf16 v[192:195], a[132:135], a[12:15], v[192:195]\n"
"v_mfma_f32_16x16x32_bf16 v[192:195], a[136:139], a[16:19], v[192:195]\n"
"v_mfma_f32_16x16x32_bf16 v[192:195], a[140:143], a[20:23], v[192:195]\n"
"v_mfma_f32_16x16x32_bf16 v[196:199], a[120:123], a[24:27], 0\n"
"v_mfma_f32_16x16x32_bf16 v[196:199], a[124:127], a[28:31], v[196:199]\n"
"v_mfma_f32_16x16x32_bf16 v[196:199], a[128:131], a[32:35], v[196:199]\n"
"v_mfma_f32_16x16x32_bf16 v[196:199], a[132:135], a[36:39], v[196:199]\n"
"v_mfma_f32_16x16x32_bf16 v[196:199], a[136:139], a[40:43], v[196:199]\n"
"v_mfma_f32_16x16x32_bf16 v[196:199], a[140:143], a[44:47], v[196:199]\n"
"v_mfma_f32_16x16x32_bf16 v[200:203], a[120:123], a[48:51], 0\n"
"v_mfma_f32_16x16x32_bf16 v[200:203], a[124:127], a[52:55], v[200:203]\n"
"v_mfma_f32_16x16x32_bf16 v[200:203], a[128:131], a[56:59], v[200:203]\n"
"v_mfma_f32_16x16x32_bf16 v[200:203], a[132:135], a[60:63], v[200:203]\n"
"v_mfma_f32_16x16x32_bf16 v[200:203], a[136:139], a[64:67], v[200:203]\n"
"v_mfma_f32_16x16x32_bf16 v[200:203], a[140:143], a[68:71], v[200:203]\n"
"v_mfma_f32_16x16x32_bf16 v[204:207], a[120:123], a[72:75], 0\n"
"v_mfma_f32_16x16x32_bf16 v[204:207], a[124:127], a[76:79], v[204:207]\n"
"v_mfma_f32_16x16x32_bf16 v[204:207], a[128:131], a[80:83], v[204:207]\n"
"v_mfma_f32_16x16x32_bf16 v[204:207], a[132:135], a[84:87], v[204:207]\n"
"v_mfma_f32_16x16x32_bf16 v[204:207], a[136:139], a[88:91], v[204:207]\n"
"v_mfma_f32_16x16x32_bf16 v[204:207], a[140:143], a[92:95], v[204:207]\n"
"s_nop 15\n"
"v_max3_f32 v128, v128, v192, v193\n"
"v_max3_f32 v128, v128, v194, v195\n"
"v_max3_f32 v132, v132, v196, v197\n"
"v_max3_f32 v132, v132, v198, v199\n"
"v_max3_f32 v136, v136, v200, v201\n"
"v_max3_f32 v136, v136, v202, v203\n"
"v_max3_f32 v140, v140, v204, v205\n"
"v_max3_f32 v140, v140, v206, v207\n"
"s_sub_u32 s58, s58, 1\n"
"s_cmp_lg_u32 s58, 0\n"
"s_cbranch_scc1 L_fbl_%=\n"
"v_mov_b32 v129, v128\n"
"v_mov_b32 v133, v132\n"
"v_mov_b32 v137, v136\n"
"v_mov_b32 v141, v140\n"
"v_permlane32_swap_b32 v128, v129\n"
"v_permlane32_swap_b32 v132, v133\n"
"v_permlane32_swap_b32 v136, v137\n"
"v_permlane32_swap_b32 v140, v141\n"
"v_max_f32 v128, v128, v129\n"
"v_max_f32 v132, v132, v133\n"
"v_max_f32 v136, v136, v137\n"
"v_max_f32 v140, v140, v141\n"
"v_mov_b32 v129, v128\n"
"v_mov_b32 v133, v132\n"
"v_mov_b32 v137, v136\n"
"v_mov_b32 v141, v140\n"
"v_permlane16_swap_b32 v128, v129\n"
"v_permlane16_swap_b32 v132, v133\n"
"v_permlane16_swap_b32 v136, v137\n"
"v_permlane16_swap_b32 v140, v141\n"
"v_max_f32 v128, v128, v129\n"
"v_max_f32 v132, v132, v133\n"
"v_max_f32 v136, v136, v137\n"
"v_max_f32 v140, v140, v141\n"
"v_sub_f32 v208, 0, v128\n"
"v_sub_f32 v209, 0, v128\n"
"v_sub_f32 v210, 0, v128\n"
"v_sub_f32 v211, 0, v128\n"
"v_sub_f32 v212, 0, v132\n"
"v_sub_f32 v213, 0, v132\n"
"v_sub_f32 v214, 0, v132\n"
"v_sub_f32 v215, 0, v132\n"
"v_sub_f32 v216, 0, v136\n"
"v_sub_f32 v217, 0, v136\n"
"v_sub_f32 v218, 0, v136\n"
"v_sub_f32 v219, 0, v136\n"
"v_sub_f32 v220, 0, v140\n"
"v_sub_f32 v221, 0, v140\n"
"v_sub_f32 v222, 0, v140\n"
"v_sub_f32 v223, 0, v140\n"
"v_accvgpr_write_b32 a172, 0\n"
"v_accvgpr_write_b32 a173, 0\n"
"v_accvgpr_write_b32 a174, 0\n"
"v_accvgpr_write_b32 a175, 0\n"
"v_accvgpr_write_b32 a176, 0\n"
"v_accvgpr_write_b32 a177, 0\n"
"v_accvgpr_write_b32 a178, 0\n"
"v_accvgpr_write_b32 a179, 0\n"
"v_accvgpr_write_b32 a180, 0\n"
"v_accvgpr_write_b32 a181, 0\n"
"v_accvgpr_write_b32 a182, 0\n"
"v_accvgpr_write_b32 a183, 0\n"
"v_accvgpr_write_b32 a184, 0\n"
"v_accvgpr_write_b32 a185, 0\n"
"v_accvgpr_write_b32 a186, 0\n"
"v_accvgpr_write_b32 a187, 0\n"
"s_barrier\n"
"s_mul_i32 s88, s53, 786432\n"
"s_mov_b64 s[16:17], s[26:27]\n"
"s_add_u32 s16, s16, s88\n"
"s_addc_u32 s17, s17, 0\n"
"s_sub_u32 s16, s16, 1792\n"
"s_subb_u32 s17, s17, 0\n"
"s_and_b32 s17, s17, 0xffff\n"
"s_mov_b32 s18, 788224\n"
"s_mov_b32 s19, 0x20000\n"
"s_mov_b64 s[20:21], s[28:29]\n"
"s_add_u32 s20, s20, s88\n"
"s_addc_u32 s21, s21, 0\n"
"s_sub_u32 s20, s20, 1792\n"
"s_subb_u32 s21, s21, 0\n"
"s_and_b32 s21, s21, 0xffff\n"
"s_mov_b32 s22, 788224\n"
"s_mov_b32 s23, 0x20000\n"
"s_mov_b32 s61, s57\n"
"s_add_u32 s62, s57, 3072\n"
"s_add_u32 s88, s57, 24576\n"
"s_add_u32 s89, s57, 27648\n"
"s_mov_b32 m0, s63\n"
"s_nop 0\n"
"buffer_load_dwordx4 v224, s[16:19], s61 offen offset:0 lds\n"
"buffer_load_dwordx4 v225, s[16:19], s61 offen offset:1024 lds\n"
"buffer_load_dwordx4 v226, s[16:19], s61 offen offset:2048 lds\n"
"s_mov_b32 m0, s64\n"
"s_nop 0\n"
"buffer_load_dwordx4 v224, s[16:19], s62 offen offset:0 lds\n"
"buffer_load_dwordx4 v225, s[16:19], s62 offen offset:1024 lds\n"
"buffer_load_dwordx4 v226, s[16:19], s62 offen offset:2048 lds\n"
"s_mov_b32 m0, s67\n"
"s_nop 0\n"
"buffer_load_dwordx4 v224, s[20:23], s61 offen offset:0 lds\n"
"buffer_load_dwordx4 v225, s[20:23], s61 offen offset:1024 lds\n"
"buffer_load_dwordx4 v226, s[20:23], s61 offen offset:2048 lds\n"
"s_mov_b32 m0, s68\n"
"s_nop 0\n"
"buffer_load_dwordx4 v224, s[20:23], s62 offen offset:0 lds\n"
"buffer_load_dwordx4 v225, s[20:23], s62 offen offset:1024 lds\n"
"buffer_load_dwordx4 v226, s[20:23], s62 offen offset:2048 lds\n"
"s_mov_b32 m0, s65\n"
"s_nop 0\n"
"buffer_load_dwordx4 v224, s[16:19], s88 offen offset:0 lds\n"
"buffer_load_dwordx4 v225, s[16:19], s88 offen offset:1024 lds\n"
"buffer_load_dwordx4 v226, s[16:19], s88 offen offset:2048 lds\n"
"s_mov_b32 m0, s66\n"
"s_nop 0\n"
"buffer_load_dwordx4 v224, s[16:19], s89 offen offset:0 lds\n"
"buffer_load_dwordx4 v225, s[16:19], s89 offen offset:1024 lds\n"
"buffer_load_dwordx4 v226, s[16:19], s89 offen offset:2048 lds\n"
"s_waitcnt vmcnt(0)\n"
"s_barrier\n"
"ds_read_b128 a[96:99], v227 offset:0\n"
"ds_read_b128 a[100:103], v228 offset:0\n"
"ds_read_b128 a[104:107], v227 offset:1024\n"
"ds_read_b128 a[108:111], v228 offset:1024\n"
"ds_read_b128 a[112:115], v227 offset:2048\n"
"ds_read_b128 a[116:119], v228 offset:2048\n"
"s_waitcnt lgkmcnt(0)\n"
"s_mov_b32 s59, 0\n"
"v_mfma_f32_16x16x32_bf16 v[192:195], a[96:99], a[0:3], v[208:211]\n"
"ds_read_b128 a[120:123], v227 offset:6144\n"
"v_mfma_f32_16x16x32_bf16 v[192:195], a[100:103], a[4:7], v[192:195]\n"
"ds_read_b128 a[124:127], v228 offset:6144\n"
"v_mfma_f32_16x16x32_bf16 v[192:195], a[104:107], a[8:11], v[192:195]\n"
"ds_read_b128 a[128:131], v227 offset:7168\n"
"v_mfma_f32_16x16x32_bf16 v[192:195], a[108:111], a[12:15], v[192:195]\n"
"ds_read_b128 a[132:135], v228 offset:7168\n"
"v_mfma_f32_16x16x32_bf16 v[192:195], a[112:115], a[16:19], v[192:195]\n"
"ds_read_b128 a[136:139], v227 offset:8192\n"
"v_mfma_f32_16x16x32_bf16 v[192:195], a[116:119], a[20:23], v[192:195]\n"
"ds_read_b128 a[140:143], v228 offset:8192\n"
"v_mfma_f32_16x16x32_bf16 v[196:199], a[96:99], a[24:27], v[212:215]\n"
"v_mfma_f32_16x16x32_bf16 v[196:199], a[100:103], a[28:31], v[196:199]\n"
"v_mfma_f32_16x16x32_bf16 v[196:199], a[104:107], a[32:35], v[196:199]\n"
"v_mfma_f32_16x16x32_bf16 v[196:199], a[108:111], a[36:39], v[196:199]\n"
"v_mfma_f32_16x16x32_bf16 v[196:199], a[112:115], a[40:43], v[196:199]\n"
"s_nop 1\n"
"v_exp_f32 v192, v192\n"
"v_mfma_f32_16x16x32_bf16 v[196:199], a[116:119], a[44:47], v[196:199]\n"
"v_exp_f32 v193, v193\n"
"v_mfma_f32_16x16x32_bf16 v[200:203], a[96:99], a[48:51], v[216:219]\n"
"v_cvt_pk_bf16_f32 v128, v192, v193\n"
"v_mfma_f32_16x16x32_bf16 v[200:203], a[100:103], a[52:55], v[200:203]\n"
"v_exp_f32 v194, v194\n"
"v_mfma_f32_16x16x32_bf16 v[200:203], a[104:107], a[56:59], v[200:203]\n"
"v_exp_f32 v195, v195\n"
"v_mfma_f32_16x16x32_bf16 v[200:203], a[108:111], a[60:63], v[200:203]\n"
"v_cvt_pk_bf16_f32 v129, v194, v195\n"
"v_mfma_f32_16x16x32_bf16 v[200:203], a[112:115], a[64:67], v[200:203]\n"
"v_exp_f32 v196, v196\n"
"v_mfma_f32_16x16x32_bf16 v[200:203], a[116:119], a[68:71], v[200:203]\n"
"v_exp_f32 v197, v197\n"
"v_mfma_f32_16x16x32_bf16 v[204:207], a[96:99], a[72:75], v[220:223]\n"
"v_cvt_pk_bf16_f32 v136, v196, v197\n"
"v_mfma_f32_16x16x32_bf16 v[204:207], a[100:103], a[76:79], v[204:207]\n"
"v_exp_f32 v198, v198\n"
"v_mfma_f32_16x16x32_bf16 v[204:207], a[104:107], a[80:83], v[204:207]\n"
"v_exp_f32 v199, v199\n"
"v_mfma_f32_16x16x32_bf16 v[204:207], a[108:111], a[84:87], v[204:207]\n"
"v_cvt_pk_bf16_f32 v137, v198, v199\n"
"v_mfma_f32_16x16x32_bf16 v[204:207], a[112:115], a[88:91], v[204:207]\n"
"v_exp_f32 v200, v200\n"
"v_mfma_f32_16x16x32_bf16 v[204:207], a[116:119], a[92:95], v[204:207]\n"
"v_exp_f32 v201, v201\n"
"s_waitcnt lgkmcnt(0)\n"
"v_mfma_f32_16x16x32_bf16 v[192:195], a[120:123], a[0:3], v[208:211]\n"
"v_cvt_pk_bf16_f32 v144, v200, v201\n"
"ds_read_b128 a[96:99], v227 offset:12288\n"
"v_mfma_f32_16x16x32_bf16 v[192:195], a[124:127], a[4:7], v[192:195]\n"
"v_exp_f32 v202, v202\n"
"v_mfma_f32_16x16x32_bf16 v[192:195], a[128:131], a[8:11], v[192:195]\n"
"v_exp_f32 v203, v203\n"
"v_mfma_f32_16x16x32_bf16 v[192:195], a[132:135], a[12:15], v[192:195]\n"
"v_cvt_pk_bf16_f32 v145, v202, v203\n"
"ds_read_b128 a[100:103], v228 offset:12288\n"
"v_mfma_f32_16x16x32_bf16 v[192:195], a[136:139], a[16:19], v[192:195]\n"
"v_exp_f32 v204, v204\n"
"v_mfma_f32_16x16x32_bf16 v[192:195], a[140:143], a[20:23], v[192:195]\n"
"v_exp_f32 v205, v205\n"
"v_mfma_f32_16x16x32_bf16 v[196:199], a[120:123], a[24:27], v[212:215]\n"
"v_cvt_pk_bf16_f32 v152, v204, v205\n"
"ds_read_b128 a[104:107], v227 offset:13312\n"
"v_mfma_f32_16x16x32_bf16 v[196:199], a[124:127], a[28:31], v[196:199]\n"
"v_exp_f32 v206, v206\n"
"v_mfma_f32_16x16x32_bf16 v[196:199], a[128:131], a[32:35], v[196:199]\n"
"v_exp_f32 v207, v207\n"
"v_mfma_f32_16x16x32_bf16 v[196:199], a[132:135], a[36:39], v[196:199]\n"
"v_cvt_pk_bf16_f32 v153, v206, v207\n"
"ds_read_b128 a[108:111], v228 offset:13312\n"
"v_mfma_f32_16x16x32_bf16 v[196:199], a[136:139], a[40:43], v[196:199]\n"
"v_exp_f32 v192, v192\n"
"v_mfma_f32_16x16x32_bf16 v[196:199], a[140:143], a[44:47], v[196:199]\n"
"v_exp_f32 v193, v193\n"
"v_mfma_f32_16x16x32_bf16 v[200:203], a[120:123], a[48:51], v[216:219]\n"
"v_cvt_pk_bf16_f32 v130, v192, v193\n"
"ds_read_b128 a[112:115], v227 offset:14336\n"
"v_mfma_f32_16x16x32_bf16 v[200:203], a[124:127], a[52:55], v[200:203]\n"
"v_exp_f32 v194, v194\n"
"v_mfma_f32_16x16x32_bf16 v[200:203], a[128:131], a[56:59], v[200:203]\n"
"v_exp_f32 v195, v195\n"
"v_mfma_f32_16x16x32_bf16 v[200:203], a[132:135], a[60:63], v[200:203]\n"
"v_cvt_pk_bf16_f32 v131, v194, v195\n"
"ds_read_b128 a[116:119], v228 offset:14336\n"
"v_mfma_f32_16x16x32_bf16 v[200:203], a[136:139], a[64:67], v[200:203]\n"
"v_exp_f32 v196, v196\n"
"v_mfma_f32_16x16x32_bf16 v[200:203], a[140:143], a[68:71], v[200:203]\n"
"v_exp_f32 v197, v197\n"
"v_mfma_f32_16x16x32_bf16 v[204:207], a[120:123], a[72:75], v[220:223]\n"
"v_cvt_pk_bf16_f32 v138, v196, v197\n"
"v_mfma_f32_16x16x32_bf16 v[204:207], a[124:127], a[76:79], v[204:207]\n"
"v_exp_f32 v198, v198\n"
"v_mfma_f32_16x16x32_bf16 v[204:207], a[128:131], a[80:83], v[204:207]\n"
"v_exp_f32 v199, v199\n"
"v_mfma_f32_16x16x32_bf16 v[204:207], a[132:135], a[84:87], v[204:207]\n"
"v_cvt_pk_bf16_f32 v139, v198, v199\n"
"v_mfma_f32_16x16x32_bf16 v[204:207], a[136:139], a[88:91], v[204:207]\n"
"v_exp_f32 v200, v200\n"
"v_mfma_f32_16x16x32_bf16 v[204:207], a[140:143], a[92:95], v[204:207]\n"
"v_exp_f32 v201, v201\n"
"s_waitcnt lgkmcnt(0)\n"
"v_mfma_f32_16x16x32_bf16 v[192:195], a[96:99], a[0:3], v[208:211]\n"
"v_cvt_pk_bf16_f32 v146, v200, v201\n"
"ds_read_b128 a[120:123], v227 offset:18432\n"
"v_mfma_f32_16x16x32_bf16 v[192:195], a[100:103], a[4:7], v[192:195]\n"
"v_exp_f32 v202, v202\n"
"v_mfma_f32_16x16x32_bf16 v[192:195], a[104:107], a[8:11], v[192:195]\n"
"v_exp_f32 v203, v203\n"
"v_mfma_f32_16x16x32_bf16 v[192:195], a[108:111], a[12:15], v[192:195]\n"
"v_cvt_pk_bf16_f32 v147, v202, v203\n"
"ds_read_b128 a[124:127], v228 offset:18432\n"
"v_mfma_f32_16x16x32_bf16 v[192:195], a[112:115], a[16:19], v[192:195]\n"
"v_exp_f32 v204, v204\n"
"v_mfma_f32_16x16x32_bf16 v[192:195], a[116:119], a[20:23], v[192:195]\n"
"v_exp_f32 v205, v205\n"
"v_mfma_f32_16x16x32_bf16 v[196:199], a[96:99], a[24:27], v[212:215]\n"
"v_cvt_pk_bf16_f32 v154, v204, v205\n"
"ds_read_b128 a[128:131], v227 offset:19456\n"
"v_mfma_f32_16x16x32_bf16 v[196:199], a[100:103], a[28:31], v[196:199]\n"
"v_exp_f32 v206, v206\n"
"v_mfma_f32_16x16x32_bf16 v[196:199], a[104:107], a[32:35], v[196:199]\n"
"v_exp_f32 v207, v207\n"
"v_mfma_f32_16x16x32_bf16 v[196:199], a[108:111], a[36:39], v[196:199]\n"
"v_cvt_pk_bf16_f32 v155, v206, v207\n"
"ds_read_b128 a[132:135], v228 offset:19456\n"
"v_mfma_f32_16x16x32_bf16 v[196:199], a[112:115], a[40:43], v[196:199]\n"
"v_exp_f32 v192, v192\n"
"v_mfma_f32_16x16x32_bf16 v[196:199], a[116:119], a[44:47], v[196:199]\n"
"v_exp_f32 v193, v193\n"
"v_mfma_f32_16x16x32_bf16 v[200:203], a[96:99], a[48:51], v[216:219]\n"
"v_cvt_pk_bf16_f32 v132, v192, v193\n"
"ds_read_b128 a[136:139], v227 offset:20480\n"
"v_mfma_f32_16x16x32_bf16 v[200:203], a[100:103], a[52:55], v[200:203]\n"
"v_exp_f32 v194, v194\n"
"v_mfma_f32_16x16x32_bf16 v[200:203], a[104:107], a[56:59], v[200:203]\n"
"v_exp_f32 v195, v195\n"
"v_mfma_f32_16x16x32_bf16 v[200:203], a[108:111], a[60:63], v[200:203]\n"
"v_cvt_pk_bf16_f32 v133, v194, v195\n"
"ds_read_b128 a[140:143], v228 offset:20480\n"
"v_mfma_f32_16x16x32_bf16 v[200:203], a[112:115], a[64:67], v[200:203]\n"
"v_exp_f32 v196, v196\n"
"v_mfma_f32_16x16x32_bf16 v[200:203], a[116:119], a[68:71], v[200:203]\n"
"v_exp_f32 v197, v197\n"
"v_mfma_f32_16x16x32_bf16 v[204:207], a[96:99], a[72:75], v[220:223]\n"
"v_cvt_pk_bf16_f32 v140, v196, v197\n"
"v_mfma_f32_16x16x32_bf16 v[204:207], a[100:103], a[76:79], v[204:207]\n"
"v_exp_f32 v198, v198\n"
"v_mfma_f32_16x16x32_bf16 v[204:207], a[104:107], a[80:83], v[204:207]\n"
"v_exp_f32 v199, v199\n"
"v_mfma_f32_16x16x32_bf16 v[204:207], a[108:111], a[84:87], v[204:207]\n"
"v_cvt_pk_bf16_f32 v141, v198, v199\n"
"v_mfma_f32_16x16x32_bf16 v[204:207], a[112:115], a[88:91], v[204:207]\n"
"v_exp_f32 v200, v200\n"
"v_mfma_f32_16x16x32_bf16 v[204:207], a[116:119], a[92:95], v[204:207]\n"
"v_exp_f32 v201, v201\n"
"s_waitcnt lgkmcnt(0)\n"
"v_mfma_f32_16x16x32_bf16 v[192:195], a[120:123], a[0:3], v[208:211]\n"
"v_cvt_pk_bf16_f32 v148, v200, v201\n"
"v_mfma_f32_16x16x32_bf16 v[192:195], a[124:127], a[4:7], v[192:195]\n"
"v_exp_f32 v202, v202\n"
"v_mfma_f32_16x16x32_bf16 v[192:195], a[128:131], a[8:11], v[192:195]\n"
"v_exp_f32 v203, v203\n"
"v_mfma_f32_16x16x32_bf16 v[192:195], a[132:135], a[12:15], v[192:195]\n"
"v_cvt_pk_bf16_f32 v149, v202, v203\n"
"v_mfma_f32_16x16x32_bf16 v[192:195], a[136:139], a[16:19], v[192:195]\n"
"v_exp_f32 v204, v204\n"
"v_mfma_f32_16x16x32_bf16 v[192:195], a[140:143], a[20:23], v[192:195]\n"
"v_exp_f32 v205, v205\n"
"s_waitcnt vmcnt(0) lgkmcnt(0)\n"
"s_barrier\n"
"v_mfma_f32_16x16x32_bf16 v[196:199], a[120:123], a[24:27], v[212:215]\n"
"v_cvt_pk_bf16_f32 v156, v204, v205\n"
"ds_read_b128 a[96:99], v227 offset:24576\n"
"v_mfma_f32_16x16x32_bf16 v[196:199], a[124:127], a[28:31], v[196:199]\n"
"v_exp_f32 v206, v206\n"
"v_mfma_f32_16x16x32_bf16 v[196:199], a[128:131], a[32:35], v[196:199]\n"
"v_exp_f32 v207, v207\n"
"v_mfma_f32_16x16x32_bf16 v[196:199], a[132:135], a[36:39], v[196:199]\n"
"v_cvt_pk_bf16_f32 v157, v206, v207\n"
"ds_read_b128 a[100:103], v228 offset:24576\n"
"v_mfma_f32_16x16x32_bf16 v[196:199], a[136:139], a[40:43], v[196:199]\n"
"v_exp_f32 v192, v192\n"
"v_mfma_f32_16x16x32_bf16 v[196:199], a[140:143], a[44:47], v[196:199]\n"
"v_exp_f32 v193, v193\n"
"v_mfma_f32_16x16x32_bf16 v[200:203], a[120:123], a[48:51], v[216:219]\n"
"v_cvt_pk_bf16_f32 v134, v192, v193\n"
"ds_read_b128 a[104:107], v227 offset:25600\n"
"v_mfma_f32_16x16x32_bf16 v[200:203], a[124:127], a[52:55], v[200:203]\n"
"v_exp_f32 v194, v194\n"
"v_mfma_f32_16x16x32_bf16 v[200:203], a[128:131], a[56:59], v[200:203]\n"
"v_exp_f32 v195, v195\n"
"v_mfma_f32_16x16x32_bf16 v[200:203], a[132:135], a[60:63], v[200:203]\n"
"v_cvt_pk_bf16_f32 v135, v194, v195\n"
"ds_read_b128 a[108:111], v228 offset:25600\n"
"v_mfma_f32_16x16x32_bf16 v[200:203], a[136:139], a[64:67], v[200:203]\n"
"v_exp_f32 v196, v196\n"
"v_mfma_f32_16x16x32_bf16 v[200:203], a[140:143], a[68:71], v[200:203]\n"
"v_exp_f32 v197, v197\n"
"v_mfma_f32_16x16x32_bf16 v[204:207], a[120:123], a[72:75], v[220:223]\n"
"v_cvt_pk_bf16_f32 v142, v196, v197\n"
"ds_read_b128 a[112:115], v227 offset:26624\n"
"v_mfma_f32_16x16x32_bf16 v[204:207], a[124:127], a[76:79], v[204:207]\n"
"v_exp_f32 v198, v198\n"
"v_mfma_f32_16x16x32_bf16 v[204:207], a[128:131], a[80:83], v[204:207]\n"
"v_exp_f32 v199, v199\n"
"v_mfma_f32_16x16x32_bf16 v[204:207], a[132:135], a[84:87], v[204:207]\n"
"v_cvt_pk_bf16_f32 v143, v198, v199\n"
"v_mfma_f32_16x16x32_bf16 v[204:207], a[136:139], a[88:91], v[204:207]\n"
"ds_read_b128 a[116:119], v228 offset:26624\n"
"v_mfma_f32_16x16x32_bf16 v[204:207], a[140:143], a[92:95], v[204:207]\n"
"ds_read_b64_tr_b16 a[144:145], v229 offset:0\n"
"ds_read_b64_tr_b16 a[146:147], v229 offset:6144\n"
"ds_read_b64_tr_b16 a[148:149], v229 offset:12288\n"
"ds_read_b64_tr_b16 a[150:151], v229 offset:18432\n"
"ds_read_b64_tr_b16 a[152:153], v230 offset:0\n"
"ds_read_b64_tr_b16 a[154:155], v230 offset:6144\n"
"ds_read_b64_tr_b16 a[156:157], v230 offset:12288\n"
"ds_read_b64_tr_b16 a[158:159], v230 offset:18432\n"
"v_exp_f32 v200, v200\n"
"v_exp_f32 v201, v201\n"
"v_exp_f32 v202, v202\n"
"v_exp_f32 v203, v203\n"
"v_exp_f32 v204, v204\n"
"v_cvt_pk_bf16_f32 v150, v200, v201\n"
"v_cvt_pk_bf16_f32 v151, v202, v203\n"
"s_add_u32 s16, s16, 24576\n"
"s_addc_u32 s17, s17, 0\n"
"s_sub_u32 s18, s18, 24576\n"
"s_waitcnt lgkmcnt(0)\n"
"s_nop 7\n"
"s_branch L_it0_%=\n"
    :: "s"(__builtin_amdgcn_kernarg_segment_ptr()), "s"(wid), "s"(bid) : "v0","v1","v2","v3","v4","v5","v6","v7","v8","v9","v10","v11","v12","v13","v14","v15","v16","v17","v18","v19","v20","v21","v22","v23","v24","v25","v26","v27","v28","v29","v30","v31","v32","v33","v34","v35","v36","v37","v38","v39","v40","v41","v42","v43","v44","v45","v46","v47","v48","v49","v50","v51","v52","v53","v54","v55","v56","v57","v58","v59","v60","v61","v62","v63","v64","v65","v66","v67","v68","v69","v70","v71","v72","v73","v74","v75","v76","v77","v78","v79","v80","v81","v82","v83","v84","v85","v86","v87","v88","v89","v90","v91","v92","v93","v94","v95","v96","v97","v98","v99","v100","v101","v102","v103","v104","v105","v106","v107","v108","v109","v110","v111","v112","v113","v114","v115","v116","v117","v118","v119","v120","v121","v122","v123","v124","v125","v126","v127","v128","v129","v130","v131","v132","v133","v134","v135","v136","v137","v138","v139","v140","v141","v142","v143","v144","v145","v146","v147","v148","v149","v150","v151","v152","v153","v154","v155","v156","v157","v158","v159","v160","v161","v162","v163","v164","v165","v166","v167","v168","v169","v170","v171","v172","v173","v174","v175","v176","v177","v178","v179","v180","v181","v182","v183","v184","v185","v186","v187","v188","v189","v190","v191","v192","v193","v194","v195","v196","v197","v198","v199","v200","v201","v202","v203","v204","v205","v206","v207","v208","v209","v210","v211","v212","v213","v214","v215","v216","v217","v218","v219","v220","v221","v222","v223","v224","v225","v226","v227","v228","v229","v230","v231","v232","v233","v234","v235","v236","v237","v238","v239","v240","v241","v242","v243","v244","v245","v246","v247","v248","v249","v250","v251","v252","v253","v254","v255","a0","a1","a2","a3","a4","a5","a6","a7","a8","a9","a10","a11","a12","a13","a14","a15","a16","a17","a18","a19","a20","a21","a22","a23","a24","a25","a26","a27","a28","a29","a30","a31","a32","a33","a34","a35","a36","a37","a38","a39","a40","a41","a42","a43","a44","a45","a46","a47","a48","a49","a50","a51","a52","a53","a54","a55","a56","a57","a58","a59","a60","a61","a62","a63","a64","a65","a66","a67","a68","a69","a70","a71","a72","a73","a74","a75","a76","a77","a78","a79","a80","a81","a82","a83","a84","a85","a86","a87","a88","a89","a90","a91","a92","a93","a94","a95","a96","a97","a98","a99","a100","a101","a102","a103","a104","a105","a106","a107","a108","a109","a110","a111","a112","a113","a114","a115","a116","a117","a118","a119","a120","a121","a122","a123","a124","a125","a126","a127","a128","a129","a130","a131","a132","a133","a134","a135","a136","a137","a138","a139","a140","a141","a142","a143","a144","a145","a146","a147","a148","a149","a150","a151","a152","a153","a154","a155","a156","a157","a158","a159","a160","a161","a162","a163","a164","a165","a166","a167","a168","a169","a170","a171","a172","a173","a174","a175","a176","a177","a178","a179","a180","a181","a182","a183","a184","a185","a186","a187","a188","a189","a190","a191","a192","a193","a194","a195","a196","a197","a198","a199","a200","a201","a202","a203","a204","a205","a206","a207","a208","a209","a210","a211","a212","a213","a214","a215","a216","a217","a218","a219","a220","a221","a222","a223","a224","a225","a226","a227","a228","a229","a230","a231","a232","a233","a234","a235","a236","a237","a238","a239","a240","a241","a242","a243","a244","a245","a246","a247","a248","a249","a250","a251","a252","a253","a254","a255","s4","s5","s6","s7","s8","s9","s10","s11","s12","s13","s14","s15","s16","s17","s18","s19","s20","s21","s22","s23","s24","s25","s26","s27","s28","s29","s30","s31","s33","s34","s35","s36","s37","s38","s39","s40","s41","s42","s43","s44","s45","s46","s47","s48","s49","s50","s51","s52","s53","s54","s55","s56","s57","s58","s59","s60","s61","s62","s63","s64","s65","s66","s67","s68","s69","s70","s71","s72","s73","s74","s75","s76","s77","s78","s79","s80","s81","s82","s83","s84","s85","s86","s87","s88","s89","s90","s91","s92","s93","s94","s95","s96","s97","s98","s99","memory","scc","vcc");
}

extern "C" void kernel_launch(void* const* d_in, const int* in_sizes, int n_in, void* d_out, int out_size, void* d_ws, size_t ws_size,
                              hipStream_t stream) {
  (void)in_sizes; (void)n_in; (void)out_size; (void)ws_size;
  (void)hipFuncSetAttribute((const void*)fused_kernel, hipFuncAttributeMaxDynamicSharedMemorySize, 163840);
  fused_kernel<<<256, 256, 163840, stream>>>(d_in[0], d_in[1], d_in[2], d_in[3], d_in[4], d_in[8], d_in[9], d_out, d_ws, d_in[6], d_in[7]);
}
```
